```python
import torch

def _softmax(x, axis=-1):
    return torch.softmax(x, dim=axis)
def _gelu(x):
    return torch.nn.functional.gelu(x, approximate="tanh")
def _silu(x):
    return torch.nn.functional.silu(x)
def _sigmoid(x):
    return torch.sigmoid(x)
def _layernorm(x, axis=-1, eps=1e-5):
    m = x.mean(dim=axis, keepdim=True)
    v = x.var(dim=axis, keepdim=True, unbiased=False)
    return (x - m) / torch.sqrt(v + eps)
def _scatter_add(tgt, idx, src, axis=0):
    return tgt.clone().index_add_(axis, idx, src)
def _segment_sum(src, seg, n):
    out = torch.zeros((n,) + src.shape[1:], dtype=src.dtype)
    return out.index_add_(0, seg, src)
def _pad(x, pw):
    flat = tuple(v for pair in reversed(pw) for v in pair)
    return torch.nn.functional.pad(x, flat)

kernel_name = 'gated_mix__ssm_conv_s5__moe_arch_mod__prompt'

def setup_inputs():

    _w = (lambda a: (lambda t: t.to(torch.bfloat16) if t.is_floating_point() else t)(torch.as_tensor(a)))
    def _A_setup():
        B, S, D = 1, 2048, 1024
        P = D
        Lambda_re = -0.5 * (lambda _x: torch.exp(torch.as_tensor(_x)))((lambda *s: torch.randn(*s, dtype=torch.bfloat16) * 0.8)(P))
        Lambda_im = (lambda *s: torch.randn(*s, dtype=torch.bfloat16) * 0.8)(P) * 1.0
        B_re = (lambda *s: torch.randn(*s, dtype=torch.bfloat16) * 0.8)(P, D) * (D ** -0.5)
        B_im = (lambda *s: torch.randn(*s, dtype=torch.bfloat16) * 0.8)(P, D) * (D ** -0.5)
        C_re = (lambda *s: torch.randn(*s, dtype=torch.bfloat16) * 0.8)(D, P) * (P ** -0.5)
        C_im = (lambda *s: torch.randn(*s, dtype=torch.bfloat16) * 0.8)(D, P) * (P ** -0.5)
        log_dt = (lambda *s: torch.randn(*s, dtype=torch.bfloat16) * 0.8)(P) * 0.3 - 2.0
        Dskip = (lambda *s: torch.randn(*s, dtype=torch.bfloat16) * 0.8)(D)
        return {
            "x": _w((lambda *s: torch.randn(*s, dtype=torch.bfloat16) * 0.05)(B, S, D)),
            "Lambda_re": _w(Lambda_re),
            "Lambda_im": _w(Lambda_im),
            "B_re": _w(B_re),
            "B_im": _w(B_im),
            "C_re": _w(C_re),
            "C_im": _w(C_im),
            "log_dt": _w(log_dt),
            "Dskip": _w(Dskip),
        }
    _A_in = _A_setup()

    def _B_setup():
        B, S, D, F, C = 1, 2048, 1024, 3584, 8
        return {
            "x": _w((lambda *s: torch.randn(*s, dtype=torch.bfloat16) * 0.05)(B, S, D)),
            "w_r": _w((lambda *s: torch.randn(*s, dtype=torch.bfloat16) * 1.6)(D)),
            "w_gate": _w((lambda *s: torch.randn(*s, dtype=torch.bfloat16) * 0.1)(D, F)),
            "w_up": _w((lambda *s: torch.randn(*s, dtype=torch.bfloat16) * 0.1)(D, F)),
            "w_down": _w((lambda *s: torch.randn(*s, dtype=torch.bfloat16) * 0.1)(F, D)),
        }
    _B_in = _B_setup()

    return {
        "A_x": _A_in["x"],
        "A_Lambda_re": _A_in["Lambda_re"],
        "A_Lambda_im": _A_in["Lambda_im"],
        "A_B_re": _A_in["B_re"],
        "A_B_im": _A_in["B_im"],
        "A_C_re": _A_in["C_re"],
        "A_C_im": _A_in["C_im"],
        "A_log_dt": _A_in["log_dt"],
        "A_Dskip": _A_in["Dskip"],
        "B_x": _B_in["x"],
        "B_w_r": _B_in["w_r"],
        "B_w_gate": _B_in["w_gate"],
        "B_w_up": _B_in["w_up"],
        "B_w_down": _B_in["w_down"],
        "gate_w": _w((lambda *s: torch.randn(*s, dtype=torch.bfloat16) * 0.05)(1024) * 16.0),
    }

def reference(A_x, A_Lambda_re, A_Lambda_im, A_B_re, A_B_im, A_C_re, A_C_im, A_log_dt, A_Dskip, B_x, B_w_r, B_w_gate, B_w_up, B_w_down, gate_w):
    def _A_ref(x, Lambda_re, Lambda_im, B_re, B_im, C_re, C_im, log_dt, Dskip):
        Bsz, L, H = x.shape
        Lam = Lambda_re + 1j * Lambda_im
        Bmat = B_re + 1j * B_im
        Cmat = C_re + 1j * C_im
        dt = torch.exp(log_dt)
        Lam_bar = torch.exp(dt * Lam)
        B_bar = ((Lam_bar - 1.0) / Lam)[:, None] * Bmat
        Bu = torch.einsum("bld,pd->blp", x + 0j, B_bar)
        h = torch.zeros_like(Bu[:, 0, :])
        hs = []
        for t in range(L):
            h = Lam_bar[None, :] * h + Bu[:, t, :]
            hs.append(h)
        hseq = torch.stack(hs, 1)
        y = 2.0 * torch.einsum("blp,dp->bld", hseq, Cmat).real + x * Dskip[None, None, :]
        return y

    def _B_ref(x, w_r, w_gate, w_up, w_down):
        B, S, D = x.shape
        C = 8
        r = x @ w_r
        idx = torch.argsort(-r, axis=-1)[:, :C]
        idx_e = (lambda x, n, axis: torch.repeat_interleave(x, n, dim=axis))(idx.reshape(B, C, 1), D, axis=2)
        x_sel = (lambda x, i, axis: torch.take_along_dim(x, i, dim=axis))(x, idx_e, axis=1)
        g = _silu(x_sel @ w_gate)
        u = x_sel @ w_up
        y = (g * u) @ w_down
        r_sel = _sigmoid((lambda x, i, axis: torch.take_along_dim(x, i, dim=axis))(r, idx, axis=1))
        y = y * r_sel.reshape(B, C, 1)
        b_off = (torch.arange(B) * S).reshape(B, 1)
        flat_idx = (idx + b_off).reshape(B * C)
        zero = (x * 0.0).reshape(B * S, D)
        upd = _scatter_add(zero, flat_idx, y.reshape(B * C, D), axis=0)
        return x + upd.reshape(B, S, D)

    x = A_x
    g = _sigmoid((x * gate_w).sum(-1, keepdims=True))
    a = _A_ref(x, A_Lambda_re, A_Lambda_im, A_B_re, A_B_im, A_C_re, A_C_im, A_log_dt, A_Dskip)
    b = _B_ref(x, B_w_r, B_w_gate, B_w_up, B_w_down)
    return g * a + (1 - g) * b
```

```cpp
#include <hip/hip_runtime.h>
#include <hip/hip_bf16.h>
#include <stdint.h>

typedef __attribute__((ext_vector_type(8))) short bf16x8;
typedef __attribute__((ext_vector_type(4))) float f32x4;
typedef __attribute__((ext_vector_type(2))) float f32x2;
typedef __attribute__((ext_vector_type(4))) unsigned u32x4;
typedef __attribute__((ext_vector_type(2))) unsigned u32x2;
typedef __attribute__((ext_vector_type(32))) unsigned u32x32;
typedef unsigned short u16;
typedef unsigned long long u64;
typedef __attribute__((ext_vector_type(8))) unsigned long long u64x8;
#define LAS __attribute__((address_space(3)))
#define DEV __device__ __forceinline__

#define ASM_GEMM1 \
  "s_mov_b32 %[it], 0\n\t" \
  "s_barrier\n\t" \
  "ds_read_b128 %[a00], %[pa0] offset:0\n\t" \
  "ds_read_b128 %[a01], %[pa0] offset:2048\n\t" \
  "ds_read_b128 %[b0], %[pb0] offset:0\n\t" \
  "ds_read_b128 %[b1], %[pb0] offset:2048\n\t" \
  "ds_read_b128 %[b2], %[pb0] offset:4096\n\t" \
  "ds_read_b128 %[b3], %[pb0] offset:6144\n\t" \
  "ds_read_b128 %[b4], %[pb0] offset:8192\n\t" \
  "ds_read_b128 %[b5], %[pb0] offset:10240\n\t" \
  "ds_read_b128 %[b6], %[pb0] offset:12288\n\t" \
  "ds_read_b128 %[b7], %[pb0] offset:14336\n\t" \
  "Lg1_%=:\n\t" \
  "ds_read_b128 %[a10], %[pa1] offset:0\n\t" \
  "ds_read_b128 %[a11], %[pa1] offset:2048\n\t" \
  "s_waitcnt lgkmcnt(8)\n\t" \
  "v_mfma_f32_16x16x32_bf16 %[c00], %[a00], %[b0], %[c00]\n\t" \
  "v_mfma_f32_16x16x32_bf16 %[c10], %[a01], %[b0], %[c10]\n\t" \
  "ds_read_b128 %[b0], %[pb1] offset:0\n\t" \
  "v_mfma_f32_16x16x32_bf16 %[c01], %[a00], %[b1], %[c01]\n\t" \
  "v_mfma_f32_16x16x32_bf16 %[c11], %[a01], %[b1], %[c11]\n\t" \
  "ds_read_b128 %[b1], %[pb1] offset:2048\n\t" \
  "s_waitcnt lgkmcnt(8)\n\t" \
  "v_mfma_f32_16x16x32_bf16 %[c02], %[a00], %[b2], %[c02]\n\t" \
  "v_mfma_f32_16x16x32_bf16 %[c12], %[a01], %[b2], %[c12]\n\t" \
  "ds_read_b128 %[b2], %[pb1] offset:4096\n\t" \
  "v_mfma_f32_16x16x32_bf16 %[c03], %[a00], %[b3], %[c03]\n\t" \
  "v_mfma_f32_16x16x32_bf16 %[c13], %[a01], %[b3], %[c13]\n\t" \
  "ds_read_b128 %[b3], %[pb1] offset:6144\n\t" \
  "s_waitcnt lgkmcnt(8)\n\t" \
  "v_mfma_f32_16x16x32_bf16 %[c04], %[a00], %[b4], %[c04]\n\t" \
  "v_mfma_f32_16x16x32_bf16 %[c14], %[a01], %[b4], %[c14]\n\t" \
  "ds_read_b128 %[b4], %[pb1] offset:8192\n\t" \
  "v_mfma_f32_16x16x32_bf16 %[c05], %[a00], %[b5], %[c05]\n\t" \
  "v_mfma_f32_16x16x32_bf16 %[c15], %[a01], %[b5], %[c15]\n\t" \
  "ds_read_b128 %[b5], %[pb1] offset:10240\n\t" \
  "s_waitcnt lgkmcnt(8)\n\t" \
  "v_mfma_f32_16x16x32_bf16 %[c06], %[a00], %[b6], %[c06]\n\t" \
  "v_mfma_f32_16x16x32_bf16 %[c16], %[a01], %[b6], %[c16]\n\t" \
  "ds_read_b128 %[b6], %[pb1] offset:12288\n\t" \
  "v_mfma_f32_16x16x32_bf16 %[c07], %[a00], %[b7], %[c07]\n\t" \
  "v_mfma_f32_16x16x32_bf16 %[c17], %[a01], %[b7], %[c17]\n\t" \
  "ds_read_b128 %[b7], %[pb1] offset:14336\n\t" \
  "s_waitcnt lgkmcnt(6)\n\t" \
  "v_mfma_f32_16x16x32_bf16 %[c00], %[a10], %[b0], %[c00]\n\t" \
  "v_mfma_f32_16x16x32_bf16 %[c10], %[a11], %[b0], %[c10]\n\t" \
  "v_mfma_f32_16x16x32_bf16 %[c01], %[a10], %[b1], %[c01]\n\t" \
  "v_mfma_f32_16x16x32_bf16 %[c11], %[a11], %[b1], %[c11]\n\t" \
  "s_waitcnt lgkmcnt(4)\n\t" \
  "v_mfma_f32_16x16x32_bf16 %[c02], %[a10], %[b2], %[c02]\n\t" \
  "v_mfma_f32_16x16x32_bf16 %[c12], %[a11], %[b2], %[c12]\n\t" \
  "s_waitcnt lgkmcnt(0)\n\t" \
  "s_barrier\n\t" \
  "ds_read_b128 %[a00], %[pa0] offset:32768\n\t" \
  "ds_read_b128 %[a01], %[pa0] offset:34816\n\t" \
  "v_mfma_f32_16x16x32_bf16 %[c03], %[a10], %[b3], %[c03]\n\t" \
  "v_mfma_f32_16x16x32_bf16 %[c13], %[a11], %[b3], %[c13]\n\t" \
  "ds_read_b128 %[b0], %[pb0] offset:32768\n\t" \
  "ds_read_b128 %[b1], %[pb0] offset:34816\n\t" \
  "v_mfma_f32_16x16x32_bf16 %[c04], %[a10], %[b4], %[c04]\n\t" \
  "v_mfma_f32_16x16x32_bf16 %[c14], %[a11], %[b4], %[c14]\n\t" \
  "ds_read_b128 %[b2], %[pb0] offset:36864\n\t" \
  "ds_read_b128 %[b3], %[pb0] offset:38912\n\t" \
  "v_mfma_f32_16x16x32_bf16 %[c05], %[a10], %[b5], %[c05]\n\t" \
  "v_mfma_f32_16x16x32_bf16 %[c15], %[a11], %[b5], %[c15]\n\t" \
  "ds_read_b128 %[b4], %[pb0] offset:40960\n\t" \
  "ds_read_b128 %[b5], %[pb0] offset:43008\n\t" \
  "v_mfma_f32_16x16x32_bf16 %[c06], %[a10], %[b6], %[c06]\n\t" \
  "v_mfma_f32_16x16x32_bf16 %[c16], %[a11], %[b6], %[c16]\n\t" \
  "ds_read_b128 %[b6], %[pb0] offset:45056\n\t" \
  "v_mfma_f32_16x16x32_bf16 %[c07], %[a10], %[b7], %[c07]\n\t" \
  "v_mfma_f32_16x16x32_bf16 %[c17], %[a11], %[b7], %[c17]\n\t" \
  "ds_read_b128 %[b7], %[pb0] offset:47104\n\t" \
  "ds_read_b128 %[a10], %[pa1] offset:32768\n\t" \
  "ds_read_b128 %[a11], %[pa1] offset:34816\n\t" \
  "s_waitcnt lgkmcnt(8)\n\t" \
  "v_mfma_f32_16x16x32_bf16 %[c00], %[a00], %[b0], %[c00]\n\t" \
  "v_mfma_f32_16x16x32_bf16 %[c10], %[a01], %[b0], %[c10]\n\t" \
  "ds_read_b128 %[b0], %[pb1] offset:32768\n\t" \
  "v_mfma_f32_16x16x32_bf16 %[c01], %[a00], %[b1], %[c01]\n\t" \
  "v_mfma_f32_16x16x32_bf16 %[c11], %[a01], %[b1], %[c11]\n\t" \
  "ds_read_b128 %[b1], %[pb1] offset:34816\n\t" \
  "s_waitcnt lgkmcnt(8)\n\t" \
  "v_mfma_f32_16x16x32_bf16 %[c02], %[a00], %[b2], %[c02]\n\t" \
  "v_mfma_f32_16x16x32_bf16 %[c12], %[a01], %[b2], %[c12]\n\t" \
  "ds_read_b128 %[b2], %[pb1] offset:36864\n\t" \
  "v_mfma_f32_16x16x32_bf16 %[c03], %[a00], %[b3], %[c03]\n\t" \
  "v_mfma_f32_16x16x32_bf16 %[c13], %[a01], %[b3], %[c13]\n\t" \
  "ds_read_b128 %[b3], %[pb1] offset:38912\n\t" \
  "v_xor_b32 %[pa0], 0x10000, %[pa0]\n\t" \
  "s_waitcnt lgkmcnt(8)\n\t" \
  "v_mfma_f32_16x16x32_bf16 %[c04], %[a00], %[b4], %[c04]\n\t" \
  "v_mfma_f32_16x16x32_bf16 %[c14], %[a01], %[b4], %[c14]\n\t" \
  "ds_read_b128 %[b4], %[pb1] offset:40960\n\t" \
  "v_xor_b32 %[pb0], 0x10000, %[pb0]\n\t" \
  "v_mfma_f32_16x16x32_bf16 %[c05], %[a00], %[b5], %[c05]\n\t" \
  "v_mfma_f32_16x16x32_bf16 %[c15], %[a01], %[b5], %[c15]\n\t" \
  "ds_read_b128 %[b5], %[pb1] offset:43008\n\t" \
  "s_waitcnt lgkmcnt(8)\n\t" \
  "v_mfma_f32_16x16x32_bf16 %[c06], %[a00], %[b6], %[c06]\n\t" \
  "v_mfma_f32_16x16x32_bf16 %[c16], %[a01], %[b6], %[c16]\n\t" \
  "ds_read_b128 %[b6], %[pb1] offset:45056\n\t" \
  "s_cmp_eq_u32 %[it], 7\n\t" \
  "v_mfma_f32_16x16x32_bf16 %[c07], %[a00], %[b7], %[c07]\n\t" \
  "v_mfma_f32_16x16x32_bf16 %[c17], %[a01], %[b7], %[c17]\n\t" \
  "ds_read_b128 %[b7], %[pb1] offset:47104\n\t" \
  "s_cbranch_scc1 Lxe_%=\n\t" \
  "Lxb_%=:\n\t" \
  "s_waitcnt lgkmcnt(6)\n\t" \
  "v_mfma_f32_16x16x32_bf16 %[c00], %[a10], %[b0], %[c00]\n\t" \
  "v_mfma_f32_16x16x32_bf16 %[c10], %[a11], %[b0], %[c10]\n\t" \
  "v_mfma_f32_16x16x32_bf16 %[c01], %[a10], %[b1], %[c01]\n\t" \
  "v_mfma_f32_16x16x32_bf16 %[c11], %[a11], %[b1], %[c11]\n\t" \
  "v_xor_b32 %[pa1], 0x10000, %[pa1]\n\t" \
  "s_waitcnt lgkmcnt(4)\n\t" \
  "v_mfma_f32_16x16x32_bf16 %[c02], %[a10], %[b2], %[c02]\n\t" \
  "v_mfma_f32_16x16x32_bf16 %[c12], %[a11], %[b2], %[c12]\n\t" \
  "v_xor_b32 %[pb1], 0x10000, %[pb1]\n\t" \
  "s_waitcnt lgkmcnt(0)\n\t" \
  "s_barrier\n\t" \
  "s_cbranch_scc1 Lt1_%=\n\t" \
  "ds_read_b128 %[a00], %[pa0] offset:0\n\t" \
  "ds_read_b128 %[a01], %[pa0] offset:2048\n\t" \
  "v_mfma_f32_16x16x32_bf16 %[c03], %[a10], %[b3], %[c03]\n\t" \
  "v_mfma_f32_16x16x32_bf16 %[c13], %[a11], %[b3], %[c13]\n\t" \
  "ds_read_b128 %[b0], %[pb0] offset:0\n\t" \
  "ds_read_b128 %[b1], %[pb0] offset:2048\n\t" \
  "v_mfma_f32_16x16x32_bf16 %[c04], %[a10], %[b4], %[c04]\n\t" \
  "v_mfma_f32_16x16x32_bf16 %[c14], %[a11], %[b4], %[c14]\n\t" \
  "ds_read_b128 %[b2], %[pb0] offset:4096\n\t" \
  "ds_read_b128 %[b3], %[pb0] offset:6144\n\t" \
  "v_mfma_f32_16x16x32_bf16 %[c05], %[a10], %[b5], %[c05]\n\t" \
  "v_mfma_f32_16x16x32_bf16 %[c15], %[a11], %[b5], %[c15]\n\t" \
  "ds_read_b128 %[b4], %[pb0] offset:8192\n\t" \
  "ds_read_b128 %[b5], %[pb0] offset:10240\n\t" \
  "v_mfma_f32_16x16x32_bf16 %[c06], %[a10], %[b6], %[c06]\n\t" \
  "v_mfma_f32_16x16x32_bf16 %[c16], %[a11], %[b6], %[c16]\n\t" \
  "ds_read_b128 %[b6], %[pb0] offset:12288\n\t" \
  "s_add_u32 %[it], %[it], 1\n\t" \
  "v_mfma_f32_16x16x32_bf16 %[c07], %[a10], %[b7], %[c07]\n\t" \
  "v_mfma_f32_16x16x32_bf16 %[c17], %[a11], %[b7], %[c17]\n\t" \
  "ds_read_b128 %[b7], %[pb0] offset:14336\n\t" \
  "s_branch Lg1_%=\n\t" \
  "Lxe_%=:\n\t" \
  "ds_read_b64 %[xe00], %[xa0] offset:0\n\t" \
  "ds_read_b64 %[xe01], %[xa1] offset:0\n\t" \
  "ds_read_b64 %[xe02], %[xa2] offset:0\n\t" \
  "ds_read_b64 %[xe03], %[xa3] offset:0\n\t" \
  "ds_read_b64 %[xe10], %[xa0] offset:2048\n\t" \
  "ds_read_b64 %[xe11], %[xa1] offset:2048\n\t" \
  "ds_read_b64 %[xe12], %[xa2] offset:2048\n\t" \
  "ds_read_b64 %[xe13], %[xa3] offset:2048\n\t" \
  "s_branch Lxb_%=\n\t" \
  "Lt1_%=:\n\t" \
  "v_mfma_f32_16x16x32_bf16 %[c03], %[a10], %[b3], %[c03]\n\t" \
  "v_mfma_f32_16x16x32_bf16 %[c13], %[a11], %[b3], %[c13]\n\t" \
  "v_mfma_f32_16x16x32_bf16 %[c04], %[a10], %[b4], %[c04]\n\t" \
  "v_mfma_f32_16x16x32_bf16 %[c14], %[a11], %[b4], %[c14]\n\t" \
  "v_mfma_f32_16x16x32_bf16 %[c05], %[a10], %[b5], %[c05]\n\t" \
  "v_mfma_f32_16x16x32_bf16 %[c15], %[a11], %[b5], %[c15]\n\t" \
  "v_mfma_f32_16x16x32_bf16 %[c06], %[a10], %[b6], %[c06]\n\t" \
  "v_mfma_f32_16x16x32_bf16 %[c16], %[a11], %[b6], %[c16]\n\t" \
  "v_mfma_f32_16x16x32_bf16 %[c07], %[a10], %[b7], %[c07]\n\t" \
  "v_mfma_f32_16x16x32_bf16 %[c17], %[a11], %[b7], %[c17]\n\t" \
  "s_nop 7\n\t"
#define ASM_GEMM2 \
  "s_nop 4\n\t" \
  "s_mov_b32 %[to], 0x40000\n\t" \
  "s_mov_b32 %[cn], 1\n\t" \
  "Lh1_%=:\n\t" \
  "ds_read_b128 v[252:255], %[fa]\n\t" \
  "s_cmp_eq_u32 %[to], 0\n\t" \
  "s_cbranch_scc1 Lh1_%=x\n\t" \
  "s_sub_u32 %[to], %[to], 1\n\t" \
  "s_waitcnt lgkmcnt(0)\n\t" \
  "v_readfirstlane_b32 %[s], v254\n\t" \
  "s_cmp_ge_u32 %[s], 1\n\t" \
  "s_cbranch_scc0 Lh1_%=\n\t" \
  "Lh1_%=x:\n\t" \
  "s_cmp_gt_u32 1, %[own]\n\t" \
  "s_addc_u32 %[hb0], 1, -1\n\t" \
  "s_lshl_b32 %[hb0], %[hb0], 15\n\t" \
  "s_or_b32 %[hb1], %[hb0], 0x2000\n\t" \
  "s_or_b32 %[hb2], %[hb0], 0x4000\n\t" \
  "s_or_b32 %[hb3], %[hb0], 0x6000\n\t" \
  "buffer_load_dwordx4 v[216:219], %[vo], %[srd], %[hb0] offen offset:0 sc1\n\t" \
  "buffer_load_dwordx4 v[220:223], %[vo], %[srd], %[hb0] offen offset:1024 sc1\n\t" \
  "buffer_load_dwordx4 v[224:227], %[vo], %[srd], %[hb1] offen offset:0 sc1\n\t" \
  "buffer_load_dwordx4 v[228:231], %[vo], %[srd], %[hb1] offen offset:1024 sc1\n\t" \
  "buffer_load_dwordx4 v[232:235], %[vo], %[srd], %[hb2] offen offset:0 sc1\n\t" \
  "buffer_load_dwordx4 v[236:239], %[vo], %[srd], %[hb2] offen offset:1024 sc1\n\t" \
  "buffer_load_dwordx4 v[240:243], %[vo], %[srd], %[hb3] offen offset:0 sc1\n\t" \
  "buffer_load_dwordx4 v[244:247], %[vo], %[srd], %[hb3] offen offset:1024 sc1\n\t" \
  "Lh2_%=:\n\t" \
  "ds_read_b128 v[252:255], %[fa]\n\t" \
  "s_cmp_eq_u32 %[to], 0\n\t" \
  "s_cbranch_scc1 Lh2_%=x\n\t" \
  "s_sub_u32 %[to], %[to], 1\n\t" \
  "s_waitcnt lgkmcnt(0)\n\t" \
  "v_readfirstlane_b32 %[s], v254\n\t" \
  "s_cmp_ge_u32 %[s], 2\n\t" \
  "s_cbranch_scc0 Lh2_%=\n\t" \
  "Lh2_%=x:\n\t" \
  "s_cmp_gt_u32 2, %[own]\n\t" \
  "s_addc_u32 %[hb0], 2, -1\n\t" \
  "s_lshl_b32 %[hb0], %[hb0], 15\n\t" \
  "s_or_b32 %[hb1], %[hb0], 0x2000\n\t" \
  "s_or_b32 %[hb2], %[hb0], 0x4000\n\t" \
  "s_or_b32 %[hb3], %[hb0], 0x6000\n\t" \
  "buffer_load_dwordx4 v[184:187], %[vo], %[srd], %[hb0] offen offset:0 sc1\n\t" \
  "buffer_load_dwordx4 v[188:191], %[vo], %[srd], %[hb0] offen offset:1024 sc1\n\t" \
  "buffer_load_dwordx4 v[192:195], %[vo], %[srd], %[hb1] offen offset:0 sc1\n\t" \
  "buffer_load_dwordx4 v[196:199], %[vo], %[srd], %[hb1] offen offset:1024 sc1\n\t" \
  "buffer_load_dwordx4 v[200:203], %[vo], %[srd], %[hb2] offen offset:0 sc1\n\t" \
  "buffer_load_dwordx4 v[204:207], %[vo], %[srd], %[hb2] offen offset:1024 sc1\n\t" \
  "buffer_load_dwordx4 v[208:211], %[vo], %[srd], %[hb3] offen offset:0 sc1\n\t" \
  "buffer_load_dwordx4 v[212:215], %[vo], %[srd], %[hb3] offen offset:1024 sc1\n\t" \
  "Lc0_%=:\n\t" \
  "ds_read_b128 v[252:255], %[fa]\n\t" \
  "s_cmp_eq_u32 %[to], 0\n\t" \
  "s_cbranch_scc1 Lc0_%=x\n\t" \
  "s_sub_u32 %[to], %[to], 1\n\t" \
  "s_waitcnt lgkmcnt(0)\n\t" \
  "v_readfirstlane_b32 %[s], v253\n\t" \
  "s_cmp_ge_u32 %[s], 2\n\t" \
  "s_cbranch_scc0 Lc0_%=\n\t" \
  "Lc0_%=x:\n\t" \
  "ds_read_b128 %[k00], %[pc0] offset:0\n\t" \
  "ds_read_b128 %[k01], %[pc0] offset:2048\n\t" \
  "ds_read_b128 %[k02], %[pc0] offset:4096\n\t" \
  "ds_read_b128 %[k03], %[pc0] offset:6144\n\t" \
  "ds_read_b128 %[k10], %[pc1] offset:0\n\t" \
  "ds_read_b128 %[k11], %[pc1] offset:2048\n\t" \
  "ds_read_b128 %[k12], %[pc1] offset:4096\n\t" \
  "ds_read_b128 %[k13], %[pc1] offset:6144\n\t" \
  "s_mov_b32 %[hn], 3\n\t" \
  "Lh3_%=:\n\t" \
  "ds_read_b128 v[252:255], %[fa]\n\t" \
  "s_cmp_eq_u32 %[to], 0\n\t" \
  "s_cbranch_scc1 Lh3_%=x\n\t" \
  "s_sub_u32 %[to], %[to], 1\n\t" \
  "s_waitcnt lgkmcnt(0)\n\t" \
  "v_readfirstlane_b32 %[s], v254\n\t" \
  "s_cmp_ge_u32 %[s], 3\n\t" \
  "s_cbranch_scc0 Lh3_%=\n\t" \
  "Lh3_%=x:\n\t" \
  "s_branch Lin_%=\n\t" \
  "Lg2_%=:\n\t" \
  "s_waitcnt vmcnt(14) lgkmcnt(7)\n\t" \
  "ds_read_b128 v[252:255], %[fa]\n\t" \
  "v_mfma_f32_16x16x32_bf16 %[c00], v[184:187], %[k00], %[c00]\n\t" \
  "s_cmp_gt_u32 %[hn], %[own]\n\t" \
  "s_addc_u32 %[hb0], %[hn], -1\n\t" \
  "v_mfma_f32_16x16x32_bf16 %[c10], v[188:191], %[k00], %[c10]\n\t" \
  "ds_read_b128 %[k00], %[pc2] offset:0\n\t" \
  "s_waitcnt lgkmcnt(8)\n\t" \
  "v_mfma_f32_16x16x32_bf16 %[c01], v[184:187], %[k01], %[c01]\n\t" \
  "s_lshl_b32 %[hb0], %[hb0], 15\n\t" \
  "v_mfma_f32_16x16x32_bf16 %[c11], v[188:191], %[k01], %[c11]\n\t" \
  "ds_read_b128 %[k01], %[pc2] offset:2048\n\t" \
  "s_waitcnt lgkmcnt(8)\n\t" \
  "v_mfma_f32_16x16x32_bf16 %[c02], v[184:187], %[k02], %[c02]\n\t" \
  "s_or_b32 %[hb1], %[hb0], 0x2000\n\t" \
  "v_mfma_f32_16x16x32_bf16 %[c12], v[188:191], %[k02], %[c12]\n\t" \
  "ds_read_b128 %[k02], %[pc2] offset:4096\n\t" \
  "s_waitcnt lgkmcnt(8)\n\t" \
  "v_mfma_f32_16x16x32_bf16 %[c03], v[184:187], %[k03], %[c03]\n\t" \
  "s_add_u32 %[cn], %[cn], 1\n\t" \
  "v_mfma_f32_16x16x32_bf16 %[c13], v[188:191], %[k03], %[c13]\n\t" \
  "ds_read_b128 %[k03], %[pc2] offset:6144\n\t" \
  "buffer_load_dwordx4 v[184:187], %[vo], %[srd], %[hb0] offen offset:0 sc1\n\t" \
  "s_waitcnt vmcnt(13) lgkmcnt(8)\n\t" \
  "v_mfma_f32_16x16x32_bf16 %[c00], v[192:195], %[k10], %[c00]\n\t" \
  "s_cmp_eq_u32 %[cn], 9\n\t" \
  "s_cselect_b32 %[dl], 0x20000, %[d16]\n\t" \
  "v_mfma_f32_16x16x32_bf16 %[c10], v[196:199], %[k10], %[c10]\n\t" \
  "s_bitcmp1_b32 %[wr], %[cn]\n\t" \
  "s_cselect_b32 %[dl], 0xfffe4000, %[dl]\n\t" \
  "ds_read_b128 %[k10], %[pc3] offset:0\n\t" \
  "s_waitcnt lgkmcnt(8)\n\t" \
  "v_mfma_f32_16x16x32_bf16 %[c01], v[192:195], %[k11], %[c01]\n\t" \
  "v_add_u32 %[pc0], %[dl], %[pc0]\n\t" \
  "v_mfma_f32_16x16x32_bf16 %[c11], v[196:199], %[k11], %[c11]\n\t" \
  "s_or_b32 %[hb2], %[hb0], 0x4000\n\t" \
  "ds_read_b128 %[k11], %[pc3] offset:2048\n\t" \
  "buffer_load_dwordx4 v[188:191], %[vo], %[srd], %[hb0] offen offset:1024 sc1\n\t" \
  "s_waitcnt lgkmcnt(8)\n\t" \
  "v_mfma_f32_16x16x32_bf16 %[c02], v[192:195], %[k12], %[c02]\n\t" \
  "v_add_u32 %[pc1], %[dl], %[pc1]\n\t" \
  "v_mfma_f32_16x16x32_bf16 %[c12], v[196:199], %[k12], %[c12]\n\t" \
  "v_add_u32 %[fv], 1, %[fv]\n\t" \
  "ds_read_b128 %[k12], %[pc3] offset:4096\n\t" \
  "s_waitcnt lgkmcnt(7)\n\t" \
  "v_mfma_f32_16x16x32_bf16 %[c03], v[192:195], %[k13], %[c03]\n\t" \
  "v_readfirstlane_b32 %[s], v253\n\t" \
  "v_mfma_f32_16x16x32_bf16 %[c13], v[196:199], %[k13], %[c13]\n\t" \
  "ds_read_b128 %[k13], %[pc3] offset:6144\n\t" \
  "ds_write_b32 %[fw], %[fv]\n\t" \
  "s_cmp_gt_u32 %[s], %[cn]\n\t" \
  "s_cbranch_scc0 Lcs0_%=\n\t" \
  "Lcb0_%=:\n\t" \
  "buffer_load_dwordx4 v[192:195], %[vo], %[srd], %[hb1] offen offset:0 sc1\n\t" \
  "s_waitcnt vmcnt(13) lgkmcnt(8)\n\t" \
  "ds_read_b128 v[252:255], %[fa]\n\t" \
  "v_mfma_f32_16x16x32_bf16 %[c00], v[200:203], %[k00], %[c00]\n\t" \
  "s_or_b32 %[hb3], %[hb0], 0x6000\n\t" \
  "v_mfma_f32_16x16x32_bf16 %[c10], v[204:207], %[k00], %[c10]\n\t" \
  "ds_read_b128 %[k00], %[pc0] offset:0\n\t" \
  "s_waitcnt lgkmcnt(9)\n\t" \
  "v_mfma_f32_16x16x32_bf16 %[c01], v[200:203], %[k01], %[c01]\n\t" \
  "v_mfma_f32_16x16x32_bf16 %[c11], v[204:207], %[k01], %[c11]\n\t" \
  "ds_read_b128 %[k01], %[pc0] offset:2048\n\t" \
  "buffer_load_dwordx4 v[196:199], %[vo], %[srd], %[hb1] offen offset:1024 sc1\n\t" \
  "s_waitcnt lgkmcnt(9)\n\t" \
  "v_mfma_f32_16x16x32_bf16 %[c02], v[200:203], %[k02], %[c02]\n\t" \
  "v_mfma_f32_16x16x32_bf16 %[c12], v[204:207], %[k02], %[c12]\n\t" \
  "ds_read_b128 %[k02], %[pc0] offset:4096\n\t" \
  "s_waitcnt lgkmcnt(9)\n\t" \
  "v_mfma_f32_16x16x32_bf16 %[c03], v[200:203], %[k03], %[c03]\n\t" \
  "v_mfma_f32_16x16x32_bf16 %[c13], v[204:207], %[k03], %[c13]\n\t" \
  "ds_read_b128 %[k03], %[pc0] offset:6144\n\t" \
  "buffer_load_dwordx4 v[200:203], %[vo], %[srd], %[hb2] offen offset:0 sc1\n\t" \
  "s_waitcnt vmcnt(13) lgkmcnt(9)\n\t" \
  "v_mfma_f32_16x16x32_bf16 %[c00], v[208:211], %[k10], %[c00]\n\t" \
  "v_add_u32 %[pc2], %[dl], %[pc2]\n\t" \
  "v_mfma_f32_16x16x32_bf16 %[c10], v[212:215], %[k10], %[c10]\n\t" \
  "ds_read_b128 %[k10], %[pc1] offset:0\n\t" \
  "s_waitcnt lgkmcnt(9)\n\t" \
  "v_mfma_f32_16x16x32_bf16 %[c01], v[208:211], %[k11], %[c01]\n\t" \
  "v_add_u32 %[pc3], %[dl], %[pc3]\n\t" \
  "v_mfma_f32_16x16x32_bf16 %[c11], v[212:215], %[k11], %[c11]\n\t" \
  "ds_read_b128 %[k11], %[pc1] offset:2048\n\t" \
  "buffer_load_dwordx4 v[204:207], %[vo], %[srd], %[hb2] offen offset:1024 sc1\n\t" \
  "s_waitcnt lgkmcnt(6)\n\t" \
  "v_mfma_f32_16x16x32_bf16 %[c02], v[208:211], %[k12], %[c02]\n\t" \
  "v_readfirstlane_b32 %[s], v254\n\t" \
  "v_mfma_f32_16x16x32_bf16 %[c12], v[212:215], %[k12], %[c12]\n\t" \
  "ds_read_b128 %[k12], %[pc1] offset:4096\n\t" \
  "v_mfma_f32_16x16x32_bf16 %[c03], v[208:211], %[k13], %[c03]\n\t" \
  "s_add_u32 %[hn], %[hn], 1\n\t" \
  "v_mfma_f32_16x16x32_bf16 %[c13], v[212:215], %[k13], %[c13]\n\t" \
  "ds_read_b128 %[k13], %[pc1] offset:6144\n\t" \
  "buffer_load_dwordx4 v[208:211], %[vo], %[srd], %[hb3] offen offset:0 sc1\n\t" \
  "buffer_load_dwordx4 v[212:215], %[vo], %[srd], %[hb3] offen offset:1024 sc1\n\t" \
  "s_cmp_ge_u32 %[s], %[hn]\n\t" \
  "s_cbranch_scc0 Lhs0_%=\n\t" \
  "Lhb0_%=:\n\t" \
  "Lin_%=:\n\t" \
  "s_waitcnt vmcnt(14) lgkmcnt(7)\n\t" \
  "ds_read_b128 v[252:255], %[fa]\n\t" \
  "v_mfma_f32_16x16x32_bf16 %[c00], v[216:219], %[k00], %[c00]\n\t" \
  "s_cmp_gt_u32 %[hn], %[own]\n\t" \
  "s_addc_u32 %[hb0], %[hn], -1\n\t" \
  "v_mfma_f32_16x16x32_bf16 %[c10], v[220:223], %[k00], %[c10]\n\t" \
  "ds_read_b128 %[k00], %[pc2] offset:0\n\t" \
  "s_waitcnt lgkmcnt(8)\n\t" \
  "v_mfma_f32_16x16x32_bf16 %[c01], v[216:219], %[k01], %[c01]\n\t" \
  "s_lshl_b32 %[hb0], %[hb0], 15\n\t" \
  "v_mfma_f32_16x16x32_bf16 %[c11], v[220:223], %[k01], %[c11]\n\t" \
  "ds_read_b128 %[k01], %[pc2] offset:2048\n\t" \
  "s_waitcnt lgkmcnt(8)\n\t" \
  "v_mfma_f32_16x16x32_bf16 %[c02], v[216:219], %[k02], %[c02]\n\t" \
  "s_or_b32 %[hb1], %[hb0], 0x2000\n\t" \
  "v_mfma_f32_16x16x32_bf16 %[c12], v[220:223], %[k02], %[c12]\n\t" \
  "ds_read_b128 %[k02], %[pc2] offset:4096\n\t" \
  "s_waitcnt lgkmcnt(8)\n\t" \
  "v_mfma_f32_16x16x32_bf16 %[c03], v[216:219], %[k03], %[c03]\n\t" \
  "s_add_u32 %[cn], %[cn], 1\n\t" \
  "v_mfma_f32_16x16x32_bf16 %[c13], v[220:223], %[k03], %[c13]\n\t" \
  "ds_read_b128 %[k03], %[pc2] offset:6144\n\t" \
  "buffer_load_dwordx4 v[216:219], %[vo], %[srd], %[hb0] offen offset:0 sc1\n\t" \
  "s_waitcnt vmcnt(13) lgkmcnt(8)\n\t" \
  "v_mfma_f32_16x16x32_bf16 %[c00], v[224:227], %[k10], %[c00]\n\t" \
  "s_cmp_eq_u32 %[cn], 9\n\t" \
  "s_cselect_b32 %[dl], 0x20000, %[d16]\n\t" \
  "v_mfma_f32_16x16x32_bf16 %[c10], v[228:231], %[k10], %[c10]\n\t" \
  "s_bitcmp1_b32 %[wr], %[cn]\n\t" \
  "s_cselect_b32 %[dl], 0xfffe4000, %[dl]\n\t" \
  "ds_read_b128 %[k10], %[pc3] offset:0\n\t" \
  "s_waitcnt lgkmcnt(8)\n\t" \
  "v_mfma_f32_16x16x32_bf16 %[c01], v[224:227], %[k11], %[c01]\n\t" \
  "v_add_u32 %[pc0], %[dl], %[pc0]\n\t" \
  "v_mfma_f32_16x16x32_bf16 %[c11], v[228:231], %[k11], %[c11]\n\t" \
  "s_or_b32 %[hb2], %[hb0], 0x4000\n\t" \
  "ds_read_b128 %[k11], %[pc3] offset:2048\n\t" \
  "buffer_load_dwordx4 v[220:223], %[vo], %[srd], %[hb0] offen offset:1024 sc1\n\t" \
  "s_waitcnt lgkmcnt(8)\n\t" \
  "v_mfma_f32_16x16x32_bf16 %[c02], v[224:227], %[k12], %[c02]\n\t" \
  "v_add_u32 %[pc1], %[dl], %[pc1]\n\t" \
  "v_mfma_f32_16x16x32_bf16 %[c12], v[228:231], %[k12], %[c12]\n\t" \
  "v_add_u32 %[fv], 1, %[fv]\n\t" \
  "ds_read_b128 %[k12], %[pc3] offset:4096\n\t" \
  "s_waitcnt lgkmcnt(7)\n\t" \
  "v_mfma_f32_16x16x32_bf16 %[c03], v[224:227], %[k13], %[c03]\n\t" \
  "v_readfirstlane_b32 %[s], v252\n\t" \
  "v_mfma_f32_16x16x32_bf16 %[c13], v[228:231], %[k13], %[c13]\n\t" \
  "ds_read_b128 %[k13], %[pc3] offset:6144\n\t" \
  "ds_write_b32 %[fw], %[fv]\n\t" \
  "s_cmp_gt_u32 %[s], %[cn]\n\t" \
  "s_cbranch_scc0 Lcs1_%=\n\t" \
  "Lcb1_%=:\n\t" \
  "buffer_load_dwordx4 v[224:227], %[vo], %[srd], %[hb1] offen offset:0 sc1\n\t" \
  "s_waitcnt vmcnt(13) lgkmcnt(8)\n\t" \
  "ds_read_b128 v[252:255], %[fa]\n\t" \
  "v_mfma_f32_16x16x32_bf16 %[c00], v[232:235], %[k00], %[c00]\n\t" \
  "s_or_b32 %[hb3], %[hb0], 0x6000\n\t" \
  "v_mfma_f32_16x16x32_bf16 %[c10], v[236:239], %[k00], %[c10]\n\t" \
  "ds_read_b128 %[k00], %[pc0] offset:0\n\t" \
  "s_waitcnt lgkmcnt(9)\n\t" \
  "v_mfma_f32_16x16x32_bf16 %[c01], v[232:235], %[k01], %[c01]\n\t" \
  "v_mfma_f32_16x16x32_bf16 %[c11], v[236:239], %[k01], %[c11]\n\t" \
  "ds_read_b128 %[k01], %[pc0] offset:2048\n\t" \
  "buffer_load_dwordx4 v[228:231], %[vo], %[srd], %[hb1] offen offset:1024 sc1\n\t" \
  "s_waitcnt lgkmcnt(9)\n\t" \
  "v_mfma_f32_16x16x32_bf16 %[c02], v[232:235], %[k02], %[c02]\n\t" \
  "v_mfma_f32_16x16x32_bf16 %[c12], v[236:239], %[k02], %[c12]\n\t" \
  "ds_read_b128 %[k02], %[pc0] offset:4096\n\t" \
  "s_waitcnt lgkmcnt(9)\n\t" \
  "v_mfma_f32_16x16x32_bf16 %[c03], v[232:235], %[k03], %[c03]\n\t" \
  "v_mfma_f32_16x16x32_bf16 %[c13], v[236:239], %[k03], %[c13]\n\t" \
  "ds_read_b128 %[k03], %[pc0] offset:6144\n\t" \
  "buffer_load_dwordx4 v[232:235], %[vo], %[srd], %[hb2] offen offset:0 sc1\n\t" \
  "s_waitcnt vmcnt(13) lgkmcnt(9)\n\t" \
  "v_mfma_f32_16x16x32_bf16 %[c00], v[240:243], %[k10], %[c00]\n\t" \
  "v_add_u32 %[pc2], %[dl], %[pc2]\n\t" \
  "v_mfma_f32_16x16x32_bf16 %[c10], v[244:247], %[k10], %[c10]\n\t" \
  "ds_read_b128 %[k10], %[pc1] offset:0\n\t" \
  "s_waitcnt lgkmcnt(9)\n\t" \
  "v_mfma_f32_16x16x32_bf16 %[c01], v[240:243], %[k11], %[c01]\n\t" \
  "v_add_u32 %[pc3], %[dl], %[pc3]\n\t" \
  "v_mfma_f32_16x16x32_bf16 %[c11], v[244:247], %[k11], %[c11]\n\t" \
  "ds_read_b128 %[k11], %[pc1] offset:2048\n\t" \
  "buffer_load_dwordx4 v[236:239], %[vo], %[srd], %[hb2] offen offset:1024 sc1\n\t" \
  "s_waitcnt lgkmcnt(6)\n\t" \
  "v_mfma_f32_16x16x32_bf16 %[c02], v[240:243], %[k12], %[c02]\n\t" \
  "v_readfirstlane_b32 %[s], v254\n\t" \
  "v_mfma_f32_16x16x32_bf16 %[c12], v[244:247], %[k12], %[c12]\n\t" \
  "ds_read_b128 %[k12], %[pc1] offset:4096\n\t" \
  "v_mfma_f32_16x16x32_bf16 %[c03], v[240:243], %[k13], %[c03]\n\t" \
  "s_add_u32 %[hn], %[hn], 1\n\t" \
  "v_mfma_f32_16x16x32_bf16 %[c13], v[244:247], %[k13], %[c13]\n\t" \
  "ds_read_b128 %[k13], %[pc1] offset:6144\n\t" \
  "buffer_load_dwordx4 v[240:243], %[vo], %[srd], %[hb3] offen offset:0 sc1\n\t" \
  "buffer_load_dwordx4 v[244:247], %[vo], %[srd], %[hb3] offen offset:1024 sc1\n\t" \
  "s_cmp_ge_u32 %[s], %[hn]\n\t" \
  "s_cbranch_scc0 Lhs1_%=\n\t" \
  "Lhb1_%=:\n\t" \
  "s_cmp_lt_u32 %[cn], 14\n\t" \
  "s_cbranch_scc1 Lg2_%=\n\t" \
  "s_waitcnt vmcnt(14) lgkmcnt(7)\n\t" \
  "ds_read_b128 v[252:255], %[fa]\n\t" \
  "v_mfma_f32_16x16x32_bf16 %[c00], v[184:187], %[k00], %[c00]\n\t" \
  "v_mfma_f32_16x16x32_bf16 %[c10], v[188:191], %[k00], %[c10]\n\t" \
  "ds_read_b128 %[k00], %[pc2] offset:0\n\t" \
  "s_waitcnt lgkmcnt(8)\n\t" \
  "v_mfma_f32_16x16x32_bf16 %[c01], v[184:187], %[k01], %[c01]\n\t" \
  "v_mfma_f32_16x16x32_bf16 %[c11], v[188:191], %[k01], %[c11]\n\t" \
  "ds_read_b128 %[k01], %[pc2] offset:2048\n\t" \
  "s_waitcnt lgkmcnt(8)\n\t" \
  "v_mfma_f32_16x16x32_bf16 %[c02], v[184:187], %[k02], %[c02]\n\t" \
  "v_mfma_f32_16x16x32_bf16 %[c12], v[188:191], %[k02], %[c12]\n\t" \
  "ds_read_b128 %[k02], %[pc2] offset:4096\n\t" \
  "s_waitcnt lgkmcnt(8)\n\t" \
  "v_mfma_f32_16x16x32_bf16 %[c03], v[184:187], %[k03], %[c03]\n\t" \
  "s_add_u32 %[cn], %[cn], 1\n\t" \
  "v_mfma_f32_16x16x32_bf16 %[c13], v[188:191], %[k03], %[c13]\n\t" \
  "ds_read_b128 %[k03], %[pc2] offset:6144\n\t" \
  "s_waitcnt vmcnt(12) lgkmcnt(8)\n\t" \
  "v_mfma_f32_16x16x32_bf16 %[c00], v[192:195], %[k10], %[c00]\n\t" \
  "s_cmp_eq_u32 %[cn], 9\n\t" \
  "s_cselect_b32 %[dl], 0x20000, %[d16]\n\t" \
  "v_mfma_f32_16x16x32_bf16 %[c10], v[196:199], %[k10], %[c10]\n\t" \
  "s_bitcmp1_b32 %[wr], %[cn]\n\t" \
  "s_cselect_b32 %[dl], 0xfffe4000, %[dl]\n\t" \
  "ds_read_b128 %[k10], %[pc3] offset:0\n\t" \
  "s_waitcnt lgkmcnt(8)\n\t" \
  "v_mfma_f32_16x16x32_bf16 %[c01], v[192:195], %[k11], %[c01]\n\t" \
  "v_add_u32 %[pc0], %[dl], %[pc0]\n\t" \
  "v_mfma_f32_16x16x32_bf16 %[c11], v[196:199], %[k11], %[c11]\n\t" \
  "ds_read_b128 %[k11], %[pc3] offset:2048\n\t" \
  "s_waitcnt lgkmcnt(8)\n\t" \
  "v_mfma_f32_16x16x32_bf16 %[c02], v[192:195], %[k12], %[c02]\n\t" \
  "v_add_u32 %[pc1], %[dl], %[pc1]\n\t" \
  "v_mfma_f32_16x16x32_bf16 %[c12], v[196:199], %[k12], %[c12]\n\t" \
  "ds_read_b128 %[k12], %[pc3] offset:4096\n\t" \
  "s_waitcnt lgkmcnt(7)\n\t" \
  "v_mfma_f32_16x16x32_bf16 %[c03], v[192:195], %[k13], %[c03]\n\t" \
  "v_readfirstlane_b32 %[s], v253\n\t" \
  "v_mfma_f32_16x16x32_bf16 %[c13], v[196:199], %[k13], %[c13]\n\t" \
  "ds_read_b128 %[k13], %[pc3] offset:6144\n\t" \
  "s_cmp_gt_u32 %[s], %[cn]\n\t" \
  "s_cbranch_scc0 Lcs0t_%=\n\t" \
  "Lcb0t_%=:\n\t" \
  "s_waitcnt vmcnt(10) lgkmcnt(7)\n\t" \
  "v_mfma_f32_16x16x32_bf16 %[c00], v[200:203], %[k00], %[c00]\n\t" \
  "v_mfma_f32_16x16x32_bf16 %[c10], v[204:207], %[k00], %[c10]\n\t" \
  "ds_read_b128 %[k00], %[pc0] offset:0\n\t" \
  "s_waitcnt lgkmcnt(7)\n\t" \
  "v_mfma_f32_16x16x32_bf16 %[c01], v[200:203], %[k01], %[c01]\n\t" \
  "v_mfma_f32_16x16x32_bf16 %[c11], v[204:207], %[k01], %[c11]\n\t" \
  "ds_read_b128 %[k01], %[pc0] offset:2048\n\t" \
  "s_waitcnt lgkmcnt(7)\n\t" \
  "v_mfma_f32_16x16x32_bf16 %[c02], v[200:203], %[k02], %[c02]\n\t" \
  "v_mfma_f32_16x16x32_bf16 %[c12], v[204:207], %[k02], %[c12]\n\t" \
  "ds_read_b128 %[k02], %[pc0] offset:4096\n\t" \
  "s_waitcnt lgkmcnt(7)\n\t" \
  "v_mfma_f32_16x16x32_bf16 %[c03], v[200:203], %[k03], %[c03]\n\t" \
  "v_mfma_f32_16x16x32_bf16 %[c13], v[204:207], %[k03], %[c13]\n\t" \
  "ds_read_b128 %[k03], %[pc0] offset:6144\n\t" \
  "s_waitcnt vmcnt(8) lgkmcnt(7)\n\t" \
  "v_mfma_f32_16x16x32_bf16 %[c00], v[208:211], %[k10], %[c00]\n\t" \
  "v_add_u32 %[pc2], %[dl], %[pc2]\n\t" \
  "v_mfma_f32_16x16x32_bf16 %[c10], v[212:215], %[k10], %[c10]\n\t" \
  "ds_read_b128 %[k10], %[pc1] offset:0\n\t" \
  "s_waitcnt lgkmcnt(7)\n\t" \
  "v_mfma_f32_16x16x32_bf16 %[c01], v[208:211], %[k11], %[c01]\n\t" \
  "v_add_u32 %[pc3], %[dl], %[pc3]\n\t" \
  "v_mfma_f32_16x16x32_bf16 %[c11], v[212:215], %[k11], %[c11]\n\t" \
  "ds_read_b128 %[k11], %[pc1] offset:2048\n\t" \
  "s_waitcnt lgkmcnt(7)\n\t" \
  "v_mfma_f32_16x16x32_bf16 %[c02], v[208:211], %[k12], %[c02]\n\t" \
  "v_mfma_f32_16x16x32_bf16 %[c12], v[212:215], %[k12], %[c12]\n\t" \
  "ds_read_b128 %[k12], %[pc1] offset:4096\n\t" \
  "s_waitcnt lgkmcnt(7)\n\t" \
  "v_mfma_f32_16x16x32_bf16 %[c03], v[208:211], %[k13], %[c03]\n\t" \
  "v_mfma_f32_16x16x32_bf16 %[c13], v[212:215], %[k13], %[c13]\n\t" \
  "ds_read_b128 %[k13], %[pc1] offset:6144\n\t" \
  "s_waitcnt vmcnt(6) lgkmcnt(7)\n\t" \
  "v_mfma_f32_16x16x32_bf16 %[c00], v[216:219], %[k00], %[c00]\n\t" \
  "v_mfma_f32_16x16x32_bf16 %[c10], v[220:223], %[k00], %[c10]\n\t" \
  "ds_read_b128 %[k00], %[pc2] offset:0\n\t" \
  "s_waitcnt lgkmcnt(7)\n\t" \
  "v_mfma_f32_16x16x32_bf16 %[c01], v[216:219], %[k01], %[c01]\n\t" \
  "v_mfma_f32_16x16x32_bf16 %[c11], v[220:223], %[k01], %[c11]\n\t" \
  "ds_read_b128 %[k01], %[pc2] offset:2048\n\t" \
  "s_waitcnt lgkmcnt(7)\n\t" \
  "v_mfma_f32_16x16x32_bf16 %[c02], v[216:219], %[k02], %[c02]\n\t" \
  "v_mfma_f32_16x16x32_bf16 %[c12], v[220:223], %[k02], %[c12]\n\t" \
  "ds_read_b128 %[k02], %[pc2] offset:4096\n\t" \
  "s_waitcnt lgkmcnt(7)\n\t" \
  "v_mfma_f32_16x16x32_bf16 %[c03], v[216:219], %[k03], %[c03]\n\t" \
  "v_mfma_f32_16x16x32_bf16 %[c13], v[220:223], %[k03], %[c13]\n\t" \
  "ds_read_b128 %[k03], %[pc2] offset:6144\n\t" \
  "s_waitcnt vmcnt(4) lgkmcnt(7)\n\t" \
  "v_mfma_f32_16x16x32_bf16 %[c00], v[224:227], %[k10], %[c00]\n\t" \
  "v_mfma_f32_16x16x32_bf16 %[c10], v[228:231], %[k10], %[c10]\n\t" \
  "ds_read_b128 %[k10], %[pc3] offset:0\n\t" \
  "s_waitcnt lgkmcnt(7)\n\t" \
  "v_mfma_f32_16x16x32_bf16 %[c01], v[224:227], %[k11], %[c01]\n\t" \
  "v_mfma_f32_16x16x32_bf16 %[c11], v[228:231], %[k11], %[c11]\n\t" \
  "ds_read_b128 %[k11], %[pc3] offset:2048\n\t" \
  "s_waitcnt lgkmcnt(7)\n\t" \
  "v_mfma_f32_16x16x32_bf16 %[c02], v[224:227], %[k12], %[c02]\n\t" \
  "v_mfma_f32_16x16x32_bf16 %[c12], v[228:231], %[k12], %[c12]\n\t" \
  "ds_read_b128 %[k12], %[pc3] offset:4096\n\t" \
  "s_waitcnt lgkmcnt(7)\n\t" \
  "v_mfma_f32_16x16x32_bf16 %[c03], v[224:227], %[k13], %[c03]\n\t" \
  "v_mfma_f32_16x16x32_bf16 %[c13], v[228:231], %[k13], %[c13]\n\t" \
  "ds_read_b128 %[k13], %[pc3] offset:6144\n\t" \
  "s_waitcnt vmcnt(2) lgkmcnt(7)\n\t" \
  "v_mfma_f32_16x16x32_bf16 %[c00], v[232:235], %[k00], %[c00]\n\t" \
  "v_mfma_f32_16x16x32_bf16 %[c10], v[236:239], %[k00], %[c10]\n\t" \
  "s_waitcnt lgkmcnt(6)\n\t" \
  "v_mfma_f32_16x16x32_bf16 %[c01], v[232:235], %[k01], %[c01]\n\t" \
  "v_mfma_f32_16x16x32_bf16 %[c11], v[236:239], %[k01], %[c11]\n\t" \
  "s_waitcnt lgkmcnt(5)\n\t" \
  "v_mfma_f32_16x16x32_bf16 %[c02], v[232:235], %[k02], %[c02]\n\t" \
  "v_mfma_f32_16x16x32_bf16 %[c12], v[236:239], %[k02], %[c12]\n\t" \
  "s_waitcnt lgkmcnt(4)\n\t" \
  "v_mfma_f32_16x16x32_bf16 %[c03], v[232:235], %[k03], %[c03]\n\t" \
  "v_mfma_f32_16x16x32_bf16 %[c13], v[236:239], %[k03], %[c13]\n\t" \
  "s_waitcnt vmcnt(0) lgkmcnt(3)\n\t" \
  "v_mfma_f32_16x16x32_bf16 %[c00], v[240:243], %[k10], %[c00]\n\t" \
  "v_mfma_f32_16x16x32_bf16 %[c10], v[244:247], %[k10], %[c10]\n\t" \
  "s_waitcnt lgkmcnt(2)\n\t" \
  "v_mfma_f32_16x16x32_bf16 %[c01], v[240:243], %[k11], %[c01]\n\t" \
  "v_mfma_f32_16x16x32_bf16 %[c11], v[244:247], %[k11], %[c11]\n\t" \
  "s_waitcnt lgkmcnt(1)\n\t" \
  "v_mfma_f32_16x16x32_bf16 %[c02], v[240:243], %[k12], %[c02]\n\t" \
  "v_mfma_f32_16x16x32_bf16 %[c12], v[244:247], %[k12], %[c12]\n\t" \
  "s_waitcnt lgkmcnt(0)\n\t" \
  "v_mfma_f32_16x16x32_bf16 %[c03], v[240:243], %[k13], %[c03]\n\t" \
  "v_mfma_f32_16x16x32_bf16 %[c13], v[244:247], %[k13], %[c13]\n\t" \
  "s_branch Lend_%=\n\t" \
  "Lcs0_%=:\n\t" \
  "s_add_u32 %[s2], %[cn], 1\n\t" \
  "Lcr0_%=:\n\t" \
  "ds_read_b128 v[252:255], %[fa]\n\t" \
  "s_cmp_eq_u32 %[to], 0\n\t" \
  "s_cbranch_scc1 Lcb0_%=\n\t" \
  "s_sub_u32 %[to], %[to], 1\n\t" \
  "s_waitcnt lgkmcnt(0)\n\t" \
  "v_readfirstlane_b32 %[s], v253\n\t" \
  "s_cmp_ge_u32 %[s], %[s2]\n\t" \
  "s_cbranch_scc0 Lcr0_%=\n\t" \
  "s_branch Lcb0_%=\n\t" \
  "Lhs0_%=:\n\t" \
  "ds_read_b128 v[252:255], %[fa]\n\t" \
  "s_cmp_eq_u32 %[to], 0\n\t" \
  "s_cbranch_scc1 Lhb0_%=\n\t" \
  "s_sub_u32 %[to], %[to], 1\n\t" \
  "s_waitcnt lgkmcnt(0)\n\t" \
  "v_readfirstlane_b32 %[s], v254\n\t" \
  "s_cmp_ge_u32 %[s], %[hn]\n\t" \
  "s_cbranch_scc0 Lhs0_%=\n\t" \
  "s_branch Lhb0_%=\n\t" \
  "Lcs1_%=:\n\t" \
  "s_add_u32 %[s2], %[cn], 1\n\t" \
  "Lcr1_%=:\n\t" \
  "ds_read_b128 v[252:255], %[fa]\n\t" \
  "s_cmp_eq_u32 %[to], 0\n\t" \
  "s_cbranch_scc1 Lcb1_%=\n\t" \
  "s_sub_u32 %[to], %[to], 1\n\t" \
  "s_waitcnt lgkmcnt(0)\n\t" \
  "v_readfirstlane_b32 %[s], v252\n\t" \
  "s_cmp_ge_u32 %[s], %[s2]\n\t" \
  "s_cbranch_scc0 Lcr1_%=\n\t" \
  "s_branch Lcb1_%=\n\t" \
  "Lhs1_%=:\n\t" \
  "ds_read_b128 v[252:255], %[fa]\n\t" \
  "s_cmp_eq_u32 %[to], 0\n\t" \
  "s_cbranch_scc1 Lhb1_%=\n\t" \
  "s_sub_u32 %[to], %[to], 1\n\t" \
  "s_waitcnt lgkmcnt(0)\n\t" \
  "v_readfirstlane_b32 %[s], v254\n\t" \
  "s_cmp_ge_u32 %[s], %[hn]\n\t" \
  "s_cbranch_scc0 Lhs1_%=\n\t" \
  "s_branch Lhb1_%=\n\t" \
  "Lcs0t_%=:\n\t" \
  "s_add_u32 %[s2], %[cn], 1\n\t" \
  "Lcr0t_%=:\n\t" \
  "ds_read_b128 v[252:255], %[fa]\n\t" \
  "s_cmp_eq_u32 %[to], 0\n\t" \
  "s_cbranch_scc1 Lcb0t_%=\n\t" \
  "s_sub_u32 %[to], %[to], 1\n\t" \
  "s_waitcnt lgkmcnt(0)\n\t" \
  "v_readfirstlane_b32 %[s], v253\n\t" \
  "s_cmp_ge_u32 %[s], %[s2]\n\t" \
  "s_cbranch_scc0 Lcr0t_%=\n\t" \
  "s_branch Lcb0t_%=\n\t" \
  "Lend_%=:\n\t" \
  "s_nop 7\n\t"
#define ASM_REQ \
  "s_cmp_gt_u32 1, %[own]\n\t" \
  "s_addc_u32 %[hb0], 1, -1\n\t" \
  "s_lshl_b32 %[hb0], %[hb0], 15\n\t" \
  "s_or_b32 %[hb1], %[hb0], 0x2000\n\t" \
  "s_or_b32 %[hb2], %[hb0], 0x4000\n\t" \
  "s_or_b32 %[hb3], %[hb0], 0x6000\n\t" \
  "buffer_load_dwordx4 v[216:219], %[vo], %[srd], %[hb0] offen offset:0 sc1\n\t" \
  "buffer_load_dwordx4 v[220:223], %[vo], %[srd], %[hb0] offen offset:1024 sc1\n\t" \
  "buffer_load_dwordx4 v[224:227], %[vo], %[srd], %[hb1] offen offset:0 sc1\n\t" \
  "buffer_load_dwordx4 v[228:231], %[vo], %[srd], %[hb1] offen offset:1024 sc1\n\t" \
  "buffer_load_dwordx4 v[232:235], %[vo], %[srd], %[hb2] offen offset:0 sc1\n\t" \
  "buffer_load_dwordx4 v[236:239], %[vo], %[srd], %[hb2] offen offset:1024 sc1\n\t" \
  "buffer_load_dwordx4 v[240:243], %[vo], %[srd], %[hb3] offen offset:0 sc1\n\t" \
  "buffer_load_dwordx4 v[244:247], %[vo], %[srd], %[hb3] offen offset:1024 sc1\n\t" \
  "s_cmp_gt_u32 2, %[own]\n\t" \
  "s_addc_u32 %[hb0], 2, -1\n\t" \
  "s_lshl_b32 %[hb0], %[hb0], 15\n\t" \
  "s_or_b32 %[hb1], %[hb0], 0x2000\n\t" \
  "s_or_b32 %[hb2], %[hb0], 0x4000\n\t" \
  "s_or_b32 %[hb3], %[hb0], 0x6000\n\t" \
  "buffer_load_dwordx4 v[184:187], %[vo], %[srd], %[hb0] offen offset:0 sc1\n\t" \
  "buffer_load_dwordx4 v[188:191], %[vo], %[srd], %[hb0] offen offset:1024 sc1\n\t" \
  "buffer_load_dwordx4 v[192:195], %[vo], %[srd], %[hb1] offen offset:0 sc1\n\t" \
  "buffer_load_dwordx4 v[196:199], %[vo], %[srd], %[hb1] offen offset:1024 sc1\n\t" \
  "buffer_load_dwordx4 v[200:203], %[vo], %[srd], %[hb2] offen offset:0 sc1\n\t" \
  "buffer_load_dwordx4 v[204:207], %[vo], %[srd], %[hb2] offen offset:1024 sc1\n\t" \
  "buffer_load_dwordx4 v[208:211], %[vo], %[srd], %[hb3] offen offset:0 sc1\n\t" \
  "buffer_load_dwordx4 v[212:215], %[vo], %[srd], %[hb3] offen offset:1024 sc1\n\t"
#define NS 0

#define OST " sc1 nt"
#define XPRIO 2
#define XPRIO2 1
#define MPRIO2 2
#define HDLY 8
#define LPRIO 2
#define POLLN 8
#define YGATE 11
#define LTHR 16
#define CPK 9
#define W2P 0
#define SWP 0
#define CP0 1
#define CROT 0
#define NRG0 3
#define NRG1 3
#define CSL 0
#define NSI 8
#define DSL 1
#define MGP 1
#define RQD 0
#define XG2 2
#define NFW 28
#define SPINLOG 18
constexpr int D_ = 1024, F_ = 3584;
constexpr int L_XH = 131072;
constexpr int MISC = 155648;
constexpr int L_GT = MISC;
constexpr int L_FLG = L_GT + 512;
constexpr int L_JNK = L_FLG + 256;
constexpr int L_SEL = L_JNK + 256;
constexpr int L_BROW = L_SEL + 128;
constexpr int L_ACT = L_BROW + 2048;
constexpr int L_CAND = L_ACT + 1024;
constexpr int L_ZERO = L_CAND + 256;
constexpr int L_MAP = L_ZERO + 256;
constexpr int L_TAG = L_MAP + 512;
constexpr int L_RT = L_TAG + 16;
constexpr int LDS_BYTES = 163840;
static_assert(L_RT + 32 <= LDS_BYTES, "lds");
constexpr int FL_FULL = L_FLG, FL_FREE = L_FLG + 16, FL_C = L_FLG + 32  , FL_CFREE = L_FLG + 48, FL_TAG = L_FLG + 64  ,
              FL_XD = L_FLG + 80, FL_TK = L_FLG + 96, FL_SEL = L_FLG + 112, FL_G = L_FLG + 128, FL_W = L_FLG + 136, FL_HS = L_FLG + 144, FL_C8 = L_FLG + 160;
constexpr unsigned W_EPOCH = 0;
constexpr unsigned W_R2 = 256;
constexpr unsigned W_G2 = W_R2 + 8192;
constexpr unsigned W_HF = W_G2 + 8192;
constexpr unsigned W_AGG = W_HF + 8192;
constexpr unsigned W_SEL = W_AGG + 262144;
constexpr unsigned W_GU = W_SEL + 4096;
constexpr unsigned W_YP = W_GU + 56 * 16384;
constexpr unsigned W_H = 8u << 20;
constexpr unsigned W_DBG = 20u << 20;
constexpr unsigned W_END = 21u << 20;
static_assert(W_YP + 256 * 7168 <= W_H, "ws");
#define WS_BASE(n) (((n) - W_END) & ~(size_t)0xfffff)
constexpr int SPIN_MAX = 1 << SPINLOG;

DEV float bf2f(unsigned v) { return __uint_as_float(v << 16); }
DEV float bfhi(unsigned v) { return __uint_as_float(v & 0xffff0000u); }
DEV u16 f2bf(float f) { return __builtin_bit_cast(u16, (__bf16)f); }
DEV float rbf(float f) { return bf2f(f2bf(f)); }
typedef __bf16 bf16x2_t __attribute__((ext_vector_type(2)));
DEV unsigned pk2(float a, float b) { return __builtin_bit_cast(unsigned, __builtin_convertvector((f32x2){a, b}, bf16x2_t)); }
DEV float fma1(float a, float b, float c) { float d; asm("v_fma_f32 %0, %1, %2, %3" : "=v"(d) : "v"(a), "v"(b), "v"(c)); return d; }
DEV float fnma1(float a, float b, float c) { float d; asm("v_fma_f32 %0, -%1, %2, %3" : "=v"(d) : "v"(a), "v"(b), "v"(c)); return d; }
DEV float fnmn1(float a, float b, float c) { float d; asm("v_fma_f32 %0, -%1, %2, -%3" : "=v"(d) : "v"(a), "v"(b), "v"(c)); return d; }
DEV float mul1(float a, float b) { float d; asm("v_mul_f32 %0, %1, %2" : "=v"(d) : "v"(a), "v"(b)); return d; }
DEV f32x2 cmul(f32x2 a, f32x2 b) { return (f32x2){fnma1(a.y, b.y, mul1(a.x, b.x)), fma1(a.y, b.x, mul1(a.x, b.y))}; }
DEV f32x2 cfma(f32x2 a, f32x2 b, f32x2 c) { return (f32x2){fma1(a.x, b.x, fnma1(a.y, b.y, c.x)), fma1(a.x, b.y, fma1(a.y, b.x, c.y))}; }
template <int N> DEV void waitv() { asm volatile("s_waitcnt vmcnt(%0)" ::"n"(N) : "memory"); }
DEV void bar() { asm volatile("s_barrier" ::: "memory"); }
DEV unsigned lds_rd32(unsigned a) { unsigned v; asm volatile("ds_read_b32 %0, %1\n\ts_waitcnt lgkmcnt(0)" : "=v"(v) : "v"(a) : "memory"); return v; }
DEV u32x4 lds_rd128(unsigned a) { u32x4 v; asm volatile("ds_read_b128 %0, %1\n\ts_waitcnt lgkmcnt(0)" : "=v"(v) : "v"(a) : "memory"); return v; }
DEV void lds_wr32(unsigned a, unsigned v) { asm volatile("ds_write_b32 %0, %1" ::"v"(a), "v"(v) : "memory"); }
DEV void lds_wait_ge(unsigned a, unsigned need) {
#pragma unroll 1
  for (int it = 0; it < SPIN_MAX * 4; ++it) { if (__builtin_amdgcn_readfirstlane(lds_rd32(a)) >= need) break; __builtin_amdgcn_s_sleep(1); }
}
typedef __amdgpu_buffer_rsrc_t rsrc_t;
DEV u32x4 ld128(rsrc_t r, unsigned vo, unsigned so = 0) { return __builtin_amdgcn_raw_buffer_load_b128(r, vo, so, 0); }
#define FRESH() asm volatile("" ::: "memory")
DEV u32x4 ld128c(rsrc_t r, unsigned vo) { return __builtin_amdgcn_raw_buffer_load_b128(r, vo, 0, 16); }
DEV u32x2 ld64c(rsrc_t r, unsigned vo) { return __builtin_amdgcn_raw_buffer_load_b64(r, vo, 0, 16); }
DEV void st128c(rsrc_t r, unsigned vo, u32x4 v) {
  asm volatile("s_nop 4\n\tbuffer_store_dwordx4 %0, %1, %2, 0 offen sc1\n\ts_nop 7\n\ts_nop 7" ::"v"(v), "v"(vo), "s"(r) : "memory");
}
DEV void st64c(rsrc_t r, unsigned vo, u32x2 v) { __builtin_amdgcn_raw_buffer_store_b64(v, r, vo, 0, 16); }
DEV rsrc_t mk(const void* p, unsigned bytes) { return __builtin_amdgcn_make_buffer_rsrc((void*)p, 0, bytes, 0x00020000); }

DEV unsigned wave_maxu(unsigned v) {
  v = max(v, (unsigned)__builtin_amdgcn_update_dpp((int)v, (int)v, 0x111, 0xf, 0xf, false));
  v = max(v, (unsigned)__builtin_amdgcn_update_dpp((int)v, (int)v, 0x112, 0xf, 0xf, false));
  v = max(v, (unsigned)__builtin_amdgcn_update_dpp((int)v, (int)v, 0x114, 0xf, 0xf, false));
  v = max(v, (unsigned)__builtin_amdgcn_update_dpp((int)v, (int)v, 0x118, 0xf, 0xf, false));
  v = max(v, (unsigned)__builtin_amdgcn_update_dpp((int)v, (int)v, 0x142, 0xa, 0xf, false));
  v = max(v, (unsigned)__builtin_amdgcn_update_dpp((int)v, (int)v, 0x143, 0xc, 0xf, false));
  return (unsigned)__builtin_amdgcn_readlane((int)v, 63);
}
DEV float sum16_32(float v) {
  { auto p = __builtin_amdgcn_permlane16_swap(__float_as_uint(v), __float_as_uint(v), false, false); v = __uint_as_float(p[0]) + __uint_as_float(p[1]); }
  { auto p = __builtin_amdgcn_permlane32_swap(__float_as_uint(v), __float_as_uint(v), false, false); v = __uint_as_float(p[0]) + __uint_as_float(p[1]); }
  return v;
}
DEV float wave_sum(float v) {
  v += __uint_as_float((unsigned)__builtin_amdgcn_update_dpp(0, (int)__float_as_uint(v), 0x128, 0xf, 0xf, false));
  v += __uint_as_float((unsigned)__builtin_amdgcn_update_dpp(0, (int)__float_as_uint(v), 0x124, 0xf, 0xf, false));
  v += __uint_as_float((unsigned)__builtin_amdgcn_update_dpp(0, (int)__float_as_uint(v), 0x122, 0xf, 0xf, false));
  v += __uint_as_float((unsigned)__builtin_amdgcn_update_dpp(0, (int)__float_as_uint(v), 0x121, 0xf, 0xf, false));
  return sum16_32(v);
}
DEV unsigned route_key(float r, int idx) {
  unsigned u = __float_as_uint(r) >> 16;
  u = (u & 0x8000u) ? (~u & 0xffffu) : (u | 0x8000u);
  return (u << 16) | (unsigned)(2047 - idx);
}
DEV float key_val(unsigned key) {
  unsigned u = key >> 16;
  u = (u & 0x8000u) ? (u & 0x7fffu) : (~u & 0xffffu);
  return __uint_as_float(u << 16);
}

DEV void unit_trn(u32x4 (&w)[8]) {
  u32x4 b[8];
#pragma unroll
  for (int j = 0; j < 8; ++j)
#pragma unroll
    for (int t = 0; t < 4; ++t) b[j][t] = __builtin_amdgcn_perm(w[2 * t + 1][j >> 1], w[2 * t][j >> 1], (j & 1) ? 0x07060302u : 0x05040100u);
#pragma unroll
  for (int j = 0; j < 8; ++j) w[j] = b[j];
}
DEV f32x4 fma4(f32x4 a, f32x4 b, f32x4 c) { return __builtin_elementwise_fma(a, b, c); }
DEV f32x4 fnm4(f32x4 a, f32x4 b, f32x4 c) {
  const f32x2 l = __builtin_elementwise_fma(-a.lo, b.lo, c.lo), h = __builtin_elementwise_fma(-a.hi, b.hi, c.hi);
  return (f32x4){l.x, l.y, h.x, h.y};
}
DEV f32x4 nml4(f32x4 a, f32x4 b) { const f32x2 l = -a.lo * b.lo, h = -a.hi * b.hi; return (f32x4){l.x, l.y, h.x, h.y}; }
template <int CTRL> DEV float dpp1(float v) { return __uint_as_float((unsigned)__builtin_amdgcn_update_dpp(0, (int)__float_as_uint(v), CTRL, 0xf, 0xf, true)); }
template <int CTRL> DEV f32x4 dpp4(f32x4 v) { return (f32x4){dpp1<CTRL>(v.x), dpp1<CTRL>(v.y), dpp1<CTRL>(v.z), dpp1<CTRL>(v.w)}; }
DEV f32x4 splat4(float v) { f32x2 p = {v, v}; asm volatile("" : "+v"(p)); return (f32x4){p.x, p.y, p.x, p.y}; }
DEV void cexp4(f32x4 a2, f32x4 bv, f32x4& re, f32x4& im) {
  f32x4 m, cs, sn;
#pragma unroll
  for (int r = 0; r < 4; ++r) { const float ph = __builtin_amdgcn_fractf(bv[r]); m[r] = __builtin_amdgcn_exp2f(a2[r]); cs[r] = __builtin_amdgcn_cosf(ph); sn[r] = __builtin_amdgcn_sinf(ph); }
  re = m * cs; im = m * sn;
}
DEV f32x4 unpk4(u32x2 v) { return (f32x4){bf2f(v.x & 0xffffu), bfhi(v.x), bf2f(v.y & 0xffffu), bfhi(v.y)}; }
DEV void bcast8(int n, f32x4 v, f32x4& o) {
  switch (n) {
    case 0: o = dpp4<0x150>(v); break; case 1: o = dpp4<0x151>(v); break; case 2: o = dpp4<0x152>(v); break; case 3: o = dpp4<0x153>(v); break;
    case 4: o = dpp4<0x154>(v); break; case 5: o = dpp4<0x155>(v); break; case 6: o = dpp4<0x156>(v); break; default: o = dpp4<0x157>(v); break;
  }
}
DEV f32x4 lds4(LAS char* smem, int a) { return *(const LAS f32x4*)(smem + a); }
DEV void lds_wait_min4(unsigned a, unsigned need) {
#pragma unroll 1
  for (int it = 0; it < SPIN_MAX * 4; ++it) {
    const u32x4 f = lds_rd128(a);
    if (__builtin_amdgcn_readfirstlane(min(min(f.x, f.y), min(f.z, f.w))) >= need) break;
    __builtin_amdgcn_s_sleep(1);
  }
}

DEV void dma4(rsrc_t r0, rsrc_t r1, LAS char* la, const unsigned (&v)[4], unsigned so) {
  __builtin_amdgcn_raw_ptr_buffer_load_lds(r0, (LAS void*)la, 16, v[0], so, 0, 0);
  __builtin_amdgcn_raw_ptr_buffer_load_lds(r0, (LAS void*)la, 16, v[1], so, 1024, 0);
  __builtin_amdgcn_raw_ptr_buffer_load_lds(r1, (LAS void*)la, 16, v[2], so, 2048, 0);
  __builtin_amdgcn_raw_ptr_buffer_load_lds(r1, (LAS void*)la, 16, v[3], so, 3072, 0);
}

__global__ __launch_bounds__(512, 2) void fused(const u16*, const u16*, const u16*, const u16*, const u16*, const u16*, const u16*, const u16*, const u16*, const u16*,
                                                const u16*, const u16*, const u16*, const u16*, char*, float*) {
#if __HIP_DEVICE_COMPILE__
  unsigned kw0, kw1;
  asm volatile("s_load_dword %0, %2, 0x0\n\ts_load_dword %1, %2, 0x40" : "=&s"(kw0), "=&s"(kw1) : "s"(__builtin_amdgcn_kernarg_segment_ptr()));
  extern __shared__ __attribute__((aligned(16))) char smem_[];
  LAS char* smem = (LAS char*)smem_;
  const int tid = threadIdx.x, hw = __builtin_amdgcn_readfirstlane(tid >> 6), wid = SWP ? hw ^ ((hw >> 1) & 2) : hw;
  const int bid = blockIdx.x, xcd = bid & 7, loc = bid >> 3;
  const int c = (xcd >> 1) * 4 + (loc >> 3), j = (xcd & 1) * 8 + (loc & 7);
  int lane, fr, fq;
  auto relane = [&]() {
    asm volatile("v_mbcnt_lo_u32_b32 %0, -1, 0\n\tv_mbcnt_hi_u32_b32 %0, -1, %0" : "=v"(lane));
    __builtin_assume(lane >= 0 && lane < 64);
    fr = lane & 15; fq = lane >> 4;
  };
  const int crot = CROT == 0 ? c + 1 : CROT == 1 ? 4 * c + (c >> 2) : CROT == 2 ? (c & 1) + 4 * (c & 2) + (c >> 2) * 2 : CROT == 3 ? 2 * c : (c & 1) * 2 + 4 * (c & 2) + (c >> 2);
  const int pown = (j - crot) & 15;
  auto upos = [&](int u) { return u == 0 ? pown : u - 1 + (u - 1 >= pown ? 1 : 0); };
  u64 pc0;
  unsigned l0, l1, l2;
  asm volatile("s_getpc_b64 %0\n\tLk_pc:\n\ts_mov_b32 %1, Lk_m-Lk_pc\n\ts_mov_b32 %2, Lk_l-Lk_pc\n\ts_mov_b32 %3, Lk_x-Lk_pc" : "=s"(pc0), "=s"(l0), "=s"(l1), "=s"(l2));
  auto codepull = [&]() {
    const unsigned po = min((unsigned)(loc * 16 + (lane & 15)) * 128u, max(l0, max(l1, l2)) - 4u);
    if (lane < 16) __builtin_amdgcn_global_load_lds((const __attribute__((address_space(1))) void*)(pc0 + po), (LAS void*)(smem + L_JNK), 4, 0, 0);
  };
  if (CP0 && wid == 0) { relane(); codepull(); }
  const int l = wid & 1;
#define FFNDEF() int b_ = bid; asm volatile("" : "+s"(b_)); \
  const bool ffn = b_ < 8 * NFW; \
  const int sg = (b_ & 7) * 7 + (b_ >> 5), kq = (b_ >> 3) & 3, sg1 = (sg & ~1) + l, gu1 = sg & 1; \
  const unsigned gub = W_GU + ((sg1 * 4 + kq) * 2 + gu1) * 2048, ypb = W_YP + (kq * 4 + l * 2) * 16 * 7168 + sg * 128
  if (__builtin_expect(wid >= 4, 1)) {
  u64x8 ka, kb;
  asm volatile("s_load_dwordx16 %0, %2, 0x0\n\ts_load_dwordx16 %1, %2, 0x40" : "=&s"(ka), "=&s"(kb) : "s"(__builtin_amdgcn_kernarg_segment_ptr()));
  relane();
  unsigned vo1[4];
#pragma unroll
  for (int e = 0; e < 4; ++e)
    vo1[e] = (l == 0 ? c * 128 + 8 * (lane >> 3) + 64 * (e & 1) + (e >> 1) : j * 64 + (lane >> 3) + 8 * (e & 1)) * 2048 + (((lane & 7) ^ (lane >> 3)) * 16) + 4096 - e * 1024;
  const unsigned s4 = (l == 0 ? 2 : 16) * 2048, so1 = wid >= 6 ? 2 * s4 : 0, la1 = l * 16384 + (wid >= 6 ? 8192 : 0);
  unsigned sl0 = ((c + j + (c == 0 ? 1 : 0)) & 15) * 128 + so1;
  asm volatile("" : "+v"(vo1[0]), "+v"(vo1[1]), "+v"(vo1[2]), "+v"(vo1[3]) : "s"(sl0), "s"(s4), "s"(la1));
  asm volatile("s_waitcnt lgkmcnt(0)" : "+s"(ka), "+s"(kb) : "s"(kw0), "s"(kw1));
  const char *x = (const char*)ka[0], *Bre = (const char*)ka[3], *Bim = (const char*)ka[4], *Cre = (const char*)ka[5], *Cim = (const char*)ka[6], *w_gate = (const char*)kb[2],
             *w_up = (const char*)kb[3], *w_down = (const char*)kb[4];
  char* ws = (char*)kb[6];
  const rsrc_t r0 = mk((l == 0 ? x : Bre) - 4096, (l == 0 ? 1 << 22 : 1 << 21) + 4096), r1 = mk((l == 0 ? x : Bim) - 4096, (l == 0 ? 1 << 22 : 1 << 21) + 4096);
  auto issue = [&](unsigned sk, unsigned slot) { dma4(r0, r1, smem + slot + la1, vo1, sk); dma4(r0, r1, smem + slot + la1 + 4096, vo1, sk + s4); };
  issue(sl0, 0);
  __builtin_amdgcn_sched_barrier(0);
  if (!CP0 && wid == 4) codepull();
  const rsrc_t rws = mk(ws, W_END);
  const rsrc_t rx = mk(x, 2048 * 1024 * 2);
  u64 tbl;
  {
    const int s0 = (c + j) & 15, n = (16 - c) & 15;
    const u64 seq = 0xfedcba9876543210ull, rot = s0 ? (seq >> (4 * s0)) | (seq << (64 - 4 * s0)) : seq, lo = (1ull << (4 * n)) - 1;
    tbl = (rot & lo) | ((rot >> 4) & ~lo) | ((u64)j << 60);
  }
  auto slice = [&](int k) { return ((unsigned)(tbl >> (4 * k)) & 15u) * 128u; };

  auto flag = [&](int a, unsigned v) { lds_wr32(lane == 0 ? (unsigned)a : (unsigned)(L_JNK + lane * 4), v); };
  auto flagmax = [&](int a, unsigned v) { asm volatile("ds_max_u32 %0, %1" ::"v"(lane == 0 ? (unsigned)a : (unsigned)(L_JNK + lane * 4)), "v"(v) : "memory"); };
  auto hpoll = [&](unsigned tagA, unsigned tagB) {
    lds_wait_min4(FL_FREE, 9u);
    __builtin_amdgcn_s_sleep(HDLY);
#pragma unroll 1
    for (int it = 0; it < SPIN_MAX; ++it) {
      FRESH();
      const u32x2 f = ld64c(rws, W_HF + (c * 64 + upos(lane >> 2) * 4 + (lane & 3)) * 8);
      const u64 m = ~__ballot(lane < 4 * NS + 4 || (f.x == tagA && f.y == tagB));
      if (!m) break;
      flag(FL_C + 8, ((unsigned)__builtin_ctzll(m) >> 2) - 1u);
    }
    flag(FL_C + 8, 99u);
  };
    const rsrc_t rcr = mk(Cre, 1 << 21), rci = mk(Cim, 1 << 21);
    const unsigned ce = (j * 64 + 4 * (lane >> 3)) * 2048 + (((lane & 7) ^ (lane >> 4)) * 16);
    const unsigned co = (j * 64 + 32 + 4 * (lane >> 3)) * 2048 + (((lane & 7) ^ (4 + (lane >> 4))) * 16);
    auto cfill = [&](int u, int slot, int q0, int q1) {
      LAS char* dst = smem + slot * 16384;
#pragma unroll
      for (int q = q0; q < q1; ++q)
        __builtin_amdgcn_raw_ptr_buffer_load_lds(q < 8 ? rcr : rci, (LAS void*)(dst + q * 1024), 16, (q & 1) ? co : ce, ((q & 7) >> 1) * 2048 + ((upos(u) + crot) & 15) * 128, 0, 0);
    };
  if (__builtin_expect(wid == 4 || wid == 5, 1)) {
    __builtin_amdgcn_s_setprio(LPRIO);
    issue(slice(1), 32768);
    asm volatile("s_waitcnt vmcnt(8) lgkmcnt(0)" ::: "memory"); bar();
    issue(slice(2), 65536);
    issue(slice(3), 98304);
    constexpr int K0 = 1;
    {
      u64 t = tbl >> (4 * K0 + 12);
#pragma unroll 1
      for (int k = K0; k < 13; ++k) {
        const unsigned sk = ((unsigned)t & 15u) << 7;
        waitv<16>();
        bar();
        issue(sk, ((k + 3) & 3) * 32768);
        if (k == CPK && l == 1) codepull();
        t >>= 4;
      }
    }
    waitv<16>(); bar();
    waitv<8>(); bar();
    const unsigned tagA = lds_rd32(L_TAG), tagB = tagA + 0x40000000u;
    waitv<0>(); bar();
    bar();
    lds_wait_ge(FL_W + l * 4, 1u);
    __builtin_amdgcn_s_setprio(1);
#pragma unroll 1
    for (int u = l; u < 8; u += 2) { cfill(u, u, 0, 8); waitv<LTHR>(); cfill(u, u, 8, 16); waitv<LTHR>(); if (u >= 2) flag(FL_C + l * 4, (unsigned)(u - 1)); }
    u32x4 cq[3][16];
    auto creg = [&](int g) {
#pragma unroll
      for (int q = 0; q < 16; ++q) {
        const unsigned so = ((q & 7) >> 1) * 2048 + ((upos(10 + l + 2 * g) + crot) & 15) * 128;
        if (q < 8) asm volatile("buffer_load_dwordx4 %0, %1, %2, %3 offen" : "=v"(cq[g][q]) : "v"((q & 1) ? co : ce), "s"(rcr), "s"(so) : "memory");
        else asm volatile("buffer_load_dwordx4 %0, %1, %2, %3 offen" : "=v"(cq[g][q]) : "v"((q & 1) ? co : ce), "s"(rci), "s"(so) : "memory");
        if ((q & 7) == 7) waitv<LTHR>();
      }
    };
#pragma unroll
    for (int g = 0; g < 3; ++g) {
      if (g >= (l ? NRG1 : NRG0)) break;
      waitv<16>();
      if (g == 1) flag(FL_C + l * 4, (unsigned)(7 + l));
      creg(g);
    }
    waitv<0>();
    if ((l ? NRG1 : NRG0) < 2) flag(FL_C + l * 4, (unsigned)(7 + l));
    if (l == 0) {
      __builtin_amdgcn_s_setprio(3);
      hpoll(tagA, tagB);
    }
    else {
      lds_wait_min4(FL_CFREE, 1u);
      cfill(8, 0, 0, 16);
      cfill(9, 8, 0, 16);
      waitv<0>();
      flagmax(FL_C8, 1u);
      flag(FL_C + 4, 10u);
    }
    __builtin_amdgcn_s_setprio(0);
    if (l == 0) { lds_wait_ge(FL_C8, 1u); flag(FL_C, 9u); }
#pragma unroll
    for (int g = 0; g < 3; ++g) {
      asm volatile("" : "+v"(cq[g][0]), "+v"(cq[g][1]), "+v"(cq[g][2]), "+v"(cq[g][3]), "+v"(cq[g][4]), "+v"(cq[g][5]), "+v"(cq[g][6]), "+v"(cq[g][7]),
                   "+v"(cq[g][8]), "+v"(cq[g][9]), "+v"(cq[g][10]), "+v"(cq[g][11]), "+v"(cq[g][12]), "+v"(cq[g][13]), "+v"(cq[g][14]), "+v"(cq[g][15]));
      const int u = 10 + l + 2 * g;
      lds_wait_min4(FL_CFREE, (unsigned)(u - 8));
      const int slot = u - 9;
      if (g < (l ? NRG1 : NRG0)) {
#pragma unroll
        for (int q = 0; q < 16; ++q)
          asm volatile("ds_write_b128 %0, %1 offset:%2" ::"v"(slot * 16384 + lane * 16), "v"(cq[g][q]), "n"(q * 1024) : "memory");
      } else {
#pragma unroll
        for (int q = 0; q < 16; q += 4) { cfill(u, slot, q, q + 4); if (CSL) __builtin_amdgcn_s_sleep(CSL); }
        waitv<0>();
      }
      flag(FL_C + l * 4, g == 2 ? 99u : (unsigned)(u + 1));
    }
    asm volatile("Lk_l:");
    return;
  }
  {
    const int xw = wid - 6;
    __builtin_amdgcn_s_setprio(XPRIO);
    u32x2 ep;
    unsigned tagA, tagB;
    if (xw == 0) lds_wr32(L_FLG + lane * 4, 0u);
    else asm volatile("ds_write_b64 %0, %1\n\tds_write_b64 %2, %3" ::"v"(L_CAND + lane * 8), "v"((u32x2){0u, 0u}), "v"(L_MAP + lane * 8), "v"((u32x2){L_ZERO, L_ZERO}) : "memory");
    FFNDEF();
    u32x4 w1[4][8], w2[2][8];
    const rsrc_t rw1 = mk(gu1 ? w_up : w_gate, ffn ? D_ * F_ * 2 : 0), r2 = mk(w_down, ffn ? D_ * F_ * 2 : 0);
    unsigned o1 = (kq * 256 + 32 * (fr >> 3) + 8 * fq) * (F_ * 2) + (sg1 * 64 + 8 * (fr & 7)) * 2;
    unsigned o2 = (sg * 64 + 32 * (fr >> 3) + 8 * fq) * 2048 + (kq * 256 + xw * 128 + 8 * (fr & 7)) * 2;
#define LDW1(b) { _Pragma("unroll") for (int i = 4 * ((b) & 1); i < 4 * ((b) & 1) + 4; ++i) \
      asm volatile("buffer_load_dwordx4 %0, %1, %2, %3 offen" : "=v"(w1[(b) >> 1][i]) : "v"(o1), "s"(rw1), "s"((((b) >> 1) * 64 + i) * (F_ * 2)) : "memory"); }
#define LDW2(a, b) { _Pragma("unroll") for (int i = a; i < b; ++i) \
      asm volatile("buffer_load_dwordx4 %0, %1, %2, %3 offen" : "=v"(w2[i >> 3][i & 7]) : "v"(o2), "s"(r2), "s"((i & 7) * 2048 + (i >> 3) * 128) : "memory"); }
#define TRN(w, n) { asm volatile("s_waitcnt vmcnt(%8)" : "+v"(w[0]), "+v"(w[1]), "+v"(w[2]), "+v"(w[3]), "+v"(w[4]), "+v"(w[5]), "+v"(w[6]), "+v"(w[7]) : "n"(n) : "memory"); \
      unit_trn(w); asm volatile("" : "+v"(w[0]), "+v"(w[1]), "+v"(w[2]), "+v"(w[3]), "+v"(w[4]), "+v"(w[5]), "+v"(w[6]), "+v"(w[7])); }
    unsigned key[16];
    unsigned rgo = W_R2 + l * 4096 + lane * 16, ggo = W_G2 + c * 512 + (lane & 31) * 16;
    u32x4 g[4], a;
    asm volatile("" : "+v"(o1), "+v"(o2), "+v"(rgo), "+v"(ggo) : "s"(rw1), "s"(r2));
    auto A_EP = [&]() { asm volatile("buffer_load_dwordx2 %0, off, %1, 0 sc1" : "=v"(ep) : "s"(rws) : "memory"); };
    auto A_TAG = [&]() {
      asm volatile("" : "+v"(ep));
      tagA = ep.x + 1u; tagB = tagA + 0x40000000u;
      if (xw == 0 && lane == 0) lds_wr32(L_TAG, tagA);
    };
    auto A_CLR = [&]() {
      asm volatile("s_nop 1\n\t"
                   "buffer_store_dwordx4 %0, %1, %3, 0 offen sc1\n\tbuffer_store_dwordx4 %0, %1, %3, 0 offen offset:1024 sc1\n\t"
                   "buffer_store_dwordx4 %0, %2, %3, 0 offen sc1\n\tbuffer_store_dwordx4 %0, %2, %3, %4 offen sc1\n\t"
                   "buffer_store_dwordx4 %0, %2, %3, %5 offen sc1\n\tbuffer_store_dwordx4 %0, %2, %3, %6 offen sc1\n\t"
                   "buffer_store_dwordx4 %0, %7, %3, %8 offen sc1\n\tbuffer_store_dwordx4 %0, %7, %3, %8 offen offset:1024 sc1\n\t"
                   "buffer_store_dwordx4 %0, %7, %3, %8 offen offset:2048 sc1\n\tbuffer_store_dwordx4 %0, %7, %3, %8 offen offset:3072 sc1\n\ts_nop 7"
                   ::"v"((u32x4){~0u, ~0u, ~0u, ~0u}), "v"(ffn ? gub + lane * 16 : 0xf0000000u), "v"(ffn ? ypb + (lane >> 3) * 7168 + (lane & 7) * 16 : 0xf0000000u), "s"(rws),
                   "s"(8 * 7168), "s"(16 * 7168), "s"(24 * 7168), "v"(NS ? lane * 16 : 0xf0000000u), "s"(W_H + c * 524288 + (j * 2 + xw) * 4096) : "memory");
    };
    auto A_PUB = [&]() {
      u32x2 rt;
      asm volatile("ds_read_b64 %0, %1\n\ts_waitcnt lgkmcnt(0)" : "=v"(rt) : "v"(L_RT + (lane & 1) * 16 + xw * 8) : "memory");
      st128c(rws, lane < 2 ? (lane ? W_G2 : W_R2) + (c * 128 + j * 8 + xw * 4) * 4 : 0xf0000000u, (u32x4){rt.x, tagA, rt.y, tagB});
    };
    auto A_GAT = [&]() {
#pragma unroll
      for (int q = 0; q < 4; ++q) asm volatile("buffer_load_dwordx4 %0, %1, %2, 0 offen offset:%3 sc1" : "=v"(g[q]) : "v"(rgo), "s"(rws), "n"(q * 1024) : "memory");
      asm volatile("buffer_load_dwordx4 %0, %1, %2, 0 offen sc1" : "=v"(a) : "v"(ggo), "s"(rws) : "memory");
    };
    static_assert(NS <= 3, "cleared blocks");
    A_EP();
    issue(slice(1) + so1, 32768);
    A_CLR();
    asm volatile("s_waitcnt vmcnt(19) lgkmcnt(0)" ::: "memory"); bar();
    issue(slice(2) + so1, 65536);
    issue(slice(3) + so1, 98304);
    waitv<34>(); A_TAG();
    asm volatile("s_waitcnt vmcnt(26) lgkmcnt(0)" ::: "memory"); bar();
    issue(slice(4) + so1, 0);
    asm volatile("s_waitcnt vmcnt(16) lgkmcnt(0)" ::: "memory"); bar();
    issue(slice(5) + so1, 32768);
    waitv<32>(); A_PUB();
    LDW2(0, 16)
    asm volatile("s_waitcnt vmcnt(33) lgkmcnt(0)" ::: "memory"); bar();
    issue(slice(6) + so1, 65536);
    asm volatile("s_waitcnt vmcnt(33) lgkmcnt(0)" ::: "memory"); bar();
    issue(slice(7) + so1, 98304);
    asm volatile("s_waitcnt vmcnt(33) lgkmcnt(0)" ::: "memory"); bar();
    issue(slice(8) + so1, 0);
    asm volatile("s_waitcnt vmcnt(16) lgkmcnt(0)" ::: "memory"); bar();
    issue(slice(9) + so1, 32768);
    asm volatile("s_waitcnt vmcnt(16) lgkmcnt(0)" ::: "memory"); bar();
    issue(slice(10) + so1, 65536);
    A_GAT();
    asm volatile("s_waitcnt vmcnt(21) lgkmcnt(0)" ::: "memory"); bar();
    issue(slice(11) + so1, 98304);
    asm volatile("s_waitcnt vmcnt(21) lgkmcnt(0)" ::: "memory"); bar();
    issue(slice(12) + so1, 0);
    asm volatile("s_waitcnt vmcnt(21) lgkmcnt(0)" ::: "memory"); bar();
    issue(slice(13) + so1, 32768);
    constexpr int K0 = 11;
#pragma unroll
    for (int k = K0; k < 13; ++k) { waitv<16>(); bar(); issue(slice(k + 3) + so1, ((k + 3) & 3) * 32768); }
    asm volatile("" : "+v"(g[0]), "+v"(g[1]), "+v"(g[2]), "+v"(g[3]), "+v"(a));
    LDW1(0)
    waitv<20>(); bar(); LDW1(1)
    waitv<16>(); bar(); LDW1(2)
    waitv<12>(); bar(); LDW1(3)
    bar();
    __builtin_amdgcn_s_setprio(XPRIO2);
    LDW1(4) LDW1(5) LDW1(6) LDW1(7)
    if (W2P == 10) LDW2(0, 16)
    if (lane == 0) lds_wr32(FL_W + xw * 4, 1u);
    {
#pragma unroll 1
      for (int it = 0; it < SPIN_MAX; ++it) {
        bool ok = true;
#pragma unroll
        for (int q = 0; q < 4; ++q) ok = ok && g[q].y == tagA && g[q].w == tagB;
        if (__all(ok && a.y == tagA && a.w == tagB)) break;
        __builtin_amdgcn_s_sleep(8);
        FRESH();
#pragma unroll
        for (int q = 0; q < 4; ++q) g[q] = ld128c(rws, rgo + q * 1024);
        a = ld128c(rws, ggo);
      }
      if (l == 0) {
        const unsigned gp = lane < 32 ? a.x : a.z, ga = L_GT + ((4 * (lane & 1) + 2 * (lane >> 5)) * 16 + ((lane & 31) >> 1)) * 4;
        lds_wr32(ga, gp << 16);
        lds_wr32(ga + 64, gp & 0xffff0000u);
      }
      asm volatile("ds_add_u32 %0, %1" ::"v"(lane == 0 ? (unsigned)FL_G : (unsigned)(L_JNK + lane * 4)), "v"(1u) : "memory");
#pragma unroll
      for (int q = 0; q < 4; ++q) {
        const unsigned r0 = 2047 - (l * 1024 + q * 256 + lane * 4);
        key[4 * q] = (g[q].x << 16) + r0; key[4 * q + 1] = (g[q].x & 0xffff0000u) + (r0 - 1);
        key[4 * q + 2] = (g[q].z << 16) + (r0 - 2); key[4 * q + 3] = (g[q].z & 0xffff0000u) + (r0 - 3);
        asm volatile("" : "+v"(key[4 * q]), "+v"(key[4 * q + 1]), "+v"(key[4 * q + 2]), "+v"(key[4 * q + 3]));
      }
      unsigned m = key[0], m2 = 0, m3 = 0;
#pragma unroll
      for (int i = 1; i < 16; ++i) {
        asm("v_med3_u32 %0, %1, %0, %3\n\tv_med3_u32 %1, %2, %1, %3\n\tv_max_u32 %2, %2, %3" : "+v"(m3), "+v"(m2), "+v"(m) : "v"(key[i]));
      }
      asm volatile("" : "+v"(m), "+v"(m2), "+v"(m3));
      unsigned T = 0;
#pragma unroll
      for (int b = 31; b >= 16; --b) {
        const unsigned t2 = T | (1u << b);
        T = __popcll(__ballot(m >= t2)) >= 8 ? t2 : T;
      }
      asm volatile("" : "+s"(T));
      const u64 k1 = __ballot(m >= T), k2 = __ballot(m2 >= T);
      const int n1 = __popcll(k1);
      if (__ballot(m3 >= T) || n1 + __popcll(k2) > 16) {
        unsigned cand = 0;
#pragma unroll 1
        for (int s = 0; s < 8; ++s) {
          unsigned mm = key[0];
#pragma unroll
          for (int i = 1; i < 16; ++i) mm = max(mm, key[i]);
          mm = wave_maxu(mm);
          cand = lane == s ? mm : cand;
#pragma unroll
          for (int i = 0; i < 16; ++i) key[i] = key[i] == mm ? 0u : key[i];
        }
        if (lane < 8) lds_wr32(L_CAND + l * 64 + lane * 4, cand);
      } else {
        const unsigned p1 = __builtin_amdgcn_mbcnt_hi((unsigned)(k1 >> 32), __builtin_amdgcn_mbcnt_lo((unsigned)k1, 0u));
        const unsigned p2 = __builtin_amdgcn_mbcnt_hi((unsigned)(k2 >> 32), __builtin_amdgcn_mbcnt_lo((unsigned)k2, (unsigned)n1));
        lds_wr32(m >= T ? L_CAND + l * 64 + p1 * 4 : L_JNK + lane * 4, m);
        if (k2) lds_wr32(m2 >= T ? L_CAND + l * 64 + p2 * 4 : L_JNK + lane * 4, m2);
      }
      if (lane == 0) lds_wr32(FL_TK + 8 + l * 4, 1u);
      lds_wait_ge(FL_TK + 8 + (l ^ 1) * 4, 1u);
      const unsigned ck = *(const volatile LAS unsigned*)(smem + L_CAND + (lane & 31) * 4);
      int rank = 0;
#pragma unroll
      for (int i = 0; i < 4; ++i) {
        const u32x4 o = *(const volatile LAS u32x4*)(smem + L_CAND + (lane >> 5) * 64 + i * 16);
        rank += (o.x > ck) + (o.y > ck) + (o.z > ck) + (o.w > ck);
      }
      { auto p = __builtin_amdgcn_permlane32_swap((unsigned)rank, (unsigned)rank, false, false); rank = (int)(p[0] + p[1]); }
      if (l == 0) {
        if (lane < 32 && ck != 0u && rank < 8) {
          const int t = 2047 - (int)(ck & 0xffffu);
          lds_wr32(L_SEL + rank * 4, ck);
          if ((t >> 7) == c) lds_wr32(L_MAP + ((t & 7) * 16 + ((t & 127) >> 3)) * 4, (unsigned)(L_BROW + rank * 256));
        }
        const unsigned any = __ballot(ck != 0u && rank < 8 && ((2047 - (int)(ck & 0xffffu)) >> 7) == c) ? 2u : 1u;
        flag(FL_SEL, any);
        if (bid == 0 && lane == 0) st64c(rws, W_EPOCH, (u32x2){tagA, tagB});
      }
    }
    lds_wait_ge(FL_SEL, 1u);
    const unsigned mine = lds_rd32(L_SEL + (lane & 7) * 4);
    {
      {
        u32x4 xa[4];
#pragma unroll
        for (int u = 0; u < 4; ++u)
          asm volatile("buffer_load_dwordx4 %0, %1, %2, 0 offen offset:%3" : "=v"(xa[u]) : "v"((2047 - (mine & 0xffffu)) * 2048 + (kq * 256 + 32 * (fr >> 3) + 8 * fq) * 2), "s"(rx), "n"(u * 128) : "memory");
        TRN(w1[0], W2P == 10 ? 36 : 20) TRN(w1[1], W2P == 10 ? 36 : 20) TRN(w1[2], W2P == 10 ? 28 : 12) TRN(w1[3], 0)
        asm volatile("" : "+v"(xa[0]), "+v"(xa[1]), "+v"(xa[2]), "+v"(xa[3]));
        const bool val = (fq >> 1) == (fr >> 3), lo = lane < 32;
        auto publish = [&](f32x4 (&d)[8], unsigned o, unsigned sp) {
          u32x4 pv[4];
#pragma unroll
          for (int rr = 0; rr < 2; ++rr)
#pragma unroll
            for (int q = 0; q < 8; ++q) {
              auto p = __builtin_amdgcn_permlane32_swap(__float_as_uint(d[q][rr]), __float_as_uint(d[q][2 + rr]), false, false);
              const unsigned own = lo ? p[0] : p[1], oth = lo ? p[1] : p[0];
              pv[rr * 2 + (q >> 2)][q & 3] = min(__float_as_uint(__uint_as_float(own) + dpp1<0x128>(__uint_as_float(oth))), 0xfffffffeu);
            }
          asm volatile("s_nop 4\n\t"
                       "buffer_store_dwordx4 %0, %4, %5, 0 offen sc1\n\tbuffer_store_dwordx4 %1, %4, %5, %6 offen sc1\n\t"
                       "buffer_store_dwordx4 %2, %4, %5, %7 offen sc1\n\tbuffer_store_dwordx4 %3, %4, %5, %8 offen sc1\n\t"
                       "s_nop 7\n\ts_nop 7"
                       ::"v"(pv[0]), "v"(pv[1]), "v"(pv[2]), "v"(pv[3]), "v"(val && ffn ? o : 0xf0000000u), "s"(rws), "s"(sp), "s"(2 * sp), "s"(3 * sp) : "memory");
        };
        f32x4 d[8];
#pragma unroll
        for (int q = 0; q < 8; ++q) d[q] = (f32x4){0.f, 0.f, 0.f, 0.f};
#pragma unroll
        for (int u = 0; u < 4; ++u)
#pragma unroll
          for (int q = 0; q < 8; ++q) d[q] = __builtin_amdgcn_mfma_f32_16x16x32_bf16(__builtin_bit_cast(bf16x8, xa[u]), __builtin_bit_cast(bf16x8, w1[u][q]), d[q], 0, 0, 0);
        publish(d, gub + (16 * (fr >> 3) + 8 * (fq & 1) + (fr & 7)) * 16, 512);
        TRN(w2[0], 63) TRN(w2[1], 63)
        lds_wait_ge(FL_CFREE, XG2);
        __builtin_amdgcn_s_setprio(3);
        if (ffn) {
          f32x4 G, U;
#pragma unroll 1
          for (int it = 0; it < SPIN_MAX; ++it) {
            FRESH();
            u32x4 a_[8];
#pragma unroll
            for (int k = 0; k < 8; ++k) a_[k] = __builtin_amdgcn_raw_buffer_load_b128(rws, W_GU + sg * 16384 + xw * 1024 + lane * 16, k * 2048, 16);
            unsigned m = 0;
#pragma unroll
            for (int k = 0; k < 8; ++k) m = max(max(m, max(a_[k].x, a_[k].y)), max(a_[k].z, a_[k].w));
            G = (__builtin_bit_cast(f32x4, a_[0]) + __builtin_bit_cast(f32x4, a_[2])) + (__builtin_bit_cast(f32x4, a_[4]) + __builtin_bit_cast(f32x4, a_[6]));
            U = (__builtin_bit_cast(f32x4, a_[1]) + __builtin_bit_cast(f32x4, a_[3])) + (__builtin_bit_cast(f32x4, a_[5]) + __builtin_bit_cast(f32x4, a_[7]));
            if (__all(m != ~0u)) break;
            __builtin_amdgcn_s_sleep(8);
          }
          float a4[4];
#pragma unroll
          for (int e = 0; e < 4; ++e) { const float g = rbf(G[e]); a4[e] = rbf(g * __builtin_amdgcn_rcpf(1.f + __expf(-g))) * rbf(U[e]); }
          asm volatile("s_nop 0\n\tds_write_b64 %0, %1" ::"v"(L_ACT + (4 * ((lane >> 3) & 1) + 2 * ((lane >> 4) & 1) + xw) * 128 + (lane & 7) * 16 + (lane >> 5) * 8),
                       "v"((u32x2){pk2(a4[0], a4[1]), pk2(a4[2], a4[3])}) : "memory");
          if (lane == 0) lds_wr32(FL_TK + xw * 4, 1u);
          lds_wait_ge(FL_TK + (xw ^ 1) * 4, 1u);
        }
        const u32x4 af = lds_rd128(L_ACT + ((fr & 7) * 8 + 4 * (fr >> 3) + fq) * 16);
#pragma unroll
        for (int u = 0; u < 2; ++u) {
#pragma unroll
          for (int q = 0; q < 8; ++q) d[q] = __builtin_amdgcn_mfma_f32_16x16x32_bf16(__builtin_bit_cast(bf16x8, af), __builtin_bit_cast(bf16x8, w2[u][q]), (f32x4){0.f, 0.f, 0.f, 0.f}, 0, 0, 0);
          publish(d, ypb + (u * 16 + 2 * (fr >> 3) + (fq & 1)) * 7168 + (fr & 7) * 16, 4 * 7168);
        }
      }
    }
    __builtin_amdgcn_s_setprio(3);
    unsigned rest = (unsigned)__ballot(((2047 - (int)(mine & 0xffffu)) >> 7) == c) & 0xffu;
    if (xw) rest &= rest - 1;
#pragma unroll 1
    for (; rest; rest &= rest - 1, rest &= rest - 1) {
      const int s = __builtin_ctz(rest);
      const unsigned ks = (unsigned)__builtin_amdgcn_readlane((int)mine, s);
      const int t = 2047 - (int)(ks & 0xffffu);
      const float rs = rbf(1.f / (1.f + __expf(-key_val(ks))));
      const unsigned yb = W_YP + (j * 16 + (s & 1) * 8 + 2 * ((s >> 1) & 1) + (s >> 2)) * 7168;
      const u32x2 xr = __builtin_amdgcn_raw_buffer_load_b64(rx, t * 2048 + (j * 64 + 8 * (lane & 7) + 4 * ((lane >> 3) & 1)) * 2, 0, 0);
      lds_wait_ge(FL_CFREE, YGATE);
      const float omg = rbf(1.f - __uint_as_float(lds_rd32(L_GT + ((t & 7) * 16 + ((t & 127) >> 3)) * 4)));
      f32x4 y[2];
#pragma unroll 1
      for (int it = 0; it < SPIN_MAX; ++it) {
        FRESH();
        unsigned m = 0;
#pragma unroll
        for (int h = 0; h < 2; ++h) {
          y[h] = (f32x4){0.f, 0.f, 0.f, 0.f};
#pragma unroll
          for (int k = 0; k < 7; ++k) {
            const u32x4 a = __builtin_amdgcn_raw_buffer_load_b128(rws, yb + lane * 16, h * 4 * 7168 + k * 1024, 16);
            m = max(max(m, max(a.x, a.y)), max(a.z, a.w));
            y[h] += __builtin_bit_cast(f32x4, a);
          }
        }
        if (__all(m != ~0u)) break;
        __builtin_amdgcn_s_sleep(6);
      }
      const bool o8 = lane & 8;
      const f32x4 xf = unpk4(xr);
      f32x4 dl;
#pragma unroll
      for (int e = 0; e < 4; ++e) {
        const float v = sum16_32((o8 ? y[1][e] : y[0][e]) + dpp1<0x128>(o8 ? y[0][e] : y[1][e]));
        dl[e] = rbf(mul1(omg, rbf(xf[e] + rbf(mul1(rbf(v), rs))))) - rbf(mul1(omg, xf[e]));
      }
      if (lane < 16) asm volatile("s_nop 0\n\tds_write_b128 %0, %1\n\ts_nop 3" ::"v"(L_BROW + s * 256 + (lane & 7) * 32 + (lane >> 3) * 16), "v"(dl) : "memory");
    }
    if (lane == 0) lds_wr32(FL_XD + xw * 4, 1u);
    asm volatile("Lk_x:");
    return;
  }
  }

  __builtin_amdgcn_s_setprio(3);
  relane();
  u64x8 ma, mb;
  asm volatile("s_load_dwordx16 %0, %2, 0x0\n\ts_load_dwordx16 %1, %2, 0x40\n\ts_waitcnt lgkmcnt(0)" : "=&s"(ma), "=&s"(mb) : "s"(__builtin_amdgcn_kernarg_segment_ptr()), "s"(kw0), "s"(kw1));
  const char *Lre = (const char*)ma[1], *Lim = (const char*)ma[2], *logdt = (const char*)ma[7], *Dskip = (const char*)mb[0], *w_r = (const char*)mb[1], *gate_w = (const char*)mb[5],
             *out = (const char*)mb[7];
  char* ws = (char*)mb[6];
  const rsrc_t rws = mk(ws, W_END);
  u32x2 p0, p1, p2;
  u32x4 xv[2][2], wv[2], gv[2];
  {
    const rsrc_t rx = mk((const char*)ma[0], 2048 * 1024 * 2);
    const unsigned pmo = (j * 64 + wid * 16 + 4 * fq) * 2;
    asm volatile("buffer_load_dwordx2 %0, %4, %5, 0 offen\n\tbuffer_load_dwordx2 %1, %4, %6, 0 offen\n\tbuffer_load_dwordx2 %2, %4, %7, 0 offen"
                 : "=&v"(p0), "=&v"(p1), "=&v"(p2) : "n"(0), "v"(pmo), "s"(mk(Lre, 2048)), "s"(mk(Lim, 2048)), "s"(mk(logdt, 2048)) : "memory");
    asm volatile("buffer_load_dwordx4 %0, %8, %9, %12 offen\n\tbuffer_load_dwordx4 %1, %8, %9, %12 offen offset:1024\n\tbuffer_load_dwordx4 %2, %8, %9, %12 offen offset:2048\n\t"
                 "buffer_load_dwordx4 %3, %8, %9, %12 offen offset:3072\n\tbuffer_load_dwordx4 %4, %8, %10, 0 offen\n\tbuffer_load_dwordx4 %5, %8, %10, 0 offen offset:1024\n\t"
                 "buffer_load_dwordx4 %6, %8, %11, 0 offen\n\tbuffer_load_dwordx4 %7, %8, %11, 0 offen offset:1024"
                 : "=&v"(xv[0][0]), "=&v"(xv[0][1]), "=&v"(xv[1][0]), "=&v"(xv[1][1]), "=&v"(wv[0]), "=&v"(wv[1]), "=&v"(gv[0]), "=&v"(gv[1])
                 : "v"(lane * 16), "s"(rx), "s"(mk(w_r, 2048)), "s"(mk(gate_w, 2048)), "s"((c * 128 + j * 8 + wid * 2) * 2048) : "memory");
  }
  f32x4 lr, li, cr, ci, Er, Ei, Fr, Fi, br_, bi_;
  u32x2 xe[2][4], dv;
  {
    asm volatile("buffer_load_dwordx2 %0, %4, %5, 0 offen\n\ts_waitcnt vmcnt(9)" : "=&v"(dv), "+v"(p0), "+v"(p1), "+v"(p2) : "v"((j * 64 + 4 * fr) * 2), "s"(mk(Dskip, 2048)) : "memory");
    const f32x4 lre = unpk4(p0), lim = unpk4(p1), ld = unpk4(p2) * splat4(1.44269504f);
    f32x4 dt, id = fma4(lre, lre, lim * lim);
#pragma unroll
    for (int r = 0; r < 4; ++r) { dt[r] = rbf(__builtin_amdgcn_exp2f(ld[r])); id[r] = __builtin_amdgcn_rcpf(id[r]); }
    const f32x4 A2 = dt * lre * splat4(1.44269504f), Bv = dt * lim * splat4(0.159154943f);
    cexp4(A2, Bv, lr, li);
    const f32x4 nr = lr - splat4(1.f);
    cr = fma4(nr, lre, li * lim) * id; ci = fma4(li, lre, nml4(nr, lim)) * id;
    const f32x4 ne = splat4((float)(8 * fr)), nf = splat4((float)((fr & 7) + 1));
    cexp4(A2 * ne, Bv * ne, Er, Ei);
    f32x4 tr, ti;
    cexp4(A2 * nf, Bv * nf, tr, ti);
    Fr = fma4(cr, tr, nml4(ci, ti)); Fi = fma4(cr, ti, ci * tr);
    const float a1 = (fr & 2) ? ((fr & 1) ? A2.w : A2.z) : ((fr & 1) ? A2.y : A2.x), b1 = (fr & 2) ? ((fr & 1) ? Bv.w : Bv.z) : ((fr & 1) ? Bv.y : Bv.x);
    const int e0 = c - 1 - (fr >> 2);
    const f32x4 nl = {(float)(128 * max(e0, 0)), (float)(128 * max(e0 - 4, 0)), (float)(128 * max(e0 - 8, 0)), (float)(128 * max(e0 - 12, 0))};
    cexp4(splat4(a1) * nl, splat4(b1) * nl, br_, bi_);
    asm volatile("" : "+v"(lr), "+v"(li), "+v"(cr), "+v"(ci), "+v"(Er), "+v"(Ei), "+v"(Fr), "+v"(Fi), "+v"(br_), "+v"(bi_));
  }
  asm volatile("s_waitcnt vmcnt(0)" : "+v"(dv), "+v"(xv[0][0]), "+v"(xv[0][1]), "+v"(xv[1][0]), "+v"(xv[1][1]), "+v"(wv[0]), "+v"(wv[1]), "+v"(gv[0]), "+v"(gv[1])::"memory");
  {
    float sv[4];
#pragma unroll
    for (int r = 0; r < 2; ++r) {
      float sr = 0.f, sg_ = 0.f;
#pragma unroll
      for (int h = 0; h < 2; ++h)
#pragma unroll
        for (int e = 0; e < 4; ++e) {
          const unsigned xx = xv[r][h][e], ww = wv[h][e], gw = gv[h][e];
          sr = __builtin_amdgcn_fdot2_f32_bf16(__builtin_bit_cast(bf16x2_t, xx), __builtin_bit_cast(bf16x2_t, ww), sr, false);
          sg_ = __builtin_amdgcn_fdot2_f32_bf16(__builtin_bit_cast(bf16x2_t, xx), __builtin_bit_cast(bf16x2_t, gw), sg_, false);
        }
      sv[r] = sr; sv[2 + r] = sg_;
    }
    const bool b1 = lane & 1, b2 = lane & 2;
    const float k0 = (b1 ? sv[1] : sv[0]) + dpp1<0xb1>(b1 ? sv[0] : sv[1]), k1 = (b1 ? sv[3] : sv[2]) + dpp1<0xb1>(b1 ? sv[2] : sv[3]);
    float k2 = (b2 ? k1 : k0) + dpp1<0x4e>(b2 ? k0 : k1);
    k2 += dpp1<0x124>(k2);
    k2 += dpp1<0x128>(k2);
    k2 = sum16_32(k2);
    const unsigned vb = b2 ? f2bf(1.f / (1.f + __expf(-rbf(k2)))) : route_key(rbf(k2), 0) >> 16;
    const unsigned p01 = vb | ((unsigned)__builtin_amdgcn_update_dpp(0, (int)vb, 0xb1, 0xf, 0xf, true) << 16);
    asm volatile("ds_write_b32 %0, %1\n\ts_waitcnt lgkmcnt(0)" ::"v"((lane & 61) == 0 ? (unsigned)(L_RT + (lane * 2 + wid) * 4) : (unsigned)(L_JNK + lane * 4)), "v"(p01) : "memory");
  }
  __builtin_amdgcn_s_setprio(2);
  f32x4 acc[2][8];
  const unsigned ago = fr < 4 ? W_AGG + ((j * 16 + c) * 64 + wid * 16 + 4 * fq + fr) * 16u : 0xf0000000u;
  {
#pragma unroll
    for (int m = 0; m < 2; ++m)
#pragma unroll
      for (int n = 0; n < 8; ++n) acc[m][n] = (f32x4){0.f, 0.f, 0.f, 0.f};
    const int x0 = (fq ^ (fr & 7)) * 16, x1 = ((4 + fq) ^ (fr & 7)) * 16;
    unsigned pa0 = 16384 + (wid * 32 + fr) * 128 + x0, pa1 = 16384 + (wid * 32 + fr) * 128 + x1, pb0 = fr * 128 + x0, pb1 = fr * 128 + x1;
    unsigned ss, sit, xad[4];
#pragma unroll
    for (int r = 0; r < 4; ++r) xad[r] = 98304 + (32 * wid + 4 * fq + r) * 128 + (((fr >> 1) ^ (4 * (fq & 1) + r)) * 16) + (fr & 1) * 8;
    bf16x8 fa[2][2], fb[8];
    asm volatile(ASM_GEMM1 : [c00] "+v"(acc[0][0]), [c01] "+v"(acc[0][1]), [c02] "+v"(acc[0][2]), [c03] "+v"(acc[0][3]), [c04] "+v"(acc[0][4]), [c05] "+v"(acc[0][5]), [c06] "+v"(acc[0][6]), [c07] "+v"(acc[0][7]), [c10] "+v"(acc[1][0]), [c11] "+v"(acc[1][1]), [c12] "+v"(acc[1][2]), [c13] "+v"(acc[1][3]), [c14] "+v"(acc[1][4]), [c15] "+v"(acc[1][5]), [c16] "+v"(acc[1][6]), [c17] "+v"(acc[1][7]), [a00] "=&v"(fa[0][0]), [a01] "=&v"(fa[0][1]), [a10] "=&v"(fa[1][0]), [a11] "=&v"(fa[1][1]), [b0] "=&v"(fb[0]), [b1] "=&v"(fb[1]), [b2] "=&v"(fb[2]), [b3] "=&v"(fb[3]), [b4] "=&v"(fb[4]), [b5] "=&v"(fb[5]), [b6] "=&v"(fb[6]), [b7] "=&v"(fb[7]), [pa0] "+v"(pa0), [pa1] "+v"(pa1), [pb0] "+v"(pb0), [pb1] "+v"(pb1), [it] "=&s"(sit), [s] "=&s"(ss), [xe00] "=&v"(xe[0][0]), [xe01] "=&v"(xe[0][1]), [xe02] "=&v"(xe[0][2]), [xe03] "=&v"(xe[0][3]), [xe10] "=&v"(xe[1][0]), [xe11] "=&v"(xe[1][1]), [xe12] "=&v"(xe[1][2]), [xe13] "=&v"(xe[1][3]) : [xa0] "v"(xad[0]), [xa1] "v"(xad[1]), [xa2] "v"(xad[2]), [xa3] "v"(xad[3]) : "memory", "scc", "v250", "v251");
  }
  __builtin_amdgcn_s_setprio(3);
  const unsigned tagA = *(const volatile LAS unsigned*)(smem + L_TAG), tagB = tagA + 0x40000000u;
  {
#pragma unroll
    for (int n = 1; n < 8; ++n) {
      const f32x4 pr = acc[0][n - 1], pi = acc[1][n - 1];
      acc[0][n] = fma4(lr, pr, fnm4(li, pi, acc[0][n]));
      acc[1][n] = fma4(lr, pi, fma4(li, pr, acc[1][n]));
    }
    const f32x4 wr = dpp4<0x140>(Er), wi = dpp4<0x140>(Ei);
    const f32x4 zr = fma4(wr, acc[0][7], nml4(wi, acc[1][7])), zi = fma4(wr, acc[1][7], wi * acc[0][7]);
    const bool o1 = fr & 1, o2 = fr & 2;
    const float r01 = (o1 ? zr.y : zr.x) + dpp1<0xb1>(o1 ? zr.x : zr.y), r23 = (o1 ? zr.w : zr.z) + dpp1<0xb1>(o1 ? zr.z : zr.w);
    const float i01 = (o1 ? zi.y : zi.x) + dpp1<0xb1>(o1 ? zi.x : zi.y), i23 = (o1 ? zi.w : zi.z) + dpp1<0xb1>(o1 ? zi.z : zi.w);
    float sr = (o2 ? r23 : r01) + dpp1<0x4e>(o2 ? r01 : r23), si = (o2 ? i23 : i01) + dpp1<0x4e>(o2 ? i01 : i23);
    sr += dpp1<0x124>(sr); si += dpp1<0x124>(si);
    sr += dpp1<0x128>(sr); si += dpp1<0x128>(si);
    u32x4 av = {__float_as_uint(sr), tagA, __float_as_uint(si), tagB};
    asm volatile("s_nop 1\n\tbuffer_store_dwordx4 %0, %1, %2, 0 offen sc1\n\ts_nop 7" : "+v"(av) : "v"(ago), "s"(rws) : "memory");
  }
  unsigned ao[4];
  u32x4 g[4];
#pragma unroll
  for (int i = 0; i < 4; ++i) {
    ao[i] = (fr >> 2) + 4 * i < c ? W_AGG + ((j * 16 + (fr >> 2) + 4 * i) * 64 + wid * 16 + 4 * fq + (fr & 3)) * 16u : 0xf0000000u;
    g[i] = (u32x4){0u, 0u, 0u, 0u};
  }
  if (POLLN < 0) {
    FRESH();
#pragma unroll
    for (int i = 0; i < 4; ++i)
      if (4 * i < c) g[i] = ld128c(rws, ao[i]);
    FRESH();
  }
  f32x4 Pr, Pi, qr[8], qi[8];
  {
    f32x4 yr = acc[0][7], yi = acc[1][7];
#define KS_STEP(S, CTRL) { const f32x4 mr = dpp4<0x150 + S>(Er), mi = dpp4<0x150 + S>(Ei); const f32x4 sr = dpp4<CTRL>(yr), si = dpp4<CTRL>(yi); \
      yr = fma4(mr, sr, fnm4(mi, si, yr)); yi = fma4(mr, si, fma4(mi, sr, yi)); }
    KS_STEP(1, 0x111) KS_STEP(2, 0x112) KS_STEP(4, 0x114) KS_STEP(8, 0x118)
    Pr = dpp4<0x111>(yr); Pi = dpp4<0x111>(yi);
    asm volatile("" : "+v"(Pr), "+v"(Pi));
#pragma unroll
    for (int n = 0; n < 8; ++n) {
      if (n == POLLN) {
        FRESH();
#pragma unroll
        for (int i = 0; i < 4; ++i)
          if (4 * i < c) g[i] = ld128c(rws, ao[i]);
        FRESH();
      }
      bcast8(n, Fr, qr[n]); bcast8(n, Fi, qi[n]);
      const f32x4 br = fma4(cr, acc[0][n], nml4(ci, acc[1][n])), bi = fnm4(cr, acc[1][n], nml4(ci, acc[0][n]));
      acc[0][n] = br; acc[1][n] = bi;
      asm volatile("" : "+v"(acc[0][n]), "+v"(acc[1][n]), "+v"(qr[n]), "+v"(qi[n]));
    }
  }
  f32x4 Xr = Pr, Xi = Pi;
  if (POLLN == 8) {
    FRESH();
#pragma unroll
    for (int i = 0; i < 4; ++i)
      if (4 * i < c) g[i] = ld128c(rws, ao[i]);
    FRESH();
  }
  u32x2 xd[2][4];
  lds_wait_ge(FL_G, 2u);
  {
    const f32x4 df = unpk4(dv) * splat4(0.5f);
#pragma unroll
    for (int m = 0; m < 2; ++m) {
      const f32x4 ge = __builtin_bit_cast(f32x4, lds_rd128(L_GT + ((2 * wid + m) * 16 + 4 * fq) * 4));
#pragma unroll
      for (int r = 0; r < 4; ++r) {
        const f32x4 xf = unpk4(xe[m][r]), p = xf * df, o = xf * splat4(rbf(1.f - ge[r]));
        xd[m][r] = (u32x2){pk2(p.x, p.y), pk2(p.z, p.w)};
        xe[m][r] = (u32x2){pk2(o.x, o.y), pk2(o.z, o.w)};
        asm volatile("" : "+v"(xd[m][r]), "+v"(xe[m][r]));
      }
    }
  }
  if (c > 0) {
#pragma unroll 1
    for (int it = 0; it < SPIN_MAX; ++it) {
      bool ok = true;
#pragma unroll
      for (int i = 0; i < 4; ++i) ok = ok && (ao[i] == 0xf0000000u || (g[i].y == tagA && g[i].w == tagB));
      if (__all(ok)) break;
      FRESH();
#pragma unroll
      for (int i = 0; i < 4; ++i)
        if (4 * i < c) g[i] = ld128c(rws, ao[i]);
    }
    const f32x4 sx = {__uint_as_float(g[0].x), __uint_as_float(g[1].x), __uint_as_float(g[2].x), __uint_as_float(g[3].x)};
    const f32x4 sy = {__uint_as_float(g[0].z), __uint_as_float(g[1].z), __uint_as_float(g[2].z), __uint_as_float(g[3].z)};
    const f32x4 tx = fma4(br_, sx, nml4(bi_, sy)), ty = fma4(br_, sy, bi_ * sx);
    float gx = (tx.x + tx.z) + (tx.y + tx.w), gy = (ty.x + ty.z) + (ty.y + ty.w);
    gx += dpp1<0x124>(gx); gy += dpp1<0x124>(gy);
    gx += dpp1<0x128>(gx); gy += dpp1<0x128>(gy);
    const f32x4 Gr = {dpp1<0x00>(gx), dpp1<0x55>(gx), dpp1<0xaa>(gx), dpp1<0xff>(gx)}, Gi = {dpp1<0x00>(gy), dpp1<0x55>(gy), dpp1<0xaa>(gy), dpp1<0xff>(gy)};
    Xr = fma4(Er, Gr, fnm4(Ei, Gi, Pr)); Xi = fma4(Er, Gi, fma4(Ei, Gr, Pi));
  }
  relane();
  const unsigned jnk = L_JNK + lane * 4;
  const unsigned ho = W_H + (c * 64 + 4 * ((j - crot) & 15) + wid) * 8192 + lane * 16;
  u32x4 hv[8];
  __builtin_amdgcn_sched_barrier(0);
#define HST(t_) asm volatile("s_nop 1\n\tbuffer_store_dwordx4 %0, %1, %2, %3 offen offset:%4 sc1" : "+v"(hv[t_]) : "v"(ho), "s"(rws), "s"(((t_) >> 2) * 4096), "n"(((t_) & 3) * 1024) : "memory")
#pragma unroll
  for (int n = 0; n < 8; ++n) {
    const f32x4 hr = fma4(qr[n], Xr, fnm4(qi[n], Xi, acc[0][n])), hi = fnm4(qr[n], Xi, fnm4(qi[n], Xr, acc[1][n]));
    const auto p = __builtin_amdgcn_permlane16_swap(pk2(hr.x, hr.y), pk2(hi.x, hi.y), false, false);
    const auto q = __builtin_amdgcn_permlane16_swap(pk2(hr.z, hr.w), pk2(hi.z, hi.w), false, false);
    hv[n] = (u32x4){NS ? min(p[0], 0xfffffffeu) : p[0], q[0], NS ? min(p[1], 0xfffffffeu) : p[1], q[1]};
    if (NSI == 8) HST(n); else if (NSI == 4 ? (n & 1) : n < NSI) HST(NSI == 4 ? n >> 1 : n);
    __builtin_amdgcn_sched_barrier(0);
    if (!DSL && (n >> 1) != wid) asm volatile("ds_write_b128 %0, %1" ::"v"(L_XH + (((n >> 1) * 3 + wid - (wid > (n >> 1) ? 1 : 0)) * 2 + (n & 1)) * 1024 + lane * 16), "v"(hv[n]) : "memory");
  }
  if (DSL) {
#pragma unroll
    for (int w = 0; w < 4; ++w)
      if (w != wid) asm volatile("ds_write_b128 %0, %1\n\tds_write_b128 %0, %2 offset:1024" ::"v"(L_XH + (w * 3 + wid - (wid > w ? 1 : 0)) * 2048 + lane * 16), "v"(hv[2 * w]), "v"(hv[2 * w + 1]) : "memory");
  }
  if (lane == 0) lds_wr32(FL_HS + wid * 4, 1u);
  const rsrc_t rh = mk(ws + W_H + c * 524288, 524288);
  const unsigned vo = wid * 2048 + lane * 16;
  u32x32 hw0, hw1;
  unsigned st[11], pc[4];
  f32x4 ac2[2][4];
#define ACK() do { asm volatile("s_waitcnt vmcnt(0)" : "+v"(hv[0]), "+v"(hv[1]), "+v"(hv[2]), "+v"(hv[3]), "+v"(hv[4]), "+v"(hv[5]), "+v"(hv[6]), "+v"(hv[7])::"memory"); \
    asm volatile("buffer_store_dwordx2 %0, %1, %2, 0 offen sc1" ::"v"((u32x2){tagA, tagB}), "v"(lane == 0 ? W_HF + (c * 64 + 4 * ((j - crot) & 15) + wid) * 8 : 0xf0000000u), "s"(rws) : "memory"); \
    lds_wr32(lane == 0 ? (unsigned)(FL_FREE + wid * 4) : jnk, 9u); \
    if (NS) { if (RQD) __builtin_amdgcn_s_sleep(RQD); asm volatile(ASM_REQ : "=&{v[184:215]}"(hw0), "=&{v[216:247]}"(hw1), [hb0] "=&s"(st[0]), [hb1] "=&s"(st[1]), [hb2] "=&s"(st[2]), [hb3] "=&s"(st[3]) : [vo] "v"(vo), [own] "s"(pown), [srd] "s"(rh) : "memory", "scc"); } } while (0)
#pragma unroll
  for (int sg = 0; sg < 4; ++sg) pc[sg] = (fq & 1) * 8192 + fr * 128 + (((2 * sg + (fq >> 1)) ^ ((fr >> 1) & 7)) * 16);
  {
    lds_wait_ge(FL_C, 1u);
    u32x4 k[4][4], h[4][2];
#pragma unroll
    for (int i = 0; i < 4; ++i) {
      const int sw = i == 0 ? wid : i - (i <= wid ? 1 : 0);
      asm volatile("ds_read_b128 %0, %4\n\tds_read_b128 %1, %4 offset:2048\n\tds_read_b128 %2, %4 offset:4096\n\tds_read_b128 %3, %4 offset:6144"
                   : "=&v"(k[i][0]), "=&v"(k[i][1]), "=&v"(k[i][2]), "=&v"(k[i][3]) : "v"(pc[0] ^ (sw * 32)) : "memory");
    }
#pragma unroll
    for (int m = 0; m < 2; ++m)
#pragma unroll
      for (int r = 0; r < 4; ++r) {
        const f32x4 t = unpk4(xd[m][r]);
#pragma unroll
        for (int n = 0; n < 4; ++n) ac2[m][n][r] = t[n];
      }
    h[0][0] = hv[0]; h[0][1] = hv[1];
    if (wid == 1) { h[0][0] = hv[2]; h[0][1] = hv[3]; }
    if (wid == 2) { h[0][0] = hv[4]; h[0][1] = hv[5]; }
    if (wid == 3) { h[0][0] = hv[6]; h[0][1] = hv[7]; }
    asm volatile("" : "+v"(ac2[0][0]), "+v"(ac2[0][1]), "+v"(ac2[0][2]), "+v"(ac2[0][3]), "+v"(ac2[1][0]), "+v"(ac2[1][1]), "+v"(ac2[1][2]), "+v"(ac2[1][3]), "+v"(h[0][0]), "+v"(h[0][1]));
    if (NSI < 8) HST(NSI);
    if (MGP == 1) ACK();
    lds_wait_min4(FL_HS, 1u);
    asm volatile("ds_read_b128 %0, %6\n\tds_read_b128 %1, %6 offset:1024\n\tds_read_b128 %2, %6 offset:2048\n\tds_read_b128 %3, %6 offset:3072\n\tds_read_b128 %4, %6 offset:4096\n\tds_read_b128 %5, %6 offset:5120"
                 : "=&v"(h[1][0]), "=&v"(h[1][1]), "=&v"(h[2][0]), "=&v"(h[2][1]), "=&v"(h[3][0]), "=&v"(h[3][1]) : "v"(L_XH + wid * 6144 + lane * 16) : "memory");
    lds_wr32(lane == 0 ? (unsigned)(FL_CFREE + wid * 4) : jnk, 1u);
#pragma unroll
    for (int i = 0; i < 4; ++i) {
      if (i == 1) asm volatile("s_waitcnt lgkmcnt(0)" : "+v"(h[1][0]), "+v"(h[1][1]), "+v"(h[2][0]), "+v"(h[2][1]), "+v"(h[3][0]), "+v"(h[3][1])::"memory");
      asm volatile("" : "+v"(k[i][0]), "+v"(k[i][1]), "+v"(k[i][2]), "+v"(k[i][3]));
#pragma unroll
      for (int n = 0; n < 4; ++n) {
        ac2[0][n] = __builtin_amdgcn_mfma_f32_16x16x32_bf16(__builtin_bit_cast(bf16x8, h[i][0]), __builtin_bit_cast(bf16x8, k[i][n]), ac2[0][n], 0, 0, 0);
        ac2[1][n] = __builtin_amdgcn_mfma_f32_16x16x32_bf16(__builtin_bit_cast(bf16x8, h[i][1]), __builtin_bit_cast(bf16x8, k[i][n]), ac2[1][n], 0, 0, 0);
      }
      asm volatile("" : "+v"(ac2[0][0]), "+v"(ac2[0][1]), "+v"(ac2[0][2]), "+v"(ac2[0][3]), "+v"(ac2[1][0]), "+v"(ac2[1][1]), "+v"(ac2[1][2]), "+v"(ac2[1][3])::"memory");
      if (NSI + 1 + i < 8) HST(NSI + 1 + i < 8 ? NSI + 1 + i : 0);
    }
    static_assert(NSI >= 3 && (MGP == 0 || NSI >= 7), "all stores");
  }
  if (MGP == 0) ACK();
  f32x4 ov[2][4];
  float g2[2][4];
#pragma unroll
  for (int m = 0; m < 2; ++m) {
    const f32x4 ge = lds4(smem, L_GT + ((2 * wid + m) * 16 + 4 * fq) * 4);
#pragma unroll
    for (int r = 0; r < 4; ++r) {
      ov[m][r] = unpk4(xe[m][r]);
      g2[m][r] = ge[r] + ge[r];
      asm volatile("" : "+v"(ov[m][r]), "+v"(g2[m][r]));
    }
  }
  __builtin_amdgcn_s_setprio(MPRIO2);

  {
    const unsigned fad = FL_C, fwd = lane == 0 ? (unsigned)(FL_CFREE + wid * 4) : jnk;
    bf16x8 kb[2][4];
    unsigned fv = 1;
#pragma unroll
    for (int sg = 0; sg < 4; ++sg) pc[sg] += 16384;
    asm volatile(ASM_GEMM2 : [c00] "+v"(ac2[0][0]), [c01] "+v"(ac2[0][1]), [c02] "+v"(ac2[0][2]), [c03] "+v"(ac2[0][3]), [c10] "+v"(ac2[1][0]), [c11] "+v"(ac2[1][1]), [c12] "+v"(ac2[1][2]), [c13] "+v"(ac2[1][3]), "=&{v[184:215]}"(hw0), "=&{v[216:247]}"(hw1), [k00] "=&v"(kb[0][0]), [k01] "=&v"(kb[0][1]), [k02] "=&v"(kb[0][2]), [k03] "=&v"(kb[0][3]), [k10] "=&v"(kb[1][0]), [k11] "=&v"(kb[1][1]), [k12] "=&v"(kb[1][2]), [k13] "=&v"(kb[1][3]), [pc0] "+v"(pc[0]), [pc1] "+v"(pc[1]), [pc2] "+v"(pc[2]), [pc3] "+v"(pc[3]), [fv] "+v"(fv), [s] "=&s"(st[0]), [s2] "=&s"(st[1]), [to] "=&s"(st[2]), [hn] "=&s"(st[3]), [cn] "=&s"(st[4]), [dl] "=&s"(st[5]), [nf] "=&s"(st[6]), [hb0] "=&s"(st[7]), [hb1] "=&s"(st[8]), [hb2] "=&s"(st[9]), [hb3] "=&s"(st[10]) : [vo] "v"(vo), [own] "s"(pown), [srd] "s"(rh), [fa] "v"(fad), [fw] "v"(fwd), [d16] "s"(0x4000), [wr] "s"(0x500) : "memory", "scc", "vcc", "v250", "v252", "v253", "v254", "v255");
  }
  relane();
  const unsigned sel = lds_rd32(FL_SEL);
#pragma unroll
  for (int m = 0; m < 2; ++m)
#pragma unroll
    for (int r = 0; r < 4; ++r)
#pragma unroll
      for (int n = 0; n < 4; ++n) ov[m][r][n] = fma1(g2[m][r], ac2[m][n][r], ov[m][r][n]);
  if (__builtin_amdgcn_readfirstlane(sel) != 1u) {
#pragma unroll 1
    for (int it = 0; it < SPIN_MAX * 4; ++it) {
      const u32x2 f = *(const volatile LAS u32x2*)(smem + FL_XD);
      if (__builtin_amdgcn_readfirstlane(f.x & f.y)) break;
      __builtin_amdgcn_s_sleep(1);
    }
    FRESH();
#pragma unroll
    for (int m = 0; m < 2; ++m) {
      const u32x4 me = *(const LAS u32x4*)(smem + L_MAP + ((2 * wid + m) * 16 + 4 * fq) * 4);
#pragma unroll
      for (int r = 0; r < 4; ++r) ov[m][r] += lds4(smem, me[r] + fr * 16);
    }
  }
  {
    const rsrc_t ro = mk(out, 2048 * 1024 * 4);
    const unsigned oo = (c * 128 + 32 * fq + 2 * wid) * 4096 + (j * 64 + 4 * fr) * 4, oo2 = oo + 4096;
    asm volatile("s_nop 4\n\t"
                 "buffer_store_dwordx4 %0, %8, %10, 0 offen" OST "\n\tbuffer_store_dwordx4 %4, %9, %10, 0 offen" OST "\n\t"
                 "buffer_store_dwordx4 %1, %8, %10, %11 offen" OST "\n\tbuffer_store_dwordx4 %5, %9, %10, %11 offen" OST "\n\t"
                 "buffer_store_dwordx4 %2, %8, %10, %12 offen" OST "\n\tbuffer_store_dwordx4 %6, %9, %10, %12 offen" OST "\n\t"
                 "buffer_store_dwordx4 %3, %8, %10, %13 offen" OST "\n\tbuffer_store_dwordx4 %7, %9, %10, %13 offen" OST "\n\t"
                 "s_nop 7\n\ts_nop 7"
                 ::"v"(ov[0][0]), "v"(ov[0][1]), "v"(ov[0][2]), "v"(ov[0][3]), "v"(ov[1][0]), "v"(ov[1][1]), "v"(ov[1][2]), "v"(ov[1][3]), "v"(oo), "v"(oo2), "s"(ro),
                 "s"(32768), "s"(65536), "s"(98304) : "memory");
  }
  asm volatile("Lk_m:");
#endif
}

extern "C" void kernel_launch(void* const* d_in, const int* in_sizes, int n_in, void* d_out, int out_size, void* d_ws, size_t ws_size,
                              hipStream_t stream) {
  (void)hipFuncSetAttribute((const void*)fused, hipFuncAttributeMaxDynamicSharedMemorySize, LDS_BYTES);
  fused<<<256, 512, LDS_BYTES, stream>>>((const u16*)d_in[0], (const u16*)d_in[1], (const u16*)d_in[2], (const u16*)d_in[3], (const u16*)d_in[4],
                                         (const u16*)d_in[5], (const u16*)d_in[6], (const u16*)d_in[7], (const u16*)d_in[8], (const u16*)d_in[10],
                                         (const u16*)d_in[11], (const u16*)d_in[12], (const u16*)d_in[13], (const u16*)d_in[14], (char*)d_ws + WS_BASE(ws_size),
                                         (float*)d_out);
}
```

```cpp
#include <hip/hip_runtime.h>
#include <hip/hip_bf16.h>
#include <stdint.h>

typedef __attribute__((ext_vector_type(8))) short bf16x8;
typedef __attribute__((ext_vector_type(4))) float f32x4;
typedef __attribute__((ext_vector_type(2))) float f32x2;
typedef __attribute__((ext_vector_type(4))) unsigned u32x4;
typedef __attribute__((ext_vector_type(2))) unsigned u32x2;
typedef __attribute__((ext_vector_type(32))) unsigned u32x32;
typedef unsigned short u16;
typedef unsigned long long u64;
typedef __attribute__((ext_vector_type(8))) unsigned long long u64x8;
#define LAS __attribute__((address_space(3)))
#define DEV __device__ __forceinline__

#define ASM_GEMM1 \
  "s_mov_b32 %[it], 0\n\t" \
  "s_barrier\n\t" \
  "ds_read_b128 %[a00], %[pa0] offset:0\n\t" \
  "ds_read_b128 %[a01], %[pa0] offset:2048\n\t" \
  "ds_read_b128 %[b0], %[pb0] offset:0\n\t" \
  "ds_read_b128 %[b1], %[pb0] offset:2048\n\t" \
  "ds_read_b128 %[b2], %[pb0] offset:4096\n\t" \
  "ds_read_b128 %[b3], %[pb0] offset:6144\n\t" \
  "ds_read_b128 %[b4], %[pb0] offset:8192\n\t" \
  "ds_read_b128 %[b5], %[pb0] offset:10240\n\t" \
  "ds_read_b128 %[b6], %[pb0] offset:12288\n\t" \
  "ds_read_b128 %[b7], %[pb0] offset:14336\n\t" \
  "Lg1_%=:\n\t" \
  "ds_read_b128 %[a10], %[pa1] offset:0\n\t" \
  "ds_read_b128 %[a11], %[pa1] offset:2048\n\t" \
  "s_waitcnt lgkmcnt(8)\n\t" \
  "v_mfma_f32_16x16x32_bf16 %[c00], %[a00], %[b0], %[c00]\n\t" \
  "v_mfma_f32_16x16x32_bf16 %[c10], %[a01], %[b0], %[c10]\n\t" \
  "ds_read_b128 %[b0], %[pb1] offset:0\n\t" \
  "v_mfma_f32_16x16x32_bf16 %[c01], %[a00], %[b1], %[c01]\n\t" \
  "v_mfma_f32_16x16x32_bf16 %[c11], %[a01], %[b1], %[c11]\n\t" \
  "ds_read_b128 %[b1], %[pb1] offset:2048\n\t" \
  "s_waitcnt lgkmcnt(8)\n\t" \
  "v_mfma_f32_16x16x32_bf16 %[c02], %[a00], %[b2], %[c02]\n\t" \
  "v_mfma_f32_16x16x32_bf16 %[c12], %[a01], %[b2], %[c12]\n\t" \
  "ds_read_b128 %[b2], %[pb1] offset:4096\n\t" \
  "v_mfma_f32_16x16x32_bf16 %[c03], %[a00], %[b3], %[c03]\n\t" \
  "v_mfma_f32_16x16x32_bf16 %[c13], %[a01], %[b3], %[c13]\n\t" \
  "ds_read_b128 %[b3], %[pb1] offset:6144\n\t" \
  "s_waitcnt lgkmcnt(8)\n\t" \
  "v_mfma_f32_16x16x32_bf16 %[c04], %[a00], %[b4], %[c04]\n\t" \
  "v_mfma_f32_16x16x32_bf16 %[c14], %[a01], %[b4], %[c14]\n\t" \
  "ds_read_b128 %[b4], %[pb1] offset:8192\n\t" \
  "v_mfma_f32_16x16x32_bf16 %[c05], %[a00], %[b5], %[c05]\n\t" \
  "v_mfma_f32_16x16x32_bf16 %[c15], %[a01], %[b5], %[c15]\n\t" \
  "ds_read_b128 %[b5], %[pb1] offset:10240\n\t" \
  "s_waitcnt lgkmcnt(8)\n\t" \
  "v_mfma_f32_16x16x32_bf16 %[c06], %[a00], %[b6], %[c06]\n\t" \
  "v_mfma_f32_16x16x32_bf16 %[c16], %[a01], %[b6], %[c16]\n\t" \
  "ds_read_b128 %[b6], %[pb1] offset:12288\n\t" \
  "v_mfma_f32_16x16x32_bf16 %[c07], %[a00], %[b7], %[c07]\n\t" \
  "v_mfma_f32_16x16x32_bf16 %[c17], %[a01], %[b7], %[c17]\n\t" \
  "ds_read_b128 %[b7], %[pb1] offset:14336\n\t" \
  "s_waitcnt lgkmcnt(6)\n\t" \
  "v_mfma_f32_16x16x32_bf16 %[c00], %[a10], %[b0], %[c00]\n\t" \
  "v_mfma_f32_16x16x32_bf16 %[c10], %[a11], %[b0], %[c10]\n\t" \
  "v_mfma_f32_16x16x32_bf16 %[c01], %[a10], %[b1], %[c01]\n\t" \
  "v_mfma_f32_16x16x32_bf16 %[c11], %[a11], %[b1], %[c11]\n\t" \
  "s_waitcnt lgkmcnt(4)\n\t" \
  "v_mfma_f32_16x16x32_bf16 %[c02], %[a10], %[b2], %[c02]\n\t" \
  "v_mfma_f32_16x16x32_bf16 %[c12], %[a11], %[b2], %[c12]\n\t" \
  "s_waitcnt lgkmcnt(0)\n\t" \
  "s_barrier\n\t" \
  "ds_read_b128 %[a00], %[pa0] offset:32768\n\t" \
  "ds_read_b128 %[a01], %[pa0] offset:34816\n\t" \
  "v_mfma_f32_16x16x32_bf16 %[c03], %[a10], %[b3], %[c03]\n\t" \
  "v_mfma_f32_16x16x32_bf16 %[c13], %[a11], %[b3], %[c13]\n\t" \
  "ds_read_b128 %[b0], %[pb0] offset:32768\n\t" \
  "ds_read_b128 %[b1], %[pb0] offset:34816\n\t" \
  "v_mfma_f32_16x16x32_bf16 %[c04], %[a10], %[b4], %[c04]\n\t" \
  "v_mfma_f32_16x16x32_bf16 %[c14], %[a11], %[b4], %[c14]\n\t" \
  "ds_read_b128 %[b2], %[pb0] offset:36864\n\t" \
  "ds_read_b128 %[b3], %[pb0] offset:38912\n\t" \
  "v_mfma_f32_16x16x32_bf16 %[c05], %[a10], %[b5], %[c05]\n\t" \
  "v_mfma_f32_16x16x32_bf16 %[c15], %[a11], %[b5], %[c15]\n\t" \
  "ds_read_b128 %[b4], %[pb0] offset:40960\n\t" \
  "ds_read_b128 %[b5], %[pb0] offset:43008\n\t" \
  "v_mfma_f32_16x16x32_bf16 %[c06], %[a10], %[b6], %[c06]\n\t" \
  "v_mfma_f32_16x16x32_bf16 %[c16], %[a11], %[b6], %[c16]\n\t" \
  "ds_read_b128 %[b6], %[pb0] offset:45056\n\t" \
  "v_mfma_f32_16x16x32_bf16 %[c07], %[a10], %[b7], %[c07]\n\t" \
  "v_mfma_f32_16x16x32_bf16 %[c17], %[a11], %[b7], %[c17]\n\t" \
  "ds_read_b128 %[b7], %[pb0] offset:47104\n\t" \
  "ds_read_b128 %[a10], %[pa1] offset:32768\n\t" \
  "ds_read_b128 %[a11], %[pa1] offset:34816\n\t" \
  "s_waitcnt lgkmcnt(8)\n\t" \
  "v_mfma_f32_16x16x32_bf16 %[c00], %[a00], %[b0], %[c00]\n\t" \
  "v_mfma_f32_16x16x32_bf16 %[c10], %[a01], %[b0], %[c10]\n\t" \
  "ds_read_b128 %[b0], %[pb1] offset:32768\n\t" \
  "v_mfma_f32_16x16x32_bf16 %[c01], %[a00], %[b1], %[c01]\n\t" \
  "v_mfma_f32_16x16x32_bf16 %[c11], %[a01], %[b1], %[c11]\n\t" \
  "ds_read_b128 %[b1], %[pb1] offset:34816\n\t" \
  "s_waitcnt lgkmcnt(8)\n\t" \
  "v_mfma_f32_16x16x32_bf16 %[c02], %[a00], %[b2], %[c02]\n\t" \
  "v_mfma_f32_16x16x32_bf16 %[c12], %[a01], %[b2], %[c12]\n\t" \
  "ds_read_b128 %[b2], %[pb1] offset:36864\n\t" \
  "v_mfma_f32_16x16x32_bf16 %[c03], %[a00], %[b3], %[c03]\n\t" \
  "v_mfma_f32_16x16x32_bf16 %[c13], %[a01], %[b3], %[c13]\n\t" \
  "ds_read_b128 %[b3], %[pb1] offset:38912\n\t" \
  "v_xor_b32 %[pa0], 0x10000, %[pa0]\n\t" \
  "s_waitcnt lgkmcnt(8)\n\t" \
  "v_mfma_f32_16x16x32_bf16 %[c04], %[a00], %[b4], %[c04]\n\t" \
  "v_mfma_f32_16x16x32_bf16 %[c14], %[a01], %[b4], %[c14]\n\t" \
  "ds_read_b128 %[b4], %[pb1] offset:40960\n\t" \
  "v_xor_b32 %[pb0], 0x10000, %[pb0]\n\t" \
  "v_mfma_f32_16x16x32_bf16 %[c05], %[a00], %[b5], %[c05]\n\t" \
  "v_mfma_f32_16x16x32_bf16 %[c15], %[a01], %[b5], %[c15]\n\t" \
  "ds_read_b128 %[b5], %[pb1] offset:43008\n\t" \
  "s_waitcnt lgkmcnt(8)\n\t" \
  "v_mfma_f32_16x16x32_bf16 %[c06], %[a00], %[b6], %[c06]\n\t" \
  "v_mfma_f32_16x16x32_bf16 %[c16], %[a01], %[b6], %[c16]\n\t" \
  "ds_read_b128 %[b6], %[pb1] offset:45056\n\t" \
  "s_cmp_eq_u32 %[it], 7\n\t" \
  "v_mfma_f32_16x16x32_bf16 %[c07], %[a00], %[b7], %[c07]\n\t" \
  "v_mfma_f32_16x16x32_bf16 %[c17], %[a01], %[b7], %[c17]\n\t" \
  "ds_read_b128 %[b7], %[pb1] offset:47104\n\t" \
  "s_cbranch_scc1 Lxe_%=\n\t" \
  "Lxb_%=:\n\t" \
  "s_waitcnt lgkmcnt(6)\n\t" \
  "v_mfma_f32_16x16x32_bf16 %[c00], %[a10], %[b0], %[c00]\n\t" \
  "v_mfma_f32_16x16x32_bf16 %[c10], %[a11], %[b0], %[c10]\n\t" \
  "v_mfma_f32_16x16x32_bf16 %[c01], %[a10], %[b1], %[c01]\n\t" \
  "v_mfma_f32_16x16x32_bf16 %[c11], %[a11], %[b1], %[c11]\n\t" \
  "v_xor_b32 %[pa1], 0x10000, %[pa1]\n\t" \
  "s_waitcnt lgkmcnt(4)\n\t" \
  "v_mfma_f32_16x16x32_bf16 %[c02], %[a10], %[b2], %[c02]\n\t" \
  "v_mfma_f32_16x16x32_bf16 %[c12], %[a11], %[b2], %[c12]\n\t" \
  "v_xor_b32 %[pb1], 0x10000, %[pb1]\n\t" \
  "s_waitcnt lgkmcnt(0)\n\t" \
  "s_barrier\n\t" \
  "s_cbranch_scc1 Lt1_%=\n\t" \
  "ds_read_b128 %[a00], %[pa0] offset:0\n\t" \
  "ds_read_b128 %[a01], %[pa0] offset:2048\n\t" \
  "v_mfma_f32_16x16x32_bf16 %[c03], %[a10], %[b3], %[c03]\n\t" \
  "v_mfma_f32_16x16x32_bf16 %[c13], %[a11], %[b3], %[c13]\n\t" \
  "ds_read_b128 %[b0], %[pb0] offset:0\n\t" \
  "ds_read_b128 %[b1], %[pb0] offset:2048\n\t" \
  "v_mfma_f32_16x16x32_bf16 %[c04], %[a10], %[b4], %[c04]\n\t" \
  "v_mfma_f32_16x16x32_bf16 %[c14], %[a11], %[b4], %[c14]\n\t" \
  "ds_read_b128 %[b2], %[pb0] offset:4096\n\t" \
  "ds_read_b128 %[b3], %[pb0] offset:6144\n\t" \
  "v_mfma_f32_16x16x32_bf16 %[c05], %[a10], %[b5], %[c05]\n\t" \
  "v_mfma_f32_16x16x32_bf16 %[c15], %[a11], %[b5], %[c15]\n\t" \
  "ds_read_b128 %[b4], %[pb0] offset:8192\n\t" \
  "ds_read_b128 %[b5], %[pb0] offset:10240\n\t" \
  "v_mfma_f32_16x16x32_bf16 %[c06], %[a10], %[b6], %[c06]\n\t" \
  "v_mfma_f32_16x16x32_bf16 %[c16], %[a11], %[b6], %[c16]\n\t" \
  "ds_read_b128 %[b6], %[pb0] offset:12288\n\t" \
  "s_add_u32 %[it], %[it], 1\n\t" \
  "v_mfma_f32_16x16x32_bf16 %[c07], %[a10], %[b7], %[c07]\n\t" \
  "v_mfma_f32_16x16x32_bf16 %[c17], %[a11], %[b7], %[c17]\n\t" \
  "ds_read_b128 %[b7], %[pb0] offset:14336\n\t" \
  "s_branch Lg1_%=\n\t" \
  "Lxe_%=:\n\t" \
  "ds_read_b64 %[xe00], %[xa0] offset:0\n\t" \
  "ds_read_b64 %[xe01], %[xa1] offset:0\n\t" \
  "ds_read_b64 %[xe02], %[xa2] offset:0\n\t" \
  "ds_read_b64 %[xe03], %[xa3] offset:0\n\t" \
  "ds_read_b64 %[xe10], %[xa0] offset:2048\n\t" \
  "ds_read_b64 %[xe11], %[xa1] offset:2048\n\t" \
  "ds_read_b64 %[xe12], %[xa2] offset:2048\n\t" \
  "ds_read_b64 %[xe13], %[xa3] offset:2048\n\t" \
  "ds_read_b32 %[tg], %[tg]\n\t" \
  "s_branch Lxb_%=\n\t" \
  "Lt1_%=:\n\t" \
  "v_mfma_f32_16x16x32_bf16 %[c03], %[a10], %[b3], %[c03]\n\t" \
  "v_mfma_f32_16x16x32_bf16 %[c13], %[a11], %[b3], %[c13]\n\t" \
  "v_mfma_f32_16x16x32_bf16 %[c04], %[a10], %[b4], %[c04]\n\t" \
  "v_mfma_f32_16x16x32_bf16 %[c14], %[a11], %[b4], %[c14]\n\t" \
  "v_mfma_f32_16x16x32_bf16 %[c05], %[a10], %[b5], %[c05]\n\t" \
  "v_mfma_f32_16x16x32_bf16 %[c15], %[a11], %[b5], %[c15]\n\t" \
  "v_mfma_f32_16x16x32_bf16 %[c06], %[a10], %[b6], %[c06]\n\t" \
  "v_mfma_f32_16x16x32_bf16 %[c16], %[a11], %[b6], %[c16]\n\t" \
  "v_mfma_f32_16x16x32_bf16 %[c07], %[a10], %[b7], %[c07]\n\t" \
  "v_mfma_f32_16x16x32_bf16 %[c17], %[a11], %[b7], %[c17]\n\t" \
  "s_nop 7\n\t"
#define ASM_GEMM2 \
  "s_nop 4\n\t" \
  "s_mov_b32 %[to], 0x40000\n\t" \
  "s_mov_b32 %[cn], 1\n\t" \
  "Lc0_%=:\n\t" \
  "ds_read_b128 v[252:255], %[fa]\n\t" \
  "s_cmp_eq_u32 %[to], 0\n\t" \
  "s_cbranch_scc1 Lc0_%=x\n\t" \
  "s_sub_u32 %[to], %[to], 1\n\t" \
  "s_waitcnt lgkmcnt(0)\n\t" \
  "v_readfirstlane_b32 %[s], v253\n\t" \
  "s_cmp_ge_u32 %[s], 2\n\t" \
  "s_cbranch_scc0 Lc0_%=\n\t" \
  "Lc0_%=x:\n\t" \
  "ds_read_b128 %[k00], %[pc0] offset:0\n\t" \
  "ds_read_b128 %[k01], %[pc0] offset:2048\n\t" \
  "ds_read_b128 %[k02], %[pc0] offset:4096\n\t" \
  "ds_read_b128 %[k03], %[pc0] offset:6144\n\t" \
  "ds_read_b128 %[k10], %[pc1] offset:0\n\t" \
  "ds_read_b128 %[k11], %[pc1] offset:2048\n\t" \
  "ds_read_b128 %[k12], %[pc1] offset:4096\n\t" \
  "ds_read_b128 %[k13], %[pc1] offset:6144\n\t" \
  "s_mov_b32 %[hn], 3\n\t" \
  "s_cmp_gt_u32 1, %[own]\n\t" \
  "s_addc_u32 %[hb0], 1, -1\n\t" \
  "s_lshl_b32 %[hb0], %[hb0], 15\n\t" \
  "s_or_b32 %[hb1], %[hb0], 0x2000\n\t" \
  "s_or_b32 %[hb2], %[hb0], 0x4000\n\t" \
  "s_or_b32 %[hb3], %[hb0], 0x6000\n\t" \
  "Lh1_%=:\n\t" \
  "ds_read_b128 v[252:255], %[fa]\n\t" \
  "s_cmp_eq_u32 %[to], 0\n\t" \
  "s_cbranch_scc1 Lh1_%=x\n\t" \
  "s_sub_u32 %[to], %[to], 1\n\t" \
  "s_waitcnt lgkmcnt(0)\n\t" \
  "v_readfirstlane_b32 %[s], v254\n\t" \
  "s_cmp_ge_u32 %[s], 1\n\t" \
  "s_cbranch_scc0 Lh1_%=\n\t" \
  "Lh1_%=x:\n\t" \
  "buffer_load_dwordx4 v[216:219], %[vo], %[srd], %[hb0] offen offset:0 sc1\n\t" \
  "buffer_load_dwordx4 v[220:223], %[vo], %[srd], %[hb0] offen offset:1024 sc1\n\t" \
  "buffer_load_dwordx4 v[224:227], %[vo], %[srd], %[hb1] offen offset:0 sc1\n\t" \
  "buffer_load_dwordx4 v[228:231], %[vo], %[srd], %[hb1] offen offset:1024 sc1\n\t" \
  "buffer_load_dwordx4 v[232:235], %[vo], %[srd], %[hb2] offen offset:0 sc1\n\t" \
  "buffer_load_dwordx4 v[236:239], %[vo], %[srd], %[hb2] offen offset:1024 sc1\n\t" \
  "buffer_load_dwordx4 v[240:243], %[vo], %[srd], %[hb3] offen offset:0 sc1\n\t" \
  "buffer_load_dwordx4 v[244:247], %[vo], %[srd], %[hb3] offen offset:1024 sc1\n\t" \
  "s_cmp_gt_u32 2, %[own]\n\t" \
  "s_addc_u32 %[hb0], 2, -1\n\t" \
  "s_lshl_b32 %[hb0], %[hb0], 15\n\t" \
  "s_or_b32 %[hb1], %[hb0], 0x2000\n\t" \
  "s_or_b32 %[hb2], %[hb0], 0x4000\n\t" \
  "s_or_b32 %[hb3], %[hb0], 0x6000\n\t" \
  "s_cmp_ge_u32 %[s], 2\n\t" \
  "s_cbranch_scc1 Lh2_%=x\n\t" \
  "Lh2_%=:\n\t" \
  "ds_read_b128 v[252:255], %[fa]\n\t" \
  "s_cmp_eq_u32 %[to], 0\n\t" \
  "s_cbranch_scc1 Lh2_%=x\n\t" \
  "s_sub_u32 %[to], %[to], 1\n\t" \
  "s_waitcnt lgkmcnt(0)\n\t" \
  "v_readfirstlane_b32 %[s], v254\n\t" \
  "s_cmp_ge_u32 %[s], 2\n\t" \
  "s_cbranch_scc0 Lh2_%=\n\t" \
  "Lh2_%=x:\n\t" \
  "buffer_load_dwordx4 v[184:187], %[vo], %[srd], %[hb0] offen offset:0 sc1\n\t" \
  "buffer_load_dwordx4 v[188:191], %[vo], %[srd], %[hb0] offen offset:1024 sc1\n\t" \
  "buffer_load_dwordx4 v[192:195], %[vo], %[srd], %[hb1] offen offset:0 sc1\n\t" \
  "buffer_load_dwordx4 v[196:199], %[vo], %[srd], %[hb1] offen offset:1024 sc1\n\t" \
  "buffer_load_dwordx4 v[200:203], %[vo], %[srd], %[hb2] offen offset:0 sc1\n\t" \
  "buffer_load_dwordx4 v[204:207], %[vo], %[srd], %[hb2] offen offset:1024 sc1\n\t" \
  "buffer_load_dwordx4 v[208:211], %[vo], %[srd], %[hb3] offen offset:0 sc1\n\t" \
  "buffer_load_dwordx4 v[212:215], %[vo], %[srd], %[hb3] offen offset:1024 sc1\n\t" \
  "s_cmp_ge_u32 %[s], 3\n\t" \
  "s_cbranch_scc1 Lh3_%=x\n\t" \
  "Lh3_%=:\n\t" \
  "ds_read_b128 v[252:255], %[fa]\n\t" \
  "s_cmp_eq_u32 %[to], 0\n\t" \
  "s_cbranch_scc1 Lh3_%=x\n\t" \
  "s_sub_u32 %[to], %[to], 1\n\t" \
  "s_waitcnt lgkmcnt(0)\n\t" \
  "v_readfirstlane_b32 %[s], v254\n\t" \
  "s_cmp_ge_u32 %[s], 3\n\t" \
  "s_cbranch_scc0 Lh3_%=\n\t" \
  "Lh3_%=x:\n\t" \
  "s_branch Lin_%=\n\t" \
  "Lg2_%=:\n\t" \
  "s_waitcnt vmcnt(14) lgkmcnt(7)\n\t" \
  "ds_read_b128 v[252:255], %[fa]\n\t" \
  "v_mfma_f32_16x16x32_bf16 %[c00], v[184:187], %[k00], %[c00]\n\t" \
  "s_cmp_gt_u32 %[hn], %[own]\n\t" \
  "s_addc_u32 %[hb0], %[hn], -1\n\t" \
  "v_mfma_f32_16x16x32_bf16 %[c10], v[188:191], %[k00], %[c10]\n\t" \
  "ds_read_b128 %[k00], %[pc2] offset:0\n\t" \
  "s_waitcnt lgkmcnt(8)\n\t" \
  "v_mfma_f32_16x16x32_bf16 %[c01], v[184:187], %[k01], %[c01]\n\t" \
  "s_lshl_b32 %[hb0], %[hb0], 15\n\t" \
  "v_mfma_f32_16x16x32_bf16 %[c11], v[188:191], %[k01], %[c11]\n\t" \
  "ds_read_b128 %[k01], %[pc2] offset:2048\n\t" \
  "s_waitcnt lgkmcnt(8)\n\t" \
  "v_mfma_f32_16x16x32_bf16 %[c02], v[184:187], %[k02], %[c02]\n\t" \
  "s_or_b32 %[hb1], %[hb0], 0x2000\n\t" \
  "v_mfma_f32_16x16x32_bf16 %[c12], v[188:191], %[k02], %[c12]\n\t" \
  "ds_read_b128 %[k02], %[pc2] offset:4096\n\t" \
  "s_waitcnt lgkmcnt(8)\n\t" \
  "v_mfma_f32_16x16x32_bf16 %[c03], v[184:187], %[k03], %[c03]\n\t" \
  "s_add_u32 %[cn], %[cn], 1\n\t" \
  "v_mfma_f32_16x16x32_bf16 %[c13], v[188:191], %[k03], %[c13]\n\t" \
  "ds_read_b128 %[k03], %[pc2] offset:6144\n\t" \
  "buffer_load_dwordx4 v[184:187], %[vo], %[srd], %[hb0] offen offset:0 sc1\n\t" \
  "s_waitcnt vmcnt(13) lgkmcnt(8)\n\t" \
  "v_mfma_f32_16x16x32_bf16 %[c00], v[192:195], %[k10], %[c00]\n\t" \
  "s_cmp_eq_u32 %[cn], 9\n\t" \
  "s_cselect_b32 %[dl], 0x20000, %[d16]\n\t" \
  "v_mfma_f32_16x16x32_bf16 %[c10], v[196:199], %[k10], %[c10]\n\t" \
  "s_bitcmp1_b32 %[wr], %[cn]\n\t" \
  "s_cselect_b32 %[dl], 0xfffe4000, %[dl]\n\t" \
  "ds_read_b128 %[k10], %[pc3] offset:0\n\t" \
  "s_waitcnt lgkmcnt(8)\n\t" \
  "v_mfma_f32_16x16x32_bf16 %[c01], v[192:195], %[k11], %[c01]\n\t" \
  "v_add_u32 %[pc0], %[dl], %[pc0]\n\t" \
  "v_mfma_f32_16x16x32_bf16 %[c11], v[196:199], %[k11], %[c11]\n\t" \
  "s_or_b32 %[hb2], %[hb0], 0x4000\n\t" \
  "ds_read_b128 %[k11], %[pc3] offset:2048\n\t" \
  "buffer_load_dwordx4 v[188:191], %[vo], %[srd], %[hb0] offen offset:1024 sc1\n\t" \
  "s_waitcnt lgkmcnt(8)\n\t" \
  "v_mfma_f32_16x16x32_bf16 %[c02], v[192:195], %[k12], %[c02]\n\t" \
  "v_add_u32 %[pc1], %[dl], %[pc1]\n\t" \
  "v_mfma_f32_16x16x32_bf16 %[c12], v[196:199], %[k12], %[c12]\n\t" \
  "v_add_u32 %[fv], 1, %[fv]\n\t" \
  "ds_read_b128 %[k12], %[pc3] offset:4096\n\t" \
  "s_waitcnt lgkmcnt(7)\n\t" \
  "v_mfma_f32_16x16x32_bf16 %[c03], v[192:195], %[k13], %[c03]\n\t" \
  "v_readfirstlane_b32 %[s], v253\n\t" \
  "v_mfma_f32_16x16x32_bf16 %[c13], v[196:199], %[k13], %[c13]\n\t" \
  "ds_read_b128 %[k13], %[pc3] offset:6144\n\t" \
  "ds_write_b32 %[fw], %[fv]\n\t" \
  "s_cmp_gt_u32 %[s], %[cn]\n\t" \
  "s_cbranch_scc0 Lcs0_%=\n\t" \
  "Lcb0_%=:\n\t" \
  "buffer_load_dwordx4 v[192:195], %[vo], %[srd], %[hb1] offen offset:0 sc1\n\t" \
  "s_waitcnt vmcnt(13) lgkmcnt(8)\n\t" \
  "ds_read_b128 v[252:255], %[fa]\n\t" \
  "v_mfma_f32_16x16x32_bf16 %[c00], v[200:203], %[k00], %[c00]\n\t" \
  "s_or_b32 %[hb3], %[hb0], 0x6000\n\t" \
  "v_mfma_f32_16x16x32_bf16 %[c10], v[204:207], %[k00], %[c10]\n\t" \
  "ds_read_b128 %[k00], %[pc0] offset:0\n\t" \
  "s_waitcnt lgkmcnt(9)\n\t" \
  "v_mfma_f32_16x16x32_bf16 %[c01], v[200:203], %[k01], %[c01]\n\t" \
  "v_mfma_f32_16x16x32_bf16 %[c11], v[204:207], %[k01], %[c11]\n\t" \
  "ds_read_b128 %[k01], %[pc0] offset:2048\n\t" \
  "buffer_load_dwordx4 v[196:199], %[vo], %[srd], %[hb1] offen offset:1024 sc1\n\t" \
  "s_waitcnt lgkmcnt(9)\n\t" \
  "v_mfma_f32_16x16x32_bf16 %[c02], v[200:203], %[k02], %[c02]\n\t" \
  "v_mfma_f32_16x16x32_bf16 %[c12], v[204:207], %[k02], %[c12]\n\t" \
  "ds_read_b128 %[k02], %[pc0] offset:4096\n\t" \
  "s_waitcnt lgkmcnt(9)\n\t" \
  "v_mfma_f32_16x16x32_bf16 %[c03], v[200:203], %[k03], %[c03]\n\t" \
  "v_mfma_f32_16x16x32_bf16 %[c13], v[204:207], %[k03], %[c13]\n\t" \
  "ds_read_b128 %[k03], %[pc0] offset:6144\n\t" \
  "buffer_load_dwordx4 v[200:203], %[vo], %[srd], %[hb2] offen offset:0 sc1\n\t" \
  "s_waitcnt vmcnt(13) lgkmcnt(9)\n\t" \
  "v_mfma_f32_16x16x32_bf16 %[c00], v[208:211], %[k10], %[c00]\n\t" \
  "v_add_u32 %[pc2], %[dl], %[pc2]\n\t" \
  "v_mfma_f32_16x16x32_bf16 %[c10], v[212:215], %[k10], %[c10]\n\t" \
  "ds_read_b128 %[k10], %[pc1] offset:0\n\t" \
  "s_waitcnt lgkmcnt(9)\n\t" \
  "v_mfma_f32_16x16x32_bf16 %[c01], v[208:211], %[k11], %[c01]\n\t" \
  "v_add_u32 %[pc3], %[dl], %[pc3]\n\t" \
  "v_mfma_f32_16x16x32_bf16 %[c11], v[212:215], %[k11], %[c11]\n\t" \
  "ds_read_b128 %[k11], %[pc1] offset:2048\n\t" \
  "buffer_load_dwordx4 v[204:207], %[vo], %[srd], %[hb2] offen offset:1024 sc1\n\t" \
  "s_waitcnt lgkmcnt(6)\n\t" \
  "v_mfma_f32_16x16x32_bf16 %[c02], v[208:211], %[k12], %[c02]\n\t" \
  "v_readfirstlane_b32 %[s], v254\n\t" \
  "v_mfma_f32_16x16x32_bf16 %[c12], v[212:215], %[k12], %[c12]\n\t" \
  "ds_read_b128 %[k12], %[pc1] offset:4096\n\t" \
  "v_mfma_f32_16x16x32_bf16 %[c03], v[208:211], %[k13], %[c03]\n\t" \
  "s_add_u32 %[hn], %[hn], 1\n\t" \
  "v_mfma_f32_16x16x32_bf16 %[c13], v[212:215], %[k13], %[c13]\n\t" \
  "ds_read_b128 %[k13], %[pc1] offset:6144\n\t" \
  "buffer_load_dwordx4 v[208:211], %[vo], %[srd], %[hb3] offen offset:0 sc1\n\t" \
  "buffer_load_dwordx4 v[212:215], %[vo], %[srd], %[hb3] offen offset:1024 sc1\n\t" \
  "s_cmp_ge_u32 %[s], %[hn]\n\t" \
  "s_cbranch_scc0 Lhs0_%=\n\t" \
  "Lhb0_%=:\n\t" \
  "Lin_%=:\n\t" \
  "s_waitcnt vmcnt(14) lgkmcnt(7)\n\t" \
  "ds_read_b128 v[252:255], %[fa]\n\t" \
  "v_mfma_f32_16x16x32_bf16 %[c00], v[216:219], %[k00], %[c00]\n\t" \
  "s_cmp_gt_u32 %[hn], %[own]\n\t" \
  "s_addc_u32 %[hb0], %[hn], -1\n\t" \
  "v_mfma_f32_16x16x32_bf16 %[c10], v[220:223], %[k00], %[c10]\n\t" \
  "ds_read_b128 %[k00], %[pc2] offset:0\n\t" \
  "s_waitcnt lgkmcnt(8)\n\t" \
  "v_mfma_f32_16x16x32_bf16 %[c01], v[216:219], %[k01], %[c01]\n\t" \
  "s_lshl_b32 %[hb0], %[hb0], 15\n\t" \
  "v_mfma_f32_16x16x32_bf16 %[c11], v[220:223], %[k01], %[c11]\n\t" \
  "ds_read_b128 %[k01], %[pc2] offset:2048\n\t" \
  "s_waitcnt lgkmcnt(8)\n\t" \
  "v_mfma_f32_16x16x32_bf16 %[c02], v[216:219], %[k02], %[c02]\n\t" \
  "s_or_b32 %[hb1], %[hb0], 0x2000\n\t" \
  "v_mfma_f32_16x16x32_bf16 %[c12], v[220:223], %[k02], %[c12]\n\t" \
  "ds_read_b128 %[k02], %[pc2] offset:4096\n\t" \
  "s_waitcnt lgkmcnt(8)\n\t" \
  "v_mfma_f32_16x16x32_bf16 %[c03], v[216:219], %[k03], %[c03]\n\t" \
  "s_add_u32 %[cn], %[cn], 1\n\t" \
  "v_mfma_f32_16x16x32_bf16 %[c13], v[220:223], %[k03], %[c13]\n\t" \
  "ds_read_b128 %[k03], %[pc2] offset:6144\n\t" \
  "buffer_load_dwordx4 v[216:219], %[vo], %[srd], %[hb0] offen offset:0 sc1\n\t" \
  "s_waitcnt vmcnt(13) lgkmcnt(8)\n\t" \
  "v_mfma_f32_16x16x32_bf16 %[c00], v[224:227], %[k10], %[c00]\n\t" \
  "s_cmp_eq_u32 %[cn], 9\n\t" \
  "s_cselect_b32 %[dl], 0x20000, %[d16]\n\t" \
  "v_mfma_f32_16x16x32_bf16 %[c10], v[228:231], %[k10], %[c10]\n\t" \
  "s_bitcmp1_b32 %[wr], %[cn]\n\t" \
  "s_cselect_b32 %[dl], 0xfffe4000, %[dl]\n\t" \
  "ds_read_b128 %[k10], %[pc3] offset:0\n\t" \
  "s_waitcnt lgkmcnt(8)\n\t" \
  "v_mfma_f32_16x16x32_bf16 %[c01], v[224:227], %[k11], %[c01]\n\t" \
  "v_add_u32 %[pc0], %[dl], %[pc0]\n\t" \
  "v_mfma_f32_16x16x32_bf16 %[c11], v[228:231], %[k11], %[c11]\n\t" \
  "s_or_b32 %[hb2], %[hb0], 0x4000\n\t" \
  "ds_read_b128 %[k11], %[pc3] offset:2048\n\t" \
  "buffer_load_dwordx4 v[220:223], %[vo], %[srd], %[hb0] offen offset:1024 sc1\n\t" \
  "s_waitcnt lgkmcnt(8)\n\t" \
  "v_mfma_f32_16x16x32_bf16 %[c02], v[224:227], %[k12], %[c02]\n\t" \
  "v_add_u32 %[pc1], %[dl], %[pc1]\n\t" \
  "v_mfma_f32_16x16x32_bf16 %[c12], v[228:231], %[k12], %[c12]\n\t" \
  "v_add_u32 %[fv], 1, %[fv]\n\t" \
  "ds_read_b128 %[k12], %[pc3] offset:4096\n\t" \
  "s_waitcnt lgkmcnt(7)\n\t" \
  "v_mfma_f32_16x16x32_bf16 %[c03], v[224:227], %[k13], %[c03]\n\t" \
  "v_readfirstlane_b32 %[s], v252\n\t" \
  "v_mfma_f32_16x16x32_bf16 %[c13], v[228:231], %[k13], %[c13]\n\t" \
  "ds_read_b128 %[k13], %[pc3] offset:6144\n\t" \
  "ds_write_b32 %[fw], %[fv]\n\t" \
  "s_cmp_gt_u32 %[s], %[cn]\n\t" \
  "s_cbranch_scc0 Lcs1_%=\n\t" \
  "Lcb1_%=:\n\t" \
  "buffer_load_dwordx4 v[224:227], %[vo], %[srd], %[hb1] offen offset:0 sc1\n\t" \
  "s_waitcnt vmcnt(13) lgkmcnt(8)\n\t" \
  "ds_read_b128 v[252:255], %[fa]\n\t" \
  "v_mfma_f32_16x16x32_bf16 %[c00], v[232:235], %[k00], %[c00]\n\t" \
  "s_or_b32 %[hb3], %[hb0], 0x6000\n\t" \
  "v_mfma_f32_16x16x32_bf16 %[c10], v[236:239], %[k00], %[c10]\n\t" \
  "ds_read_b128 %[k00], %[pc0] offset:0\n\t" \
  "s_waitcnt lgkmcnt(9)\n\t" \
  "v_mfma_f32_16x16x32_bf16 %[c01], v[232:235], %[k01], %[c01]\n\t" \
  "v_mfma_f32_16x16x32_bf16 %[c11], v[236:239], %[k01], %[c11]\n\t" \
  "ds_read_b128 %[k01], %[pc0] offset:2048\n\t" \
  "buffer_load_dwordx4 v[228:231], %[vo], %[srd], %[hb1] offen offset:1024 sc1\n\t" \
  "s_waitcnt lgkmcnt(9)\n\t" \
  "v_mfma_f32_16x16x32_bf16 %[c02], v[232:235], %[k02], %[c02]\n\t" \
  "v_mfma_f32_16x16x32_bf16 %[c12], v[236:239], %[k02], %[c12]\n\t" \
  "ds_read_b128 %[k02], %[pc0] offset:4096\n\t" \
  "s_waitcnt lgkmcnt(9)\n\t" \
  "v_mfma_f32_16x16x32_bf16 %[c03], v[232:235], %[k03], %[c03]\n\t" \
  "v_mfma_f32_16x16x32_bf16 %[c13], v[236:239], %[k03], %[c13]\n\t" \
  "ds_read_b128 %[k03], %[pc0] offset:6144\n\t" \
  "buffer_load_dwordx4 v[232:235], %[vo], %[srd], %[hb2] offen offset:0 sc1\n\t" \
  "s_waitcnt vmcnt(13) lgkmcnt(9)\n\t" \
  "v_mfma_f32_16x16x32_bf16 %[c00], v[240:243], %[k10], %[c00]\n\t" \
  "v_add_u32 %[pc2], %[dl], %[pc2]\n\t" \
  "v_mfma_f32_16x16x32_bf16 %[c10], v[244:247], %[k10], %[c10]\n\t" \
  "ds_read_b128 %[k10], %[pc1] offset:0\n\t" \
  "s_waitcnt lgkmcnt(9)\n\t" \
  "v_mfma_f32_16x16x32_bf16 %[c01], v[240:243], %[k11], %[c01]\n\t" \
  "v_add_u32 %[pc3], %[dl], %[pc3]\n\t" \
  "v_mfma_f32_16x16x32_bf16 %[c11], v[244:247], %[k11], %[c11]\n\t" \
  "ds_read_b128 %[k11], %[pc1] offset:2048\n\t" \
  "buffer_load_dwordx4 v[236:239], %[vo], %[srd], %[hb2] offen offset:1024 sc1\n\t" \
  "s_waitcnt lgkmcnt(6)\n\t" \
  "v_mfma_f32_16x16x32_bf16 %[c02], v[240:243], %[k12], %[c02]\n\t" \
  "v_readfirstlane_b32 %[s], v254\n\t" \
  "v_mfma_f32_16x16x32_bf16 %[c12], v[244:247], %[k12], %[c12]\n\t" \
  "ds_read_b128 %[k12], %[pc1] offset:4096\n\t" \
  "v_mfma_f32_16x16x32_bf16 %[c03], v[240:243], %[k13], %[c03]\n\t" \
  "s_add_u32 %[hn], %[hn], 1\n\t" \
  "v_mfma_f32_16x16x32_bf16 %[c13], v[244:247], %[k13], %[c13]\n\t" \
  "ds_read_b128 %[k13], %[pc1] offset:6144\n\t" \
  "buffer_load_dwordx4 v[240:243], %[vo], %[srd], %[hb3] offen offset:0 sc1\n\t" \
  "buffer_load_dwordx4 v[244:247], %[vo], %[srd], %[hb3] offen offset:1024 sc1\n\t" \
  "s_cmp_ge_u32 %[s], %[hn]\n\t" \
  "s_cbranch_scc0 Lhs1_%=\n\t" \
  "Lhb1_%=:\n\t" \
  "s_cmp_lt_u32 %[cn], 14\n\t" \
  "s_cbranch_scc1 Lg2_%=\n\t" \
  "s_waitcnt vmcnt(14) lgkmcnt(7)\n\t" \
  "ds_read_b128 v[252:255], %[fa]\n\t" \
  "v_mfma_f32_16x16x32_bf16 %[c00], v[184:187], %[k00], %[c00]\n\t" \
  "v_mfma_f32_16x16x32_bf16 %[c10], v[188:191], %[k00], %[c10]\n\t" \
  "ds_read_b128 %[k00], %[pc2] offset:0\n\t" \
  "s_waitcnt lgkmcnt(8)\n\t" \
  "v_mfma_f32_16x16x32_bf16 %[c01], v[184:187], %[k01], %[c01]\n\t" \
  "v_mfma_f32_16x16x32_bf16 %[c11], v[188:191], %[k01], %[c11]\n\t" \
  "ds_read_b128 %[k01], %[pc2] offset:2048\n\t" \
  "s_waitcnt lgkmcnt(8)\n\t" \
  "v_mfma_f32_16x16x32_bf16 %[c02], v[184:187], %[k02], %[c02]\n\t" \
  "v_mfma_f32_16x16x32_bf16 %[c12], v[188:191], %[k02], %[c12]\n\t" \
  "ds_read_b128 %[k02], %[pc2] offset:4096\n\t" \
  "s_waitcnt lgkmcnt(8)\n\t" \
  "v_mfma_f32_16x16x32_bf16 %[c03], v[184:187], %[k03], %[c03]\n\t" \
  "s_add_u32 %[cn], %[cn], 1\n\t" \
  "v_mfma_f32_16x16x32_bf16 %[c13], v[188:191], %[k03], %[c13]\n\t" \
  "ds_read_b128 %[k03], %[pc2] offset:6144\n\t" \
  "s_waitcnt vmcnt(12) lgkmcnt(8)\n\t" \
  "v_mfma_f32_16x16x32_bf16 %[c00], v[192:195], %[k10], %[c00]\n\t" \
  "s_cmp_eq_u32 %[cn], 9\n\t" \
  "s_cselect_b32 %[dl], 0x20000, %[d16]\n\t" \
  "v_mfma_f32_16x16x32_bf16 %[c10], v[196:199], %[k10], %[c10]\n\t" \
  "s_bitcmp1_b32 %[wr], %[cn]\n\t" \
  "s_cselect_b32 %[dl], 0xfffe4000, %[dl]\n\t" \
  "ds_read_b128 %[k10], %[pc3] offset:0\n\t" \
  "s_waitcnt lgkmcnt(8)\n\t" \
  "v_mfma_f32_16x16x32_bf16 %[c01], v[192:195], %[k11], %[c01]\n\t" \
  "v_add_u32 %[pc0], %[dl], %[pc0]\n\t" \
  "v_mfma_f32_16x16x32_bf16 %[c11], v[196:199], %[k11], %[c11]\n\t" \
  "ds_read_b128 %[k11], %[pc3] offset:2048\n\t" \
  "s_waitcnt lgkmcnt(8)\n\t" \
  "v_mfma_f32_16x16x32_bf16 %[c02], v[192:195], %[k12], %[c02]\n\t" \
  "v_add_u32 %[pc1], %[dl], %[pc1]\n\t" \
  "v_mfma_f32_16x16x32_bf16 %[c12], v[196:199], %[k12], %[c12]\n\t" \
  "ds_read_b128 %[k12], %[pc3] offset:4096\n\t" \
  "s_waitcnt lgkmcnt(7)\n\t" \
  "v_mfma_f32_16x16x32_bf16 %[c03], v[192:195], %[k13], %[c03]\n\t" \
  "v_readfirstlane_b32 %[s], v253\n\t" \
  "v_mfma_f32_16x16x32_bf16 %[c13], v[196:199], %[k13], %[c13]\n\t" \
  "ds_read_b128 %[k13], %[pc3] offset:6144\n\t" \
  "s_cmp_gt_u32 %[s], %[cn]\n\t" \
  "s_cbranch_scc0 Lcs0t_%=\n\t" \
  "Lcb0t_%=:\n\t" \
  "s_waitcnt vmcnt(10) lgkmcnt(7)\n\t" \
  "v_mfma_f32_16x16x32_bf16 %[c00], v[200:203], %[k00], %[c00]\n\t" \
  "v_mfma_f32_16x16x32_bf16 %[c10], v[204:207], %[k00], %[c10]\n\t" \
  "ds_read_b128 %[k00], %[pc0] offset:0\n\t" \
  "s_waitcnt lgkmcnt(7)\n\t" \
  "v_mfma_f32_16x16x32_bf16 %[c01], v[200:203], %[k01], %[c01]\n\t" \
  "v_mfma_f32_16x16x32_bf16 %[c11], v[204:207], %[k01], %[c11]\n\t" \
  "ds_read_b128 %[k01], %[pc0] offset:2048\n\t" \
  "s_waitcnt lgkmcnt(7)\n\t" \
  "v_mfma_f32_16x16x32_bf16 %[c02], v[200:203], %[k02], %[c02]\n\t" \
  "v_mfma_f32_16x16x32_bf16 %[c12], v[204:207], %[k02], %[c12]\n\t" \
  "ds_read_b128 %[k02], %[pc0] offset:4096\n\t" \
  "s_waitcnt lgkmcnt(7)\n\t" \
  "v_mfma_f32_16x16x32_bf16 %[c03], v[200:203], %[k03], %[c03]\n\t" \
  "v_mfma_f32_16x16x32_bf16 %[c13], v[204:207], %[k03], %[c13]\n\t" \
  "ds_read_b128 %[k03], %[pc0] offset:6144\n\t" \
  "s_waitcnt vmcnt(8) lgkmcnt(7)\n\t" \
  "v_mfma_f32_16x16x32_bf16 %[c00], v[208:211], %[k10], %[c00]\n\t" \
  "v_add_u32 %[pc2], %[dl], %[pc2]\n\t" \
  "v_mfma_f32_16x16x32_bf16 %[c10], v[212:215], %[k10], %[c10]\n\t" \
  "ds_read_b128 %[k10], %[pc1] offset:0\n\t" \
  "s_waitcnt lgkmcnt(7)\n\t" \
  "v_mfma_f32_16x16x32_bf16 %[c01], v[208:211], %[k11], %[c01]\n\t" \
  "v_add_u32 %[pc3], %[dl], %[pc3]\n\t" \
  "v_mfma_f32_16x16x32_bf16 %[c11], v[212:215], %[k11], %[c11]\n\t" \
  "ds_read_b128 %[k11], %[pc1] offset:2048\n\t" \
  "s_waitcnt lgkmcnt(7)\n\t" \
  "v_mfma_f32_16x16x32_bf16 %[c02], v[208:211], %[k12], %[c02]\n\t" \
  "v_mfma_f32_16x16x32_bf16 %[c12], v[212:215], %[k12], %[c12]\n\t" \
  "ds_read_b128 %[k12], %[pc1] offset:4096\n\t" \
  "s_waitcnt lgkmcnt(7)\n\t" \
  "v_mfma_f32_16x16x32_bf16 %[c03], v[208:211], %[k13], %[c03]\n\t" \
  "v_mfma_f32_16x16x32_bf16 %[c13], v[212:215], %[k13], %[c13]\n\t" \
  "ds_read_b128 %[k13], %[pc1] offset:6144\n\t" \
  "s_waitcnt vmcnt(6) lgkmcnt(7)\n\t" \
  "v_mfma_f32_16x16x32_bf16 %[c00], v[216:219], %[k00], %[c00]\n\t" \
  "v_mfma_f32_16x16x32_bf16 %[c10], v[220:223], %[k00], %[c10]\n\t" \
  "ds_read_b128 %[k00], %[pc2] offset:0\n\t" \
  "s_waitcnt lgkmcnt(7)\n\t" \
  "v_mfma_f32_16x16x32_bf16 %[c01], v[216:219], %[k01], %[c01]\n\t" \
  "v_mfma_f32_16x16x32_bf16 %[c11], v[220:223], %[k01], %[c11]\n\t" \
  "ds_read_b128 %[k01], %[pc2] offset:2048\n\t" \
  "s_waitcnt lgkmcnt(7)\n\t" \
  "v_mfma_f32_16x16x32_bf16 %[c02], v[216:219], %[k02], %[c02]\n\t" \
  "v_mfma_f32_16x16x32_bf16 %[c12], v[220:223], %[k02], %[c12]\n\t" \
  "ds_read_b128 %[k02], %[pc2] offset:4096\n\t" \
  "s_waitcnt lgkmcnt(7)\n\t" \
  "v_mfma_f32_16x16x32_bf16 %[c03], v[216:219], %[k03], %[c03]\n\t" \
  "v_mfma_f32_16x16x32_bf16 %[c13], v[220:223], %[k03], %[c13]\n\t" \
  "ds_read_b128 %[k03], %[pc2] offset:6144\n\t" \
  "s_waitcnt vmcnt(4) lgkmcnt(7)\n\t" \
  "v_mfma_f32_16x16x32_bf16 %[c00], v[224:227], %[k10], %[c00]\n\t" \
  "v_mfma_f32_16x16x32_bf16 %[c10], v[228:231], %[k10], %[c10]\n\t" \
  "ds_read_b128 %[k10], %[pc3] offset:0\n\t" \
  "s_waitcnt lgkmcnt(7)\n\t" \
  "v_mfma_f32_16x16x32_bf16 %[c01], v[224:227], %[k11], %[c01]\n\t" \
  "v_mfma_f32_16x16x32_bf16 %[c11], v[228:231], %[k11], %[c11]\n\t" \
  "ds_read_b128 %[k11], %[pc3] offset:2048\n\t" \
  "s_waitcnt lgkmcnt(7)\n\t" \
  "v_mfma_f32_16x16x32_bf16 %[c02], v[224:227], %[k12], %[c02]\n\t" \
  "v_mfma_f32_16x16x32_bf16 %[c12], v[228:231], %[k12], %[c12]\n\t" \
  "ds_read_b128 %[k12], %[pc3] offset:4096\n\t" \
  "s_waitcnt lgkmcnt(7)\n\t" \
  "v_mfma_f32_16x16x32_bf16 %[c03], v[224:227], %[k13], %[c03]\n\t" \
  "v_mfma_f32_16x16x32_bf16 %[c13], v[228:231], %[k13], %[c13]\n\t" \
  "ds_read_b128 %[k13], %[pc3] offset:6144\n\t" \
  "s_waitcnt vmcnt(2) lgkmcnt(7)\n\t" \
  "v_mfma_f32_16x16x32_bf16 %[c00], v[232:235], %[k00], %[c00]\n\t" \
  "v_mfma_f32_16x16x32_bf16 %[c10], v[236:239], %[k00], %[c10]\n\t" \
  "s_waitcnt lgkmcnt(6)\n\t" \
  "v_mfma_f32_16x16x32_bf16 %[c01], v[232:235], %[k01], %[c01]\n\t" \
  "v_mfma_f32_16x16x32_bf16 %[c11], v[236:239], %[k01], %[c11]\n\t" \
  "s_waitcnt lgkmcnt(5)\n\t" \
  "v_mfma_f32_16x16x32_bf16 %[c02], v[232:235], %[k02], %[c02]\n\t" \
  "v_mfma_f32_16x16x32_bf16 %[c12], v[236:239], %[k02], %[c12]\n\t" \
  "s_waitcnt lgkmcnt(4)\n\t" \
  "v_mfma_f32_16x16x32_bf16 %[c03], v[232:235], %[k03], %[c03]\n\t" \
  "v_mfma_f32_16x16x32_bf16 %[c13], v[236:239], %[k03], %[c13]\n\t" \
  "s_waitcnt vmcnt(0) lgkmcnt(3)\n\t" \
  "v_mfma_f32_16x16x32_bf16 %[c00], v[240:243], %[k10], %[c00]\n\t" \
  "v_mfma_f32_16x16x32_bf16 %[c10], v[244:247], %[k10], %[c10]\n\t" \
  "s_waitcnt lgkmcnt(2)\n\t" \
  "v_mfma_f32_16x16x32_bf16 %[c01], v[240:243], %[k11], %[c01]\n\t" \
  "v_mfma_f32_16x16x32_bf16 %[c11], v[244:247], %[k11], %[c11]\n\t" \
  "s_waitcnt lgkmcnt(1)\n\t" \
  "v_mfma_f32_16x16x32_bf16 %[c02], v[240:243], %[k12], %[c02]\n\t" \
  "v_mfma_f32_16x16x32_bf16 %[c12], v[244:247], %[k12], %[c12]\n\t" \
  "s_waitcnt lgkmcnt(0)\n\t" \
  "v_mfma_f32_16x16x32_bf16 %[c03], v[240:243], %[k13], %[c03]\n\t" \
  "v_mfma_f32_16x16x32_bf16 %[c13], v[244:247], %[k13], %[c13]\n\t" \
  "s_branch Lend_%=\n\t" \
  "Lcs0_%=:\n\t" \
  "s_add_u32 %[s2], %[cn], 1\n\t" \
  "Lcr0_%=:\n\t" \
  "ds_read_b128 v[252:255], %[fa]\n\t" \
  "s_cmp_eq_u32 %[to], 0\n\t" \
  "s_cbranch_scc1 Lcb0_%=\n\t" \
  "s_sub_u32 %[to], %[to], 1\n\t" \
  "s_waitcnt lgkmcnt(0)\n\t" \
  "v_readfirstlane_b32 %[s], v253\n\t" \
  "s_cmp_ge_u32 %[s], %[s2]\n\t" \
  "s_cbranch_scc0 Lcr0_%=\n\t" \
  "s_branch Lcb0_%=\n\t" \
  "Lhs0_%=:\n\t" \
  "ds_read_b128 v[252:255], %[fa]\n\t" \
  "s_cmp_eq_u32 %[to], 0\n\t" \
  "s_cbranch_scc1 Lhb0_%=\n\t" \
  "s_sub_u32 %[to], %[to], 1\n\t" \
  "s_waitcnt lgkmcnt(0)\n\t" \
  "v_readfirstlane_b32 %[s], v254\n\t" \
  "s_cmp_ge_u32 %[s], %[hn]\n\t" \
  "s_cbranch_scc0 Lhs0_%=\n\t" \
  "s_branch Lhb0_%=\n\t" \
  "Lcs1_%=:\n\t" \
  "s_add_u32 %[s2], %[cn], 1\n\t" \
  "Lcr1_%=:\n\t" \
  "ds_read_b128 v[252:255], %[fa]\n\t" \
  "s_cmp_eq_u32 %[to], 0\n\t" \
  "s_cbranch_scc1 Lcb1_%=\n\t" \
  "s_sub_u32 %[to], %[to], 1\n\t" \
  "s_waitcnt lgkmcnt(0)\n\t" \
  "v_readfirstlane_b32 %[s], v252\n\t" \
  "s_cmp_ge_u32 %[s], %[s2]\n\t" \
  "s_cbranch_scc0 Lcr1_%=\n\t" \
  "s_branch Lcb1_%=\n\t" \
  "Lhs1_%=:\n\t" \
  "ds_read_b128 v[252:255], %[fa]\n\t" \
  "s_cmp_eq_u32 %[to], 0\n\t" \
  "s_cbranch_scc1 Lhb1_%=\n\t" \
  "s_sub_u32 %[to], %[to], 1\n\t" \
  "s_waitcnt lgkmcnt(0)\n\t" \
  "v_readfirstlane_b32 %[s], v254\n\t" \
  "s_cmp_ge_u32 %[s], %[hn]\n\t" \
  "s_cbranch_scc0 Lhs1_%=\n\t" \
  "s_branch Lhb1_%=\n\t" \
  "Lcs0t_%=:\n\t" \
  "s_add_u32 %[s2], %[cn], 1\n\t" \
  "Lcr0t_%=:\n\t" \
  "ds_read_b128 v[252:255], %[fa]\n\t" \
  "s_cmp_eq_u32 %[to], 0\n\t" \
  "s_cbranch_scc1 Lcb0t_%=\n\t" \
  "s_sub_u32 %[to], %[to], 1\n\t" \
  "s_waitcnt lgkmcnt(0)\n\t" \
  "v_readfirstlane_b32 %[s], v253\n\t" \
  "s_cmp_ge_u32 %[s], %[s2]\n\t" \
  "s_cbranch_scc0 Lcr0t_%=\n\t" \
  "s_branch Lcb0t_%=\n\t" \
  "Lend_%=:\n\t" \
  "s_nop 7\n\t"
#define ASM_REQ \
  "s_cmp_gt_u32 1, %[own]\n\t" \
  "s_addc_u32 %[hb0], 1, -1\n\t" \
  "s_lshl_b32 %[hb0], %[hb0], 15\n\t" \
  "s_or_b32 %[hb1], %[hb0], 0x2000\n\t" \
  "s_or_b32 %[hb2], %[hb0], 0x4000\n\t" \
  "s_or_b32 %[hb3], %[hb0], 0x6000\n\t" \
  "buffer_load_dwordx4 v[216:219], %[vo], %[srd], %[hb0] offen offset:0 sc1\n\t" \
  "buffer_load_dwordx4 v[220:223], %[vo], %[srd], %[hb0] offen offset:1024 sc1\n\t" \
  "buffer_load_dwordx4 v[224:227], %[vo], %[srd], %[hb1] offen offset:0 sc1\n\t" \
  "buffer_load_dwordx4 v[228:231], %[vo], %[srd], %[hb1] offen offset:1024 sc1\n\t" \
  "buffer_load_dwordx4 v[232:235], %[vo], %[srd], %[hb2] offen offset:0 sc1\n\t" \
  "buffer_load_dwordx4 v[236:239], %[vo], %[srd], %[hb2] offen offset:1024 sc1\n\t" \
  "buffer_load_dwordx4 v[240:243], %[vo], %[srd], %[hb3] offen offset:0 sc1\n\t" \
  "buffer_load_dwordx4 v[244:247], %[vo], %[srd], %[hb3] offen offset:1024 sc1\n\t" \
  "s_cmp_gt_u32 2, %[own]\n\t" \
  "s_addc_u32 %[hb0], 2, -1\n\t" \
  "s_lshl_b32 %[hb0], %[hb0], 15\n\t" \
  "s_or_b32 %[hb1], %[hb0], 0x2000\n\t" \
  "s_or_b32 %[hb2], %[hb0], 0x4000\n\t" \
  "s_or_b32 %[hb3], %[hb0], 0x6000\n\t" \
  "buffer_load_dwordx4 v[184:187], %[vo], %[srd], %[hb0] offen offset:0 sc1\n\t" \
  "buffer_load_dwordx4 v[188:191], %[vo], %[srd], %[hb0] offen offset:1024 sc1\n\t" \
  "buffer_load_dwordx4 v[192:195], %[vo], %[srd], %[hb1] offen offset:0 sc1\n\t" \
  "buffer_load_dwordx4 v[196:199], %[vo], %[srd], %[hb1] offen offset:1024 sc1\n\t" \
  "buffer_load_dwordx4 v[200:203], %[vo], %[srd], %[hb2] offen offset:0 sc1\n\t" \
  "buffer_load_dwordx4 v[204:207], %[vo], %[srd], %[hb2] offen offset:1024 sc1\n\t" \
  "buffer_load_dwordx4 v[208:211], %[vo], %[srd], %[hb3] offen offset:0 sc1\n\t" \
  "buffer_load_dwordx4 v[212:215], %[vo], %[srd], %[hb3] offen offset:1024 sc1\n\t"
#define NS 0

#define OST " sc1 nt"
#define XPRIO 2
#define XPRIO2 1
#define MPRIO2 2
#define HDLY 8
#define LPRIO 2
#define POLLN 8
#define YGATE 11
#define LTHR 16
#define CPK 9
#define W2P 0
#define SWP 0
#define MTOP 1
#define CROT 0
#define NRG0 3
#define NRG1 3
#define CSL 0
#define NSI 8
#define DSL 1
#define FSB 128
#define FSW 8
#define XP3 3
#define XP4 3
#define LZ 1
#define KROT 0
#define LGT 0
#define XGT 0
#define W1N 8
#define W1P 1
#define WIF 0
#define W2G 0
#define W2S 0
#define MGP 1
#define RQD 0
#define XG2 2
#define NFW 28
#define SPINLOG 18
constexpr int D_ = 1024, F_ = 3584;
constexpr int L_XH = 131072;
constexpr int MISC = 155648;
constexpr int L_GT = MISC;
constexpr int L_FLG = L_GT + 512;
constexpr int L_JNK = L_FLG + 256;
constexpr int L_SEL = L_JNK + 256;
constexpr int L_BROW = L_SEL + 128;
constexpr int L_ACT = L_BROW + 2048;
constexpr int L_CAND = L_ACT + 1024;
constexpr int L_ZERO = L_CAND + 256;
constexpr int L_MAP = L_ZERO + 256;
constexpr int L_TAG = L_MAP + 512;
constexpr int L_RT = L_TAG + 16;
constexpr int LDS_BYTES = 163840;
static_assert(L_RT + 32 <= LDS_BYTES, "lds");
constexpr int FL_FULL = L_FLG, FL_FREE = L_FLG + 16, FL_C = L_FLG + 32  , FL_CFREE = L_FLG + 48, FL_TAG = L_FLG + 64  ,
              FL_XD = L_FLG + 80, FL_TK = L_FLG + 96, FL_SEL = L_FLG + 112, FL_G = L_FLG + 128, FL_W = L_FLG + 136, FL_HS = L_FLG + 144, FL_C8 = L_FLG + 160;
constexpr unsigned W_EPOCH = 0;
constexpr unsigned W_R2 = 256;
constexpr unsigned W_G2 = W_R2 + 8192;
constexpr unsigned W_HF = W_G2 + 8192;
constexpr unsigned W_AGG = W_HF + 256 * FSB;
constexpr unsigned W_SEL = W_AGG + 262144;
constexpr unsigned W_GU = W_SEL + 4096;
constexpr unsigned W_YP = W_GU + 56 * 16384;
constexpr unsigned W_H = 8u << 20;
constexpr unsigned W_DBG = 20u << 20;
constexpr unsigned W_END = 21u << 20;
static_assert(W_YP + 256 * 7168 <= W_H, "ws");
#define WS_BASE(n) (((n) - W_END) & ~(size_t)0xfffff)
constexpr int SPIN_MAX = 1 << SPINLOG;

DEV float bf2f(unsigned v) { return __uint_as_float(v << 16); }
DEV float bfhi(unsigned v) { return __uint_as_float(v & 0xffff0000u); }
DEV u16 f2bf(float f) { return __builtin_bit_cast(u16, (__bf16)f); }
DEV float rbf(float f) { return bf2f(f2bf(f)); }
typedef __bf16 bf16x2_t __attribute__((ext_vector_type(2)));
DEV unsigned pk2(float a, float b) { return __builtin_bit_cast(unsigned, __builtin_convertvector((f32x2){a, b}, bf16x2_t)); }
DEV float fma1(float a, float b, float c) { float d; asm("v_fma_f32 %0, %1, %2, %3" : "=v"(d) : "v"(a), "v"(b), "v"(c)); return d; }
DEV float fnma1(float a, float b, float c) { float d; asm("v_fma_f32 %0, -%1, %2, %3" : "=v"(d) : "v"(a), "v"(b), "v"(c)); return d; }
DEV float fnmn1(float a, float b, float c) { float d; asm("v_fma_f32 %0, -%1, %2, -%3" : "=v"(d) : "v"(a), "v"(b), "v"(c)); return d; }
DEV float mul1(float a, float b) { float d; asm("v_mul_f32 %0, %1, %2" : "=v"(d) : "v"(a), "v"(b)); return d; }
DEV f32x2 cmul(f32x2 a, f32x2 b) { return (f32x2){fnma1(a.y, b.y, mul1(a.x, b.x)), fma1(a.y, b.x, mul1(a.x, b.y))}; }
DEV f32x2 cfma(f32x2 a, f32x2 b, f32x2 c) { return (f32x2){fma1(a.x, b.x, fnma1(a.y, b.y, c.x)), fma1(a.x, b.y, fma1(a.y, b.x, c.y))}; }
template <int N> DEV void waitv() { asm volatile("s_waitcnt vmcnt(%0)" ::"n"(N) : "memory"); }
DEV void bar() { asm volatile("s_barrier" ::: "memory"); }
DEV unsigned lds_rd32(unsigned a) { unsigned v; asm volatile("ds_read_b32 %0, %1\n\ts_waitcnt lgkmcnt(0)" : "=v"(v) : "v"(a) : "memory"); return v; }
DEV u32x4 lds_rd128(unsigned a) { u32x4 v; asm volatile("ds_read_b128 %0, %1\n\ts_waitcnt lgkmcnt(0)" : "=v"(v) : "v"(a) : "memory"); return v; }
DEV void lds_wr32(unsigned a, unsigned v) { asm volatile("ds_write_b32 %0, %1" ::"v"(a), "v"(v) : "memory"); }
template <int S = 1> DEV void lds_wait_ge(unsigned a, unsigned need) {
#pragma unroll 1
  for (int it = 0; it < SPIN_MAX * 4; ++it) { if (__builtin_amdgcn_readfirstlane(lds_rd32(a)) >= need) break; __builtin_amdgcn_s_sleep(S); }
}
typedef __amdgpu_buffer_rsrc_t rsrc_t;
DEV u32x4 ld128(rsrc_t r, unsigned vo, unsigned so = 0) { return __builtin_amdgcn_raw_buffer_load_b128(r, vo, so, 0); }
#define FRESH() asm volatile("" ::: "memory")
DEV u32x4 ld128c(rsrc_t r, unsigned vo) { return __builtin_amdgcn_raw_buffer_load_b128(r, vo, 0, 16); }
DEV u32x2 ld64c(rsrc_t r, unsigned vo) { return __builtin_amdgcn_raw_buffer_load_b64(r, vo, 0, 16); }
DEV void st128c(rsrc_t r, unsigned vo, u32x4 v) {
  asm volatile("s_nop 4\n\tbuffer_store_dwordx4 %0, %1, %2, 0 offen sc1\n\ts_nop 7\n\ts_nop 7" ::"v"(v), "v"(vo), "s"(r) : "memory");
}
DEV void st64c(rsrc_t r, unsigned vo, u32x2 v) { __builtin_amdgcn_raw_buffer_store_b64(v, r, vo, 0, 16); }
DEV rsrc_t mk(const void* p, unsigned bytes) { return __builtin_amdgcn_make_buffer_rsrc((void*)p, 0, bytes, 0x00020000); }

DEV unsigned wave_maxu(unsigned v) {
  v = max(v, (unsigned)__builtin_amdgcn_update_dpp((int)v, (int)v, 0x111, 0xf, 0xf, false));
  v = max(v, (unsigned)__builtin_amdgcn_update_dpp((int)v, (int)v, 0x112, 0xf, 0xf, false));
  v = max(v, (unsigned)__builtin_amdgcn_update_dpp((int)v, (int)v, 0x114, 0xf, 0xf, false));
  v = max(v, (unsigned)__builtin_amdgcn_update_dpp((int)v, (int)v, 0x118, 0xf, 0xf, false));
  v = max(v, (unsigned)__builtin_amdgcn_update_dpp((int)v, (int)v, 0x142, 0xa, 0xf, false));
  v = max(v, (unsigned)__builtin_amdgcn_update_dpp((int)v, (int)v, 0x143, 0xc, 0xf, false));
  return (unsigned)__builtin_amdgcn_readlane((int)v, 63);
}
DEV float sum16_32(float v) {
  { auto p = __builtin_amdgcn_permlane16_swap(__float_as_uint(v), __float_as_uint(v), false, false); v = __uint_as_float(p[0]) + __uint_as_float(p[1]); }
  { auto p = __builtin_amdgcn_permlane32_swap(__float_as_uint(v), __float_as_uint(v), false, false); v = __uint_as_float(p[0]) + __uint_as_float(p[1]); }
  return v;
}
DEV float wave_sum(float v) {
  v += __uint_as_float((unsigned)__builtin_amdgcn_update_dpp(0, (int)__float_as_uint(v), 0x128, 0xf, 0xf, false));
  v += __uint_as_float((unsigned)__builtin_amdgcn_update_dpp(0, (int)__float_as_uint(v), 0x124, 0xf, 0xf, false));
  v += __uint_as_float((unsigned)__builtin_amdgcn_update_dpp(0, (int)__float_as_uint(v), 0x122, 0xf, 0xf, false));
  v += __uint_as_float((unsigned)__builtin_amdgcn_update_dpp(0, (int)__float_as_uint(v), 0x121, 0xf, 0xf, false));
  return sum16_32(v);
}
DEV unsigned route_key(float r, int idx) {
  unsigned u = __float_as_uint(r) >> 16;
  u = (u & 0x8000u) ? (~u & 0xffffu) : (u | 0x8000u);
  return (u << 16) | (unsigned)(2047 - idx);
}
DEV float key_val(unsigned key) {
  unsigned u = key >> 16;
  u = (u & 0x8000u) ? (u & 0x7fffu) : (~u & 0xffffu);
  return __uint_as_float(u << 16);
}

DEV void unit_trn(u32x4 (&w)[8]) {
  u32x4 b[8];
#pragma unroll
  for (int j = 0; j < 8; ++j)
#pragma unroll
    for (int t = 0; t < 4; ++t) b[j][t] = __builtin_amdgcn_perm(w[2 * t + 1][j >> 1], w[2 * t][j >> 1], (j & 1) ? 0x07060302u : 0x05040100u);
#pragma unroll
  for (int j = 0; j < 8; ++j) w[j] = b[j];
}
DEV f32x4 fma4(f32x4 a, f32x4 b, f32x4 c) { return __builtin_elementwise_fma(a, b, c); }
DEV f32x4 fnm4(f32x4 a, f32x4 b, f32x4 c) {
  const f32x2 l = __builtin_elementwise_fma(-a.lo, b.lo, c.lo), h = __builtin_elementwise_fma(-a.hi, b.hi, c.hi);
  return (f32x4){l.x, l.y, h.x, h.y};
}
DEV f32x4 nml4(f32x4 a, f32x4 b) { const f32x2 l = -a.lo * b.lo, h = -a.hi * b.hi; return (f32x4){l.x, l.y, h.x, h.y}; }
template <int CTRL> DEV float dpp1(float v) { return __uint_as_float((unsigned)__builtin_amdgcn_update_dpp(0, (int)__float_as_uint(v), CTRL, 0xf, 0xf, true)); }
template <int CTRL> DEV f32x4 dpp4(f32x4 v) { return (f32x4){dpp1<CTRL>(v.x), dpp1<CTRL>(v.y), dpp1<CTRL>(v.z), dpp1<CTRL>(v.w)}; }
DEV f32x4 splat4(float v) { f32x2 p = {v, v}; asm volatile("" : "+v"(p)); return (f32x4){p.x, p.y, p.x, p.y}; }
DEV void cexp4(f32x4 a2, f32x4 bv, f32x4& re, f32x4& im) {
  f32x4 m, cs, sn;
#pragma unroll
  for (int r = 0; r < 4; ++r) { const float ph = __builtin_amdgcn_fractf(bv[r]); m[r] = __builtin_amdgcn_exp2f(a2[r]); cs[r] = __builtin_amdgcn_cosf(ph); sn[r] = __builtin_amdgcn_sinf(ph); }
  re = m * cs; im = m * sn;
}
DEV f32x4 unpk4(u32x2 v) { return (f32x4){bf2f(v.x & 0xffffu), bfhi(v.x), bf2f(v.y & 0xffffu), bfhi(v.y)}; }
DEV void bcast8(int n, f32x4 v, f32x4& o) {
  switch (n) {
    case 0: o = dpp4<0x150>(v); break; case 1: o = dpp4<0x151>(v); break; case 2: o = dpp4<0x152>(v); break; case 3: o = dpp4<0x153>(v); break;
    case 4: o = dpp4<0x154>(v); break; case 5: o = dpp4<0x155>(v); break; case 6: o = dpp4<0x156>(v); break; default: o = dpp4<0x157>(v); break;
  }
}
DEV f32x4 lds4(LAS char* smem, int a) { return *(const LAS f32x4*)(smem + a); }
template <int S = 1> DEV void lds_wait_min4(unsigned a, unsigned need) {
#pragma unroll 1
  for (int it = 0; it < SPIN_MAX * 4; ++it) {
    const u32x4 f = lds_rd128(a);
    if (__builtin_amdgcn_readfirstlane(min(min(f.x, f.y), min(f.z, f.w))) >= need) break;
    __builtin_amdgcn_s_sleep(S);
  }
}

DEV void dma4(rsrc_t r0, rsrc_t r1, LAS char* la, const unsigned (&v)[4], unsigned so) {
  __builtin_amdgcn_raw_ptr_buffer_load_lds(r0, (LAS void*)la, 16, v[0], so, 0, 0);
  __builtin_amdgcn_raw_ptr_buffer_load_lds(r0, (LAS void*)la, 16, v[1], so, 1024, 0);
  __builtin_amdgcn_raw_ptr_buffer_load_lds(r1, (LAS void*)la, 16, v[2], so, 2048, 0);
  __builtin_amdgcn_raw_ptr_buffer_load_lds(r1, (LAS void*)la, 16, v[3], so, 3072, 0);
}

__global__ __launch_bounds__(512, 2) void fused(const u16*, const u16*, const u16*, const u16*, const u16*, const u16*, const u16*, const u16*, const u16*, const u16*,
                                                const u16*, const u16*, const u16*, const u16*, char*, float*) {
#if __HIP_DEVICE_COMPILE__
  unsigned kw0, kw1;
  asm volatile("s_load_dword %0, %2, 0x0\n\ts_load_dword %1, %2, 0x40" : "=&s"(kw0), "=&s"(kw1) : "s"(__builtin_amdgcn_kernarg_segment_ptr()));
  extern __shared__ __attribute__((aligned(16))) char smem_[];
  LAS char* smem = (LAS char*)smem_;
  const int tid = threadIdx.x, hw = __builtin_amdgcn_readfirstlane(tid >> 6), wid = SWP ? hw ^ ((hw >> 1) & 2) : hw;
  const int bid = blockIdx.x, xcd = bid & 7, loc = bid >> 3;
  const int c = (xcd >> 1) * 4 + (loc >> 3), j = (xcd & 1) * 8 + (loc & 7);
  int lane, fr, fq;
  auto relane = [&]() {
    asm volatile("v_mbcnt_lo_u32_b32 %0, -1, 0\n\tv_mbcnt_hi_u32_b32 %0, -1, %0" : "=v"(lane));
    __builtin_assume(lane >= 0 && lane < 64);
    fr = lane & 15; fq = lane >> 4;
  };
  const int crot = CROT == 0 ? c + 1 : CROT == 1 ? 4 * c + (c >> 2) : CROT == 2 ? (c & 1) + 4 * (c & 2) + (c >> 2) * 2 : CROT == 3 ? 2 * c : (c & 1) * 2 + 4 * (c & 2) + (c >> 2);
  const int pown = (j - crot) & 15;
  auto upos = [&](int u) { return u == 0 ? pown : u - 1 + (u - 1 >= pown ? 1 : 0); };
  u64 pc0;
  unsigned l0, l1, l2;
  asm volatile("s_getpc_b64 %0\n\tLk_pc:\n\ts_mov_b32 %1, Lk_m-Lk_pc\n\ts_mov_b32 %2, Lk_l-Lk_pc\n\ts_mov_b32 %3, Lk_x-Lk_pc" : "=s"(pc0), "=s"(l0), "=s"(l1), "=s"(l2));
  auto codepull = [&]() {
    const unsigned po = min((unsigned)(loc * 16 + (lane & 15)) * 128u, max(l0, max(l1, l2)) - 4u);
    if (lane < 16) __builtin_amdgcn_global_load_lds((const __attribute__((address_space(1))) void*)(pc0 + po), (LAS void*)(smem + L_JNK), 4, 0, 0);
  };
  u32x2 p0, p1, p2, dv;
  u32x4 xv[2][2], wv[2], gv[2];
#define MLOADS(A_, B_) \
    asm volatile("global_load_dwordx2 %0, %12, %16\n\tglobal_load_dwordx2 %1, %12, %17\n\tglobal_load_dwordx2 %2, %12, %18\n\t" \
                 "global_load_dwordx4 %4, %13, %19\n\tglobal_load_dwordx4 %5, %13, %19 offset:1024\n\tglobal_load_dwordx4 %6, %13, %19 offset:2048\n\tglobal_load_dwordx4 %7, %13, %19 offset:3072\n\t" \
                 "global_load_dwordx4 %8, %14, %20\n\tglobal_load_dwordx4 %9, %14, %20 offset:1024\n\tglobal_load_dwordx4 %10, %14, %21\n\tglobal_load_dwordx4 %11, %14, %21 offset:1024\n\t" \
                 "global_load_dwordx2 %3, %15, %22" \
                 : "=&v"(p0), "=&v"(p1), "=&v"(p2), "=&v"(dv), "=&v"(xv[0][0]), "=&v"(xv[0][1]), "=&v"(xv[1][0]), "=&v"(xv[1][1]), "=&v"(wv[0]), "=&v"(wv[1]), "=&v"(gv[0]), "=&v"(gv[1]) \
                 : "v"((j * 64 + wid * 16 + 4 * fq) * 2), "v"((c * 128 + j * 8 + wid * 2) * 2048 + lane * 16), "v"(lane * 16), "v"((j * 64 + 4 * fr) * 2), \
                   "s"(A_[1]), "s"(A_[2]), "s"(A_[7]), "s"(A_[0]), "s"(B_[1]), "s"(B_[5]), "s"(B_[0]) : "memory")
  if (wid < 4) {
    u64x8 ta, tb;
    if (MTOP) asm volatile("s_load_dwordx16 %0, %2, 0x0\n\ts_load_dwordx16 %1, %2, 0x40" : "=&s"(ta), "=&s"(tb) : "s"(__builtin_amdgcn_kernarg_segment_ptr()));
    relane();
    if (wid == 0) codepull();
    if (MTOP) {
      asm volatile("s_waitcnt lgkmcnt(0)" : "+s"(ta), "+s"(tb));
      MLOADS(ta, tb);
    }
  }
  const int l = wid & 1;
#define FFNDEF() int b_ = bid; asm volatile("" : "+s"(b_)); \
  const bool ffn = b_ < 8 * NFW; \
  const int sg = (b_ & 7) * 7 + (b_ >> 5), kq = (b_ >> 3) & 3, sg1 = (sg & ~1) + l, gu1 = sg & 1; \
  const unsigned gub = W_GU + ((sg1 * 4 + kq) * 2 + gu1) * 2048, ypb = W_YP + (kq * 4 + l * 2) * 16 * 7168 + sg * 128
  if (__builtin_expect(wid >= 4, 1)) {
  u64x8 ka, kb;
  asm volatile("s_load_dwordx16 %0, %2, 0x0\n\ts_load_dwordx16 %1, %2, 0x40" : "=&s"(ka), "=&s"(kb) : "s"(__builtin_amdgcn_kernarg_segment_ptr()));
  relane();
  unsigned vo1[4];
#pragma unroll
  for (int e = 0; e < 4; ++e)
    vo1[e] = (l == 0 ? c * 128 + 8 * (lane >> 3) + 64 * (e & 1) + (e >> 1) : j * 64 + (lane >> 3) + 8 * (e & 1)) * 2048 + (((lane & 7) ^ (lane >> 3)) * 16) + 4096 - e * 1024;
  const unsigned s4 = (l == 0 ? 2 : 16) * 2048, so1 = wid >= 6 ? 2 * s4 : 0, la1 = l * 16384 + (wid >= 6 ? 8192 : 0);
  const int s0 = (KROT == 0 ? c + j : KROT == 1 ? (c >> 1) + (j >> 1) + 4 * (c >> 2) : KROT == 2 ? c + (j >> 1) + 4 * (j >> 3) : KROT == 3 ? 2 * c + j : KROT == 4 ? 4 * c + 2 * j + (c >> 2) : KROT == 5 ? (c >> 1) + j : 3 * c + j) & 15;
  unsigned sl0 = ((s0 + (s0 == j ? 1 : 0)) & 15) * 128 + so1;
  asm volatile("" : "+v"(vo1[0]), "+v"(vo1[1]), "+v"(vo1[2]), "+v"(vo1[3]) : "s"(sl0), "s"(s4), "s"(la1));
  asm volatile("s_waitcnt lgkmcnt(0)" : "+s"(ka), "+s"(kb) : "s"(kw0), "s"(kw1));
  const char *x = (const char*)ka[0], *Bre = (const char*)ka[3], *Bim = (const char*)ka[4], *Cre = (const char*)ka[5], *Cim = (const char*)ka[6], *w_gate = (const char*)kb[2],
             *w_up = (const char*)kb[3], *w_down = (const char*)kb[4];
  char* ws = (char*)kb[6];
  const rsrc_t r0 = mk((l == 0 ? x : Bre) - 4096, (l == 0 ? 1 << 22 : 1 << 21) + 4096), r1 = mk((l == 0 ? x : Bim) - 4096, (l == 0 ? 1 << 22 : 1 << 21) + 4096);
  auto issue = [&](unsigned sk, unsigned slot) { dma4(r0, r1, smem + slot + la1, vo1, sk); dma4(r0, r1, smem + slot + la1 + 4096, vo1, sk + s4); };
  issue(sl0, 0);
  __builtin_amdgcn_sched_barrier(0);
  const rsrc_t rws = mk(ws, W_END);
  const rsrc_t rx = mk(x, 2048 * 1024 * 2);
  u64 tbl;
  {
    const int n = (j - s0) & 15;
    const u64 seq = 0xfedcba9876543210ull, rot = s0 ? (seq >> (4 * s0)) | (seq << (64 - 4 * s0)) : seq, lo = (1ull << (4 * n)) - 1;
    tbl = (rot & lo) | ((rot >> 4) & ~lo) | ((u64)j << 60);
  }
  auto slice = [&](int k) { return ((unsigned)(tbl >> (4 * k)) & 15u) * 128u; };

  auto flag = [&](int a, unsigned v) { lds_wr32(lane == 0 ? (unsigned)a : (unsigned)(L_JNK + lane * 4), v); };
  auto flagmax = [&](int a, unsigned v) { asm volatile("ds_max_u32 %0, %1" ::"v"(lane == 0 ? (unsigned)a : (unsigned)(L_JNK + lane * 4)), "v"(v) : "memory"); };
  auto hpoll = [&](unsigned tagA, unsigned tagB) {
    lds_wait_min4(FL_FREE, 9u);
    __builtin_amdgcn_s_sleep(HDLY);
#pragma unroll 1
    for (int it = 0; it < SPIN_MAX; ++it) {
      FRESH();
      const u32x2 f = ld64c(rws, W_HF + (c * 16 + upos(lane >> 2)) * FSB + (lane & 3) * FSW);
      const u64 m = ~__ballot(lane < 4 * NS + 4 || (f.x == tagA && f.y == tagB));
      if (!m) break;
      flag(FL_C + 8, ((unsigned)__builtin_ctzll(m) >> 2) - 1u);
    }
    flag(FL_C + 8, 99u);
  };
    const rsrc_t rcr = mk(Cre, WIF == 4 ? 0 : 1 << 21), rci = mk(Cim, WIF == 4 || WIF == 5 ? 0 : 1 << 21);
    const unsigned ce = (j * 64 + 4 * (lane >> 3)) * 2048 + (((lane & 7) ^ (lane >> 4)) * 16);
    const unsigned co = (j * 64 + 32 + 4 * (lane >> 3)) * 2048 + (((lane & 7) ^ (4 + (lane >> 4))) * 16);
    auto cfill = [&](int u, int slot, int q0, int q1) {
      LAS char* dst = smem + slot * 16384;
#pragma unroll
      for (int q = q0; q < q1; ++q)
        __builtin_amdgcn_raw_ptr_buffer_load_lds(q < 8 ? rcr : rci, (LAS void*)(dst + q * 1024), 16, (q & 1) ? co : ce, ((q & 7) >> 1) * 2048 + ((upos(u) + crot) & 15) * 128, 0, 0);
    };
  if (__builtin_expect(wid == 4 || wid == 5, 1)) {
    __builtin_amdgcn_s_setprio(LPRIO);
    issue(slice(1), 32768);
    issue(slice(2), 65536);
    constexpr int K0 = 0;
    {
      u64 t = tbl >> (4 * K0 + 12);
#pragma unroll 1
      for (int k = K0; k < 13; ++k) {
        const unsigned sk = ((unsigned)t & 15u) << 7;
        waitv<16>();
        bar();
        issue(sk, ((k + 3) & 3) * 32768);
        if (k == CPK && l == 1) codepull();
        t >>= 4;
      }
    }
    waitv<16>(); bar();
    waitv<8>(); bar();
    const unsigned tagA = lds_rd32(L_TAG), tagB = tagA + 0x40000000u;
    waitv<0>(); bar();
    bar();
    lds_wait_ge(FL_W + l * 4, 1u);
    __builtin_amdgcn_s_setprio(1);
#pragma unroll 1
    for (int u = l; u < 8; u += 2) { if (LGT && (u >> 1) == LGT - 1) lds_wait_min4(FL_FREE, 1u); cfill(u, u, 0, 8); waitv<LTHR>(); cfill(u, u, 8, 16); waitv<LTHR>(); if (u >= 2) flag(FL_C + l * 4, (unsigned)(u - 1)); }
    u32x4 cq[3][16];
    auto creg = [&](int g) {
#pragma unroll
      for (int q = 0; q < 16; ++q) {
        const unsigned so = ((q & 7) >> 1) * 2048 + ((upos(10 + l + 2 * g) + crot) & 15) * 128;
        if (q < 8) asm volatile("buffer_load_dwordx4 %0, %1, %2, %3 offen" : "=v"(cq[g][q]) : "v"((q & 1) ? co : ce), "s"(rcr), "s"(so) : "memory");
        else asm volatile("buffer_load_dwordx4 %0, %1, %2, %3 offen" : "=v"(cq[g][q]) : "v"((q & 1) ? co : ce), "s"(rci), "s"(so) : "memory");
        if ((q & 7) == 7) waitv<LTHR>();
      }
    };
#pragma unroll
    for (int g = 0; g < 3; ++g) {
      if (g >= (l ? NRG1 : NRG0)) break;
      waitv<16>();
      if (g == 1) flag(FL_C + l * 4, (unsigned)(7 + l));
      creg(g);
    }
    waitv<0>();
    if ((l ? NRG1 : NRG0) < 2) flag(FL_C + l * 4, (unsigned)(7 + l));
    if (l == 0) {
      __builtin_amdgcn_s_setprio(3);
      hpoll(tagA, tagB);
    }
    else {
      lds_wait_min4(FL_CFREE, 1u);
      cfill(8, 0, 0, 16);
      cfill(9, 8, 0, 16);
      waitv<0>();
      flagmax(FL_C8, 1u);
      flag(FL_C + 4, 10u);
    }
    __builtin_amdgcn_s_setprio(0);
    if (l == 0) { lds_wait_ge(FL_C8, 1u); flag(FL_C, 9u); }
#pragma unroll
    for (int g = 0; g < 3; ++g) {
      asm volatile("" : "+v"(cq[g][0]), "+v"(cq[g][1]), "+v"(cq[g][2]), "+v"(cq[g][3]), "+v"(cq[g][4]), "+v"(cq[g][5]), "+v"(cq[g][6]), "+v"(cq[g][7]),
                   "+v"(cq[g][8]), "+v"(cq[g][9]), "+v"(cq[g][10]), "+v"(cq[g][11]), "+v"(cq[g][12]), "+v"(cq[g][13]), "+v"(cq[g][14]), "+v"(cq[g][15]));
      const int u = 10 + l + 2 * g;
      lds_wait_min4<LZ>(FL_CFREE, (unsigned)(u - 8));
      const int slot = u - 9;
      if (g < (l ? NRG1 : NRG0)) {
#pragma unroll
        for (int q = 0; q < 16; ++q)
          asm volatile("ds_write_b128 %0, %1 offset:%2" ::"v"(slot * 16384 + lane * 16), "v"(cq[g][q]), "n"(q * 1024) : "memory");
      } else {
#pragma unroll
        for (int q = 0; q < 16; q += 4) { cfill(u, slot, q, q + 4); if (CSL) __builtin_amdgcn_s_sleep(CSL); }
        waitv<0>();
      }
      flag(FL_C + l * 4, g == 2 ? 99u : (unsigned)(u + 1));
    }
    asm volatile("Lk_l:");
    return;
  }
  {
    const int xw = wid - 6;
    __builtin_amdgcn_s_setprio(XPRIO);
    u32x2 ep;
    unsigned tagA, tagB;
    if (xw == 0) lds_wr32(L_FLG + lane * 4, 0u);
    else asm volatile("ds_write_b64 %0, %1\n\tds_write_b64 %2, %3" ::"v"(L_CAND + lane * 8), "v"((u32x2){0u, 0u}), "v"(L_MAP + lane * 8), "v"((u32x2){L_ZERO, L_ZERO}) : "memory");
    FFNDEF();
    u32x4 w1[4][8], w2[2][8];
    const rsrc_t rw1 = mk(gu1 ? w_up : w_gate, ffn ? D_ * F_ * 2 : 0), r2 = mk(w_down, ffn ? D_ * F_ * 2 : 0);
    unsigned o1 = (kq * 256 + 32 * (fr >> 3) + 8 * fq) * (F_ * 2) + (sg1 * 64 + 8 * (fr & 7)) * 2;
    unsigned o2 = (sg * 64 + 32 * (fr >> 3) + 8 * fq) * 2048 + (kq * 256 + xw * 128 + 8 * (fr & 7)) * 2;
#define LDW1(b) { _Pragma("unroll") for (int i = 4 * ((b) & 1); i < 4 * ((b) & 1) + 4; ++i) \
      asm volatile("buffer_load_dwordx4 %0, %1, %2, %3 offen" : "=v"(w1[(b) >> 1][i]) : "v"(o1), "s"(rw1), "s"((((b) >> 1) * 64 + i) * (F_ * 2)) : "memory"); }
#define LDW2(a, b) { _Pragma("unroll") for (int i = a; i < b; ++i) \
      asm volatile("buffer_load_dwordx4 %0, %1, %2, %3 offen" : "=v"(w2[i >> 3][i & 7]) : "v"(o2), "s"(r2), "s"((i & 7) * 2048 + (i >> 3) * 128) : "memory"); }
#define TRN(w, n) { asm volatile("s_waitcnt vmcnt(%8)" : "+v"(w[0]), "+v"(w[1]), "+v"(w[2]), "+v"(w[3]), "+v"(w[4]), "+v"(w[5]), "+v"(w[6]), "+v"(w[7]) : "n"(n) : "memory"); \
      unit_trn(w); asm volatile("" : "+v"(w[0]), "+v"(w[1]), "+v"(w[2]), "+v"(w[3]), "+v"(w[4]), "+v"(w[5]), "+v"(w[6]), "+v"(w[7])); }
    unsigned key[16];
    unsigned rgo = W_R2 + l * 4096 + lane * 16, ggo = W_G2 + c * 512 + (lane & 31) * 16;
    u32x4 g[4], a;
    asm volatile("" : "+v"(o1), "+v"(o2), "+v"(rgo), "+v"(ggo) : "s"(rw1), "s"(r2));
    auto A_EP = [&]() { asm volatile("buffer_load_dwordx2 %0, off, %1, 0 sc1" : "=v"(ep) : "s"(rws) : "memory"); };
    auto A_TAG = [&]() {
      asm volatile("" : "+v"(ep));
      tagA = ep.x + 1u; tagB = tagA + 0x40000000u;
      if (xw == 0 && lane == 0) lds_wr32(L_TAG, tagA);
    };
    auto A_CLR = [&]() {
      asm volatile("s_nop 1\n\t"
                   "buffer_store_dwordx4 %0, %1, %3, 0 offen sc1\n\tbuffer_store_dwordx4 %0, %1, %3, 0 offen offset:1024 sc1\n\t"
                   "buffer_store_dwordx4 %0, %2, %3, 0 offen sc1\n\tbuffer_store_dwordx4 %0, %2, %3, %4 offen sc1\n\t"
                   "buffer_store_dwordx4 %0, %2, %3, %5 offen sc1\n\tbuffer_store_dwordx4 %0, %2, %3, %6 offen sc1\n\t"
                   "buffer_store_dwordx4 %0, %7, %3, %8 offen sc1\n\tbuffer_store_dwordx4 %0, %7, %3, %8 offen offset:1024 sc1\n\t"
                   "buffer_store_dwordx4 %0, %7, %3, %8 offen offset:2048 sc1\n\tbuffer_store_dwordx4 %0, %7, %3, %8 offen offset:3072 sc1\n\ts_nop 7"
                   ::"v"((u32x4){~0u, ~0u, ~0u, ~0u}), "v"(ffn ? gub + lane * 16 : 0xf0000000u), "v"(ffn ? ypb + (lane >> 3) * 7168 + (lane & 7) * 16 : 0xf0000000u), "s"(rws),
                   "s"(8 * 7168), "s"(16 * 7168), "s"(24 * 7168), "v"(NS ? lane * 16 : 0xf0000000u), "s"(W_H + c * 524288 + (j * 2 + xw) * 4096) : "memory");
    };
    auto A_PUB = [&]() {
      u32x2 rt;
      asm volatile("ds_read_b64 %0, %1\n\ts_waitcnt lgkmcnt(0)" : "=v"(rt) : "v"(L_RT + (lane & 1) * 16 + xw * 8) : "memory");
      st128c(rws, lane < 2 ? (lane ? W_G2 : W_R2) + (c * 128 + j * 8 + xw * 4) * 4 : 0xf0000000u, (u32x4){rt.x, tagA, rt.y, tagB});
    };
    auto A_GAT = [&]() {
#pragma unroll
      for (int q = 0; q < 4; ++q) asm volatile("buffer_load_dwordx4 %0, %1, %2, 0 offen offset:%3 sc1" : "=v"(g[q]) : "v"(rgo), "s"(rws), "n"(q * 1024) : "memory");
      asm volatile("buffer_load_dwordx4 %0, %1, %2, 0 offen sc1" : "=v"(a) : "v"(ggo), "s"(rws) : "memory");
    };
    static_assert(NS <= 3, "cleared blocks");
    A_EP();
    issue(slice(1) + so1, 32768);
    issue(slice(2) + so1, 65536);
    asm volatile("s_waitcnt vmcnt(17) lgkmcnt(0)" ::: "memory"); bar();
    issue(slice(3) + so1, 98304);
    waitv<24>(); A_TAG();
    A_CLR();
    asm volatile("s_waitcnt vmcnt(26) lgkmcnt(0)" ::: "memory"); bar();
    issue(slice(4) + so1, 0);
    asm volatile("s_waitcnt vmcnt(26) lgkmcnt(0)" ::: "memory"); bar();
    issue(slice(5) + so1, 32768);
    LDW2(0, 16)
    asm volatile("s_waitcnt vmcnt(42) lgkmcnt(0)" ::: "memory"); bar();
    issue(slice(6) + so1, 65536);
    waitv<40>(); A_PUB();
    asm volatile("s_waitcnt vmcnt(33) lgkmcnt(0)" ::: "memory"); bar();
    issue(slice(7) + so1, 98304);
    asm volatile("s_waitcnt vmcnt(33) lgkmcnt(0)" ::: "memory"); bar();
    issue(slice(8) + so1, 0);
    asm volatile("s_waitcnt vmcnt(17) lgkmcnt(0)" ::: "memory"); bar();
    issue(slice(9) + so1, 32768);
    asm volatile("s_waitcnt vmcnt(16) lgkmcnt(0)" ::: "memory"); bar();
    issue(slice(10) + so1, 65536);
    asm volatile("s_waitcnt vmcnt(16) lgkmcnt(0)" ::: "memory"); bar();
    issue(slice(11) + so1, 98304);
    A_GAT();
    asm volatile("s_waitcnt vmcnt(21) lgkmcnt(0)" ::: "memory"); bar();
    issue(slice(12) + so1, 0);
    asm volatile("s_waitcnt vmcnt(21) lgkmcnt(0)" ::: "memory"); bar();
    issue(slice(13) + so1, 32768);
    asm volatile("s_waitcnt vmcnt(21) lgkmcnt(0)" ::: "memory"); bar();
    issue(slice(14) + so1, 65536);
    constexpr int K0 = 12;
#pragma unroll
    for (int k = K0; k < 13; ++k) { waitv<16>(); bar(); issue(slice(k + 3) + so1, ((k + 3) & 3) * 32768); }
    asm volatile("" : "+v"(g[0]), "+v"(g[1]), "+v"(g[2]), "+v"(g[3]), "+v"(a));
    LDW1(0)
    waitv<20>(); bar(); LDW1(1)
    waitv<16>(); bar(); LDW1(2)
    waitv<12>(); bar(); LDW1(3)
    bar();
    __builtin_amdgcn_s_setprio(XPRIO2);
#define W1B() do { if (XGT == 1) lds_wait_min4(FL_FREE, 1u); if (XGT == 2) lds_wait_min4(FL_HS, 1u); if (XGT == 3) lds_wait_ge(FL_HS, 1u); \
      _Pragma("unroll") for (int b_ = W1N; b_ < 8; ++b_) LDW1(b_) } while (0)
#pragma unroll
    for (int b_ = 4; b_ < W1N; ++b_) LDW1(b_)
    if (W2P == 10) LDW2(0, 16)
    if (lane == 0) lds_wr32(FL_W + xw * 4, 1u);
    {
#pragma unroll 1
      for (int it = 0; it < SPIN_MAX; ++it) {
        bool ok = true;
#pragma unroll
        for (int q = 0; q < 4; ++q) ok = ok && g[q].y == tagA && g[q].w == tagB;
        if (__all(ok && a.y == tagA && a.w == tagB)) break;
        __builtin_amdgcn_s_sleep(8);
        FRESH();
#pragma unroll
        for (int q = 0; q < 4; ++q) g[q] = ld128c(rws, rgo + q * 1024);
        a = ld128c(rws, ggo);
      }
      if (l == 0) {
        const unsigned gp = lane < 32 ? a.x : a.z, ga = L_GT + ((4 * (lane & 1) + 2 * (lane >> 5)) * 16 + ((lane & 31) >> 1)) * 4;
        lds_wr32(ga, gp << 16);
        lds_wr32(ga + 64, gp & 0xffff0000u);
      }
      asm volatile("ds_add_u32 %0, %1" ::"v"(lane == 0 ? (unsigned)FL_G : (unsigned)(L_JNK + lane * 4)), "v"(1u) : "memory");
      if (W1P == 1) W1B();
#pragma unroll
      for (int q = 0; q < 4; ++q) {
        const unsigned r0 = 2047 - (l * 1024 + q * 256 + lane * 4);
        key[4 * q] = (g[q].x << 16) + r0; key[4 * q + 1] = (g[q].x & 0xffff0000u) + (r0 - 1);
        key[4 * q + 2] = (g[q].z << 16) + (r0 - 2); key[4 * q + 3] = (g[q].z & 0xffff0000u) + (r0 - 3);
        asm volatile("" : "+v"(key[4 * q]), "+v"(key[4 * q + 1]), "+v"(key[4 * q + 2]), "+v"(key[4 * q + 3]));
      }
      unsigned m = key[0], m2 = 0, m3 = 0;
#pragma unroll
      for (int i = 1; i < 16; ++i) {
        asm("v_med3_u32 %0, %1, %0, %3\n\tv_med3_u32 %1, %2, %1, %3\n\tv_max_u32 %2, %2, %3" : "+v"(m3), "+v"(m2), "+v"(m) : "v"(key[i]));
      }
      asm volatile("" : "+v"(m), "+v"(m2), "+v"(m3));
      unsigned T = 0;
#pragma unroll
      for (int b = 31; b >= 16; --b) {
        const unsigned t2 = T | (1u << b);
        T = __popcll(__ballot(m >= t2)) >= 8 ? t2 : T;
      }
      asm volatile("" : "+s"(T));
      const u64 k1 = __ballot(m >= T), k2 = __ballot(m2 >= T);
      const int n1 = __popcll(k1);
      if (__ballot(m3 >= T) || n1 + __popcll(k2) > 16) {
        unsigned cand = 0;
#pragma unroll 1
        for (int s = 0; s < 8; ++s) {
          unsigned mm = key[0];
#pragma unroll
          for (int i = 1; i < 16; ++i) mm = max(mm, key[i]);
          mm = wave_maxu(mm);
          cand = lane == s ? mm : cand;
#pragma unroll
          for (int i = 0; i < 16; ++i) key[i] = key[i] == mm ? 0u : key[i];
        }
        if (lane < 8) lds_wr32(L_CAND + l * 64 + lane * 4, cand);
      } else {
        const unsigned p1 = __builtin_amdgcn_mbcnt_hi((unsigned)(k1 >> 32), __builtin_amdgcn_mbcnt_lo((unsigned)k1, 0u));
        const unsigned p2 = __builtin_amdgcn_mbcnt_hi((unsigned)(k2 >> 32), __builtin_amdgcn_mbcnt_lo((unsigned)k2, (unsigned)n1));
        lds_wr32(m >= T ? L_CAND + l * 64 + p1 * 4 : L_JNK + lane * 4, m);
        if (k2) lds_wr32(m2 >= T ? L_CAND + l * 64 + p2 * 4 : L_JNK + lane * 4, m2);
      }
      if (lane == 0) lds_wr32(FL_TK + 8 + l * 4, 1u);
      if (W1P == 2) W1B();
      lds_wait_ge(FL_TK + 8 + (l ^ 1) * 4, 1u);
      const unsigned ck = *(const volatile LAS unsigned*)(smem + L_CAND + (lane & 31) * 4);
      int rank = 0;
#pragma unroll
      for (int i = 0; i < 4; ++i) {
        const u32x4 o = *(const volatile LAS u32x4*)(smem + L_CAND + (lane >> 5) * 64 + i * 16);
        rank += (o.x > ck) + (o.y > ck) + (o.z > ck) + (o.w > ck);
      }
      { auto p = __builtin_amdgcn_permlane32_swap((unsigned)rank, (unsigned)rank, false, false); rank = (int)(p[0] + p[1]); }
      if (l == 0) {
        if (lane < 32 && ck != 0u && rank < 8) {
          const int t = 2047 - (int)(ck & 0xffffu);
          lds_wr32(L_SEL + rank * 4, ck);
          if ((t >> 7) == c) lds_wr32(L_MAP + ((t & 7) * 16 + ((t & 127) >> 3)) * 4, (unsigned)(L_BROW + rank * 256));
        }
        const unsigned any = __ballot(ck != 0u && rank < 8 && ((2047 - (int)(ck & 0xffffu)) >> 7) == c) ? 2u : 1u;
        flag(FL_SEL, any);
        if (bid == 0 && lane == 0) st64c(rws, W_EPOCH, (u32x2){tagA, tagB});
      }
    }
    if (W1P == 3) W1B();
    lds_wait_ge(FL_SEL, 1u);
    const unsigned mine = lds_rd32(L_SEL + (lane & 7) * 4);
    {
      {
        u32x4 xa[4];
#pragma unroll
        for (int u = 0; u < 4; ++u)
          asm volatile("buffer_load_dwordx4 %0, %1, %2, 0 offen offset:%3" : "=v"(xa[u]) : "v"((2047 - (mine & 0xffffu)) * 2048 + (kq * 256 + 32 * (fr >> 3) + 8 * fq) * 2), "s"(rx), "n"(u * 128) : "memory");
        TRN(w1[0], W2P == 10 ? 36 : 20) TRN(w1[1], W2P == 10 ? 36 : 20) TRN(w1[2], W2P == 10 ? 28 : 12) TRN(w1[3], 0)
        asm volatile("" : "+v"(xa[0]), "+v"(xa[1]), "+v"(xa[2]), "+v"(xa[3]));
        const bool val = (fq >> 1) == (fr >> 3), lo = lane < 32;
        auto publish = [&](f32x4 (&d)[8], unsigned o, unsigned sp) {
          u32x4 pv[4];
#pragma unroll
          for (int rr = 0; rr < 2; ++rr)
#pragma unroll
            for (int q = 0; q < 8; ++q) {
              auto p = __builtin_amdgcn_permlane32_swap(__float_as_uint(d[q][rr]), __float_as_uint(d[q][2 + rr]), false, false);
              const unsigned own = lo ? p[0] : p[1], oth = lo ? p[1] : p[0];
              pv[rr * 2 + (q >> 2)][q & 3] = min(__float_as_uint(__uint_as_float(own) + dpp1<0x128>(__uint_as_float(oth))), 0xfffffffeu);
            }
          asm volatile("s_nop 4\n\t"
                       "buffer_store_dwordx4 %0, %4, %5, 0 offen sc1\n\tbuffer_store_dwordx4 %1, %4, %5, %6 offen sc1\n\t"
                       "buffer_store_dwordx4 %2, %4, %5, %7 offen sc1\n\tbuffer_store_dwordx4 %3, %4, %5, %8 offen sc1\n\t"
                       "s_nop 7\n\ts_nop 7"
                       ::"v"(pv[0]), "v"(pv[1]), "v"(pv[2]), "v"(pv[3]), "v"(val && ffn ? o : 0xf0000000u), "s"(rws), "s"(sp), "s"(2 * sp), "s"(3 * sp) : "memory");
        };
        f32x4 d[8];
#pragma unroll
        for (int q = 0; q < 8; ++q) d[q] = (f32x4){0.f, 0.f, 0.f, 0.f};
#pragma unroll
        for (int u = 0; u < 4; ++u)
#pragma unroll
          for (int q = 0; q < 8; ++q) d[q] = __builtin_amdgcn_mfma_f32_16x16x32_bf16(__builtin_bit_cast(bf16x8, xa[u]), __builtin_bit_cast(bf16x8, w1[u][q]), d[q], 0, 0, 0);
        publish(d, gub + (16 * (fr >> 3) + 8 * (fq & 1) + (fr & 7)) * 16, 512);
        if (W2P < 11) { TRN(w2[0], 63) TRN(w2[1], 63) }
        lds_wait_ge<LZ>(FL_CFREE, XG2);
        __builtin_amdgcn_s_setprio(XP3);
        if (W2P == 11) LDW2(0, 16)
        if (ffn) {
          f32x4 G, U;
#pragma unroll 1
          for (int it = 0; it < SPIN_MAX; ++it) {
            FRESH();
            u32x4 a_[8];
#pragma unroll
            for (int k = 0; k < 8; ++k) a_[k] = __builtin_amdgcn_raw_buffer_load_b128(rws, W_GU + sg * 16384 + xw * 1024 + lane * 16, k * 2048, 16);
            unsigned m = 0;
#pragma unroll
            for (int k = 0; k < 8; ++k) m = max(max(m, max(a_[k].x, a_[k].y)), max(a_[k].z, a_[k].w));
            G = (__builtin_bit_cast(f32x4, a_[0]) + __builtin_bit_cast(f32x4, a_[2])) + (__builtin_bit_cast(f32x4, a_[4]) + __builtin_bit_cast(f32x4, a_[6]));
            U = (__builtin_bit_cast(f32x4, a_[1]) + __builtin_bit_cast(f32x4, a_[3])) + (__builtin_bit_cast(f32x4, a_[5]) + __builtin_bit_cast(f32x4, a_[7]));
            if (__all(m != ~0u)) break;
            __builtin_amdgcn_s_sleep(8);
          }
          if (W2P == 12) { lds_wait_ge<LZ>(FL_CFREE, W2G + W2S * (kq + 4 * xw) / 8); LDW2(0, 16) }
          float a4[4];
#pragma unroll
          for (int e = 0; e < 4; ++e) { const float g = rbf(G[e]); a4[e] = rbf(g * __builtin_amdgcn_rcpf(1.f + __expf(-g))) * rbf(U[e]); }
          asm volatile("s_nop 0\n\tds_write_b64 %0, %1" ::"v"(L_ACT + (4 * ((lane >> 3) & 1) + 2 * ((lane >> 4) & 1) + xw) * 128 + (lane & 7) * 16 + (lane >> 5) * 8),
                       "v"((u32x2){pk2(a4[0], a4[1]), pk2(a4[2], a4[3])}) : "memory");
          if (lane == 0) lds_wr32(FL_TK + xw * 4, 1u);
          lds_wait_ge(FL_TK + (xw ^ 1) * 4, 1u);
        }
        if (W2P >= 11) { TRN(w2[0], 8) TRN(w2[1], 0) }
        const u32x4 af = lds_rd128(L_ACT + ((fr & 7) * 8 + 4 * (fr >> 3) + fq) * 16);
#pragma unroll
        for (int u = 0; u < 2; ++u) {
#pragma unroll
          for (int q = 0; q < 8; ++q) d[q] = __builtin_amdgcn_mfma_f32_16x16x32_bf16(__builtin_bit_cast(bf16x8, af), __builtin_bit_cast(bf16x8, w2[u][q]), (f32x4){0.f, 0.f, 0.f, 0.f}, 0, 0, 0);
          publish(d, ypb + (u * 16 + 2 * (fr >> 3) + (fq & 1)) * 7168 + (fr & 7) * 16, 4 * 7168);
        }
      }
    }
    __builtin_amdgcn_s_setprio(XP4);
    unsigned rest = (unsigned)__ballot(((2047 - (int)(mine & 0xffffu)) >> 7) == c) & 0xffu;
    if (xw) rest &= rest - 1;
#pragma unroll 1
    for (; rest; rest &= rest - 1, rest &= rest - 1) {
      const int s = __builtin_ctz(rest);
      const unsigned ks = (unsigned)__builtin_amdgcn_readlane((int)mine, s);
      const int t = 2047 - (int)(ks & 0xffffu);
      const float rs = rbf(1.f / (1.f + __expf(-key_val(ks))));
      const unsigned yb = W_YP + (j * 16 + (s & 1) * 8 + 2 * ((s >> 1) & 1) + (s >> 2)) * 7168;
      const u32x2 xr = __builtin_amdgcn_raw_buffer_load_b64(rx, t * 2048 + (j * 64 + 8 * (lane & 7) + 4 * ((lane >> 3) & 1)) * 2, 0, 0);
      lds_wait_ge<LZ>(FL_CFREE, YGATE);
      const float omg = rbf(1.f - __uint_as_float(lds_rd32(L_GT + ((t & 7) * 16 + ((t & 127) >> 3)) * 4)));
      f32x4 y[2];
#pragma unroll 1
      for (int it = 0; it < SPIN_MAX; ++it) {
        FRESH();
        unsigned m = 0;
#pragma unroll
        for (int h = 0; h < 2; ++h) {
          y[h] = (f32x4){0.f, 0.f, 0.f, 0.f};
#pragma unroll
          for (int k = 0; k < 7; ++k) {
            const u32x4 a = __builtin_amdgcn_raw_buffer_load_b128(rws, yb + lane * 16, h * 4 * 7168 + k * 1024, 16);
            m = max(max(m, max(a.x, a.y)), max(a.z, a.w));
            y[h] += __builtin_bit_cast(f32x4, a);
          }
        }
        if (__all(m != ~0u)) break;
        __builtin_amdgcn_s_sleep(6);
      }
      const bool o8 = lane & 8;
      const f32x4 xf = unpk4(xr);
      f32x4 dl;
#pragma unroll
      for (int e = 0; e < 4; ++e) {
        const float v = sum16_32((o8 ? y[1][e] : y[0][e]) + dpp1<0x128>(o8 ? y[0][e] : y[1][e]));
        dl[e] = rbf(mul1(omg, rbf(xf[e] + rbf(mul1(rbf(v), rs))))) - rbf(mul1(omg, xf[e]));
      }
      if (lane < 16) asm volatile("s_nop 0\n\tds_write_b128 %0, %1\n\ts_nop 3" ::"v"(L_BROW + s * 256 + (lane & 7) * 32 + (lane >> 3) * 16), "v"(dl) : "memory");
    }
    if (lane == 0) lds_wr32(FL_XD + xw * 4, 1u);
    asm volatile("Lk_x:");
    return;
  }
  }

  __builtin_amdgcn_s_setprio(3);
  relane();
  u64x8 ma, mb;
  asm volatile("s_load_dwordx16 %0, %2, 0x0\n\ts_load_dwordx16 %1, %2, 0x40\n\ts_waitcnt lgkmcnt(0)" : "=&s"(ma), "=&s"(mb) : "s"(__builtin_amdgcn_kernarg_segment_ptr()), "s"(kw0), "s"(kw1));
  const char* out = (const char*)mb[7];
  char* ws = (char*)mb[6];
  const rsrc_t rws = mk(ws, W_END);
  if (!MTOP) MLOADS(ma, mb);
  f32x4 lr, li, cr, ci, Er, Ei, Fr, Fi, br_, bi_;
  u32x2 xe[2][4];
  {
    asm volatile("s_waitcnt vmcnt(9)" : "+v"(p0), "+v"(p1), "+v"(p2)::"memory");
    const f32x4 lre = unpk4(p0), lim = unpk4(p1), ld = unpk4(p2) * splat4(1.44269504f);
    f32x4 dt, id = fma4(lre, lre, lim * lim);
#pragma unroll
    for (int r = 0; r < 4; ++r) { dt[r] = rbf(__builtin_amdgcn_exp2f(ld[r])); id[r] = __builtin_amdgcn_rcpf(id[r]); }
    const f32x4 A2 = dt * lre * splat4(1.44269504f), Bv = dt * lim * splat4(0.159154943f);
    cexp4(A2, Bv, lr, li);
    const f32x4 nr = lr - splat4(1.f);
    cr = fma4(nr, lre, li * lim) * id; ci = fma4(li, lre, nml4(nr, lim)) * id;
    const f32x4 ne = splat4((float)(8 * fr)), nf = splat4((float)((fr & 7) + 1));
    cexp4(A2 * ne, Bv * ne, Er, Ei);
    f32x4 tr, ti;
    cexp4(A2 * nf, Bv * nf, tr, ti);
    Fr = fma4(cr, tr, nml4(ci, ti)); Fi = fma4(cr, ti, ci * tr);
    const float a1 = (fr & 2) ? ((fr & 1) ? A2.w : A2.z) : ((fr & 1) ? A2.y : A2.x), b1 = (fr & 2) ? ((fr & 1) ? Bv.w : Bv.z) : ((fr & 1) ? Bv.y : Bv.x);
    const int e0 = c - 1 - (fr >> 2);
    const f32x4 nl = {(float)(128 * max(e0, 0)), (float)(128 * max(e0 - 4, 0)), (float)(128 * max(e0 - 8, 0)), (float)(128 * max(e0 - 12, 0))};
    cexp4(splat4(a1) * nl, splat4(b1) * nl, br_, bi_);
    asm volatile("" : "+v"(lr), "+v"(li), "+v"(cr), "+v"(ci), "+v"(Er), "+v"(Ei), "+v"(Fr), "+v"(Fi), "+v"(br_), "+v"(bi_));
  }
  asm volatile("s_waitcnt vmcnt(0)" : "+v"(dv), "+v"(xv[0][0]), "+v"(xv[0][1]), "+v"(xv[1][0]), "+v"(xv[1][1]), "+v"(wv[0]), "+v"(wv[1]), "+v"(gv[0]), "+v"(gv[1])::"memory");
  {
    float sv[4];
#pragma unroll
    for (int r = 0; r < 2; ++r) {
      float sr = 0.f, sg_ = 0.f;
#pragma unroll
      for (int h = 0; h < 2; ++h)
#pragma unroll
        for (int e = 0; e < 4; ++e) {
          const unsigned xx = xv[r][h][e], ww = wv[h][e], gw = gv[h][e];
          sr = __builtin_amdgcn_fdot2_f32_bf16(__builtin_bit_cast(bf16x2_t, xx), __builtin_bit_cast(bf16x2_t, ww), sr, false);
          sg_ = __builtin_amdgcn_fdot2_f32_bf16(__builtin_bit_cast(bf16x2_t, xx), __builtin_bit_cast(bf16x2_t, gw), sg_, false);
        }
      sv[r] = sr; sv[2 + r] = sg_;
    }
    const bool b1 = lane & 1, b2 = lane & 2;
    const float k0 = (b1 ? sv[1] : sv[0]) + dpp1<0xb1>(b1 ? sv[0] : sv[1]), k1 = (b1 ? sv[3] : sv[2]) + dpp1<0xb1>(b1 ? sv[2] : sv[3]);
    float k2 = (b2 ? k1 : k0) + dpp1<0x4e>(b2 ? k0 : k1);
    k2 += dpp1<0x124>(k2);
    k2 += dpp1<0x128>(k2);
    k2 = sum16_32(k2);
    const unsigned vb = b2 ? f2bf(1.f / (1.f + __expf(-rbf(k2)))) : route_key(rbf(k2), 0) >> 16;
    const unsigned p01 = vb | ((unsigned)__builtin_amdgcn_update_dpp(0, (int)vb, 0xb1, 0xf, 0xf, true) << 16);
    asm volatile("ds_write_b32 %0, %1\n\ts_waitcnt lgkmcnt(0)" ::"v"((lane & 61) == 0 ? (unsigned)(L_RT + (lane * 2 + wid) * 4) : (unsigned)(L_JNK + lane * 4)), "v"(p01) : "memory");
  }
  __builtin_amdgcn_s_setprio(2);
  unsigned tagA = L_TAG;
  f32x4 acc[2][8];
  const unsigned ago = fr < 4 ? W_AGG + ((j * 16 + c) * 64 + wid * 16 + 4 * fq + fr) * 16u : 0xf0000000u;
  {
#pragma unroll
    for (int m = 0; m < 2; ++m)
#pragma unroll
      for (int n = 0; n < 8; ++n) acc[m][n] = (f32x4){0.f, 0.f, 0.f, 0.f};
    const int x0 = (fq ^ (fr & 7)) * 16, x1 = ((4 + fq) ^ (fr & 7)) * 16;
    unsigned pa0 = 16384 + (wid * 32 + fr) * 128 + x0, pa1 = 16384 + (wid * 32 + fr) * 128 + x1, pb0 = fr * 128 + x0, pb1 = fr * 128 + x1;
    unsigned sit, xad[4];
#pragma unroll
    for (int r = 0; r < 4; ++r) xad[r] = 98304 + (32 * wid + 4 * fq + r) * 128 + (((fr >> 1) ^ (4 * (fq & 1) + r)) * 16) + (fr & 1) * 8;
    bf16x8 fa[2][2], fb[8];
    asm volatile(ASM_GEMM1 : [c00] "+v"(acc[0][0]), [c01] "+v"(acc[0][1]), [c02] "+v"(acc[0][2]), [c03] "+v"(acc[0][3]), [c04] "+v"(acc[0][4]), [c05] "+v"(acc[0][5]), [c06] "+v"(acc[0][6]), [c07] "+v"(acc[0][7]), [c10] "+v"(acc[1][0]), [c11] "+v"(acc[1][1]), [c12] "+v"(acc[1][2]), [c13] "+v"(acc[1][3]), [c14] "+v"(acc[1][4]), [c15] "+v"(acc[1][5]), [c16] "+v"(acc[1][6]), [c17] "+v"(acc[1][7]), [a00] "=&v"(fa[0][0]), [a01] "=&v"(fa[0][1]), [a10] "=&v"(fa[1][0]), [a11] "=&v"(fa[1][1]), [b0] "=&v"(fb[0]), [b1] "=&v"(fb[1]), [b2] "=&v"(fb[2]), [b3] "=&v"(fb[3]), [b4] "=&v"(fb[4]), [b5] "=&v"(fb[5]), [b6] "=&v"(fb[6]), [b7] "=&v"(fb[7]), [pa0] "+v"(pa0), [pa1] "+v"(pa1), [pb0] "+v"(pb0), [pb1] "+v"(pb1), [it] "=&s"(sit), [tg] "+v"(tagA), [xe00] "=&v"(xe[0][0]), [xe01] "=&v"(xe[0][1]), [xe02] "=&v"(xe[0][2]), [xe03] "=&v"(xe[0][3]), [xe10] "=&v"(xe[1][0]), [xe11] "=&v"(xe[1][1]), [xe12] "=&v"(xe[1][2]), [xe13] "=&v"(xe[1][3]) : [xa0] "v"(xad[0]), [xa1] "v"(xad[1]), [xa2] "v"(xad[2]), [xa3] "v"(xad[3]) : "memory", "scc", "v250", "v251");
  }
  __builtin_amdgcn_s_setprio(3);
  const unsigned tagB = tagA + 0x40000000u;
  {
#pragma unroll
    for (int n = 1; n < 8; ++n) {
      const f32x4 pr = acc[0][n - 1], pi = acc[1][n - 1];
      acc[0][n] = fma4(lr, pr, fnm4(li, pi, acc[0][n]));
      acc[1][n] = fma4(lr, pi, fma4(li, pr, acc[1][n]));
    }
    const f32x4 wr = dpp4<0x140>(Er), wi = dpp4<0x140>(Ei);
    const f32x4 zr = fma4(wr, acc[0][7], nml4(wi, acc[1][7])), zi = fma4(wr, acc[1][7], wi * acc[0][7]);
    const bool o1 = fr & 1, o2 = fr & 2;
    const float r01 = (o1 ? zr.y : zr.x) + dpp1<0xb1>(o1 ? zr.x : zr.y), r23 = (o1 ? zr.w : zr.z) + dpp1<0xb1>(o1 ? zr.z : zr.w);
    const float i01 = (o1 ? zi.y : zi.x) + dpp1<0xb1>(o1 ? zi.x : zi.y), i23 = (o1 ? zi.w : zi.z) + dpp1<0xb1>(o1 ? zi.z : zi.w);
    float sr = (o2 ? r23 : r01) + dpp1<0x4e>(o2 ? r01 : r23), si = (o2 ? i23 : i01) + dpp1<0x4e>(o2 ? i01 : i23);
    sr += dpp1<0x124>(sr); si += dpp1<0x124>(si);
    sr += dpp1<0x128>(sr); si += dpp1<0x128>(si);
    u32x4 av = {__float_as_uint(sr), tagA, __float_as_uint(si), tagB};
    asm volatile("s_nop 1\n\tbuffer_store_dwordx4 %0, %1, %2, 0 offen sc1\n\ts_nop 7" : "+v"(av) : "v"(ago), "s"(rws) : "memory");
  }
  unsigned ao[4];
  u32x4 g[4];
#pragma unroll
  for (int i = 0; i < 4; ++i) {
    ao[i] = (fr >> 2) + 4 * i < c ? W_AGG + ((j * 16 + (fr >> 2) + 4 * i) * 64 + wid * 16 + 4 * fq + (fr & 3)) * 16u : 0xf0000000u;
    g[i] = (u32x4){0u, 0u, 0u, 0u};
  }
  if (POLLN < 0) {
    FRESH();
#pragma unroll
    for (int i = 0; i < 4; ++i)
      if (4 * i < c) g[i] = ld128c(rws, ao[i]);
    FRESH();
  }
  f32x4 Pr, Pi, qr[8], qi[8];
  {
    f32x4 yr = acc[0][7], yi = acc[1][7];
#define KS_STEP(S, CTRL) { const f32x4 mr = dpp4<0x150 + S>(Er), mi = dpp4<0x150 + S>(Ei); const f32x4 sr = dpp4<CTRL>(yr), si = dpp4<CTRL>(yi); \
      yr = fma4(mr, sr, fnm4(mi, si, yr)); yi = fma4(mr, si, fma4(mi, sr, yi)); }
    KS_STEP(1, 0x111) KS_STEP(2, 0x112) KS_STEP(4, 0x114) KS_STEP(8, 0x118)
    Pr = dpp4<0x111>(yr); Pi = dpp4<0x111>(yi);
    asm volatile("" : "+v"(Pr), "+v"(Pi));
#pragma unroll
    for (int n = 0; n < 8; ++n) {
      if (n == POLLN) {
        FRESH();
#pragma unroll
        for (int i = 0; i < 4; ++i)
          if (4 * i < c) g[i] = ld128c(rws, ao[i]);
        FRESH();
      }
      bcast8(n, Fr, qr[n]); bcast8(n, Fi, qi[n]);
      const f32x4 br = fma4(cr, acc[0][n], nml4(ci, acc[1][n])), bi = fnm4(cr, acc[1][n], nml4(ci, acc[0][n]));
      acc[0][n] = br; acc[1][n] = bi;
      asm volatile("" : "+v"(acc[0][n]), "+v"(acc[1][n]), "+v"(qr[n]), "+v"(qi[n]));
    }
  }
  f32x4 Xr = Pr, Xi = Pi;
  if (POLLN == 8) {
    FRESH();
#pragma unroll
    for (int i = 0; i < 4; ++i)
      if (4 * i < c) g[i] = ld128c(rws, ao[i]);
    FRESH();
  }
  u32x2 xd[2][4];
  lds_wait_ge(FL_G, 2u);
  {
    const f32x4 df = unpk4(dv) * splat4(0.5f);
#pragma unroll
    for (int m = 0; m < 2; ++m) {
      const f32x4 ge = __builtin_bit_cast(f32x4, lds_rd128(L_GT + ((2 * wid + m) * 16 + 4 * fq) * 4));
#pragma unroll
      for (int r = 0; r < 4; ++r) {
        const f32x4 xf = unpk4(xe[m][r]), p = xf * df, o = xf * splat4(rbf(1.f - ge[r]));
        xd[m][r] = (u32x2){pk2(p.x, p.y), pk2(p.z, p.w)};
        xe[m][r] = (u32x2){pk2(o.x, o.y), pk2(o.z, o.w)};
        asm volatile("" : "+v"(xd[m][r]), "+v"(xe[m][r]));
      }
    }
  }
  if (POLLN > 9) __builtin_amdgcn_s_sleep(POLLN > 9 ? POLLN - 9 : 0);
  if (c > 0) {
#pragma unroll 1
    for (int it = 0; it < SPIN_MAX; ++it) {
      bool ok = true;
#pragma unroll
      for (int i = 0; i < 4; ++i) ok = ok && (ao[i] == 0xf0000000u || (g[i].y == tagA && g[i].w == tagB));
      if (__all(ok) || WIF == 6) break;
      FRESH();
#pragma unroll
      for (int i = 0; i < 4; ++i)
        if (4 * i < c) g[i] = ld128c(rws, ao[i]);
    }
    const f32x4 sx = {__uint_as_float(g[0].x), __uint_as_float(g[1].x), __uint_as_float(g[2].x), __uint_as_float(g[3].x)};
    const f32x4 sy = {__uint_as_float(g[0].z), __uint_as_float(g[1].z), __uint_as_float(g[2].z), __uint_as_float(g[3].z)};
    const f32x4 tx = fma4(br_, sx, nml4(bi_, sy)), ty = fma4(br_, sy, bi_ * sx);
    float gx = (tx.x + tx.z) + (tx.y + tx.w), gy = (ty.x + ty.z) + (ty.y + ty.w);
    gx += dpp1<0x124>(gx); gy += dpp1<0x124>(gy);
    gx += dpp1<0x128>(gx); gy += dpp1<0x128>(gy);
    const f32x4 Gr = {dpp1<0x00>(gx), dpp1<0x55>(gx), dpp1<0xaa>(gx), dpp1<0xff>(gx)}, Gi = {dpp1<0x00>(gy), dpp1<0x55>(gy), dpp1<0xaa>(gy), dpp1<0xff>(gy)};
    Xr = fma4(Er, Gr, fnm4(Ei, Gi, Pr)); Xi = fma4(Er, Gi, fma4(Ei, Gr, Pi));
  }
  relane();
  const unsigned jnk = L_JNK + lane * 4;
  const unsigned ho = WIF == 3 ? 0xf0000000u : W_H + (c * 64 + 4 * ((j - crot) & 15) + wid) * 8192 + lane * 16;
  u32x4 hv[8];
  __builtin_amdgcn_sched_barrier(0);
#define HST(t_) asm volatile("s_nop 1\n\tbuffer_store_dwordx4 %0, %1, %2, %3 offen offset:%4 sc1" : "+v"(hv[t_]) : "v"(ho), "s"(rws), "s"(((t_) >> 2) * 4096), "n"(((t_) & 3) * 1024) : "memory")
#pragma unroll
  for (int n = 0; n < 8; ++n) {
    const f32x4 hr = fma4(qr[n], Xr, fnm4(qi[n], Xi, acc[0][n])), hi = fnm4(qr[n], Xi, fnm4(qi[n], Xr, acc[1][n]));
    const auto p = __builtin_amdgcn_permlane16_swap(pk2(hr.x, hr.y), pk2(hi.x, hi.y), false, false);
    const auto q = __builtin_amdgcn_permlane16_swap(pk2(hr.z, hr.w), pk2(hi.z, hi.w), false, false);
    hv[n] = (u32x4){NS ? min(p[0], 0xfffffffeu) : p[0], q[0], NS ? min(p[1], 0xfffffffeu) : p[1], q[1]};
    if (NSI == 8) HST(n); else if (NSI == 4 ? (n & 1) : n < NSI) HST(NSI == 4 ? n >> 1 : n);
    __builtin_amdgcn_sched_barrier(0);
    if (!DSL && (n >> 1) != wid) asm volatile("ds_write_b128 %0, %1" ::"v"(L_XH + (((n >> 1) * 3 + wid - (wid > (n >> 1) ? 1 : 0)) * 2 + (n & 1)) * 1024 + lane * 16), "v"(hv[n]) : "memory");
  }
  if (DSL) {
#pragma unroll
    for (int w = 0; w < 4; ++w)
      if (w != wid) asm volatile("ds_write_b128 %0, %1\n\tds_write_b128 %0, %2 offset:1024" ::"v"(L_XH + (w * 3 + wid - (wid > w ? 1 : 0)) * 2048 + lane * 16), "v"(hv[2 * w]), "v"(hv[2 * w + 1]) : "memory");
  }
  if (lane == 0) lds_wr32(FL_HS + wid * 4, 1u);
  const rsrc_t rh = mk(ws + W_H + c * 524288, 524288);
  const unsigned vo = wid * 2048 + lane * 16;
  u32x32 hw0, hw1;
  unsigned st[12], pc[4];
  f32x4 ac2[2][4];
#define ACK() do { asm volatile("s_waitcnt vmcnt(0)" : "+v"(hv[0]), "+v"(hv[1]), "+v"(hv[2]), "+v"(hv[3]), "+v"(hv[4]), "+v"(hv[5]), "+v"(hv[6]), "+v"(hv[7])::"memory"); \
    asm volatile("buffer_store_dwordx2 %0, %1, %2, 0 offen sc1" ::"v"((u32x2){tagA, tagB}), "v"(lane == 0 ? W_HF + (c * 16 + pown) * FSB + wid * FSW : 0xf0000000u), "s"(rws) : "memory"); \
    lds_wr32(lane == 0 ? (unsigned)(FL_FREE + wid * 4) : jnk, 9u); \
    if (NS) { if (RQD) __builtin_amdgcn_s_sleep(RQD); asm volatile(ASM_REQ : "=&{v[184:215]}"(hw0), "=&{v[216:247]}"(hw1), [hb0] "=&s"(st[0]), [hb1] "=&s"(st[1]), [hb2] "=&s"(st[2]), [hb3] "=&s"(st[3]) : [vo] "v"(vo), [own] "s"(pown), [srd] "s"(rh) : "memory", "scc"); } } while (0)
#pragma unroll
  for (int sg = 0; sg < 4; ++sg) pc[sg] = (fq & 1) * 8192 + fr * 128 + (((2 * sg + (fq >> 1)) ^ ((fr >> 1) & 7)) * 16);
  {
    lds_wait_ge(FL_C, 1u);
    u32x4 k[4][4], h[4][2];
#pragma unroll
    for (int i = 0; i < 4; ++i) {
      const int sw = i == 0 ? wid : i - (i <= wid ? 1 : 0);
      asm volatile("ds_read_b128 %0, %4\n\tds_read_b128 %1, %4 offset:2048\n\tds_read_b128 %2, %4 offset:4096\n\tds_read_b128 %3, %4 offset:6144"
                   : "=&v"(k[i][0]), "=&v"(k[i][1]), "=&v"(k[i][2]), "=&v"(k[i][3]) : "v"(pc[0] ^ (sw * 32)) : "memory");
    }
#pragma unroll
    for (int m = 0; m < 2; ++m)
#pragma unroll
      for (int r = 0; r < 4; ++r) {
        const f32x4 t = unpk4(xd[m][r]);
#pragma unroll
        for (int n = 0; n < 4; ++n) ac2[m][n][r] = t[n];
      }
    h[0][0] = hv[0]; h[0][1] = hv[1];
    if (wid == 1) { h[0][0] = hv[2]; h[0][1] = hv[3]; }
    if (wid == 2) { h[0][0] = hv[4]; h[0][1] = hv[5]; }
    if (wid == 3) { h[0][0] = hv[6]; h[0][1] = hv[7]; }
    asm volatile("" : "+v"(ac2[0][0]), "+v"(ac2[0][1]), "+v"(ac2[0][2]), "+v"(ac2[0][3]), "+v"(ac2[1][0]), "+v"(ac2[1][1]), "+v"(ac2[1][2]), "+v"(ac2[1][3]), "+v"(h[0][0]), "+v"(h[0][1]));
    if (NSI < 8) HST(NSI);
    if (MGP == 1) ACK();
    lds_wait_min4(FL_HS, 1u);
    asm volatile("ds_read_b128 %0, %6\n\tds_read_b128 %1, %6 offset:1024\n\tds_read_b128 %2, %6 offset:2048\n\tds_read_b128 %3, %6 offset:3072\n\tds_read_b128 %4, %6 offset:4096\n\tds_read_b128 %5, %6 offset:5120"
                 : "=&v"(h[1][0]), "=&v"(h[1][1]), "=&v"(h[2][0]), "=&v"(h[2][1]), "=&v"(h[3][0]), "=&v"(h[3][1]) : "v"(L_XH + wid * 6144 + lane * 16) : "memory");
    lds_wr32(lane == 0 ? (unsigned)(FL_CFREE + wid * 4) : jnk, 1u);
#pragma unroll
    for (int i = 0; i < 4; ++i) {
      if (i == 1) asm volatile("s_waitcnt lgkmcnt(0)" : "+v"(h[1][0]), "+v"(h[1][1]), "+v"(h[2][0]), "+v"(h[2][1]), "+v"(h[3][0]), "+v"(h[3][1])::"memory");
      asm volatile("" : "+v"(k[i][0]), "+v"(k[i][1]), "+v"(k[i][2]), "+v"(k[i][3]));
#pragma unroll
      for (int n = 0; n < 4; ++n) {
        ac2[0][n] = __builtin_amdgcn_mfma_f32_16x16x32_bf16(__builtin_bit_cast(bf16x8, h[i][0]), __builtin_bit_cast(bf16x8, k[i][n]), ac2[0][n], 0, 0, 0);
        ac2[1][n] = __builtin_amdgcn_mfma_f32_16x16x32_bf16(__builtin_bit_cast(bf16x8, h[i][1]), __builtin_bit_cast(bf16x8, k[i][n]), ac2[1][n], 0, 0, 0);
      }
      asm volatile("" : "+v"(ac2[0][0]), "+v"(ac2[0][1]), "+v"(ac2[0][2]), "+v"(ac2[0][3]), "+v"(ac2[1][0]), "+v"(ac2[1][1]), "+v"(ac2[1][2]), "+v"(ac2[1][3])::"memory");
      if (NSI + 1 + i < 8) HST(NSI + 1 + i < 8 ? NSI + 1 + i : 0);
    }
    static_assert(NSI >= 3 && (MGP == 0 || NSI >= 7), "all stores");
  }
  if (MGP == 0) ACK();
  f32x4 ov[2][4];
  float g2[2][4];
#pragma unroll
  for (int m = 0; m < 2; ++m) {
    const f32x4 ge = lds4(smem, L_GT + ((2 * wid + m) * 16 + 4 * fq) * 4);
#pragma unroll
    for (int r = 0; r < 4; ++r) {
      ov[m][r] = unpk4(xe[m][r]);
      g2[m][r] = ge[r] + ge[r];
      asm volatile("" : "+v"(ov[m][r]), "+v"(g2[m][r]));
    }
  }
  unsigned ra[2][4];
  lds_wait_ge(FL_SEL, 1u);
  const unsigned routed = __builtin_amdgcn_readfirstlane(lds_rd32(FL_SEL));
#pragma unroll
  for (int m = 0; m < 2; ++m) {
    const u32x4 me = lds_rd128(L_MAP + ((2 * wid + m) * 16 + 4 * fq) * 4);
#pragma unroll
    for (int r = 0; r < 4; ++r) { ra[m][r] = me[r] + fr * 16; asm volatile("" : "+v"(ra[m][r])); }
  }
  const rsrc_t ro = mk(out, 2048 * 1024 * 4);
  unsigned oo = (c * 128 + 32 * fq + 2 * wid) * 4096 + (j * 64 + 4 * fr) * 4;
  if (WIF == 1) oo = 0xf0000000u;
  if (WIF == 2 && (fq & 1)) oo = 0xf0000000u;
  asm volatile("" : "+v"(oo) : "s"(ro));
  __builtin_amdgcn_s_setprio(MPRIO2);

  {
    const unsigned fad = FL_C, fwd = lane == 0 ? (unsigned)(FL_CFREE + wid * 4) : jnk;
    bf16x8 kb[2][4];
    unsigned fv = 1;
#pragma unroll
    for (int sg = 0; sg < 4; ++sg) pc[sg] += 16384;
    asm volatile(ASM_GEMM2 : [c00] "+v"(ac2[0][0]), [c01] "+v"(ac2[0][1]), [c02] "+v"(ac2[0][2]), [c03] "+v"(ac2[0][3]), [c10] "+v"(ac2[1][0]), [c11] "+v"(ac2[1][1]), [c12] "+v"(ac2[1][2]), [c13] "+v"(ac2[1][3]), "=&{v[184:215]}"(hw0), "=&{v[216:247]}"(hw1), [k00] "=&v"(kb[0][0]), [k01] "=&v"(kb[0][1]), [k02] "=&v"(kb[0][2]), [k03] "=&v"(kb[0][3]), [k10] "=&v"(kb[1][0]), [k11] "=&v"(kb[1][1]), [k12] "=&v"(kb[1][2]), [k13] "=&v"(kb[1][3]), [pc0] "+v"(pc[0]), [pc1] "+v"(pc[1]), [pc2] "+v"(pc[2]), [pc3] "+v"(pc[3]), [fv] "+v"(fv), [s] "=&s"(st[0]), [s2] "=&s"(st[1]), [to] "=&s"(st[2]), [hn] "=&s"(st[3]), [cn] "=&s"(st[4]), [dl] "=&s"(st[5]), [nf] "=&s"(st[6]), [hr] "=&s"(st[7]), [hb0] "=&s"(st[8]), [hb1] "=&s"(st[9]), [hb2] "=&s"(st[10]), [hb3] "=&s"(st[11]) : [vo] "v"(vo), [own] "s"(pown), [srd] "s"(rh), [fa] "v"(fad), [fw] "v"(fwd), [d16] "s"(0x4000), [wr] "s"(0x500) : "memory", "scc", "vcc", "v250", "v251", "v252", "v253", "v254", "v255");
  }
  if (routed > 1u) {
#pragma unroll 1
    for (int it = 0; it < SPIN_MAX * 4; ++it) {
      u32x2 f;
      f32x4 d[8];
      asm volatile("ds_read_b64 %8, %17\n\tds_read_b128 %0, %9\n\tds_read_b128 %1, %10\n\tds_read_b128 %2, %11\n\tds_read_b128 %3, %12\n\t"
                   "ds_read_b128 %4, %13\n\tds_read_b128 %5, %14\n\tds_read_b128 %6, %15\n\tds_read_b128 %7, %16\n\ts_waitcnt lgkmcnt(0)"
                   : "=&v"(d[0]), "=&v"(d[1]), "=&v"(d[2]), "=&v"(d[3]), "=&v"(d[4]), "=&v"(d[5]), "=&v"(d[6]), "=&v"(d[7]), "=&v"(f)
                   : "v"(ra[0][0]), "v"(ra[0][1]), "v"(ra[0][2]), "v"(ra[0][3]), "v"(ra[1][0]), "v"(ra[1][1]), "v"(ra[1][2]), "v"(ra[1][3]), "v"(FL_XD) : "memory");
      if (__builtin_amdgcn_readfirstlane(f.x & f.y)) {
#pragma unroll
        for (int e = 0; e < 8; ++e) ov[e >> 2][e & 3] += d[e];
        break;
      }
    }
  }
#pragma unroll
  for (int r = 0; r < 4; ++r)
#pragma unroll
    for (int m = 0; m < 2; ++m) {
#pragma unroll
      for (int n = 0; n < 4; ++n) ov[m][r][n] = fma1(g2[m][r], ac2[m][n][r], ov[m][r][n]);
      asm volatile("s_nop 1\n\tbuffer_store_dwordx4 %0, %1, %2, %3 offen" OST : "+v"(ov[m][r]) : "v"(oo), "s"(ro), "s"(r * 32768 + m * 4096) : "memory");
      __builtin_amdgcn_sched_barrier(0);
    }
  asm volatile("s_nop 7\n\ts_nop 7" ::"v"(ov[0][0]), "v"(ov[0][1]), "v"(ov[0][2]), "v"(ov[0][3]), "v"(ov[1][0]), "v"(ov[1][1]), "v"(ov[1][2]), "v"(ov[1][3]));
  asm volatile("Lk_m:");
#endif
}

extern "C" void kernel_launch(void* const* d_in, const int* in_sizes, int n_in, void* d_out, int out_size, void* d_ws, size_t ws_size,
                              hipStream_t stream) {
  (void)hipFuncSetAttribute((const void*)fused, hipFuncAttributeMaxDynamicSharedMemorySize, LDS_BYTES);
  fused<<<256, 512, LDS_BYTES, stream>>>((const u16*)d_in[0], (const u16*)d_in[1], (const u16*)d_in[2], (const u16*)d_in[3], (const u16*)d_in[4],
                                         (const u16*)d_in[5], (const u16*)d_in[6], (const u16*)d_in[7], (const u16*)d_in[8], (const u16*)d_in[10],
                                         (const u16*)d_in[11], (const u16*)d_in[12], (const u16*)d_in[13], (const u16*)d_in[14], (char*)d_ws + WS_BASE(ws_size),
                                         (float*)d_out);
}
```

```cpp
#include <hip/hip_runtime.h>
#include <hip/hip_bf16.h>
#include <stdint.h>

typedef __attribute__((ext_vector_type(8))) short bf16x8;
typedef __attribute__((ext_vector_type(4))) float f32x4;
typedef __attribute__((ext_vector_type(2))) float f32x2;
typedef __attribute__((ext_vector_type(4))) unsigned u32x4;
typedef __attribute__((ext_vector_type(2))) unsigned u32x2;
typedef __attribute__((ext_vector_type(32))) unsigned u32x32;
typedef unsigned short u16;
typedef unsigned long long u64;
typedef __attribute__((ext_vector_type(8))) unsigned long long u64x8;
#define LAS __attribute__((address_space(3)))
#define DEV __device__ __forceinline__

#define ASM_GEMM1 \
  "s_mov_b32 %[it], 0\n\t" \
  "s_barrier\n\t" \
  "ds_read_b128 %[a00], %[pa0] offset:0\n\t" \
  "ds_read_b128 %[a01], %[pa0] offset:2048\n\t" \
  "ds_read_b128 %[b0], %[pb0] offset:0\n\t" \
  "ds_read_b128 %[b1], %[pb0] offset:2048\n\t" \
  "ds_read_b128 %[b2], %[pb0] offset:4096\n\t" \
  "ds_read_b128 %[b3], %[pb0] offset:6144\n\t" \
  "ds_read_b128 %[b4], %[pb0] offset:8192\n\t" \
  "ds_read_b128 %[b5], %[pb0] offset:10240\n\t" \
  "ds_read_b128 %[b6], %[pb0] offset:12288\n\t" \
  "ds_read_b128 %[b7], %[pb0] offset:14336\n\t" \
  "Lg1_%=:\n\t" \
  "ds_read_b128 %[a10], %[pa1] offset:0\n\t" \
  "ds_read_b128 %[a11], %[pa1] offset:2048\n\t" \
  "s_waitcnt lgkmcnt(8)\n\t" \
  "v_mfma_f32_16x16x32_bf16 %[c00], %[a00], %[b0], %[c00]\n\t" \
  "v_mfma_f32_16x16x32_bf16 %[c10], %[a01], %[b0], %[c10]\n\t" \
  "ds_read_b128 %[b0], %[pb1] offset:0\n\t" \
  "v_mfma_f32_16x16x32_bf16 %[c01], %[a00], %[b1], %[c01]\n\t" \
  "v_mfma_f32_16x16x32_bf16 %[c11], %[a01], %[b1], %[c11]\n\t" \
  "ds_read_b128 %[b1], %[pb1] offset:2048\n\t" \
  "s_waitcnt lgkmcnt(8)\n\t" \
  "v_mfma_f32_16x16x32_bf16 %[c02], %[a00], %[b2], %[c02]\n\t" \
  "v_mfma_f32_16x16x32_bf16 %[c12], %[a01], %[b2], %[c12]\n\t" \
  "ds_read_b128 %[b2], %[pb1] offset:4096\n\t" \
  "v_mfma_f32_16x16x32_bf16 %[c03], %[a00], %[b3], %[c03]\n\t" \
  "v_mfma_f32_16x16x32_bf16 %[c13], %[a01], %[b3], %[c13]\n\t" \
  "ds_read_b128 %[b3], %[pb1] offset:6144\n\t" \
  "s_waitcnt lgkmcnt(8)\n\t" \
  "v_mfma_f32_16x16x32_bf16 %[c04], %[a00], %[b4], %[c04]\n\t" \
  "v_mfma_f32_16x16x32_bf16 %[c14], %[a01], %[b4], %[c14]\n\t" \
  "ds_read_b128 %[b4], %[pb1] offset:8192\n\t" \
  "v_mfma_f32_16x16x32_bf16 %[c05], %[a00], %[b5], %[c05]\n\t" \
  "v_mfma_f32_16x16x32_bf16 %[c15], %[a01], %[b5], %[c15]\n\t" \
  "ds_read_b128 %[b5], %[pb1] offset:10240\n\t" \
  "s_waitcnt lgkmcnt(8)\n\t" \
  "v_mfma_f32_16x16x32_bf16 %[c06], %[a00], %[b6], %[c06]\n\t" \
  "v_mfma_f32_16x16x32_bf16 %[c16], %[a01], %[b6], %[c16]\n\t" \
  "ds_read_b128 %[b6], %[pb1] offset:12288\n\t" \
  "v_mfma_f32_16x16x32_bf16 %[c07], %[a00], %[b7], %[c07]\n\t" \
  "v_mfma_f32_16x16x32_bf16 %[c17], %[a01], %[b7], %[c17]\n\t" \
  "ds_read_b128 %[b7], %[pb1] offset:14336\n\t" \
  "s_waitcnt lgkmcnt(6)\n\t" \
  "v_mfma_f32_16x16x32_bf16 %[c00], %[a10], %[b0], %[c00]\n\t" \
  "v_mfma_f32_16x16x32_bf16 %[c10], %[a11], %[b0], %[c10]\n\t" \
  "v_mfma_f32_16x16x32_bf16 %[c01], %[a10], %[b1], %[c01]\n\t" \
  "v_mfma_f32_16x16x32_bf16 %[c11], %[a11], %[b1], %[c11]\n\t" \
  "s_waitcnt lgkmcnt(4)\n\t" \
  "v_mfma_f32_16x16x32_bf16 %[c02], %[a10], %[b2], %[c02]\n\t" \
  "v_mfma_f32_16x16x32_bf16 %[c12], %[a11], %[b2], %[c12]\n\t" \
  "s_waitcnt lgkmcnt(0)\n\t" \
  "s_barrier\n\t" \
  "ds_read_b128 %[a00], %[pa0] offset:32768\n\t" \
  "ds_read_b128 %[a01], %[pa0] offset:34816\n\t" \
  "v_mfma_f32_16x16x32_bf16 %[c03], %[a10], %[b3], %[c03]\n\t" \
  "v_mfma_f32_16x16x32_bf16 %[c13], %[a11], %[b3], %[c13]\n\t" \
  "ds_read_b128 %[b0], %[pb0] offset:32768\n\t" \
  "ds_read_b128 %[b1], %[pb0] offset:34816\n\t" \
  "v_mfma_f32_16x16x32_bf16 %[c04], %[a10], %[b4], %[c04]\n\t" \
  "v_mfma_f32_16x16x32_bf16 %[c14], %[a11], %[b4], %[c14]\n\t" \
  "ds_read_b128 %[b2], %[pb0] offset:36864\n\t" \
  "ds_read_b128 %[b3], %[pb0] offset:38912\n\t" \
  "v_mfma_f32_16x16x32_bf16 %[c05], %[a10], %[b5], %[c05]\n\t" \
  "v_mfma_f32_16x16x32_bf16 %[c15], %[a11], %[b5], %[c15]\n\t" \
  "ds_read_b128 %[b4], %[pb0] offset:40960\n\t" \
  "ds_read_b128 %[b5], %[pb0] offset:43008\n\t" \
  "v_mfma_f32_16x16x32_bf16 %[c06], %[a10], %[b6], %[c06]\n\t" \
  "v_mfma_f32_16x16x32_bf16 %[c16], %[a11], %[b6], %[c16]\n\t" \
  "ds_read_b128 %[b6], %[pb0] offset:45056\n\t" \
  "v_mfma_f32_16x16x32_bf16 %[c07], %[a10], %[b7], %[c07]\n\t" \
  "v_mfma_f32_16x16x32_bf16 %[c17], %[a11], %[b7], %[c17]\n\t" \
  "ds_read_b128 %[b7], %[pb0] offset:47104\n\t" \
  "ds_read_b128 %[a10], %[pa1] offset:32768\n\t" \
  "ds_read_b128 %[a11], %[pa1] offset:34816\n\t" \
  "s_waitcnt lgkmcnt(8)\n\t" \
  "v_mfma_f32_16x16x32_bf16 %[c00], %[a00], %[b0], %[c00]\n\t" \
  "v_mfma_f32_16x16x32_bf16 %[c10], %[a01], %[b0], %[c10]\n\t" \
  "ds_read_b128 %[b0], %[pb1] offset:32768\n\t" \
  "v_mfma_f32_16x16x32_bf16 %[c01], %[a00], %[b1], %[c01]\n\t" \
  "v_mfma_f32_16x16x32_bf16 %[c11], %[a01], %[b1], %[c11]\n\t" \
  "ds_read_b128 %[b1], %[pb1] offset:34816\n\t" \
  "s_waitcnt lgkmcnt(8)\n\t" \
  "v_mfma_f32_16x16x32_bf16 %[c02], %[a00], %[b2], %[c02]\n\t" \
  "v_mfma_f32_16x16x32_bf16 %[c12], %[a01], %[b2], %[c12]\n\t" \
  "ds_read_b128 %[b2], %[pb1] offset:36864\n\t" \
  "v_mfma_f32_16x16x32_bf16 %[c03], %[a00], %[b3], %[c03]\n\t" \
  "v_mfma_f32_16x16x32_bf16 %[c13], %[a01], %[b3], %[c13]\n\t" \
  "ds_read_b128 %[b3], %[pb1] offset:38912\n\t" \
  "v_xor_b32 %[pa0], 0x10000, %[pa0]\n\t" \
  "s_waitcnt lgkmcnt(8)\n\t" \
  "v_mfma_f32_16x16x32_bf16 %[c04], %[a00], %[b4], %[c04]\n\t" \
  "v_mfma_f32_16x16x32_bf16 %[c14], %[a01], %[b4], %[c14]\n\t" \
  "ds_read_b128 %[b4], %[pb1] offset:40960\n\t" \
  "v_xor_b32 %[pb0], 0x10000, %[pb0]\n\t" \
  "v_mfma_f32_16x16x32_bf16 %[c05], %[a00], %[b5], %[c05]\n\t" \
  "v_mfma_f32_16x16x32_bf16 %[c15], %[a01], %[b5], %[c15]\n\t" \
  "ds_read_b128 %[b5], %[pb1] offset:43008\n\t" \
  "s_waitcnt lgkmcnt(8)\n\t" \
  "v_mfma_f32_16x16x32_bf16 %[c06], %[a00], %[b6], %[c06]\n\t" \
  "v_mfma_f32_16x16x32_bf16 %[c16], %[a01], %[b6], %[c16]\n\t" \
  "ds_read_b128 %[b6], %[pb1] offset:45056\n\t" \
  "s_cmp_eq_u32 %[it], 7\n\t" \
  "v_mfma_f32_16x16x32_bf16 %[c07], %[a00], %[b7], %[c07]\n\t" \
  "v_mfma_f32_16x16x32_bf16 %[c17], %[a01], %[b7], %[c17]\n\t" \
  "ds_read_b128 %[b7], %[pb1] offset:47104\n\t" \
  "s_cbranch_scc1 Lxe_%=\n\t" \
  "Lxb_%=:\n\t" \
  "s_waitcnt lgkmcnt(6)\n\t" \
  "v_mfma_f32_16x16x32_bf16 %[c00], %[a10], %[b0], %[c00]\n\t" \
  "v_mfma_f32_16x16x32_bf16 %[c10], %[a11], %[b0], %[c10]\n\t" \
  "v_mfma_f32_16x16x32_bf16 %[c01], %[a10], %[b1], %[c01]\n\t" \
  "v_mfma_f32_16x16x32_bf16 %[c11], %[a11], %[b1], %[c11]\n\t" \
  "v_xor_b32 %[pa1], 0x10000, %[pa1]\n\t" \
  "s_waitcnt lgkmcnt(4)\n\t" \
  "v_mfma_f32_16x16x32_bf16 %[c02], %[a10], %[b2], %[c02]\n\t" \
  "v_mfma_f32_16x16x32_bf16 %[c12], %[a11], %[b2], %[c12]\n\t" \
  "v_xor_b32 %[pb1], 0x10000, %[pb1]\n\t" \
  "s_waitcnt lgkmcnt(0)\n\t" \
  "s_barrier\n\t" \
  "s_cbranch_scc1 Lt1_%=\n\t" \
  "ds_read_b128 %[a00], %[pa0] offset:0\n\t" \
  "ds_read_b128 %[a01], %[pa0] offset:2048\n\t" \
  "v_mfma_f32_16x16x32_bf16 %[c03], %[a10], %[b3], %[c03]\n\t" \
  "v_mfma_f32_16x16x32_bf16 %[c13], %[a11], %[b3], %[c13]\n\t" \
  "ds_read_b128 %[b0], %[pb0] offset:0\n\t" \
  "ds_read_b128 %[b1], %[pb0] offset:2048\n\t" \
  "v_mfma_f32_16x16x32_bf16 %[c04], %[a10], %[b4], %[c04]\n\t" \
  "v_mfma_f32_16x16x32_bf16 %[c14], %[a11], %[b4], %[c14]\n\t" \
  "ds_read_b128 %[b2], %[pb0] offset:4096\n\t" \
  "ds_read_b128 %[b3], %[pb0] offset:6144\n\t" \
  "v_mfma_f32_16x16x32_bf16 %[c05], %[a10], %[b5], %[c05]\n\t" \
  "v_mfma_f32_16x16x32_bf16 %[c15], %[a11], %[b5], %[c15]\n\t" \
  "ds_read_b128 %[b4], %[pb0] offset:8192\n\t" \
  "ds_read_b128 %[b5], %[pb0] offset:10240\n\t" \
  "v_mfma_f32_16x16x32_bf16 %[c06], %[a10], %[b6], %[c06]\n\t" \
  "v_mfma_f32_16x16x32_bf16 %[c16], %[a11], %[b6], %[c16]\n\t" \
  "ds_read_b128 %[b6], %[pb0] offset:12288\n\t" \
  "s_add_u32 %[it], %[it], 1\n\t" \
  "v_mfma_f32_16x16x32_bf16 %[c07], %[a10], %[b7], %[c07]\n\t" \
  "v_mfma_f32_16x16x32_bf16 %[c17], %[a11], %[b7], %[c17]\n\t" \
  "ds_read_b128 %[b7], %[pb0] offset:14336\n\t" \
  "s_branch Lg1_%=\n\t" \
  "Lxe_%=:\n\t" \
  "ds_read_b64 %[xe00], %[xa0] offset:0\n\t" \
  "ds_read_b64 %[xe01], %[xa1] offset:0\n\t" \
  "ds_read_b64 %[xe02], %[xa2] offset:0\n\t" \
  "ds_read_b64 %[xe03], %[xa3] offset:0\n\t" \
  "ds_read_b64 %[xe10], %[xa0] offset:2048\n\t" \
  "ds_read_b64 %[xe11], %[xa1] offset:2048\n\t" \
  "ds_read_b64 %[xe12], %[xa2] offset:2048\n\t" \
  "ds_read_b64 %[xe13], %[xa3] offset:2048\n\t" \
  "ds_read_b32 %[tg], %[tg]\n\t" \
  "s_branch Lxb_%=\n\t" \
  "Lt1_%=:\n\t" \
  "v_mfma_f32_16x16x32_bf16 %[c03], %[a10], %[b3], %[c03]\n\t" \
  "v_mfma_f32_16x16x32_bf16 %[c13], %[a11], %[b3], %[c13]\n\t" \
  "v_mfma_f32_16x16x32_bf16 %[c04], %[a10], %[b4], %[c04]\n\t" \
  "v_mfma_f32_16x16x32_bf16 %[c14], %[a11], %[b4], %[c14]\n\t" \
  "v_mfma_f32_16x16x32_bf16 %[c05], %[a10], %[b5], %[c05]\n\t" \
  "v_mfma_f32_16x16x32_bf16 %[c15], %[a11], %[b5], %[c15]\n\t" \
  "v_mfma_f32_16x16x32_bf16 %[c06], %[a10], %[b6], %[c06]\n\t" \
  "v_mfma_f32_16x16x32_bf16 %[c16], %[a11], %[b6], %[c16]\n\t" \
  "v_mfma_f32_16x16x32_bf16 %[c07], %[a10], %[b7], %[c07]\n\t" \
  "v_mfma_f32_16x16x32_bf16 %[c17], %[a11], %[b7], %[c17]\n\t" \
  "s_nop 7\n\t"
#define ASM_GEMM2 \
  "s_nop 4\n\t" \
  "s_mov_b32 %[to], 0x40000\n\t" \
  "s_mov_b32 %[cn], 1\n\t" \
  "Lc0_%=:\n\t" \
  "ds_read_b128 v[252:255], %[fa]\n\t" \
  "s_cmp_eq_u32 %[to], 0\n\t" \
  "s_cbranch_scc1 Lc0_%=x\n\t" \
  "s_sub_u32 %[to], %[to], 1\n\t" \
  "s_waitcnt lgkmcnt(0)\n\t" \
  "v_readfirstlane_b32 %[s], v253\n\t" \
  "s_cmp_ge_u32 %[s], 2\n\t" \
  "s_cbranch_scc0 Lc0_%=\n\t" \
  "Lc0_%=x:\n\t" \
  "ds_read_b128 %[k00], %[pc0] offset:0\n\t" \
  "ds_read_b128 %[k01], %[pc0] offset:2048\n\t" \
  "ds_read_b128 %[k02], %[pc0] offset:4096\n\t" \
  "ds_read_b128 %[k03], %[pc0] offset:6144\n\t" \
  "ds_read_b128 %[k10], %[pc1] offset:0\n\t" \
  "ds_read_b128 %[k11], %[pc1] offset:2048\n\t" \
  "ds_read_b128 %[k12], %[pc1] offset:4096\n\t" \
  "ds_read_b128 %[k13], %[pc1] offset:6144\n\t" \
  "s_mov_b32 %[hn], 3\n\t" \
  "s_cmp_gt_u32 1, %[own]\n\t" \
  "s_addc_u32 %[hb0], 1, -1\n\t" \
  "s_lshl_b32 %[hb0], %[hb0], 15\n\t" \
  "s_or_b32 %[hb1], %[hb0], 0x2000\n\t" \
  "s_or_b32 %[hb2], %[hb0], 0x4000\n\t" \
  "s_or_b32 %[hb3], %[hb0], 0x6000\n\t" \
  "Lh1_%=:\n\t" \
  "ds_read_b128 v[252:255], %[fa]\n\t" \
  "s_cmp_eq_u32 %[to], 0\n\t" \
  "s_cbranch_scc1 Lh1_%=x\n\t" \
  "s_sub_u32 %[to], %[to], 1\n\t" \
  "s_waitcnt lgkmcnt(0)\n\t" \
  "v_readfirstlane_b32 %[s], v254\n\t" \
  "s_cmp_ge_u32 %[s], 1\n\t" \
  "s_cbranch_scc0 Lh1_%=\n\t" \
  "Lh1_%=x:\n\t" \
  "buffer_load_dwordx4 v[216:219], %[vo], %[srd], %[hb0] offen offset:0 sc1\n\t" \
  "buffer_load_dwordx4 v[220:223], %[vo], %[srd], %[hb0] offen offset:1024 sc1\n\t" \
  "buffer_load_dwordx4 v[224:227], %[vo], %[srd], %[hb1] offen offset:0 sc1\n\t" \
  "buffer_load_dwordx4 v[228:231], %[vo], %[srd], %[hb1] offen offset:1024 sc1\n\t" \
  "buffer_load_dwordx4 v[232:235], %[vo], %[srd], %[hb2] offen offset:0 sc1\n\t" \
  "buffer_load_dwordx4 v[236:239], %[vo], %[srd], %[hb2] offen offset:1024 sc1\n\t" \
  "buffer_load_dwordx4 v[240:243], %[vo], %[srd], %[hb3] offen offset:0 sc1\n\t" \
  "buffer_load_dwordx4 v[244:247], %[vo], %[srd], %[hb3] offen offset:1024 sc1\n\t" \
  "s_cmp_gt_u32 2, %[own]\n\t" \
  "s_addc_u32 %[hb0], 2, -1\n\t" \
  "s_lshl_b32 %[hb0], %[hb0], 15\n\t" \
  "s_or_b32 %[hb1], %[hb0], 0x2000\n\t" \
  "s_or_b32 %[hb2], %[hb0], 0x4000\n\t" \
  "s_or_b32 %[hb3], %[hb0], 0x6000\n\t" \
  "s_cmp_ge_u32 %[s], 2\n\t" \
  "s_cbranch_scc1 Lh2_%=x\n\t" \
  "Lh2_%=:\n\t" \
  "ds_read_b128 v[252:255], %[fa]\n\t" \
  "s_cmp_eq_u32 %[to], 0\n\t" \
  "s_cbranch_scc1 Lh2_%=x\n\t" \
  "s_sub_u32 %[to], %[to], 1\n\t" \
  "s_waitcnt lgkmcnt(0)\n\t" \
  "v_readfirstlane_b32 %[s], v254\n\t" \
  "s_cmp_ge_u32 %[s], 2\n\t" \
  "s_cbranch_scc0 Lh2_%=\n\t" \
  "Lh2_%=x:\n\t" \
  "buffer_load_dwordx4 v[184:187], %[vo], %[srd], %[hb0] offen offset:0 sc1\n\t" \
  "buffer_load_dwordx4 v[188:191], %[vo], %[srd], %[hb0] offen offset:1024 sc1\n\t" \
  "buffer_load_dwordx4 v[192:195], %[vo], %[srd], %[hb1] offen offset:0 sc1\n\t" \
  "buffer_load_dwordx4 v[196:199], %[vo], %[srd], %[hb1] offen offset:1024 sc1\n\t" \
  "buffer_load_dwordx4 v[200:203], %[vo], %[srd], %[hb2] offen offset:0 sc1\n\t" \
  "buffer_load_dwordx4 v[204:207], %[vo], %[srd], %[hb2] offen offset:1024 sc1\n\t" \
  "buffer_load_dwordx4 v[208:211], %[vo], %[srd], %[hb3] offen offset:0 sc1\n\t" \
  "buffer_load_dwordx4 v[212:215], %[vo], %[srd], %[hb3] offen offset:1024 sc1\n\t" \
  "s_cmp_ge_u32 %[s], 3\n\t" \
  "s_cbranch_scc1 Lh3_%=x\n\t" \
  "Lh3_%=:\n\t" \
  "ds_read_b128 v[252:255], %[fa]\n\t" \
  "s_cmp_eq_u32 %[to], 0\n\t" \
  "s_cbranch_scc1 Lh3_%=x\n\t" \
  "s_sub_u32 %[to], %[to], 1\n\t" \
  "s_waitcnt lgkmcnt(0)\n\t" \
  "v_readfirstlane_b32 %[s], v254\n\t" \
  "s_cmp_ge_u32 %[s], 3\n\t" \
  "s_cbranch_scc0 Lh3_%=\n\t" \
  "Lh3_%=x:\n\t" \
  "s_branch Lin_%=\n\t" \
  "Lg2_%=:\n\t" \
  "s_waitcnt vmcnt(14) lgkmcnt(7)\n\t" \
  "ds_read_b128 v[252:255], %[fa]\n\t" \
  "v_mfma_f32_16x16x32_bf16 %[c00], v[184:187], %[k00], %[c00]\n\t" \
  "s_cmp_gt_u32 %[hn], %[own]\n\t" \
  "s_addc_u32 %[hb0], %[hn], -1\n\t" \
  "v_mfma_f32_16x16x32_bf16 %[c10], v[188:191], %[k00], %[c10]\n\t" \
  "ds_read_b128 %[k00], %[pc2] offset:0\n\t" \
  "s_waitcnt lgkmcnt(8)\n\t" \
  "v_mfma_f32_16x16x32_bf16 %[c01], v[184:187], %[k01], %[c01]\n\t" \
  "s_lshl_b32 %[hb0], %[hb0], 15\n\t" \
  "v_mfma_f32_16x16x32_bf16 %[c11], v[188:191], %[k01], %[c11]\n\t" \
  "ds_read_b128 %[k01], %[pc2] offset:2048\n\t" \
  "s_waitcnt lgkmcnt(8)\n\t" \
  "v_mfma_f32_16x16x32_bf16 %[c02], v[184:187], %[k02], %[c02]\n\t" \
  "s_or_b32 %[hb1], %[hb0], 0x2000\n\t" \
  "v_mfma_f32_16x16x32_bf16 %[c12], v[188:191], %[k02], %[c12]\n\t" \
  "ds_read_b128 %[k02], %[pc2] offset:4096\n\t" \
  "s_waitcnt lgkmcnt(8)\n\t" \
  "v_mfma_f32_16x16x32_bf16 %[c03], v[184:187], %[k03], %[c03]\n\t" \
  "s_add_u32 %[cn], %[cn], 1\n\t" \
  "v_mfma_f32_16x16x32_bf16 %[c13], v[188:191], %[k03], %[c13]\n\t" \
  "ds_read_b128 %[k03], %[pc2] offset:6144\n\t" \
  "buffer_load_dwordx4 v[184:187], %[vo], %[srd], %[hb0] offen offset:0 sc1\n\t" \
  "s_waitcnt vmcnt(13) lgkmcnt(8)\n\t" \
  "v_mfma_f32_16x16x32_bf16 %[c00], v[192:195], %[k10], %[c00]\n\t" \
  "s_cmp_eq_u32 %[cn], 9\n\t" \
  "s_cselect_b32 %[dl], 0x20000, %[d16]\n\t" \
  "v_mfma_f32_16x16x32_bf16 %[c10], v[196:199], %[k10], %[c10]\n\t" \
  "s_bitcmp1_b32 %[wr], %[cn]\n\t" \
  "s_cselect_b32 %[dl], 0xfffe4000, %[dl]\n\t" \
  "ds_read_b128 %[k10], %[pc3] offset:0\n\t" \
  "s_waitcnt lgkmcnt(8)\n\t" \
  "v_mfma_f32_16x16x32_bf16 %[c01], v[192:195], %[k11], %[c01]\n\t" \
  "v_add_u32 %[pc0], %[dl], %[pc0]\n\t" \
  "v_mfma_f32_16x16x32_bf16 %[c11], v[196:199], %[k11], %[c11]\n\t" \
  "s_or_b32 %[hb2], %[hb0], 0x4000\n\t" \
  "ds_read_b128 %[k11], %[pc3] offset:2048\n\t" \
  "buffer_load_dwordx4 v[188:191], %[vo], %[srd], %[hb0] offen offset:1024 sc1\n\t" \
  "s_waitcnt lgkmcnt(8)\n\t" \
  "v_mfma_f32_16x16x32_bf16 %[c02], v[192:195], %[k12], %[c02]\n\t" \
  "v_add_u32 %[pc1], %[dl], %[pc1]\n\t" \
  "v_mfma_f32_16x16x32_bf16 %[c12], v[196:199], %[k12], %[c12]\n\t" \
  "v_add_u32 %[fv], 1, %[fv]\n\t" \
  "ds_read_b128 %[k12], %[pc3] offset:4096\n\t" \
  "s_waitcnt lgkmcnt(7)\n\t" \
  "v_mfma_f32_16x16x32_bf16 %[c03], v[192:195], %[k13], %[c03]\n\t" \
  "v_readfirstlane_b32 %[s], v253\n\t" \
  "v_mfma_f32_16x16x32_bf16 %[c13], v[196:199], %[k13], %[c13]\n\t" \
  "ds_read_b128 %[k13], %[pc3] offset:6144\n\t" \
  "ds_write_b32 %[fw], %[fv]\n\t" \
  "s_cmp_gt_u32 %[s], %[cn]\n\t" \
  "s_cbranch_scc0 Lcs0_%=\n\t" \
  "Lcb0_%=:\n\t" \
  "buffer_load_dwordx4 v[192:195], %[vo], %[srd], %[hb1] offen offset:0 sc1\n\t" \
  "s_waitcnt vmcnt(13) lgkmcnt(8)\n\t" \
  "ds_read_b128 v[252:255], %[fa]\n\t" \
  "v_mfma_f32_16x16x32_bf16 %[c00], v[200:203], %[k00], %[c00]\n\t" \
  "s_or_b32 %[hb3], %[hb0], 0x6000\n\t" \
  "v_mfma_f32_16x16x32_bf16 %[c10], v[204:207], %[k00], %[c10]\n\t" \
  "ds_read_b128 %[k00], %[pc0] offset:0\n\t" \
  "s_waitcnt lgkmcnt(9)\n\t" \
  "v_mfma_f32_16x16x32_bf16 %[c01], v[200:203], %[k01], %[c01]\n\t" \
  "v_mfma_f32_16x16x32_bf16 %[c11], v[204:207], %[k01], %[c11]\n\t" \
  "ds_read_b128 %[k01], %[pc0] offset:2048\n\t" \
  "buffer_load_dwordx4 v[196:199], %[vo], %[srd], %[hb1] offen offset:1024 sc1\n\t" \
  "s_waitcnt lgkmcnt(9)\n\t" \
  "v_mfma_f32_16x16x32_bf16 %[c02], v[200:203], %[k02], %[c02]\n\t" \
  "v_mfma_f32_16x16x32_bf16 %[c12], v[204:207], %[k02], %[c12]\n\t" \
  "ds_read_b128 %[k02], %[pc0] offset:4096\n\t" \
  "s_waitcnt lgkmcnt(9)\n\t" \
  "v_mfma_f32_16x16x32_bf16 %[c03], v[200:203], %[k03], %[c03]\n\t" \
  "v_mfma_f32_16x16x32_bf16 %[c13], v[204:207], %[k03], %[c13]\n\t" \
  "ds_read_b128 %[k03], %[pc0] offset:6144\n\t" \
  "buffer_load_dwordx4 v[200:203], %[vo], %[srd], %[hb2] offen offset:0 sc1\n\t" \
  "s_waitcnt vmcnt(13) lgkmcnt(9)\n\t" \
  "v_mfma_f32_16x16x32_bf16 %[c00], v[208:211], %[k10], %[c00]\n\t" \
  "v_add_u32 %[pc2], %[dl], %[pc2]\n\t" \
  "v_mfma_f32_16x16x32_bf16 %[c10], v[212:215], %[k10], %[c10]\n\t" \
  "ds_read_b128 %[k10], %[pc1] offset:0\n\t" \
  "s_waitcnt lgkmcnt(9)\n\t" \
  "v_mfma_f32_16x16x32_bf16 %[c01], v[208:211], %[k11], %[c01]\n\t" \
  "v_add_u32 %[pc3], %[dl], %[pc3]\n\t" \
  "v_mfma_f32_16x16x32_bf16 %[c11], v[212:215], %[k11], %[c11]\n\t" \
  "ds_read_b128 %[k11], %[pc1] offset:2048\n\t" \
  "buffer_load_dwordx4 v[204:207], %[vo], %[srd], %[hb2] offen offset:1024 sc1\n\t" \
  "s_waitcnt lgkmcnt(6)\n\t" \
  "v_mfma_f32_16x16x32_bf16 %[c02], v[208:211], %[k12], %[c02]\n\t" \
  "v_readfirstlane_b32 %[s], v254\n\t" \
  "v_mfma_f32_16x16x32_bf16 %[c12], v[212:215], %[k12], %[c12]\n\t" \
  "ds_read_b128 %[k12], %[pc1] offset:4096\n\t" \
  "v_mfma_f32_16x16x32_bf16 %[c03], v[208:211], %[k13], %[c03]\n\t" \
  "s_add_u32 %[hn], %[hn], 1\n\t" \
  "v_mfma_f32_16x16x32_bf16 %[c13], v[212:215], %[k13], %[c13]\n\t" \
  "ds_read_b128 %[k13], %[pc1] offset:6144\n\t" \
  "buffer_load_dwordx4 v[208:211], %[vo], %[srd], %[hb3] offen offset:0 sc1\n\t" \
  "buffer_load_dwordx4 v[212:215], %[vo], %[srd], %[hb3] offen offset:1024 sc1\n\t" \
  "s_cmp_ge_u32 %[s], %[hn]\n\t" \
  "s_cbranch_scc0 Lhs0_%=\n\t" \
  "Lhb0_%=:\n\t" \
  "Lin_%=:\n\t" \
  "s_waitcnt vmcnt(14) lgkmcnt(7)\n\t" \
  "ds_read_b128 v[252:255], %[fa]\n\t" \
  "v_mfma_f32_16x16x32_bf16 %[c00], v[216:219], %[k00], %[c00]\n\t" \
  "s_cmp_gt_u32 %[hn], %[own]\n\t" \
  "s_addc_u32 %[hb0], %[hn], -1\n\t" \
  "v_mfma_f32_16x16x32_bf16 %[c10], v[220:223], %[k00], %[c10]\n\t" \
  "ds_read_b128 %[k00], %[pc2] offset:0\n\t" \
  "s_waitcnt lgkmcnt(8)\n\t" \
  "v_mfma_f32_16x16x32_bf16 %[c01], v[216:219], %[k01], %[c01]\n\t" \
  "s_lshl_b32 %[hb0], %[hb0], 15\n\t" \
  "v_mfma_f32_16x16x32_bf16 %[c11], v[220:223], %[k01], %[c11]\n\t" \
  "ds_read_b128 %[k01], %[pc2] offset:2048\n\t" \
  "s_waitcnt lgkmcnt(8)\n\t" \
  "v_mfma_f32_16x16x32_bf16 %[c02], v[216:219], %[k02], %[c02]\n\t" \
  "s_or_b32 %[hb1], %[hb0], 0x2000\n\t" \
  "v_mfma_f32_16x16x32_bf16 %[c12], v[220:223], %[k02], %[c12]\n\t" \
  "ds_read_b128 %[k02], %[pc2] offset:4096\n\t" \
  "s_waitcnt lgkmcnt(8)\n\t" \
  "v_mfma_f32_16x16x32_bf16 %[c03], v[216:219], %[k03], %[c03]\n\t" \
  "s_add_u32 %[cn], %[cn], 1\n\t" \
  "v_mfma_f32_16x16x32_bf16 %[c13], v[220:223], %[k03], %[c13]\n\t" \
  "ds_read_b128 %[k03], %[pc2] offset:6144\n\t" \
  "buffer_load_dwordx4 v[216:219], %[vo], %[srd], %[hb0] offen offset:0 sc1\n\t" \
  "s_waitcnt vmcnt(13) lgkmcnt(8)\n\t" \
  "v_mfma_f32_16x16x32_bf16 %[c00], v[224:227], %[k10], %[c00]\n\t" \
  "s_cmp_eq_u32 %[cn], 9\n\t" \
  "s_cselect_b32 %[dl], 0x20000, %[d16]\n\t" \
  "v_mfma_f32_16x16x32_bf16 %[c10], v[228:231], %[k10], %[c10]\n\t" \
  "s_bitcmp1_b32 %[wr], %[cn]\n\t" \
  "s_cselect_b32 %[dl], 0xfffe4000, %[dl]\n\t" \
  "ds_read_b128 %[k10], %[pc3] offset:0\n\t" \
  "s_waitcnt lgkmcnt(8)\n\t" \
  "v_mfma_f32_16x16x32_bf16 %[c01], v[224:227], %[k11], %[c01]\n\t" \
  "v_add_u32 %[pc0], %[dl], %[pc0]\n\t" \
  "v_mfma_f32_16x16x32_bf16 %[c11], v[228:231], %[k11], %[c11]\n\t" \
  "s_or_b32 %[hb2], %[hb0], 0x4000\n\t" \
  "ds_read_b128 %[k11], %[pc3] offset:2048\n\t" \
  "buffer_load_dwordx4 v[220:223], %[vo], %[srd], %[hb0] offen offset:1024 sc1\n\t" \
  "s_waitcnt lgkmcnt(8)\n\t" \
  "v_mfma_f32_16x16x32_bf16 %[c02], v[224:227], %[k12], %[c02]\n\t" \
  "v_add_u32 %[pc1], %[dl], %[pc1]\n\t" \
  "v_mfma_f32_16x16x32_bf16 %[c12], v[228:231], %[k12], %[c12]\n\t" \
  "v_add_u32 %[fv], 1, %[fv]\n\t" \
  "ds_read_b128 %[k12], %[pc3] offset:4096\n\t" \
  "s_waitcnt lgkmcnt(7)\n\t" \
  "v_mfma_f32_16x16x32_bf16 %[c03], v[224:227], %[k13], %[c03]\n\t" \
  "v_readfirstlane_b32 %[s], v252\n\t" \
  "v_mfma_f32_16x16x32_bf16 %[c13], v[228:231], %[k13], %[c13]\n\t" \
  "ds_read_b128 %[k13], %[pc3] offset:6144\n\t" \
  "ds_write_b32 %[fw], %[fv]\n\t" \
  "s_cmp_gt_u32 %[s], %[cn]\n\t" \
  "s_cbranch_scc0 Lcs1_%=\n\t" \
  "Lcb1_%=:\n\t" \
  "buffer_load_dwordx4 v[224:227], %[vo], %[srd], %[hb1] offen offset:0 sc1\n\t" \
  "s_waitcnt vmcnt(13) lgkmcnt(8)\n\t" \
  "ds_read_b128 v[252:255], %[fa]\n\t" \
  "v_mfma_f32_16x16x32_bf16 %[c00], v[232:235], %[k00], %[c00]\n\t" \
  "s_or_b32 %[hb3], %[hb0], 0x6000\n\t" \
  "v_mfma_f32_16x16x32_bf16 %[c10], v[236:239], %[k00], %[c10]\n\t" \
  "ds_read_b128 %[k00], %[pc0] offset:0\n\t" \
  "s_waitcnt lgkmcnt(9)\n\t" \
  "v_mfma_f32_16x16x32_bf16 %[c01], v[232:235], %[k01], %[c01]\n\t" \
  "v_mfma_f32_16x16x32_bf16 %[c11], v[236:239], %[k01], %[c11]\n\t" \
  "ds_read_b128 %[k01], %[pc0] offset:2048\n\t" \
  "buffer_load_dwordx4 v[228:231], %[vo], %[srd], %[hb1] offen offset:1024 sc1\n\t" \
  "s_waitcnt lgkmcnt(9)\n\t" \
  "v_mfma_f32_16x16x32_bf16 %[c02], v[232:235], %[k02], %[c02]\n\t" \
  "v_mfma_f32_16x16x32_bf16 %[c12], v[236:239], %[k02], %[c12]\n\t" \
  "ds_read_b128 %[k02], %[pc0] offset:4096\n\t" \
  "s_waitcnt lgkmcnt(9)\n\t" \
  "v_mfma_f32_16x16x32_bf16 %[c03], v[232:235], %[k03], %[c03]\n\t" \
  "v_mfma_f32_16x16x32_bf16 %[c13], v[236:239], %[k03], %[c13]\n\t" \
  "ds_read_b128 %[k03], %[pc0] offset:6144\n\t" \
  "buffer_load_dwordx4 v[232:235], %[vo], %[srd], %[hb2] offen offset:0 sc1\n\t" \
  "s_waitcnt vmcnt(13) lgkmcnt(9)\n\t" \
  "v_mfma_f32_16x16x32_bf16 %[c00], v[240:243], %[k10], %[c00]\n\t" \
  "v_add_u32 %[pc2], %[dl], %[pc2]\n\t" \
  "v_mfma_f32_16x16x32_bf16 %[c10], v[244:247], %[k10], %[c10]\n\t" \
  "ds_read_b128 %[k10], %[pc1] offset:0\n\t" \
  "s_waitcnt lgkmcnt(9)\n\t" \
  "v_mfma_f32_16x16x32_bf16 %[c01], v[240:243], %[k11], %[c01]\n\t" \
  "v_add_u32 %[pc3], %[dl], %[pc3]\n\t" \
  "v_mfma_f32_16x16x32_bf16 %[c11], v[244:247], %[k11], %[c11]\n\t" \
  "ds_read_b128 %[k11], %[pc1] offset:2048\n\t" \
  "buffer_load_dwordx4 v[236:239], %[vo], %[srd], %[hb2] offen offset:1024 sc1\n\t" \
  "s_waitcnt lgkmcnt(6)\n\t" \
  "v_mfma_f32_16x16x32_bf16 %[c02], v[240:243], %[k12], %[c02]\n\t" \
  "v_readfirstlane_b32 %[s], v254\n\t" \
  "v_mfma_f32_16x16x32_bf16 %[c12], v[244:247], %[k12], %[c12]\n\t" \
  "ds_read_b128 %[k12], %[pc1] offset:4096\n\t" \
  "v_mfma_f32_16x16x32_bf16 %[c03], v[240:243], %[k13], %[c03]\n\t" \
  "s_add_u32 %[hn], %[hn], 1\n\t" \
  "v_mfma_f32_16x16x32_bf16 %[c13], v[244:247], %[k13], %[c13]\n\t" \
  "ds_read_b128 %[k13], %[pc1] offset:6144\n\t" \
  "buffer_load_dwordx4 v[240:243], %[vo], %[srd], %[hb3] offen offset:0 sc1\n\t" \
  "buffer_load_dwordx4 v[244:247], %[vo], %[srd], %[hb3] offen offset:1024 sc1\n\t" \
  "s_cmp_ge_u32 %[s], %[hn]\n\t" \
  "s_cbranch_scc0 Lhs1_%=\n\t" \
  "Lhb1_%=:\n\t" \
  "s_cmp_lt_u32 %[cn], 14\n\t" \
  "s_cbranch_scc1 Lg2_%=\n\t" \
  "s_waitcnt vmcnt(14) lgkmcnt(7)\n\t" \
  "ds_read_b128 v[252:255], %[fa]\n\t" \
  "v_mfma_f32_16x16x32_bf16 %[c00], v[184:187], %[k00], %[c00]\n\t" \
  "v_mfma_f32_16x16x32_bf16 %[c10], v[188:191], %[k00], %[c10]\n\t" \
  "ds_read_b128 %[k00], %[pc2] offset:0\n\t" \
  "s_waitcnt lgkmcnt(8)\n\t" \
  "v_mfma_f32_16x16x32_bf16 %[c01], v[184:187], %[k01], %[c01]\n\t" \
  "v_mfma_f32_16x16x32_bf16 %[c11], v[188:191], %[k01], %[c11]\n\t" \
  "ds_read_b128 %[k01], %[pc2] offset:2048\n\t" \
  "s_waitcnt lgkmcnt(8)\n\t" \
  "v_mfma_f32_16x16x32_bf16 %[c02], v[184:187], %[k02], %[c02]\n\t" \
  "v_mfma_f32_16x16x32_bf16 %[c12], v[188:191], %[k02], %[c12]\n\t" \
  "ds_read_b128 %[k02], %[pc2] offset:4096\n\t" \
  "s_waitcnt lgkmcnt(8)\n\t" \
  "v_mfma_f32_16x16x32_bf16 %[c03], v[184:187], %[k03], %[c03]\n\t" \
  "s_add_u32 %[cn], %[cn], 1\n\t" \
  "v_mfma_f32_16x16x32_bf16 %[c13], v[188:191], %[k03], %[c13]\n\t" \
  "ds_read_b128 %[k03], %[pc2] offset:6144\n\t" \
  "s_waitcnt vmcnt(12) lgkmcnt(8)\n\t" \
  "v_mfma_f32_16x16x32_bf16 %[c00], v[192:195], %[k10], %[c00]\n\t" \
  "s_cmp_eq_u32 %[cn], 9\n\t" \
  "s_cselect_b32 %[dl], 0x20000, %[d16]\n\t" \
  "v_mfma_f32_16x16x32_bf16 %[c10], v[196:199], %[k10], %[c10]\n\t" \
  "s_bitcmp1_b32 %[wr], %[cn]\n\t" \
  "s_cselect_b32 %[dl], 0xfffe4000, %[dl]\n\t" \
  "ds_read_b128 %[k10], %[pc3] offset:0\n\t" \
  "s_waitcnt lgkmcnt(8)\n\t" \
  "v_mfma_f32_16x16x32_bf16 %[c01], v[192:195], %[k11], %[c01]\n\t" \
  "v_add_u32 %[pc0], %[dl], %[pc0]\n\t" \
  "v_mfma_f32_16x16x32_bf16 %[c11], v[196:199], %[k11], %[c11]\n\t" \
  "ds_read_b128 %[k11], %[pc3] offset:2048\n\t" \
  "s_waitcnt lgkmcnt(8)\n\t" \
  "v_mfma_f32_16x16x32_bf16 %[c02], v[192:195], %[k12], %[c02]\n\t" \
  "v_add_u32 %[pc1], %[dl], %[pc1]\n\t" \
  "v_mfma_f32_16x16x32_bf16 %[c12], v[196:199], %[k12], %[c12]\n\t" \
  "ds_read_b128 %[k12], %[pc3] offset:4096\n\t" \
  "s_waitcnt lgkmcnt(7)\n\t" \
  "v_mfma_f32_16x16x32_bf16 %[c03], v[192:195], %[k13], %[c03]\n\t" \
  "v_readfirstlane_b32 %[s], v253\n\t" \
  "v_mfma_f32_16x16x32_bf16 %[c13], v[196:199], %[k13], %[c13]\n\t" \
  "ds_read_b128 %[k13], %[pc3] offset:6144\n\t" \
  "s_cmp_gt_u32 %[s], %[cn]\n\t" \
  "s_cbranch_scc0 Lcs0t_%=\n\t" \
  "Lcb0t_%=:\n\t" \
  "s_waitcnt vmcnt(10) lgkmcnt(7)\n\t" \
  "v_mfma_f32_16x16x32_bf16 %[c00], v[200:203], %[k00], %[c00]\n\t" \
  "v_mfma_f32_16x16x32_bf16 %[c10], v[204:207], %[k00], %[c10]\n\t" \
  "ds_read_b128 %[k00], %[pc0] offset:0\n\t" \
  "s_waitcnt lgkmcnt(7)\n\t" \
  "v_mfma_f32_16x16x32_bf16 %[c01], v[200:203], %[k01], %[c01]\n\t" \
  "v_mfma_f32_16x16x32_bf16 %[c11], v[204:207], %[k01], %[c11]\n\t" \
  "ds_read_b128 %[k01], %[pc0] offset:2048\n\t" \
  "s_waitcnt lgkmcnt(7)\n\t" \
  "v_mfma_f32_16x16x32_bf16 %[c02], v[200:203], %[k02], %[c02]\n\t" \
  "v_mfma_f32_16x16x32_bf16 %[c12], v[204:207], %[k02], %[c12]\n\t" \
  "ds_read_b128 %[k02], %[pc0] offset:4096\n\t" \
  "s_waitcnt lgkmcnt(7)\n\t" \
  "v_mfma_f32_16x16x32_bf16 %[c03], v[200:203], %[k03], %[c03]\n\t" \
  "v_mfma_f32_16x16x32_bf16 %[c13], v[204:207], %[k03], %[c13]\n\t" \
  "ds_read_b128 %[k03], %[pc0] offset:6144\n\t" \
  "s_waitcnt vmcnt(8) lgkmcnt(7)\n\t" \
  "v_mfma_f32_16x16x32_bf16 %[c00], v[208:211], %[k10], %[c00]\n\t" \
  "v_add_u32 %[pc2], %[dl], %[pc2]\n\t" \
  "v_mfma_f32_16x16x32_bf16 %[c10], v[212:215], %[k10], %[c10]\n\t" \
  "ds_read_b128 %[k10], %[pc1] offset:0\n\t" \
  "s_waitcnt lgkmcnt(7)\n\t" \
  "v_mfma_f32_16x16x32_bf16 %[c01], v[208:211], %[k11], %[c01]\n\t" \
  "v_add_u32 %[pc3], %[dl], %[pc3]\n\t" \
  "v_mfma_f32_16x16x32_bf16 %[c11], v[212:215], %[k11], %[c11]\n\t" \
  "ds_read_b128 %[k11], %[pc1] offset:2048\n\t" \
  "s_waitcnt lgkmcnt(7)\n\t" \
  "v_mfma_f32_16x16x32_bf16 %[c02], v[208:211], %[k12], %[c02]\n\t" \
  "v_mfma_f32_16x16x32_bf16 %[c12], v[212:215], %[k12], %[c12]\n\t" \
  "ds_read_b128 %[k12], %[pc1] offset:4096\n\t" \
  "s_waitcnt lgkmcnt(7)\n\t" \
  "v_mfma_f32_16x16x32_bf16 %[c03], v[208:211], %[k13], %[c03]\n\t" \
  "v_mfma_f32_16x16x32_bf16 %[c13], v[212:215], %[k13], %[c13]\n\t" \
  "ds_read_b128 %[k13], %[pc1] offset:6144\n\t" \
  "s_waitcnt vmcnt(6) lgkmcnt(7)\n\t" \
  "v_mfma_f32_16x16x32_bf16 %[c00], v[216:219], %[k00], %[c00]\n\t" \
  "v_mfma_f32_16x16x32_bf16 %[c10], v[220:223], %[k00], %[c10]\n\t" \
  "ds_read_b128 %[k00], %[pc2] offset:0\n\t" \
  "s_waitcnt lgkmcnt(7)\n\t" \
  "v_mfma_f32_16x16x32_bf16 %[c01], v[216:219], %[k01], %[c01]\n\t" \
  "v_mfma_f32_16x16x32_bf16 %[c11], v[220:223], %[k01], %[c11]\n\t" \
  "ds_read_b128 %[k01], %[pc2] offset:2048\n\t" \
  "s_waitcnt lgkmcnt(7)\n\t" \
  "v_mfma_f32_16x16x32_bf16 %[c02], v[216:219], %[k02], %[c02]\n\t" \
  "v_mfma_f32_16x16x32_bf16 %[c12], v[220:223], %[k02], %[c12]\n\t" \
  "ds_read_b128 %[k02], %[pc2] offset:4096\n\t" \
  "s_waitcnt lgkmcnt(7)\n\t" \
  "v_mfma_f32_16x16x32_bf16 %[c03], v[216:219], %[k03], %[c03]\n\t" \
  "v_mfma_f32_16x16x32_bf16 %[c13], v[220:223], %[k03], %[c13]\n\t" \
  "ds_read_b128 %[k03], %[pc2] offset:6144\n\t" \
  "s_waitcnt vmcnt(4) lgkmcnt(7)\n\t" \
  "v_mfma_f32_16x16x32_bf16 %[c00], v[224:227], %[k10], %[c00]\n\t" \
  "v_mfma_f32_16x16x32_bf16 %[c10], v[228:231], %[k10], %[c10]\n\t" \
  "ds_read_b128 %[k10], %[pc3] offset:0\n\t" \
  "s_waitcnt lgkmcnt(7)\n\t" \
  "v_mfma_f32_16x16x32_bf16 %[c01], v[224:227], %[k11], %[c01]\n\t" \
  "v_mfma_f32_16x16x32_bf16 %[c11], v[228:231], %[k11], %[c11]\n\t" \
  "ds_read_b128 %[k11], %[pc3] offset:2048\n\t" \
  "s_waitcnt lgkmcnt(7)\n\t" \
  "v_mfma_f32_16x16x32_bf16 %[c02], v[224:227], %[k12], %[c02]\n\t" \
  "v_mfma_f32_16x16x32_bf16 %[c12], v[228:231], %[k12], %[c12]\n\t" \
  "ds_read_b128 %[k12], %[pc3] offset:4096\n\t" \
  "s_waitcnt lgkmcnt(7)\n\t" \
  "v_mfma_f32_16x16x32_bf16 %[c03], v[224:227], %[k13], %[c03]\n\t" \
  "v_mfma_f32_16x16x32_bf16 %[c13], v[228:231], %[k13], %[c13]\n\t" \
  "ds_read_b128 %[k13], %[pc3] offset:6144\n\t" \
  "s_waitcnt vmcnt(2) lgkmcnt(7)\n\t" \
  "v_mfma_f32_16x16x32_bf16 %[c00], v[232:235], %[k00], %[c00]\n\t" \
  "v_mfma_f32_16x16x32_bf16 %[c10], v[236:239], %[k00], %[c10]\n\t" \
  "s_waitcnt lgkmcnt(6)\n\t" \
  "v_mfma_f32_16x16x32_bf16 %[c01], v[232:235], %[k01], %[c01]\n\t" \
  "v_mfma_f32_16x16x32_bf16 %[c11], v[236:239], %[k01], %[c11]\n\t" \
  "s_waitcnt lgkmcnt(5)\n\t" \
  "v_mfma_f32_16x16x32_bf16 %[c02], v[232:235], %[k02], %[c02]\n\t" \
  "v_mfma_f32_16x16x32_bf16 %[c12], v[236:239], %[k02], %[c12]\n\t" \
  "s_waitcnt lgkmcnt(4)\n\t" \
  "v_mfma_f32_16x16x32_bf16 %[c03], v[232:235], %[k03], %[c03]\n\t" \
  "v_mfma_f32_16x16x32_bf16 %[c13], v[236:239], %[k03], %[c13]\n\t" \
  "s_waitcnt vmcnt(0) lgkmcnt(3)\n\t" \
  "v_mfma_f32_16x16x32_bf16 %[c00], v[240:243], %[k10], %[c00]\n\t" \
  "v_mfma_f32_16x16x32_bf16 %[c10], v[244:247], %[k10], %[c10]\n\t" \
  "s_waitcnt lgkmcnt(2)\n\t" \
  "v_mfma_f32_16x16x32_bf16 %[c01], v[240:243], %[k11], %[c01]\n\t" \
  "v_mfma_f32_16x16x32_bf16 %[c11], v[244:247], %[k11], %[c11]\n\t" \
  "s_waitcnt lgkmcnt(1)\n\t" \
  "v_mfma_f32_16x16x32_bf16 %[c02], v[240:243], %[k12], %[c02]\n\t" \
  "v_mfma_f32_16x16x32_bf16 %[c12], v[244:247], %[k12], %[c12]\n\t" \
  "s_waitcnt lgkmcnt(0)\n\t" \
  "v_mfma_f32_16x16x32_bf16 %[c03], v[240:243], %[k13], %[c03]\n\t" \
  "v_mfma_f32_16x16x32_bf16 %[c13], v[244:247], %[k13], %[c13]\n\t" \
  "s_branch Lend_%=\n\t" \
  "Lcs0_%=:\n\t" \
  "s_add_u32 %[s2], %[cn], 1\n\t" \
  "Lcr0_%=:\n\t" \
  "ds_read_b128 v[252:255], %[fa]\n\t" \
  "s_cmp_eq_u32 %[to], 0\n\t" \
  "s_cbranch_scc1 Lcb0_%=\n\t" \
  "s_sub_u32 %[to], %[to], 1\n\t" \
  "s_waitcnt lgkmcnt(0)\n\t" \
  "v_readfirstlane_b32 %[s], v253\n\t" \
  "s_cmp_ge_u32 %[s], %[s2]\n\t" \
  "s_cbranch_scc0 Lcr0_%=\n\t" \
  "s_branch Lcb0_%=\n\t" \
  "Lhs0_%=:\n\t" \
  "ds_read_b128 v[252:255], %[fa]\n\t" \
  "s_cmp_eq_u32 %[to], 0\n\t" \
  "s_cbranch_scc1 Lhb0_%=\n\t" \
  "s_sub_u32 %[to], %[to], 1\n\t" \
  "s_waitcnt lgkmcnt(0)\n\t" \
  "v_readfirstlane_b32 %[s], v254\n\t" \
  "s_cmp_ge_u32 %[s], %[hn]\n\t" \
  "s_cbranch_scc0 Lhs0_%=\n\t" \
  "s_branch Lhb0_%=\n\t" \
  "Lcs1_%=:\n\t" \
  "s_add_u32 %[s2], %[cn], 1\n\t" \
  "Lcr1_%=:\n\t" \
  "ds_read_b128 v[252:255], %[fa]\n\t" \
  "s_cmp_eq_u32 %[to], 0\n\t" \
  "s_cbranch_scc1 Lcb1_%=\n\t" \
  "s_sub_u32 %[to], %[to], 1\n\t" \
  "s_waitcnt lgkmcnt(0)\n\t" \
  "v_readfirstlane_b32 %[s], v252\n\t" \
  "s_cmp_ge_u32 %[s], %[s2]\n\t" \
  "s_cbranch_scc0 Lcr1_%=\n\t" \
  "s_branch Lcb1_%=\n\t" \
  "Lhs1_%=:\n\t" \
  "ds_read_b128 v[252:255], %[fa]\n\t" \
  "s_cmp_eq_u32 %[to], 0\n\t" \
  "s_cbranch_scc1 Lhb1_%=\n\t" \
  "s_sub_u32 %[to], %[to], 1\n\t" \
  "s_waitcnt lgkmcnt(0)\n\t" \
  "v_readfirstlane_b32 %[s], v254\n\t" \
  "s_cmp_ge_u32 %[s], %[hn]\n\t" \
  "s_cbranch_scc0 Lhs1_%=\n\t" \
  "s_branch Lhb1_%=\n\t" \
  "Lcs0t_%=:\n\t" \
  "s_add_u32 %[s2], %[cn], 1\n\t" \
  "Lcr0t_%=:\n\t" \
  "ds_read_b128 v[252:255], %[fa]\n\t" \
  "s_cmp_eq_u32 %[to], 0\n\t" \
  "s_cbranch_scc1 Lcb0t_%=\n\t" \
  "s_sub_u32 %[to], %[to], 1\n\t" \
  "s_waitcnt lgkmcnt(0)\n\t" \
  "v_readfirstlane_b32 %[s], v253\n\t" \
  "s_cmp_ge_u32 %[s], %[s2]\n\t" \
  "s_cbranch_scc0 Lcr0t_%=\n\t" \
  "s_branch Lcb0t_%=\n\t" \
  "Lend_%=:\n\t" \
  "s_nop 7\n\t"
#define ASM_REQ \
  "s_cmp_gt_u32 1, %[own]\n\t" \
  "s_addc_u32 %[hb0], 1, -1\n\t" \
  "s_lshl_b32 %[hb0], %[hb0], 15\n\t" \
  "s_or_b32 %[hb1], %[hb0], 0x2000\n\t" \
  "s_or_b32 %[hb2], %[hb0], 0x4000\n\t" \
  "s_or_b32 %[hb3], %[hb0], 0x6000\n\t" \
  "buffer_load_dwordx4 v[216:219], %[vo], %[srd], %[hb0] offen offset:0 sc1\n\t" \
  "buffer_load_dwordx4 v[220:223], %[vo], %[srd], %[hb0] offen offset:1024 sc1\n\t" \
  "buffer_load_dwordx4 v[224:227], %[vo], %[srd], %[hb1] offen offset:0 sc1\n\t" \
  "buffer_load_dwordx4 v[228:231], %[vo], %[srd], %[hb1] offen offset:1024 sc1\n\t" \
  "buffer_load_dwordx4 v[232:235], %[vo], %[srd], %[hb2] offen offset:0 sc1\n\t" \
  "buffer_load_dwordx4 v[236:239], %[vo], %[srd], %[hb2] offen offset:1024 sc1\n\t" \
  "buffer_load_dwordx4 v[240:243], %[vo], %[srd], %[hb3] offen offset:0 sc1\n\t" \
  "buffer_load_dwordx4 v[244:247], %[vo], %[srd], %[hb3] offen offset:1024 sc1\n\t" \
  "s_cmp_gt_u32 2, %[own]\n\t" \
  "s_addc_u32 %[hb0], 2, -1\n\t" \
  "s_lshl_b32 %[hb0], %[hb0], 15\n\t" \
  "s_or_b32 %[hb1], %[hb0], 0x2000\n\t" \
  "s_or_b32 %[hb2], %[hb0], 0x4000\n\t" \
  "s_or_b32 %[hb3], %[hb0], 0x6000\n\t" \
  "buffer_load_dwordx4 v[184:187], %[vo], %[srd], %[hb0] offen offset:0 sc1\n\t" \
  "buffer_load_dwordx4 v[188:191], %[vo], %[srd], %[hb0] offen offset:1024 sc1\n\t" \
  "buffer_load_dwordx4 v[192:195], %[vo], %[srd], %[hb1] offen offset:0 sc1\n\t" \
  "buffer_load_dwordx4 v[196:199], %[vo], %[srd], %[hb1] offen offset:1024 sc1\n\t" \
  "buffer_load_dwordx4 v[200:203], %[vo], %[srd], %[hb2] offen offset:0 sc1\n\t" \
  "buffer_load_dwordx4 v[204:207], %[vo], %[srd], %[hb2] offen offset:1024 sc1\n\t" \
  "buffer_load_dwordx4 v[208:211], %[vo], %[srd], %[hb3] offen offset:0 sc1\n\t" \
  "buffer_load_dwordx4 v[212:215], %[vo], %[srd], %[hb3] offen offset:1024 sc1\n\t"
#define NS 0

#define OST " sc1 nt"
#define XPRIO 2
#define XPRIO2 1
#define MPRIO2 2
#define HDLY 8
#define LPRIO 2
#define POLLN 8
#define YGATE 11
#define LTHR 16
#define CPK 9
#define W2P 0
#define SWP 0
#define MTOP 1
#define CROT 0
#define NRG0 3
#define NRG1 3
#define CSL 0
#define NSI 8
#define DSL 1
#define FSB 128
#define FSW 8
#define XP3 3
#define XP4 3
#define LZ 1
#define KROT 0
#define LGT 0
#define XGT 0
#define W1N 8
#define W1P 1
#define WIF 0
#define W2G 0
#define W2S 0
#define WNT " nt"
#define MGP 1
#define RQD 0
#define XG2 2
#define NFW 28
#define SPINLOG 18
constexpr int D_ = 1024, F_ = 3584;
constexpr int L_XH = 131072;
constexpr int MISC = 155648;
constexpr int L_GT = MISC;
constexpr int L_FLG = L_GT + 512;
constexpr int L_JNK = L_FLG + 256;
constexpr int L_SEL = L_JNK + 256;
constexpr int L_BROW = L_SEL + 128;
constexpr int L_ACT = L_BROW + 2048;
constexpr int L_CAND = L_ACT + 1024;
constexpr int L_ZERO = L_CAND + 256;
constexpr int L_MAP = L_ZERO + 256;
constexpr int L_TAG = L_MAP + 512;
constexpr int L_RT = L_TAG + 16;
constexpr int LDS_BYTES = 163840;
static_assert(L_RT + 32 <= LDS_BYTES, "lds");
constexpr int FL_FULL = L_FLG, FL_FREE = L_FLG + 16, FL_C = L_FLG + 32  , FL_CFREE = L_FLG + 48, FL_TAG = L_FLG + 64  ,
              FL_XD = L_FLG + 80, FL_TK = L_FLG + 96, FL_SEL = L_FLG + 112, FL_G = L_FLG + 128, FL_W = L_FLG + 136, FL_HS = L_FLG + 144, FL_C8 = L_FLG + 160;
constexpr unsigned W_EPOCH = 0;
constexpr unsigned W_R2 = 256;
constexpr unsigned W_G2 = W_R2 + 8192;
constexpr unsigned W_HF = W_G2 + 8192;
constexpr unsigned W_AGG = W_HF + 256 * FSB;
constexpr unsigned W_SEL = W_AGG + 262144;
constexpr unsigned W_GU = W_SEL + 4096;
constexpr unsigned W_YP = W_GU + 56 * 16384;
constexpr unsigned W_H = 8u << 20;
constexpr unsigned W_DBG = 20u << 20;
constexpr unsigned W_END = 21u << 20;
static_assert(W_YP + 256 * 7168 <= W_H, "ws");
#define WS_BASE(n) (((n) - W_END) & ~(size_t)0xfffff)
constexpr int SPIN_MAX = 1 << SPINLOG;

DEV float bf2f(unsigned v) { return __uint_as_float(v << 16); }
DEV float bfhi(unsigned v) { return __uint_as_float(v & 0xffff0000u); }
DEV u16 f2bf(float f) { return __builtin_bit_cast(u16, (__bf16)f); }
DEV float rbf(float f) { return bf2f(f2bf(f)); }
typedef __bf16 bf16x2_t __attribute__((ext_vector_type(2)));
DEV unsigned pk2(float a, float b) { return __builtin_bit_cast(unsigned, __builtin_convertvector((f32x2){a, b}, bf16x2_t)); }
DEV float fma1(float a, float b, float c) { float d; asm("v_fma_f32 %0, %1, %2, %3" : "=v"(d) : "v"(a), "v"(b), "v"(c)); return d; }
DEV float fnma1(float a, float b, float c) { float d; asm("v_fma_f32 %0, -%1, %2, %3" : "=v"(d) : "v"(a), "v"(b), "v"(c)); return d; }
DEV float fnmn1(float a, float b, float c) { float d; asm("v_fma_f32 %0, -%1, %2, -%3" : "=v"(d) : "v"(a), "v"(b), "v"(c)); return d; }
DEV float mul1(float a, float b) { float d; asm("v_mul_f32 %0, %1, %2" : "=v"(d) : "v"(a), "v"(b)); return d; }
DEV f32x2 cmul(f32x2 a, f32x2 b) { return (f32x2){fnma1(a.y, b.y, mul1(a.x, b.x)), fma1(a.y, b.x, mul1(a.x, b.y))}; }
DEV f32x2 cfma(f32x2 a, f32x2 b, f32x2 c) { return (f32x2){fma1(a.x, b.x, fnma1(a.y, b.y, c.x)), fma1(a.x, b.y, fma1(a.y, b.x, c.y))}; }
template <int N> DEV void waitv() { asm volatile("s_waitcnt vmcnt(%0)" ::"n"(N) : "memory"); }
DEV void bar() { asm volatile("s_barrier" ::: "memory"); }
DEV unsigned lds_rd32(unsigned a) { unsigned v; asm volatile("ds_read_b32 %0, %1\n\ts_waitcnt lgkmcnt(0)" : "=v"(v) : "v"(a) : "memory"); return v; }
DEV u32x4 lds_rd128(unsigned a) { u32x4 v; asm volatile("ds_read_b128 %0, %1\n\ts_waitcnt lgkmcnt(0)" : "=v"(v) : "v"(a) : "memory"); return v; }
DEV void lds_wr32(unsigned a, unsigned v) { asm volatile("ds_write_b32 %0, %1" ::"v"(a), "v"(v) : "memory"); }
template <int S = 1> DEV void lds_wait_ge(unsigned a, unsigned need) {
#pragma unroll 1
  for (int it = 0; it < SPIN_MAX * 4; ++it) { if (__builtin_amdgcn_readfirstlane(lds_rd32(a)) >= need) break; __builtin_amdgcn_s_sleep(S); }
}
typedef __amdgpu_buffer_rsrc_t rsrc_t;
DEV u32x4 ld128(rsrc_t r, unsigned vo, unsigned so = 0) { return __builtin_amdgcn_raw_buffer_load_b128(r, vo, so, 0); }
#define FRESH() asm volatile("" ::: "memory")
DEV u32x4 ld128c(rsrc_t r, unsigned vo) { return __builtin_amdgcn_raw_buffer_load_b128(r, vo, 0, 16); }
DEV u32x2 ld64c(rsrc_t r, unsigned vo) { return __builtin_amdgcn_raw_buffer_load_b64(r, vo, 0, 16); }
DEV void st128c(rsrc_t r, unsigned vo, u32x4 v) {
  asm volatile("s_nop 4\n\tbuffer_store_dwordx4 %0, %1, %2, 0 offen sc1\n\ts_nop 7\n\ts_nop 7" ::"v"(v), "v"(vo), "s"(r) : "memory");
}
DEV void st64c(rsrc_t r, unsigned vo, u32x2 v) { __builtin_amdgcn_raw_buffer_store_b64(v, r, vo, 0, 16); }
DEV rsrc_t mk(const void* p, unsigned bytes) { return __builtin_amdgcn_make_buffer_rsrc((void*)p, 0, bytes, 0x00020000); }

DEV unsigned wave_maxu(unsigned v) {
  v = max(v, (unsigned)__builtin_amdgcn_update_dpp((int)v, (int)v, 0x111, 0xf, 0xf, false));
  v = max(v, (unsigned)__builtin_amdgcn_update_dpp((int)v, (int)v, 0x112, 0xf, 0xf, false));
  v = max(v, (unsigned)__builtin_amdgcn_update_dpp((int)v, (int)v, 0x114, 0xf, 0xf, false));
  v = max(v, (unsigned)__builtin_amdgcn_update_dpp((int)v, (int)v, 0x118, 0xf, 0xf, false));
  v = max(v, (unsigned)__builtin_amdgcn_update_dpp((int)v, (int)v, 0x142, 0xa, 0xf, false));
  v = max(v, (unsigned)__builtin_amdgcn_update_dpp((int)v, (int)v, 0x143, 0xc, 0xf, false));
  return (unsigned)__builtin_amdgcn_readlane((int)v, 63);
}
DEV float sum16_32(float v) {
  { auto p = __builtin_amdgcn_permlane16_swap(__float_as_uint(v), __float_as_uint(v), false, false); v = __uint_as_float(p[0]) + __uint_as_float(p[1]); }
  { auto p = __builtin_amdgcn_permlane32_swap(__float_as_uint(v), __float_as_uint(v), false, false); v = __uint_as_float(p[0]) + __uint_as_float(p[1]); }
  return v;
}
DEV float wave_sum(float v) {
  v += __uint_as_float((unsigned)__builtin_amdgcn_update_dpp(0, (int)__float_as_uint(v), 0x128, 0xf, 0xf, false));
  v += __uint_as_float((unsigned)__builtin_amdgcn_update_dpp(0, (int)__float_as_uint(v), 0x124, 0xf, 0xf, false));
  v += __uint_as_float((unsigned)__builtin_amdgcn_update_dpp(0, (int)__float_as_uint(v), 0x122, 0xf, 0xf, false));
  v += __uint_as_float((unsigned)__builtin_amdgcn_update_dpp(0, (int)__float_as_uint(v), 0x121, 0xf, 0xf, false));
  return sum16_32(v);
}
DEV unsigned route_key(float r, int idx) {
  unsigned u = __float_as_uint(r) >> 16;
  u = (u & 0x8000u) ? (~u & 0xffffu) : (u | 0x8000u);
  return (u << 16) | (unsigned)(2047 - idx);
}
DEV float key_val(unsigned key) {
  unsigned u = key >> 16;
  u = (u & 0x8000u) ? (u & 0x7fffu) : (~u & 0xffffu);
  return __uint_as_float(u << 16);
}

DEV void unit_trn(u32x4 (&w)[8]) {
  u32x4 b[8];
#pragma unroll
  for (int j = 0; j < 8; ++j)
#pragma unroll
    for (int t = 0; t < 4; ++t) b[j][t] = __builtin_amdgcn_perm(w[2 * t + 1][j >> 1], w[2 * t][j >> 1], (j & 1) ? 0x07060302u : 0x05040100u);
#pragma unroll
  for (int j = 0; j < 8; ++j) w[j] = b[j];
}
DEV f32x4 fma4(f32x4 a, f32x4 b, f32x4 c) { return __builtin_elementwise_fma(a, b, c); }
DEV f32x4 fnm4(f32x4 a, f32x4 b, f32x4 c) {
  const f32x2 l = __builtin_elementwise_fma(-a.lo, b.lo, c.lo), h = __builtin_elementwise_fma(-a.hi, b.hi, c.hi);
  return (f32x4){l.x, l.y, h.x, h.y};
}
DEV f32x4 nml4(f32x4 a, f32x4 b) { const f32x2 l = -a.lo * b.lo, h = -a.hi * b.hi; return (f32x4){l.x, l.y, h.x, h.y}; }
template <int CTRL> DEV float dpp1(float v) { return __uint_as_float((unsigned)__builtin_amdgcn_update_dpp(0, (int)__float_as_uint(v), CTRL, 0xf, 0xf, true)); }
template <int CTRL> DEV f32x4 dpp4(f32x4 v) { return (f32x4){dpp1<CTRL>(v.x), dpp1<CTRL>(v.y), dpp1<CTRL>(v.z), dpp1<CTRL>(v.w)}; }
DEV f32x4 splat4(float v) { f32x2 p = {v, v}; asm volatile("" : "+v"(p)); return (f32x4){p.x, p.y, p.x, p.y}; }
DEV void cexp4(f32x4 a2, f32x4 bv, f32x4& re, f32x4& im) {
  f32x4 m, cs, sn;
#pragma unroll
  for (int r = 0; r < 4; ++r) { const float ph = __builtin_amdgcn_fractf(bv[r]); m[r] = __builtin_amdgcn_exp2f(a2[r]); cs[r] = __builtin_amdgcn_cosf(ph); sn[r] = __builtin_amdgcn_sinf(ph); }
  re = m * cs; im = m * sn;
}
DEV f32x4 unpk4(u32x2 v) { return (f32x4){bf2f(v.x & 0xffffu), bfhi(v.x), bf2f(v.y & 0xffffu), bfhi(v.y)}; }
DEV void bcast8(int n, f32x4 v, f32x4& o) {
  switch (n) {
    case 0: o = dpp4<0x150>(v); break; case 1: o = dpp4<0x151>(v); break; case 2: o = dpp4<0x152>(v); break; case 3: o = dpp4<0x153>(v); break;
    case 4: o = dpp4<0x154>(v); break; case 5: o = dpp4<0x155>(v); break; case 6: o = dpp4<0x156>(v); break; default: o = dpp4<0x157>(v); break;
  }
}
DEV f32x4 lds4(LAS char* smem, int a) { return *(const LAS f32x4*)(smem + a); }
template <int S = 1> DEV void lds_wait_min4(unsigned a, unsigned need) {
#pragma unroll 1
  for (int it = 0; it < SPIN_MAX * 4; ++it) {
    const u32x4 f = lds_rd128(a);
    if (__builtin_amdgcn_readfirstlane(min(min(f.x, f.y), min(f.z, f.w))) >= need) break;
    __builtin_amdgcn_s_sleep(S);
  }
}

DEV void dma4(rsrc_t r0, rsrc_t r1, LAS char* la, const unsigned (&v)[4], unsigned so) {
  __builtin_amdgcn_raw_ptr_buffer_load_lds(r0, (LAS void*)la, 16, v[0], so, 0, 0);
  __builtin_amdgcn_raw_ptr_buffer_load_lds(r0, (LAS void*)la, 16, v[1], so, 1024, 0);
  __builtin_amdgcn_raw_ptr_buffer_load_lds(r1, (LAS void*)la, 16, v[2], so, 2048, 0);
  __builtin_amdgcn_raw_ptr_buffer_load_lds(r1, (LAS void*)la, 16, v[3], so, 3072, 0);
}

__global__ __launch_bounds__(512, 2) void fused(const u16*, const u16*, const u16*, const u16*, const u16*, const u16*, const u16*, const u16*, const u16*, const u16*,
                                                const u16*, const u16*, const u16*, const u16*, char*, float*) {
#if __HIP_DEVICE_COMPILE__
  unsigned kw0, kw1;
  asm volatile("s_load_dword %0, %2, 0x0\n\ts_load_dword %1, %2, 0x40" : "=&s"(kw0), "=&s"(kw1) : "s"(__builtin_amdgcn_kernarg_segment_ptr()));
  extern __shared__ __attribute__((aligned(16))) char smem_[];
  LAS char* smem = (LAS char*)smem_;
  const int tid = threadIdx.x, hw = __builtin_amdgcn_readfirstlane(tid >> 6), wid = SWP ? hw ^ ((hw >> 1) & 2) : hw;
  const int bid = blockIdx.x, xcd = bid & 7, loc = bid >> 3;
  const int c = (xcd >> 1) * 4 + (loc >> 3), j = (xcd & 1) * 8 + (loc & 7);
  int lane, fr, fq;
  auto relane = [&]() {
    asm volatile("v_mbcnt_lo_u32_b32 %0, -1, 0\n\tv_mbcnt_hi_u32_b32 %0, -1, %0" : "=v"(lane));
    __builtin_assume(lane >= 0 && lane < 64);
    fr = lane & 15; fq = lane >> 4;
  };
  const int crot = CROT == 0 ? c + 1 : CROT == 1 ? 4 * c + (c >> 2) : CROT == 2 ? (c & 1) + 4 * (c & 2) + (c >> 2) * 2 : CROT == 3 ? 2 * c : (c & 1) * 2 + 4 * (c & 2) + (c >> 2);
  const int pown = (j - crot) & 15;
  auto upos = [&](int u) { return u == 0 ? pown : u - 1 + (u - 1 >= pown ? 1 : 0); };
  u64 pc0;
  unsigned l0, l1, l2;
  asm volatile("s_getpc_b64 %0\n\tLk_pc:\n\ts_mov_b32 %1, Lk_m-Lk_pc\n\ts_mov_b32 %2, Lk_l-Lk_pc\n\ts_mov_b32 %3, Lk_x-Lk_pc" : "=s"(pc0), "=s"(l0), "=s"(l1), "=s"(l2));
  auto codepull = [&]() {
    const unsigned po = min((unsigned)(loc * 16 + (lane & 15)) * 128u, max(l0, max(l1, l2)) - 4u);
    if (lane < 16) __builtin_amdgcn_global_load_lds((const __attribute__((address_space(1))) void*)(pc0 + po), (LAS void*)(smem + L_JNK), 4, 0, 0);
  };
  u32x2 p0, p1, p2, dv;
  u32x4 xv[2][2], wv[2], gv[2];
#define MLOADS(A_, B_) \
    asm volatile("global_load_dwordx2 %0, %12, %16\n\tglobal_load_dwordx2 %1, %12, %17\n\tglobal_load_dwordx2 %2, %12, %18\n\t" \
                 "global_load_dwordx4 %4, %13, %19\n\tglobal_load_dwordx4 %5, %13, %19 offset:1024\n\tglobal_load_dwordx4 %6, %13, %19 offset:2048\n\tglobal_load_dwordx4 %7, %13, %19 offset:3072\n\t" \
                 "global_load_dwordx4 %8, %14, %20\n\tglobal_load_dwordx4 %9, %14, %20 offset:1024\n\tglobal_load_dwordx4 %10, %14, %21\n\tglobal_load_dwordx4 %11, %14, %21 offset:1024\n\t" \
                 "global_load_dwordx2 %3, %15, %22" \
                 : "=&v"(p0), "=&v"(p1), "=&v"(p2), "=&v"(dv), "=&v"(xv[0][0]), "=&v"(xv[0][1]), "=&v"(xv[1][0]), "=&v"(xv[1][1]), "=&v"(wv[0]), "=&v"(wv[1]), "=&v"(gv[0]), "=&v"(gv[1]) \
                 : "v"((j * 64 + wid * 16 + 4 * fq) * 2), "v"((c * 128 + j * 8 + wid * 2) * 2048 + lane * 16), "v"(lane * 16), "v"((j * 64 + 4 * fr) * 2), \
                   "s"(A_[1]), "s"(A_[2]), "s"(A_[7]), "s"(A_[0]), "s"(B_[1]), "s"(B_[5]), "s"(B_[0]) : "memory")
  if (wid < 4) {
    u64x8 ta, tb;
    if (MTOP) asm volatile("s_load_dwordx16 %0, %2, 0x0\n\ts_load_dwordx16 %1, %2, 0x40" : "=&s"(ta), "=&s"(tb) : "s"(__builtin_amdgcn_kernarg_segment_ptr()));
    relane();
    if (wid == 0) codepull();
    if (MTOP) {
      asm volatile("s_waitcnt lgkmcnt(0)" : "+s"(ta), "+s"(tb));
      MLOADS(ta, tb);
    }
  }
  const int l = wid & 1;
#define FFNDEF() int b_ = bid; asm volatile("" : "+s"(b_)); \
  const bool ffn = b_ < 8 * NFW; \
  const int sg = (b_ & 7) * 7 + (b_ >> 5), kq = (b_ >> 3) & 3, sg1 = (sg & ~1) + l, gu1 = sg & 1; \
  const unsigned gub = W_GU + ((sg1 * 4 + kq) * 2 + gu1) * 2048, ypb = W_YP + (kq * 4 + l * 2) * 16 * 7168 + sg * 128
  if (__builtin_expect(wid >= 4, 1)) {
  u64x8 ka, kb;
  asm volatile("s_load_dwordx16 %0, %2, 0x0\n\ts_load_dwordx16 %1, %2, 0x40" : "=&s"(ka), "=&s"(kb) : "s"(__builtin_amdgcn_kernarg_segment_ptr()));
  relane();
  unsigned vo1[4];
#pragma unroll
  for (int e = 0; e < 4; ++e)
    vo1[e] = (l == 0 ? c * 128 + 8 * (lane >> 3) + 64 * (e & 1) + (e >> 1) : j * 64 + (lane >> 3) + 8 * (e & 1)) * 2048 + (((lane & 7) ^ (lane >> 3)) * 16) + 4096 - e * 1024;
  const unsigned s4 = (l == 0 ? 2 : 16) * 2048, so1 = wid >= 6 ? 2 * s4 : 0, la1 = l * 16384 + (wid >= 6 ? 8192 : 0);
  const int s0 = (KROT == 0 ? c + j : KROT == 1 ? (c >> 1) + (j >> 1) + 4 * (c >> 2) : KROT == 2 ? c + (j >> 1) + 4 * (j >> 3) : KROT == 3 ? 2 * c + j : KROT == 4 ? 4 * c + 2 * j + (c >> 2) : KROT == 5 ? (c >> 1) + j : 3 * c + j) & 15;
  unsigned sl0 = ((s0 + (s0 == j ? 1 : 0)) & 15) * 128 + so1;
  asm volatile("" : "+v"(vo1[0]), "+v"(vo1[1]), "+v"(vo1[2]), "+v"(vo1[3]) : "s"(sl0), "s"(s4), "s"(la1));
  asm volatile("s_waitcnt lgkmcnt(0)" : "+s"(ka), "+s"(kb) : "s"(kw0), "s"(kw1));
  const char *x = (const char*)ka[0], *Bre = (const char*)ka[3], *Bim = (const char*)ka[4], *Cre = (const char*)ka[5], *Cim = (const char*)ka[6], *w_gate = (const char*)kb[2],
             *w_up = (const char*)kb[3], *w_down = (const char*)kb[4];
  char* ws = (char*)kb[6];
  const rsrc_t r0 = mk((l == 0 ? x : Bre) - 4096, (l == 0 ? 1 << 22 : 1 << 21) + 4096), r1 = mk((l == 0 ? x : Bim) - 4096, (l == 0 ? 1 << 22 : 1 << 21) + 4096);
  auto issue = [&](unsigned sk, unsigned slot) { dma4(r0, r1, smem + slot + la1, vo1, sk); dma4(r0, r1, smem + slot + la1 + 4096, vo1, sk + s4); };
  issue(sl0, 0);
  __builtin_amdgcn_sched_barrier(0);
  const rsrc_t rws = mk(ws, W_END);
  const rsrc_t rx = mk(x, 2048 * 1024 * 2);
  u64 tbl;
  {
    const int n = (j - s0) & 15;
    const u64 seq = 0xfedcba9876543210ull, rot = s0 ? (seq >> (4 * s0)) | (seq << (64 - 4 * s0)) : seq, lo = (1ull << (4 * n)) - 1;
    tbl = (rot & lo) | ((rot >> 4) & ~lo) | ((u64)j << 60);
  }
  auto slice = [&](int k) { return ((unsigned)(tbl >> (4 * k)) & 15u) * 128u; };
  __builtin_amdgcn_sched_barrier(0);

  auto flag = [&](int a, unsigned v) { lds_wr32(lane == 0 ? (unsigned)a : (unsigned)(L_JNK + lane * 4), v); };
  auto flagmax = [&](int a, unsigned v) { asm volatile("ds_max_u32 %0, %1" ::"v"(lane == 0 ? (unsigned)a : (unsigned)(L_JNK + lane * 4)), "v"(v) : "memory"); };
  auto hpoll = [&](unsigned tagA, unsigned tagB) {
    lds_wait_min4(FL_FREE, 9u);
    __builtin_amdgcn_s_sleep(HDLY);
#pragma unroll 1
    for (int it = 0; it < SPIN_MAX; ++it) {
      FRESH();
      const u32x2 f = ld64c(rws, W_HF + (c * 16 + upos(lane >> 2)) * FSB + (lane & 3) * FSW);
      const u64 m = ~__ballot(lane < 4 * NS + 4 || (f.x == tagA && f.y == tagB));
      if (!m) break;
      flag(FL_C + 8, ((unsigned)__builtin_ctzll(m) >> 2) - 1u);
    }
    flag(FL_C + 8, 99u);
  };
    const rsrc_t rcr = mk(Cre, WIF == 4 ? 0 : 1 << 21), rci = mk(Cim, WIF == 4 || WIF == 5 ? 0 : 1 << 21);
    const unsigned ce = (j * 64 + 4 * (lane >> 3)) * 2048 + (((lane & 7) ^ (lane >> 4)) * 16);
    const unsigned co = (j * 64 + 32 + 4 * (lane >> 3)) * 2048 + (((lane & 7) ^ (4 + (lane >> 4))) * 16);
    auto cfill = [&](int u, int slot, int q0, int q1) {
      LAS char* dst = smem + slot * 16384;
#pragma unroll
      for (int q = q0; q < q1; ++q)
        __builtin_amdgcn_raw_ptr_buffer_load_lds(q < 8 ? rcr : rci, (LAS void*)(dst + q * 1024), 16, (q & 1) ? co : ce, ((q & 7) >> 1) * 2048 + ((upos(u) + crot) & 15) * 128, 0, 0);
    };
  if (__builtin_expect(wid == 4 || wid == 5, 1)) {
    __builtin_amdgcn_s_setprio(LPRIO);
    issue(slice(1), 32768);
    issue(slice(2), 65536);
    constexpr int K0 = 0;
    {
      u64 t = tbl >> (4 * K0 + 12);
#pragma unroll 1
      for (int k = K0; k < 13; ++k) {
        const unsigned sk = ((unsigned)t & 15u) << 7;
        waitv<16>();
        bar();
        issue(sk, ((k + 3) & 3) * 32768);
        if (k == CPK && l == 1) codepull();
        t >>= 4;
      }
    }
    waitv<16>(); bar();
    waitv<8>(); bar();
    const unsigned tagA = lds_rd32(L_TAG), tagB = tagA + 0x40000000u;
    waitv<0>(); bar();
    bar();
    lds_wait_ge(FL_W + l * 4, 1u);
    __builtin_amdgcn_s_setprio(1);
#pragma unroll 1
    for (int u = l; u < 8; u += 2) { if (LGT && (u >> 1) == LGT - 1) lds_wait_min4(FL_FREE, 1u); cfill(u, u, 0, 8); waitv<LTHR>(); cfill(u, u, 8, 16); waitv<LTHR>(); if (u >= 2) flag(FL_C + l * 4, (unsigned)(u - 1)); }
    u32x4 cq[3][16];
    auto creg = [&](int g) {
#pragma unroll
      for (int q = 0; q < 16; ++q) {
        const unsigned so = ((q & 7) >> 1) * 2048 + ((upos(10 + l + 2 * g) + crot) & 15) * 128;
        if (q < 8) asm volatile("buffer_load_dwordx4 %0, %1, %2, %3 offen" : "=v"(cq[g][q]) : "v"((q & 1) ? co : ce), "s"(rcr), "s"(so) : "memory");
        else asm volatile("buffer_load_dwordx4 %0, %1, %2, %3 offen" : "=v"(cq[g][q]) : "v"((q & 1) ? co : ce), "s"(rci), "s"(so) : "memory");
        if ((q & 7) == 7) waitv<LTHR>();
      }
    };
#pragma unroll
    for (int g = 0; g < 3; ++g) {
      if (g >= (l ? NRG1 : NRG0)) break;
      waitv<16>();
      if (g == 1) flag(FL_C + l * 4, (unsigned)(7 + l));
      creg(g);
    }
    waitv<0>();
    if ((l ? NRG1 : NRG0) < 2) flag(FL_C + l * 4, (unsigned)(7 + l));
    if (l == 0) {
      __builtin_amdgcn_s_setprio(3);
      hpoll(tagA, tagB);
    }
    else {
      lds_wait_min4(FL_CFREE, 1u);
      cfill(8, 0, 0, 16);
      cfill(9, 8, 0, 16);
      waitv<0>();
      flagmax(FL_C8, 1u);
      flag(FL_C + 4, 10u);
    }
    __builtin_amdgcn_s_setprio(0);
    if (l == 0) { lds_wait_ge(FL_C8, 1u); flag(FL_C, 9u); }
#pragma unroll
    for (int g = 0; g < 3; ++g) {
      asm volatile("" : "+v"(cq[g][0]), "+v"(cq[g][1]), "+v"(cq[g][2]), "+v"(cq[g][3]), "+v"(cq[g][4]), "+v"(cq[g][5]), "+v"(cq[g][6]), "+v"(cq[g][7]),
                   "+v"(cq[g][8]), "+v"(cq[g][9]), "+v"(cq[g][10]), "+v"(cq[g][11]), "+v"(cq[g][12]), "+v"(cq[g][13]), "+v"(cq[g][14]), "+v"(cq[g][15]));
      const int u = 10 + l + 2 * g;
      lds_wait_min4<LZ>(FL_CFREE, (unsigned)(u - 8));
      const int slot = u - 9;
      if (g < (l ? NRG1 : NRG0)) {
#pragma unroll
        for (int q = 0; q < 16; ++q)
          asm volatile("ds_write_b128 %0, %1 offset:%2" ::"v"(slot * 16384 + lane * 16), "v"(cq[g][q]), "n"(q * 1024) : "memory");
      } else {
#pragma unroll
        for (int q = 0; q < 16; q += 4) { cfill(u, slot, q, q + 4); if (CSL) __builtin_amdgcn_s_sleep(CSL); }
        waitv<0>();
      }
      flag(FL_C + l * 4, g == 2 ? 99u : (unsigned)(u + 1));
    }
    asm volatile("Lk_l:");
    return;
  }
  {
    const int xw = wid - 6;
    __builtin_amdgcn_s_setprio(XPRIO);
    u32x2 ep;
    unsigned tagA, tagB;
    if (xw == 0) lds_wr32(L_FLG + lane * 4, 0u);
    else asm volatile("ds_write_b64 %0, %1\n\tds_write_b64 %2, %3" ::"v"(L_CAND + lane * 8), "v"((u32x2){0u, 0u}), "v"(L_MAP + lane * 8), "v"((u32x2){L_ZERO, L_ZERO}) : "memory");
    FFNDEF();
    u32x4 w1[4][8], w2[2][8];
    const rsrc_t rw1 = mk(gu1 ? w_up : w_gate, ffn ? D_ * F_ * 2 : 0), r2 = mk(w_down, ffn ? D_ * F_ * 2 : 0);
    unsigned o1 = (kq * 256 + 32 * (fr >> 3) + 8 * fq) * (F_ * 2) + (sg1 * 64 + 8 * (fr & 7)) * 2;
    unsigned o2 = (sg * 64 + 32 * (fr >> 3) + 8 * fq) * 2048 + (kq * 256 + xw * 128 + 8 * (fr & 7)) * 2;
#define LDW1(b) { _Pragma("unroll") for (int i = 4 * ((b) & 1); i < 4 * ((b) & 1) + 4; ++i) \
      asm volatile("buffer_load_dwordx4 %0, %1, %2, %3 offen" WNT : "=v"(w1[(b) >> 1][i]) : "v"(o1), "s"(rw1), "s"((((b) >> 1) * 64 + i) * (F_ * 2)) : "memory"); }
#define LDW2(a, b) { _Pragma("unroll") for (int i = a; i < b; ++i) \
      asm volatile("buffer_load_dwordx4 %0, %1, %2, %3 offen" WNT : "=v"(w2[i >> 3][i & 7]) : "v"(o2), "s"(r2), "s"((i & 7) * 2048 + (i >> 3) * 128) : "memory"); }
#define TRN(w, n) { asm volatile("s_waitcnt vmcnt(%8)" : "+v"(w[0]), "+v"(w[1]), "+v"(w[2]), "+v"(w[3]), "+v"(w[4]), "+v"(w[5]), "+v"(w[6]), "+v"(w[7]) : "n"(n) : "memory"); \
      unit_trn(w); asm volatile("" : "+v"(w[0]), "+v"(w[1]), "+v"(w[2]), "+v"(w[3]), "+v"(w[4]), "+v"(w[5]), "+v"(w[6]), "+v"(w[7])); }
    unsigned key[16];
    unsigned rgo = W_R2 + l * 4096 + lane * 16, ggo = W_G2 + c * 512 + (lane & 31) * 16;
    u32x4 g[4], a;
    asm volatile("" : "+v"(o1), "+v"(o2), "+v"(rgo), "+v"(ggo) : "s"(rw1), "s"(r2));
    auto A_EP = [&]() { asm volatile("buffer_load_dwordx2 %0, off, %1, 0 sc1" : "=v"(ep) : "s"(rws) : "memory"); };
    auto A_TAG = [&]() {
      asm volatile("" : "+v"(ep));
      tagA = ep.x + 1u; tagB = tagA + 0x40000000u;
      if (xw == 0 && lane == 0) lds_wr32(L_TAG, tagA);
    };
    auto A_CLR = [&]() {
      asm volatile("s_nop 1\n\t"
                   "buffer_store_dwordx4 %0, %1, %3, 0 offen sc1\n\tbuffer_store_dwordx4 %0, %1, %3, 0 offen offset:1024 sc1\n\t"
                   "buffer_store_dwordx4 %0, %2, %3, 0 offen sc1\n\tbuffer_store_dwordx4 %0, %2, %3, %4 offen sc1\n\t"
                   "buffer_store_dwordx4 %0, %2, %3, %5 offen sc1\n\tbuffer_store_dwordx4 %0, %2, %3, %6 offen sc1\n\t"
                   "buffer_store_dwordx4 %0, %7, %3, %8 offen sc1\n\tbuffer_store_dwordx4 %0, %7, %3, %8 offen offset:1024 sc1\n\t"
                   "buffer_store_dwordx4 %0, %7, %3, %8 offen offset:2048 sc1\n\tbuffer_store_dwordx4 %0, %7, %3, %8 offen offset:3072 sc1\n\ts_nop 7"
                   ::"v"((u32x4){~0u, ~0u, ~0u, ~0u}), "v"(ffn ? gub + lane * 16 : 0xf0000000u), "v"(ffn ? ypb + (lane >> 3) * 7168 + (lane & 7) * 16 : 0xf0000000u), "s"(rws),
                   "s"(8 * 7168), "s"(16 * 7168), "s"(24 * 7168), "v"(NS ? lane * 16 : 0xf0000000u), "s"(W_H + c * 524288 + (j * 2 + xw) * 4096) : "memory");
    };
    auto A_PUB = [&]() {
      u32x2 rt;
      asm volatile("ds_read_b64 %0, %1\n\ts_waitcnt lgkmcnt(0)" : "=v"(rt) : "v"(L_RT + (lane & 1) * 16 + xw * 8) : "memory");
      st128c(rws, lane < 2 ? (lane ? W_G2 : W_R2) + (c * 128 + j * 8 + xw * 4) * 4 : 0xf0000000u, (u32x4){rt.x, tagA, rt.y, tagB});
    };
    auto A_GAT = [&]() {
#pragma unroll
      for (int q = 0; q < 4; ++q) asm volatile("buffer_load_dwordx4 %0, %1, %2, 0 offen offset:%3 sc1" : "=v"(g[q]) : "v"(rgo), "s"(rws), "n"(q * 1024) : "memory");
      asm volatile("buffer_load_dwordx4 %0, %1, %2, 0 offen sc1" : "=v"(a) : "v"(ggo), "s"(rws) : "memory");
    };
    static_assert(NS <= 3, "cleared blocks");
    A_EP();
    issue(slice(1) + so1, 32768);
    issue(slice(2) + so1, 65536);
    asm volatile("s_waitcnt vmcnt(17) lgkmcnt(0)" ::: "memory"); bar();
    issue(slice(3) + so1, 98304);
    waitv<24>(); A_TAG();
    A_CLR();
    asm volatile("s_waitcnt vmcnt(26) lgkmcnt(0)" ::: "memory"); bar();
    issue(slice(4) + so1, 0);
    asm volatile("s_waitcnt vmcnt(26) lgkmcnt(0)" ::: "memory"); bar();
    issue(slice(5) + so1, 32768);
    LDW2(0, 16)
    asm volatile("s_waitcnt vmcnt(42) lgkmcnt(0)" ::: "memory"); bar();
    issue(slice(6) + so1, 65536);
    waitv<40>(); A_PUB();
    asm volatile("s_waitcnt vmcnt(33) lgkmcnt(0)" ::: "memory"); bar();
    issue(slice(7) + so1, 98304);
    asm volatile("s_waitcnt vmcnt(33) lgkmcnt(0)" ::: "memory"); bar();
    issue(slice(8) + so1, 0);
    asm volatile("s_waitcnt vmcnt(17) lgkmcnt(0)" ::: "memory"); bar();
    issue(slice(9) + so1, 32768);
    asm volatile("s_waitcnt vmcnt(16) lgkmcnt(0)" ::: "memory"); bar();
    issue(slice(10) + so1, 65536);
    asm volatile("s_waitcnt vmcnt(16) lgkmcnt(0)" ::: "memory"); bar();
    issue(slice(11) + so1, 98304);
    A_GAT();
    asm volatile("s_waitcnt vmcnt(21) lgkmcnt(0)" ::: "memory"); bar();
    issue(slice(12) + so1, 0);
    asm volatile("s_waitcnt vmcnt(21) lgkmcnt(0)" ::: "memory"); bar();
    issue(slice(13) + so1, 32768);
    asm volatile("s_waitcnt vmcnt(21) lgkmcnt(0)" ::: "memory"); bar();
    issue(slice(14) + so1, 65536);
    constexpr int K0 = 12;
#pragma unroll
    for (int k = K0; k < 13; ++k) { waitv<16>(); bar(); issue(slice(k + 3) + so1, ((k + 3) & 3) * 32768); }
    asm volatile("" : "+v"(g[0]), "+v"(g[1]), "+v"(g[2]), "+v"(g[3]), "+v"(a));
    LDW1(0)
    waitv<20>(); bar(); LDW1(1)
    waitv<16>(); bar(); LDW1(2)
    waitv<12>(); bar(); LDW1(3)
    bar();
    __builtin_amdgcn_s_setprio(XPRIO2);
#define W1B() do { if (XGT == 1) lds_wait_min4(FL_FREE, 1u); if (XGT == 2) lds_wait_min4(FL_HS, 1u); if (XGT == 3) lds_wait_ge(FL_HS, 1u); \
      _Pragma("unroll") for (int b_ = W1N; b_ < 8; ++b_) LDW1(b_) } while (0)
#pragma unroll
    for (int b_ = 4; b_ < W1N; ++b_) LDW1(b_)
    if (W2P == 10) LDW2(0, 16)
    if (lane == 0) lds_wr32(FL_W + xw * 4, 1u);
    {
#pragma unroll 1
      for (int it = 0; it < SPIN_MAX; ++it) {
        bool ok = true;
#pragma unroll
        for (int q = 0; q < 4; ++q) ok = ok && g[q].y == tagA && g[q].w == tagB;
        if (__all(ok && a.y == tagA && a.w == tagB)) break;
        __builtin_amdgcn_s_sleep(8);
        FRESH();
#pragma unroll
        for (int q = 0; q < 4; ++q) g[q] = ld128c(rws, rgo + q * 1024);
        a = ld128c(rws, ggo);
      }
      if (l == 0) {
        const unsigned gp = lane < 32 ? a.x : a.z, ga = L_GT + ((4 * (lane & 1) + 2 * (lane >> 5)) * 16 + ((lane & 31) >> 1)) * 4;
        lds_wr32(ga, gp << 16);
        lds_wr32(ga + 64, gp & 0xffff0000u);
      }
      asm volatile("ds_add_u32 %0, %1" ::"v"(lane == 0 ? (unsigned)FL_G : (unsigned)(L_JNK + lane * 4)), "v"(1u) : "memory");
      if (W1P == 1) W1B();
#pragma unroll
      for (int q = 0; q < 4; ++q) {
        const unsigned r0 = 2047 - (l * 1024 + q * 256 + lane * 4);
        key[4 * q] = (g[q].x << 16) + r0; key[4 * q + 1] = (g[q].x & 0xffff0000u) + (r0 - 1);
        key[4 * q + 2] = (g[q].z << 16) + (r0 - 2); key[4 * q + 3] = (g[q].z & 0xffff0000u) + (r0 - 3);
        asm volatile("" : "+v"(key[4 * q]), "+v"(key[4 * q + 1]), "+v"(key[4 * q + 2]), "+v"(key[4 * q + 3]));
      }
      unsigned m = key[0], m2 = 0, m3 = 0;
#pragma unroll
      for (int i = 1; i < 16; ++i) {
        asm("v_med3_u32 %0, %1, %0, %3\n\tv_med3_u32 %1, %2, %1, %3\n\tv_max_u32 %2, %2, %3" : "+v"(m3), "+v"(m2), "+v"(m) : "v"(key[i]));
      }
      asm volatile("" : "+v"(m), "+v"(m2), "+v"(m3));
      unsigned T = 0;
#pragma unroll
      for (int b = 31; b >= 16; --b) {
        const unsigned t2 = T | (1u << b);
        T = __popcll(__ballot(m >= t2)) >= 8 ? t2 : T;
      }
      asm volatile("" : "+s"(T));
      const u64 k1 = __ballot(m >= T), k2 = __ballot(m2 >= T);
      const int n1 = __popcll(k1);
      if (__ballot(m3 >= T) || n1 + __popcll(k2) > 16) {
        unsigned cand = 0;
#pragma unroll 1
        for (int s = 0; s < 8; ++s) {
          unsigned mm = key[0];
#pragma unroll
          for (int i = 1; i < 16; ++i) mm = max(mm, key[i]);
          mm = wave_maxu(mm);
          cand = lane == s ? mm : cand;
#pragma unroll
          for (int i = 0; i < 16; ++i) key[i] = key[i] == mm ? 0u : key[i];
        }
        if (lane < 8) lds_wr32(L_CAND + l * 64 + lane * 4, cand);
      } else {
        const unsigned p1 = __builtin_amdgcn_mbcnt_hi((unsigned)(k1 >> 32), __builtin_amdgcn_mbcnt_lo((unsigned)k1, 0u));
        const unsigned p2 = __builtin_amdgcn_mbcnt_hi((unsigned)(k2 >> 32), __builtin_amdgcn_mbcnt_lo((unsigned)k2, (unsigned)n1));
        lds_wr32(m >= T ? L_CAND + l * 64 + p1 * 4 : L_JNK + lane * 4, m);
        if (k2) lds_wr32(m2 >= T ? L_CAND + l * 64 + p2 * 4 : L_JNK + lane * 4, m2);
      }
      if (lane == 0) lds_wr32(FL_TK + 8 + l * 4, 1u);
      if (W1P == 2) W1B();
      lds_wait_ge(FL_TK + 8 + (l ^ 1) * 4, 1u);
      const unsigned ck = *(const volatile LAS unsigned*)(smem + L_CAND + (lane & 31) * 4);
      int rank = 0;
#pragma unroll
      for (int i = 0; i < 4; ++i) {
        const u32x4 o = *(const volatile LAS u32x4*)(smem + L_CAND + (lane >> 5) * 64 + i * 16);
        rank += (o.x > ck) + (o.y > ck) + (o.z > ck) + (o.w > ck);
      }
      { auto p = __builtin_amdgcn_permlane32_swap((unsigned)rank, (unsigned)rank, false, false); rank = (int)(p[0] + p[1]); }
      if (l == 0) {
        if (lane < 32 && ck != 0u && rank < 8) {
          const int t = 2047 - (int)(ck & 0xffffu);
          lds_wr32(L_SEL + rank * 4, ck);
          if ((t >> 7) == c) lds_wr32(L_MAP + ((t & 7) * 16 + ((t & 127) >> 3)) * 4, (unsigned)(L_BROW + rank * 256));
        }
        const unsigned any = __ballot(ck != 0u && rank < 8 && ((2047 - (int)(ck & 0xffffu)) >> 7) == c) ? 2u : 1u;
        flag(FL_SEL, any);
        if (bid == 0 && lane == 0) st64c(rws, W_EPOCH, (u32x2){tagA, tagB});
      }
    }
    if (W1P == 3) W1B();
    lds_wait_ge(FL_SEL, 1u);
    const unsigned mine = lds_rd32(L_SEL + (lane & 7) * 4);
    {
      {
        u32x4 xa[4];
#pragma unroll
        for (int u = 0; u < 4; ++u)
          asm volatile("buffer_load_dwordx4 %0, %1, %2, 0 offen offset:%3" : "=v"(xa[u]) : "v"((2047 - (mine & 0xffffu)) * 2048 + (kq * 256 + 32 * (fr >> 3) + 8 * fq) * 2), "s"(rx), "n"(u * 128) : "memory");
        TRN(w1[0], W2P == 10 ? 36 : 20) TRN(w1[1], W2P == 10 ? 36 : 20) TRN(w1[2], W2P == 10 ? 28 : 12) TRN(w1[3], 0)
        asm volatile("" : "+v"(xa[0]), "+v"(xa[1]), "+v"(xa[2]), "+v"(xa[3]));
        const bool val = (fq >> 1) == (fr >> 3), lo = lane < 32;
        auto publish = [&](f32x4 (&d)[8], unsigned o, unsigned sp) {
          u32x4 pv[4];
#pragma unroll
          for (int rr = 0; rr < 2; ++rr)
#pragma unroll
            for (int q = 0; q < 8; ++q) {
              auto p = __builtin_amdgcn_permlane32_swap(__float_as_uint(d[q][rr]), __float_as_uint(d[q][2 + rr]), false, false);
              const unsigned own = lo ? p[0] : p[1], oth = lo ? p[1] : p[0];
              pv[rr * 2 + (q >> 2)][q & 3] = min(__float_as_uint(__uint_as_float(own) + dpp1<0x128>(__uint_as_float(oth))), 0xfffffffeu);
            }
          asm volatile("s_nop 4\n\t"
                       "buffer_store_dwordx4 %0, %4, %5, 0 offen sc1\n\tbuffer_store_dwordx4 %1, %4, %5, %6 offen sc1\n\t"
                       "buffer_store_dwordx4 %2, %4, %5, %7 offen sc1\n\tbuffer_store_dwordx4 %3, %4, %5, %8 offen sc1\n\t"
                       "s_nop 7\n\ts_nop 7"
                       ::"v"(pv[0]), "v"(pv[1]), "v"(pv[2]), "v"(pv[3]), "v"(val && ffn ? o : 0xf0000000u), "s"(rws), "s"(sp), "s"(2 * sp), "s"(3 * sp) : "memory");
        };
        f32x4 d[8];
#pragma unroll
        for (int q = 0; q < 8; ++q) d[q] = (f32x4){0.f, 0.f, 0.f, 0.f};
#pragma unroll
        for (int u = 0; u < 4; ++u)
#pragma unroll
          for (int q = 0; q < 8; ++q) d[q] = __builtin_amdgcn_mfma_f32_16x16x32_bf16(__builtin_bit_cast(bf16x8, xa[u]), __builtin_bit_cast(bf16x8, w1[u][q]), d[q], 0, 0, 0);
        publish(d, gub + (16 * (fr >> 3) + 8 * (fq & 1) + (fr & 7)) * 16, 512);
        if (W2P < 11) { TRN(w2[0], 63) TRN(w2[1], 63) }
        lds_wait_ge<LZ>(FL_CFREE, XG2);
        __builtin_amdgcn_s_setprio(XP3);
        if (W2P == 11) LDW2(0, 16)
        if (ffn) {
          f32x4 G, U;
#pragma unroll 1
          for (int it = 0; it < SPIN_MAX; ++it) {
            FRESH();
            u32x4 a_[8];
#pragma unroll
            for (int k = 0; k < 8; ++k) a_[k] = __builtin_amdgcn_raw_buffer_load_b128(rws, W_GU + sg * 16384 + xw * 1024 + lane * 16, k * 2048, 16);
            unsigned m = 0;
#pragma unroll
            for (int k = 0; k < 8; ++k) m = max(max(m, max(a_[k].x, a_[k].y)), max(a_[k].z, a_[k].w));
            G = (__builtin_bit_cast(f32x4, a_[0]) + __builtin_bit_cast(f32x4, a_[2])) + (__builtin_bit_cast(f32x4, a_[4]) + __builtin_bit_cast(f32x4, a_[6]));
            U = (__builtin_bit_cast(f32x4, a_[1]) + __builtin_bit_cast(f32x4, a_[3])) + (__builtin_bit_cast(f32x4, a_[5]) + __builtin_bit_cast(f32x4, a_[7]));
            if (__all(m != ~0u)) break;
            __builtin_amdgcn_s_sleep(8);
          }
          if (W2P == 12) { lds_wait_ge<LZ>(FL_CFREE, W2G + W2S * (kq + 4 * xw) / 8); LDW2(0, 16) }
          float a4[4];
#pragma unroll
          for (int e = 0; e < 4; ++e) { const float g = rbf(G[e]); a4[e] = rbf(g * __builtin_amdgcn_rcpf(1.f + __expf(-g))) * rbf(U[e]); }
          asm volatile("s_nop 0\n\tds_write_b64 %0, %1" ::"v"(L_ACT + (4 * ((lane >> 3) & 1) + 2 * ((lane >> 4) & 1) + xw) * 128 + (lane & 7) * 16 + (lane >> 5) * 8),
                       "v"((u32x2){pk2(a4[0], a4[1]), pk2(a4[2], a4[3])}) : "memory");
          if (lane == 0) lds_wr32(FL_TK + xw * 4, 1u);
          lds_wait_ge(FL_TK + (xw ^ 1) * 4, 1u);
        }
        if (W2P >= 11) { TRN(w2[0], 8) TRN(w2[1], 0) }
        const u32x4 af = lds_rd128(L_ACT + ((fr & 7) * 8 + 4 * (fr >> 3) + fq) * 16);
#pragma unroll
        for (int u = 0; u < 2; ++u) {
#pragma unroll
          for (int q = 0; q < 8; ++q) d[q] = __builtin_amdgcn_mfma_f32_16x16x32_bf16(__builtin_bit_cast(bf16x8, af), __builtin_bit_cast(bf16x8, w2[u][q]), (f32x4){0.f, 0.f, 0.f, 0.f}, 0, 0, 0);
          publish(d, ypb + (u * 16 + 2 * (fr >> 3) + (fq & 1)) * 7168 + (fr & 7) * 16, 4 * 7168);
        }
      }
    }
    __builtin_amdgcn_s_setprio(XP4);
    unsigned rest = (unsigned)__ballot(((2047 - (int)(mine & 0xffffu)) >> 7) == c) & 0xffu;
    if (xw) rest &= rest - 1;
#pragma unroll 1
    for (; rest; rest &= rest - 1, rest &= rest - 1) {
      const int s = __builtin_ctz(rest);
      const unsigned ks = (unsigned)__builtin_amdgcn_readlane((int)mine, s);
      const int t = 2047 - (int)(ks & 0xffffu);
      const float rs = rbf(1.f / (1.f + __expf(-key_val(ks))));
      const unsigned yb = W_YP + (j * 16 + (s & 1) * 8 + 2 * ((s >> 1) & 1) + (s >> 2)) * 7168;
      const u32x2 xr = __builtin_amdgcn_raw_buffer_load_b64(rx, t * 2048 + (j * 64 + 8 * (lane & 7) + 4 * ((lane >> 3) & 1)) * 2, 0, 0);
      lds_wait_ge<LZ>(FL_CFREE, YGATE);
      const float omg = rbf(1.f - __uint_as_float(lds_rd32(L_GT + ((t & 7) * 16 + ((t & 127) >> 3)) * 4)));
      f32x4 y[2];
#pragma unroll 1
      for (int it = 0; it < SPIN_MAX; ++it) {
        FRESH();
        unsigned m = 0;
#pragma unroll
        for (int h = 0; h < 2; ++h) {
          y[h] = (f32x4){0.f, 0.f, 0.f, 0.f};
#pragma unroll
          for (int k = 0; k < 7; ++k) {
            const u32x4 a = __builtin_amdgcn_raw_buffer_load_b128(rws, yb + lane * 16, h * 4 * 7168 + k * 1024, 16);
            m = max(max(m, max(a.x, a.y)), max(a.z, a.w));
            y[h] += __builtin_bit_cast(f32x4, a);
          }
        }
        if (__all(m != ~0u)) break;
        __builtin_amdgcn_s_sleep(6);
      }
      const bool o8 = lane & 8;
      const f32x4 xf = unpk4(xr);
      f32x4 dl;
#pragma unroll
      for (int e = 0; e < 4; ++e) {
        const float v = sum16_32((o8 ? y[1][e] : y[0][e]) + dpp1<0x128>(o8 ? y[0][e] : y[1][e]));
        dl[e] = rbf(mul1(omg, rbf(xf[e] + rbf(mul1(rbf(v), rs))))) - rbf(mul1(omg, xf[e]));
      }
      if (lane < 16) asm volatile("s_nop 0\n\tds_write_b128 %0, %1\n\ts_nop 3" ::"v"(L_BROW + s * 256 + (lane & 7) * 32 + (lane >> 3) * 16), "v"(dl) : "memory");
    }
    if (lane == 0) lds_wr32(FL_XD + xw * 4, 1u);
    asm volatile("Lk_x:");
    return;
  }
  }

  __builtin_amdgcn_s_setprio(3);
  relane();
  u64x8 ma, mb;
  asm volatile("s_load_dwordx16 %0, %2, 0x0\n\ts_load_dwordx16 %1, %2, 0x40\n\ts_waitcnt lgkmcnt(0)" : "=&s"(ma), "=&s"(mb) : "s"(__builtin_amdgcn_kernarg_segment_ptr()), "s"(kw0), "s"(kw1));
  const char* out = (const char*)mb[7];
  char* ws = (char*)mb[6];
  const rsrc_t rws = mk(ws, W_END);
  if (!MTOP) MLOADS(ma, mb);
  f32x4 lr, li, cr, ci, Er, Ei, Fr, Fi, br_, bi_;
  u32x2 xe[2][4];
  {
    asm volatile("s_waitcnt vmcnt(%3)" : "+v"(p0), "+v"(p1), "+v"(p2) : "n"(9) : "memory");
    const f32x4 lre = unpk4(p0), lim = unpk4(p1), ld = unpk4(p2) * splat4(1.44269504f);
    f32x4 dt, id = fma4(lre, lre, lim * lim);
#pragma unroll
    for (int r = 0; r < 4; ++r) { dt[r] = rbf(__builtin_amdgcn_exp2f(ld[r])); id[r] = __builtin_amdgcn_rcpf(id[r]); }
    const f32x4 A2 = dt * lre * splat4(1.44269504f), Bv = dt * lim * splat4(0.159154943f);
    cexp4(A2, Bv, lr, li);
    const f32x4 nr = lr - splat4(1.f);
    cr = fma4(nr, lre, li * lim) * id; ci = fma4(li, lre, nml4(nr, lim)) * id;
    const f32x4 ne = splat4((float)(8 * fr)), nf = splat4((float)((fr & 7) + 1));
    cexp4(A2 * ne, Bv * ne, Er, Ei);
    f32x4 tr, ti;
    cexp4(A2 * nf, Bv * nf, tr, ti);
    Fr = fma4(cr, tr, nml4(ci, ti)); Fi = fma4(cr, ti, ci * tr);
    const float a1 = (fr & 2) ? ((fr & 1) ? A2.w : A2.z) : ((fr & 1) ? A2.y : A2.x), b1 = (fr & 2) ? ((fr & 1) ? Bv.w : Bv.z) : ((fr & 1) ? Bv.y : Bv.x);
    const int e0 = c - 1 - (fr >> 2);
    const f32x4 nl = {(float)(128 * max(e0, 0)), (float)(128 * max(e0 - 4, 0)), (float)(128 * max(e0 - 8, 0)), (float)(128 * max(e0 - 12, 0))};
    cexp4(splat4(a1) * nl, splat4(b1) * nl, br_, bi_);
    asm volatile("" : "+v"(lr), "+v"(li), "+v"(cr), "+v"(ci), "+v"(Er), "+v"(Ei), "+v"(Fr), "+v"(Fi), "+v"(br_), "+v"(bi_));
  }
  asm volatile("s_waitcnt vmcnt(%9)" : "+v"(dv), "+v"(xv[0][0]), "+v"(xv[0][1]), "+v"(xv[1][0]), "+v"(xv[1][1]), "+v"(wv[0]), "+v"(wv[1]), "+v"(gv[0]), "+v"(gv[1]) : "n"(0) : "memory");
  {
    float sv[4];
#pragma unroll
    for (int r = 0; r < 2; ++r) {
      float sr = 0.f, sg_ = 0.f;
#pragma unroll
      for (int h = 0; h < 2; ++h)
#pragma unroll
        for (int e = 0; e < 4; ++e) {
          const unsigned xx = xv[r][h][e], ww = wv[h][e], gw = gv[h][e];
          sr = __builtin_amdgcn_fdot2_f32_bf16(__builtin_bit_cast(bf16x2_t, xx), __builtin_bit_cast(bf16x2_t, ww), sr, false);
          sg_ = __builtin_amdgcn_fdot2_f32_bf16(__builtin_bit_cast(bf16x2_t, xx), __builtin_bit_cast(bf16x2_t, gw), sg_, false);
        }
      sv[r] = sr; sv[2 + r] = sg_;
    }
    const bool b1 = lane & 1, b2 = lane & 2;
    const float k0 = (b1 ? sv[1] : sv[0]) + dpp1<0xb1>(b1 ? sv[0] : sv[1]), k1 = (b1 ? sv[3] : sv[2]) + dpp1<0xb1>(b1 ? sv[2] : sv[3]);
    float k2 = (b2 ? k1 : k0) + dpp1<0x4e>(b2 ? k0 : k1);
    k2 += dpp1<0x124>(k2);
    k2 += dpp1<0x128>(k2);
    k2 = sum16_32(k2);
    const unsigned vb = b2 ? f2bf(1.f / (1.f + __expf(-rbf(k2)))) : route_key(rbf(k2), 0) >> 16;
    const unsigned p01 = vb | ((unsigned)__builtin_amdgcn_update_dpp(0, (int)vb, 0xb1, 0xf, 0xf, true) << 16);
    asm volatile("ds_write_b32 %0, %1\n\ts_waitcnt lgkmcnt(0)" ::"v"((lane & 61) == 0 ? (unsigned)(L_RT + (lane * 2 + wid) * 4) : (unsigned)(L_JNK + lane * 4)), "v"(p01) : "memory");
  }
  __builtin_amdgcn_s_setprio(2);
  unsigned tagA = L_TAG;
  f32x4 acc[2][8];
  const unsigned ago = fr < 4 ? W_AGG + ((j * 16 + c) * 64 + wid * 16 + 4 * fq + fr) * 16u : 0xf0000000u;
  {
#pragma unroll
    for (int m = 0; m < 2; ++m)
#pragma unroll
      for (int n = 0; n < 8; ++n) acc[m][n] = (f32x4){0.f, 0.f, 0.f, 0.f};
    const int x0 = (fq ^ (fr & 7)) * 16, x1 = ((4 + fq) ^ (fr & 7)) * 16;
    unsigned pa0 = 16384 + (wid * 32 + fr) * 128 + x0, pa1 = 16384 + (wid * 32 + fr) * 128 + x1, pb0 = fr * 128 + x0, pb1 = fr * 128 + x1;
    unsigned sit, xad[4];
#pragma unroll
    for (int r = 0; r < 4; ++r) xad[r] = 98304 + (32 * wid + 4 * fq + r) * 128 + (((fr >> 1) ^ (4 * (fq & 1) + r)) * 16) + (fr & 1) * 8;
    bf16x8 fa[2][2], fb[8];
    asm volatile(ASM_GEMM1 : [c00] "+v"(acc[0][0]), [c01] "+v"(acc[0][1]), [c02] "+v"(acc[0][2]), [c03] "+v"(acc[0][3]), [c04] "+v"(acc[0][4]), [c05] "+v"(acc[0][5]), [c06] "+v"(acc[0][6]), [c07] "+v"(acc[0][7]), [c10] "+v"(acc[1][0]), [c11] "+v"(acc[1][1]), [c12] "+v"(acc[1][2]), [c13] "+v"(acc[1][3]), [c14] "+v"(acc[1][4]), [c15] "+v"(acc[1][5]), [c16] "+v"(acc[1][6]), [c17] "+v"(acc[1][7]), [a00] "=&v"(fa[0][0]), [a01] "=&v"(fa[0][1]), [a10] "=&v"(fa[1][0]), [a11] "=&v"(fa[1][1]), [b0] "=&v"(fb[0]), [b1] "=&v"(fb[1]), [b2] "=&v"(fb[2]), [b3] "=&v"(fb[3]), [b4] "=&v"(fb[4]), [b5] "=&v"(fb[5]), [b6] "=&v"(fb[6]), [b7] "=&v"(fb[7]), [pa0] "+v"(pa0), [pa1] "+v"(pa1), [pb0] "+v"(pb0), [pb1] "+v"(pb1), [it] "=&s"(sit), [tg] "+v"(tagA), [xe00] "=&v"(xe[0][0]), [xe01] "=&v"(xe[0][1]), [xe02] "=&v"(xe[0][2]), [xe03] "=&v"(xe[0][3]), [xe10] "=&v"(xe[1][0]), [xe11] "=&v"(xe[1][1]), [xe12] "=&v"(xe[1][2]), [xe13] "=&v"(xe[1][3]) : [xa0] "v"(xad[0]), [xa1] "v"(xad[1]), [xa2] "v"(xad[2]), [xa3] "v"(xad[3]) : "memory", "scc", "v250", "v251");
  }
  __builtin_amdgcn_s_setprio(3);
  const unsigned tagB = tagA + 0x40000000u;
  {
#pragma unroll
    for (int n = 1; n < 8; ++n) {
      const f32x4 pr = acc[0][n - 1], pi = acc[1][n - 1];
      acc[0][n] = fma4(lr, pr, fnm4(li, pi, acc[0][n]));
      acc[1][n] = fma4(lr, pi, fma4(li, pr, acc[1][n]));
    }
    const f32x4 wr = dpp4<0x140>(Er), wi = dpp4<0x140>(Ei);
    const f32x4 zr = fma4(wr, acc[0][7], nml4(wi, acc[1][7])), zi = fma4(wr, acc[1][7], wi * acc[0][7]);
    const bool o1 = fr & 1, o2 = fr & 2;
    const float r01 = (o1 ? zr.y : zr.x) + dpp1<0xb1>(o1 ? zr.x : zr.y), r23 = (o1 ? zr.w : zr.z) + dpp1<0xb1>(o1 ? zr.z : zr.w);
    const float i01 = (o1 ? zi.y : zi.x) + dpp1<0xb1>(o1 ? zi.x : zi.y), i23 = (o1 ? zi.w : zi.z) + dpp1<0xb1>(o1 ? zi.z : zi.w);
    float sr = (o2 ? r23 : r01) + dpp1<0x4e>(o2 ? r01 : r23), si = (o2 ? i23 : i01) + dpp1<0x4e>(o2 ? i01 : i23);
    sr += dpp1<0x124>(sr); si += dpp1<0x124>(si);
    sr += dpp1<0x128>(sr); si += dpp1<0x128>(si);
    u32x4 av = {__float_as_uint(sr), tagA, __float_as_uint(si), tagB};
    asm volatile("s_nop 1\n\tbuffer_store_dwordx4 %0, %1, %2, 0 offen sc1\n\ts_nop 7" : "+v"(av) : "v"(ago), "s"(rws) : "memory");
  }
  unsigned ao[4];
  u32x4 g[4];
#pragma unroll
  for (int i = 0; i < 4; ++i) {
    ao[i] = (fr >> 2) + 4 * i < c ? W_AGG + ((j * 16 + (fr >> 2) + 4 * i) * 64 + wid * 16 + 4 * fq + (fr & 3)) * 16u : 0xf0000000u;
    g[i] = (u32x4){0u, 0u, 0u, 0u};
  }
  if (POLLN < 0) {
    FRESH();
#pragma unroll
    for (int i = 0; i < 4; ++i)
      if (4 * i < c) g[i] = ld128c(rws, ao[i]);
    FRESH();
  }
  f32x4 Pr, Pi, qr[8], qi[8];
  {
    f32x4 yr = acc[0][7], yi = acc[1][7];
#define KS_STEP(S, CTRL) { const f32x4 mr = dpp4<0x150 + S>(Er), mi = dpp4<0x150 + S>(Ei); const f32x4 sr = dpp4<CTRL>(yr), si = dpp4<CTRL>(yi); \
      yr = fma4(mr, sr, fnm4(mi, si, yr)); yi = fma4(mr, si, fma4(mi, sr, yi)); }
    KS_STEP(1, 0x111) KS_STEP(2, 0x112) KS_STEP(4, 0x114) KS_STEP(8, 0x118)
    Pr = dpp4<0x111>(yr); Pi = dpp4<0x111>(yi);
    asm volatile("" : "+v"(Pr), "+v"(Pi));
#pragma unroll
    for (int n = 0; n < 8; ++n) {
      if (n == POLLN) {
        FRESH();
#pragma unroll
        for (int i = 0; i < 4; ++i)
          if (4 * i < c) g[i] = ld128c(rws, ao[i]);
        FRESH();
      }
      bcast8(n, Fr, qr[n]); bcast8(n, Fi, qi[n]);
      const f32x4 br = fma4(cr, acc[0][n], nml4(ci, acc[1][n])), bi = fnm4(cr, acc[1][n], nml4(ci, acc[0][n]));
      acc[0][n] = br; acc[1][n] = bi;
      asm volatile("" : "+v"(acc[0][n]), "+v"(acc[1][n]), "+v"(qr[n]), "+v"(qi[n]));
    }
  }
  f32x4 Xr = Pr, Xi = Pi;
  if (POLLN == 8) {
    FRESH();
#pragma unroll
    for (int i = 0; i < 4; ++i)
      if (4 * i < c) g[i] = ld128c(rws, ao[i]);
    FRESH();
  }
  u32x2 xd[2][4];
  lds_wait_ge(FL_G, 2u);
  {
    const f32x4 df = unpk4(dv) * splat4(0.5f);
#pragma unroll
    for (int m = 0; m < 2; ++m) {
      const f32x4 ge = __builtin_bit_cast(f32x4, lds_rd128(L_GT + ((2 * wid + m) * 16 + 4 * fq) * 4));
#pragma unroll
      for (int r = 0; r < 4; ++r) {
        const f32x4 xf = unpk4(xe[m][r]), p = xf * df, o = xf * splat4(rbf(1.f - ge[r]));
        xd[m][r] = (u32x2){pk2(p.x, p.y), pk2(p.z, p.w)};
        xe[m][r] = (u32x2){pk2(o.x, o.y), pk2(o.z, o.w)};
        asm volatile("" : "+v"(xd[m][r]), "+v"(xe[m][r]));
      }
    }
  }
  if (POLLN > 9) __builtin_amdgcn_s_sleep(POLLN > 9 ? POLLN - 9 : 0);
  if (c > 0) {
#pragma unroll 1
    for (int it = 0; it < SPIN_MAX; ++it) {
      bool ok = true;
#pragma unroll
      for (int i = 0; i < 4; ++i) ok = ok && (ao[i] == 0xf0000000u || (g[i].y == tagA && g[i].w == tagB));
      if (__all(ok) || WIF == 6) break;
      FRESH();
#pragma unroll
      for (int i = 0; i < 4; ++i)
        if (4 * i < c) g[i] = ld128c(rws, ao[i]);
    }
    const f32x4 sx = {__uint_as_float(g[0].x), __uint_as_float(g[1].x), __uint_as_float(g[2].x), __uint_as_float(g[3].x)};
    const f32x4 sy = {__uint_as_float(g[0].z), __uint_as_float(g[1].z), __uint_as_float(g[2].z), __uint_as_float(g[3].z)};
    const f32x4 tx = fma4(br_, sx, nml4(bi_, sy)), ty = fma4(br_, sy, bi_ * sx);
    float gx = (tx.x + tx.z) + (tx.y + tx.w), gy = (ty.x + ty.z) + (ty.y + ty.w);
    gx += dpp1<0x124>(gx); gy += dpp1<0x124>(gy);
    gx += dpp1<0x128>(gx); gy += dpp1<0x128>(gy);
    const f32x4 Gr = {dpp1<0x00>(gx), dpp1<0x55>(gx), dpp1<0xaa>(gx), dpp1<0xff>(gx)}, Gi = {dpp1<0x00>(gy), dpp1<0x55>(gy), dpp1<0xaa>(gy), dpp1<0xff>(gy)};
    Xr = fma4(Er, Gr, fnm4(Ei, Gi, Pr)); Xi = fma4(Er, Gi, fma4(Ei, Gr, Pi));
  }
  relane();
  const unsigned jnk = L_JNK + lane * 4;
  const unsigned ho = WIF == 3 ? 0xf0000000u : W_H + c * 524288 + (4 * pown + wid) * 8192 + lane * 16;
  u32x4 hv[8];
  __builtin_amdgcn_sched_barrier(0);
#define HST(t_) asm volatile("s_nop 1\n\tbuffer_store_dwordx4 %0, %1, %2, %3 offen offset:%4 sc1" : "+v"(hv[t_]) : "v"(ho), "s"(rws), "s"(((t_) >> 2) * 4096), "n"(((t_) & 3) * 1024) : "memory")
#pragma unroll
  for (int n = 0; n < 8; ++n) {
    const f32x4 hr = fma4(qr[n], Xr, fnm4(qi[n], Xi, acc[0][n])), hi = fnm4(qr[n], Xi, fnm4(qi[n], Xr, acc[1][n]));
    const auto p = __builtin_amdgcn_permlane16_swap(pk2(hr.x, hr.y), pk2(hi.x, hi.y), false, false);
    const auto q = __builtin_amdgcn_permlane16_swap(pk2(hr.z, hr.w), pk2(hi.z, hi.w), false, false);
    hv[n] = (u32x4){NS ? min(p[0], 0xfffffffeu) : p[0], q[0], NS ? min(p[1], 0xfffffffeu) : p[1], q[1]};
    if (NSI == 8) HST(n); else if (NSI == 4 ? (n & 1) : n < NSI) HST(NSI == 4 ? n >> 1 : n);
    __builtin_amdgcn_sched_barrier(0);
    if (!DSL && (n >> 1) != wid) asm volatile("ds_write_b128 %0, %1" ::"v"(L_XH + (((n >> 1) * 3 + wid - (wid > (n >> 1) ? 1 : 0)) * 2 + (n & 1)) * 1024 + lane * 16), "v"(hv[n]) : "memory");
  }
  if (DSL) {
#pragma unroll
    for (int w = 0; w < 4; ++w)
      if (w != wid) asm volatile("ds_write_b128 %0, %1\n\tds_write_b128 %0, %2 offset:1024" ::"v"(L_XH + (w * 3 + wid - (wid > w ? 1 : 0)) * 2048 + lane * 16), "v"(hv[2 * w]), "v"(hv[2 * w + 1]) : "memory");
  }
  if (lane == 0) lds_wr32(FL_HS + wid * 4, 1u);
  const rsrc_t rh = mk(ws + W_H + c * 524288, 524288);
  const unsigned vo = wid * 2048 + lane * 16;
  u32x32 hw0, hw1;
  unsigned st[12], pc[4];
  f32x4 ac2[2][4];
#define ACK() do { asm volatile("s_waitcnt vmcnt(0)" : "+v"(hv[0]), "+v"(hv[1]), "+v"(hv[2]), "+v"(hv[3]), "+v"(hv[4]), "+v"(hv[5]), "+v"(hv[6]), "+v"(hv[7])::"memory"); \
    asm volatile("buffer_store_dwordx2 %0, %1, %2, 0 offen sc1" ::"v"((u32x2){tagA, tagB}), "v"(lane == 0 ? W_HF + (c * 16 + pown) * FSB + wid * FSW : 0xf0000000u), "s"(rws) : "memory"); \
    lds_wr32(lane == 0 ? (unsigned)(FL_FREE + wid * 4) : jnk, 9u); \
    if (NS) { if (RQD) __builtin_amdgcn_s_sleep(RQD); asm volatile(ASM_REQ : "=&{v[184:215]}"(hw0), "=&{v[216:247]}"(hw1), [hb0] "=&s"(st[0]), [hb1] "=&s"(st[1]), [hb2] "=&s"(st[2]), [hb3] "=&s"(st[3]) : [vo] "v"(vo), [own] "s"(pown), [srd] "s"(rh) : "memory", "scc"); } } while (0)
#pragma unroll
  for (int sg = 0; sg < 4; ++sg) pc[sg] = (fq & 1) * 8192 + fr * 128 + (((2 * sg + (fq >> 1)) ^ ((fr >> 1) & 7)) * 16);
  {
    lds_wait_ge(FL_C, 1u);
    u32x4 k[4][4], h[4][2];
#pragma unroll
    for (int i = 0; i < 4; ++i) {
      const int sw = i == 0 ? wid : i - (i <= wid ? 1 : 0);
      asm volatile("ds_read_b128 %0, %4\n\tds_read_b128 %1, %4 offset:2048\n\tds_read_b128 %2, %4 offset:4096\n\tds_read_b128 %3, %4 offset:6144"
                   : "=&v"(k[i][0]), "=&v"(k[i][1]), "=&v"(k[i][2]), "=&v"(k[i][3]) : "v"(pc[0] ^ (sw * 32)) : "memory");
    }
#pragma unroll
    for (int m = 0; m < 2; ++m)
#pragma unroll
      for (int r = 0; r < 4; ++r) {
        const f32x4 t = unpk4(xd[m][r]);
#pragma unroll
        for (int n = 0; n < 4; ++n) ac2[m][n][r] = t[n];
      }
    h[0][0] = hv[0]; h[0][1] = hv[1];
    if (wid == 1) { h[0][0] = hv[2]; h[0][1] = hv[3]; }
    if (wid == 2) { h[0][0] = hv[4]; h[0][1] = hv[5]; }
    if (wid == 3) { h[0][0] = hv[6]; h[0][1] = hv[7]; }
    asm volatile("" : "+v"(ac2[0][0]), "+v"(ac2[0][1]), "+v"(ac2[0][2]), "+v"(ac2[0][3]), "+v"(ac2[1][0]), "+v"(ac2[1][1]), "+v"(ac2[1][2]), "+v"(ac2[1][3]), "+v"(h[0][0]), "+v"(h[0][1]));
    if (NSI < 8) HST(NSI);
    if (MGP == 1) ACK();
    lds_wait_min4(FL_HS, 1u);
    asm volatile("ds_read_b128 %0, %6\n\tds_read_b128 %1, %6 offset:1024\n\tds_read_b128 %2, %6 offset:2048\n\tds_read_b128 %3, %6 offset:3072\n\tds_read_b128 %4, %6 offset:4096\n\tds_read_b128 %5, %6 offset:5120"
                 : "=&v"(h[1][0]), "=&v"(h[1][1]), "=&v"(h[2][0]), "=&v"(h[2][1]), "=&v"(h[3][0]), "=&v"(h[3][1]) : "v"(L_XH + wid * 6144 + lane * 16) : "memory");
    lds_wr32(lane == 0 ? (unsigned)(FL_CFREE + wid * 4) : jnk, 1u);
#pragma unroll
    for (int i = 0; i < 4; ++i) {
      if (i == 1) asm volatile("s_waitcnt lgkmcnt(0)" : "+v"(h[1][0]), "+v"(h[1][1]), "+v"(h[2][0]), "+v"(h[2][1]), "+v"(h[3][0]), "+v"(h[3][1])::"memory");
      asm volatile("" : "+v"(k[i][0]), "+v"(k[i][1]), "+v"(k[i][2]), "+v"(k[i][3]));
#pragma unroll
      for (int n = 0; n < 4; ++n) {
        ac2[0][n] = __builtin_amdgcn_mfma_f32_16x16x32_bf16(__builtin_bit_cast(bf16x8, h[i][0]), __builtin_bit_cast(bf16x8, k[i][n]), ac2[0][n], 0, 0, 0);
        ac2[1][n] = __builtin_amdgcn_mfma_f32_16x16x32_bf16(__builtin_bit_cast(bf16x8, h[i][1]), __builtin_bit_cast(bf16x8, k[i][n]), ac2[1][n], 0, 0, 0);
      }
      asm volatile("" : "+v"(ac2[0][0]), "+v"(ac2[0][1]), "+v"(ac2[0][2]), "+v"(ac2[0][3]), "+v"(ac2[1][0]), "+v"(ac2[1][1]), "+v"(ac2[1][2]), "+v"(ac2[1][3])::"memory");
      if (NSI + 1 + i < 8) HST(NSI + 1 + i < 8 ? NSI + 1 + i : 0);
    }
    static_assert(NSI >= 3 && (MGP == 0 || NSI >= 7), "all stores");
  }
  if (MGP == 0) ACK();
  f32x4 ov[2][4];
  float g2[2][4];
#pragma unroll
  for (int m = 0; m < 2; ++m) {
    const f32x4 ge = lds4(smem, L_GT + ((2 * wid + m) * 16 + 4 * fq) * 4);
#pragma unroll
    for (int r = 0; r < 4; ++r) {
      ov[m][r] = unpk4(xe[m][r]);
      g2[m][r] = ge[r] + ge[r];
      asm volatile("" : "+v"(ov[m][r]), "+v"(g2[m][r]));
    }
  }
  unsigned ra[2][4];
  lds_wait_ge(FL_SEL, 1u);
  const unsigned routed = __builtin_amdgcn_readfirstlane(lds_rd32(FL_SEL));
#pragma unroll
  for (int m = 0; m < 2; ++m) {
    const u32x4 me = lds_rd128(L_MAP + ((2 * wid + m) * 16 + 4 * fq) * 4);
#pragma unroll
    for (int r = 0; r < 4; ++r) { ra[m][r] = me[r] + fr * 16; asm volatile("" : "+v"(ra[m][r])); }
  }
  const rsrc_t ro = mk(out, 2048 * 1024 * 4);
  unsigned oo = (c * 128 + 32 * fq + 2 * wid) * 4096 + (j * 64 + 4 * fr) * 4;
  if (WIF == 1) oo = 0xf0000000u;
  if (WIF == 2 && (fq & 1)) oo = 0xf0000000u;
  asm volatile("" : "+v"(oo) : "s"(ro));
  __builtin_amdgcn_s_setprio(MPRIO2);

  {
    const unsigned fad = FL_C, fwd = lane == 0 ? (unsigned)(FL_CFREE + wid * 4) : jnk;
    bf16x8 kb[2][4];
    unsigned fv = 1;
#pragma unroll
    for (int sg = 0; sg < 4; ++sg) pc[sg] += 16384;
    asm volatile(ASM_GEMM2 : [c00] "+v"(ac2[0][0]), [c01] "+v"(ac2[0][1]), [c02] "+v"(ac2[0][2]), [c03] "+v"(ac2[0][3]), [c10] "+v"(ac2[1][0]), [c11] "+v"(ac2[1][1]), [c12] "+v"(ac2[1][2]), [c13] "+v"(ac2[1][3]), "=&{v[184:215]}"(hw0), "=&{v[216:247]}"(hw1), [k00] "=&v"(kb[0][0]), [k01] "=&v"(kb[0][1]), [k02] "=&v"(kb[0][2]), [k03] "=&v"(kb[0][3]), [k10] "=&v"(kb[1][0]), [k11] "=&v"(kb[1][1]), [k12] "=&v"(kb[1][2]), [k13] "=&v"(kb[1][3]), [pc0] "+v"(pc[0]), [pc1] "+v"(pc[1]), [pc2] "+v"(pc[2]), [pc3] "+v"(pc[3]), [fv] "+v"(fv), [s] "=&s"(st[0]), [s2] "=&s"(st[1]), [to] "=&s"(st[2]), [hn] "=&s"(st[3]), [cn] "=&s"(st[4]), [dl] "=&s"(st[5]), [nf] "=&s"(st[6]), [hr] "=&s"(st[7]), [hb0] "=&s"(st[8]), [hb1] "=&s"(st[9]), [hb2] "=&s"(st[10]), [hb3] "=&s"(st[11]) : [vo] "v"(vo), [own] "s"(pown), [srd] "s"(rh), [fa] "v"(fad), [fw] "v"(fwd), [d16] "s"(0x4000), [wr] "s"(0x500) : "memory", "scc", "vcc", "v250", "v251", "v252", "v253", "v254", "v255");
  }
  if (routed > 1u) {
#pragma unroll 1
    for (int it = 0; it < SPIN_MAX * 4; ++it) {
      u32x2 f;
      f32x4 d[8];
      asm volatile("ds_read_b64 %8, %17\n\tds_read_b128 %0, %9\n\tds_read_b128 %1, %10\n\tds_read_b128 %2, %11\n\tds_read_b128 %3, %12\n\t"
                   "ds_read_b128 %4, %13\n\tds_read_b128 %5, %14\n\tds_read_b128 %6, %15\n\tds_read_b128 %7, %16\n\ts_waitcnt lgkmcnt(0)"
                   : "=&v"(d[0]), "=&v"(d[1]), "=&v"(d[2]), "=&v"(d[3]), "=&v"(d[4]), "=&v"(d[5]), "=&v"(d[6]), "=&v"(d[7]), "=&v"(f)
                   : "v"(ra[0][0]), "v"(ra[0][1]), "v"(ra[0][2]), "v"(ra[0][3]), "v"(ra[1][0]), "v"(ra[1][1]), "v"(ra[1][2]), "v"(ra[1][3]), "v"(FL_XD) : "memory");
      if (__builtin_amdgcn_readfirstlane(f.x & f.y)) {
#pragma unroll
        for (int e = 0; e < 8; ++e) ov[e >> 2][e & 3] += d[e];
        break;
      }
    }
  }
#pragma unroll
  for (int r = 0; r < 4; ++r)
#pragma unroll
    for (int m = 0; m < 2; ++m) {
#pragma unroll
      for (int n = 0; n < 4; ++n) ov[m][r][n] = fma1(g2[m][r], ac2[m][n][r], ov[m][r][n]);
      asm volatile("s_nop 1\n\tbuffer_store_dwordx4 %0, %1, %2, %3 offen" OST : "+v"(ov[m][r]) : "v"(oo), "s"(ro), "s"(r * 32768 + m * 4096) : "memory");
      __builtin_amdgcn_sched_barrier(0);
    }
  asm volatile("s_nop 7\n\ts_nop 7" ::"v"(ov[0][0]), "v"(ov[0][1]), "v"(ov[0][2]), "v"(ov[0][3]), "v"(ov[1][0]), "v"(ov[1][1]), "v"(ov[1][2]), "v"(ov[1][3]));
  asm volatile("Lk_m:");
#endif
}

extern "C" void kernel_launch(void* const* d_in, const int* in_sizes, int n_in, void* d_out, int out_size, void* d_ws, size_t ws_size,
                              hipStream_t stream) {
  (void)hipFuncSetAttribute((const void*)fused, hipFuncAttributeMaxDynamicSharedMemorySize, LDS_BYTES);
  fused<<<256, 512, LDS_BYTES, stream>>>((const u16*)d_in[0], (const u16*)d_in[1], (const u16*)d_in[2], (const u16*)d_in[3], (const u16*)d_in[4],
                                         (const u16*)d_in[5], (const u16*)d_in[6], (const u16*)d_in[7], (const u16*)d_in[8], (const u16*)d_in[10],
                                         (const u16*)d_in[11], (const u16*)d_in[12], (const u16*)d_in[13], (const u16*)d_in[14], (char*)d_ws + WS_BASE(ws_size),
                                         (float*)d_out);
}
```

```cpp
#include <hip/hip_runtime.h>
#include <hip/hip_bf16.h>
#include <stdint.h>

typedef __attribute__((ext_vector_type(8))) short bf16x8;
typedef __attribute__((ext_vector_type(4))) float f32x4;
typedef __attribute__((ext_vector_type(2))) float f32x2;
typedef __attribute__((ext_vector_type(4))) unsigned u32x4;
typedef __attribute__((ext_vector_type(2))) unsigned u32x2;
typedef __attribute__((ext_vector_type(32))) unsigned u32x32;
typedef unsigned short u16;
typedef unsigned long long u64;
typedef __attribute__((ext_vector_type(8))) unsigned long long u64x8;
#define LAS __attribute__((address_space(3)))
#define DEV __device__ __forceinline__

#define ASM_GEMM1 \
  "s_mov_b32 %[it], 0\n\t" \
  "s_barrier\n\t" \
  "ds_read_b128 %[a00], %[pa0] offset:0\n\t" \
  "ds_read_b128 %[a01], %[pa0] offset:2048\n\t" \
  "ds_read_b128 %[b0], %[pb0] offset:0\n\t" \
  "ds_read_b128 %[b1], %[pb0] offset:2048\n\t" \
  "ds_read_b128 %[b2], %[pb0] offset:4096\n\t" \
  "ds_read_b128 %[b3], %[pb0] offset:6144\n\t" \
  "ds_read_b128 %[b4], %[pb0] offset:8192\n\t" \
  "ds_read_b128 %[b5], %[pb0] offset:10240\n\t" \
  "ds_read_b128 %[b6], %[pb0] offset:12288\n\t" \
  "ds_read_b128 %[b7], %[pb0] offset:14336\n\t" \
  "Lg1_%=:\n\t" \
  "ds_read_b128 %[a10], %[pa1] offset:0\n\t" \
  "ds_read_b128 %[a11], %[pa1] offset:2048\n\t" \
  "s_waitcnt lgkmcnt(8)\n\t" \
  "v_mfma_f32_16x16x32_bf16 %[c00], %[a00], %[b0], %[c00]\n\t" \
  "v_mfma_f32_16x16x32_bf16 %[c10], %[a01], %[b0], %[c10]\n\t" \
  "ds_read_b128 %[b0], %[pb1] offset:0\n\t" \
  "v_mfma_f32_16x16x32_bf16 %[c01], %[a00], %[b1], %[c01]\n\t" \
  "v_mfma_f32_16x16x32_bf16 %[c11], %[a01], %[b1], %[c11]\n\t" \
  "ds_read_b128 %[b1], %[pb1] offset:2048\n\t" \
  "s_waitcnt lgkmcnt(8)\n\t" \
  "v_mfma_f32_16x16x32_bf16 %[c02], %[a00], %[b2], %[c02]\n\t" \
  "v_mfma_f32_16x16x32_bf16 %[c12], %[a01], %[b2], %[c12]\n\t" \
  "ds_read_b128 %[b2], %[pb1] offset:4096\n\t" \
  "v_mfma_f32_16x16x32_bf16 %[c03], %[a00], %[b3], %[c03]\n\t" \
  "v_mfma_f32_16x16x32_bf16 %[c13], %[a01], %[b3], %[c13]\n\t" \
  "ds_read_b128 %[b3], %[pb1] offset:6144\n\t" \
  "s_waitcnt lgkmcnt(8)\n\t" \
  "v_mfma_f32_16x16x32_bf16 %[c04], %[a00], %[b4], %[c04]\n\t" \
  "v_mfma_f32_16x16x32_bf16 %[c14], %[a01], %[b4], %[c14]\n\t" \
  "ds_read_b128 %[b4], %[pb1] offset:8192\n\t" \
  "v_mfma_f32_16x16x32_bf16 %[c05], %[a00], %[b5], %[c05]\n\t" \
  "v_mfma_f32_16x16x32_bf16 %[c15], %[a01], %[b5], %[c15]\n\t" \
  "ds_read_b128 %[b5], %[pb1] offset:10240\n\t" \
  "s_waitcnt lgkmcnt(8)\n\t" \
  "v_mfma_f32_16x16x32_bf16 %[c06], %[a00], %[b6], %[c06]\n\t" \
  "v_mfma_f32_16x16x32_bf16 %[c16], %[a01], %[b6], %[c16]\n\t" \
  "ds_read_b128 %[b6], %[pb1] offset:12288\n\t" \
  "v_mfma_f32_16x16x32_bf16 %[c07], %[a00], %[b7], %[c07]\n\t" \
  "v_mfma_f32_16x16x32_bf16 %[c17], %[a01], %[b7], %[c17]\n\t" \
  "ds_read_b128 %[b7], %[pb1] offset:14336\n\t" \
  "s_waitcnt lgkmcnt(6)\n\t" \
  "v_mfma_f32_16x16x32_bf16 %[c00], %[a10], %[b0], %[c00]\n\t" \
  "v_mfma_f32_16x16x32_bf16 %[c10], %[a11], %[b0], %[c10]\n\t" \
  "v_mfma_f32_16x16x32_bf16 %[c01], %[a10], %[b1], %[c01]\n\t" \
  "v_mfma_f32_16x16x32_bf16 %[c11], %[a11], %[b1], %[c11]\n\t" \
  "s_waitcnt lgkmcnt(4)\n\t" \
  "v_mfma_f32_16x16x32_bf16 %[c02], %[a10], %[b2], %[c02]\n\t" \
  "v_mfma_f32_16x16x32_bf16 %[c12], %[a11], %[b2], %[c12]\n\t" \
  "s_waitcnt lgkmcnt(0)\n\t" \
  "s_barrier\n\t" \
  "ds_read_b128 %[a00], %[pa0] offset:32768\n\t" \
  "ds_read_b128 %[a01], %[pa0] offset:34816\n\t" \
  "v_mfma_f32_16x16x32_bf16 %[c03], %[a10], %[b3], %[c03]\n\t" \
  "v_mfma_f32_16x16x32_bf16 %[c13], %[a11], %[b3], %[c13]\n\t" \
  "ds_read_b128 %[b0], %[pb0] offset:32768\n\t" \
  "ds_read_b128 %[b1], %[pb0] offset:34816\n\t" \
  "v_mfma_f32_16x16x32_bf16 %[c04], %[a10], %[b4], %[c04]\n\t" \
  "v_mfma_f32_16x16x32_bf16 %[c14], %[a11], %[b4], %[c14]\n\t" \
  "ds_read_b128 %[b2], %[pb0] offset:36864\n\t" \
  "ds_read_b128 %[b3], %[pb0] offset:38912\n\t" \
  "v_mfma_f32_16x16x32_bf16 %[c05], %[a10], %[b5], %[c05]\n\t" \
  "v_mfma_f32_16x16x32_bf16 %[c15], %[a11], %[b5], %[c15]\n\t" \
  "ds_read_b128 %[b4], %[pb0] offset:40960\n\t" \
  "ds_read_b128 %[b5], %[pb0] offset:43008\n\t" \
  "v_mfma_f32_16x16x32_bf16 %[c06], %[a10], %[b6], %[c06]\n\t" \
  "v_mfma_f32_16x16x32_bf16 %[c16], %[a11], %[b6], %[c16]\n\t" \
  "ds_read_b128 %[b6], %[pb0] offset:45056\n\t" \
  "v_mfma_f32_16x16x32_bf16 %[c07], %[a10], %[b7], %[c07]\n\t" \
  "v_mfma_f32_16x16x32_bf16 %[c17], %[a11], %[b7], %[c17]\n\t" \
  "ds_read_b128 %[b7], %[pb0] offset:47104\n\t" \
  "ds_read_b128 %[a10], %[pa1] offset:32768\n\t" \
  "ds_read_b128 %[a11], %[pa1] offset:34816\n\t" \
  "s_waitcnt lgkmcnt(8)\n\t" \
  "v_mfma_f32_16x16x32_bf16 %[c00], %[a00], %[b0], %[c00]\n\t" \
  "v_mfma_f32_16x16x32_bf16 %[c10], %[a01], %[b0], %[c10]\n\t" \
  "ds_read_b128 %[b0], %[pb1] offset:32768\n\t" \
  "v_mfma_f32_16x16x32_bf16 %[c01], %[a00], %[b1], %[c01]\n\t" \
  "v_mfma_f32_16x16x32_bf16 %[c11], %[a01], %[b1], %[c11]\n\t" \
  "ds_read_b128 %[b1], %[pb1] offset:34816\n\t" \
  "s_waitcnt lgkmcnt(8)\n\t" \
  "v_mfma_f32_16x16x32_bf16 %[c02], %[a00], %[b2], %[c02]\n\t" \
  "v_mfma_f32_16x16x32_bf16 %[c12], %[a01], %[b2], %[c12]\n\t" \
  "ds_read_b128 %[b2], %[pb1] offset:36864\n\t" \
  "v_mfma_f32_16x16x32_bf16 %[c03], %[a00], %[b3], %[c03]\n\t" \
  "v_mfma_f32_16x16x32_bf16 %[c13], %[a01], %[b3], %[c13]\n\t" \
  "ds_read_b128 %[b3], %[pb1] offset:38912\n\t" \
  "v_xor_b32 %[pa0], 0x10000, %[pa0]\n\t" \
  "s_waitcnt lgkmcnt(8)\n\t" \
  "v_mfma_f32_16x16x32_bf16 %[c04], %[a00], %[b4], %[c04]\n\t" \
  "v_mfma_f32_16x16x32_bf16 %[c14], %[a01], %[b4], %[c14]\n\t" \
  "ds_read_b128 %[b4], %[pb1] offset:40960\n\t" \
  "v_xor_b32 %[pb0], 0x10000, %[pb0]\n\t" \
  "v_mfma_f32_16x16x32_bf16 %[c05], %[a00], %[b5], %[c05]\n\t" \
  "v_mfma_f32_16x16x32_bf16 %[c15], %[a01], %[b5], %[c15]\n\t" \
  "ds_read_b128 %[b5], %[pb1] offset:43008\n\t" \
  "s_waitcnt lgkmcnt(8)\n\t" \
  "v_mfma_f32_16x16x32_bf16 %[c06], %[a00], %[b6], %[c06]\n\t" \
  "v_mfma_f32_16x16x32_bf16 %[c16], %[a01], %[b6], %[c16]\n\t" \
  "ds_read_b128 %[b6], %[pb1] offset:45056\n\t" \
  "s_cmp_eq_u32 %[it], 7\n\t" \
  "v_mfma_f32_16x16x32_bf16 %[c07], %[a00], %[b7], %[c07]\n\t" \
  "v_mfma_f32_16x16x32_bf16 %[c17], %[a01], %[b7], %[c17]\n\t" \
  "ds_read_b128 %[b7], %[pb1] offset:47104\n\t" \
  "s_cbranch_scc1 Lxe_%=\n\t" \
  "Lxb_%=:\n\t" \
  "s_waitcnt lgkmcnt(6)\n\t" \
  "v_mfma_f32_16x16x32_bf16 %[c00], %[a10], %[b0], %[c00]\n\t" \
  "v_mfma_f32_16x16x32_bf16 %[c10], %[a11], %[b0], %[c10]\n\t" \
  "v_mfma_f32_16x16x32_bf16 %[c01], %[a10], %[b1], %[c01]\n\t" \
  "v_mfma_f32_16x16x32_bf16 %[c11], %[a11], %[b1], %[c11]\n\t" \
  "v_xor_b32 %[pa1], 0x10000, %[pa1]\n\t" \
  "s_waitcnt lgkmcnt(4)\n\t" \
  "v_mfma_f32_16x16x32_bf16 %[c02], %[a10], %[b2], %[c02]\n\t" \
  "v_mfma_f32_16x16x32_bf16 %[c12], %[a11], %[b2], %[c12]\n\t" \
  "v_xor_b32 %[pb1], 0x10000, %[pb1]\n\t" \
  "s_waitcnt lgkmcnt(0)\n\t" \
  "s_barrier\n\t" \
  "s_cbranch_scc1 Lt1_%=\n\t" \
  "ds_read_b128 %[a00], %[pa0] offset:0\n\t" \
  "ds_read_b128 %[a01], %[pa0] offset:2048\n\t" \
  "v_mfma_f32_16x16x32_bf16 %[c03], %[a10], %[b3], %[c03]\n\t" \
  "v_mfma_f32_16x16x32_bf16 %[c13], %[a11], %[b3], %[c13]\n\t" \
  "ds_read_b128 %[b0], %[pb0] offset:0\n\t" \
  "ds_read_b128 %[b1], %[pb0] offset:2048\n\t" \
  "v_mfma_f32_16x16x32_bf16 %[c04], %[a10], %[b4], %[c04]\n\t" \
  "v_mfma_f32_16x16x32_bf16 %[c14], %[a11], %[b4], %[c14]\n\t" \
  "ds_read_b128 %[b2], %[pb0] offset:4096\n\t" \
  "ds_read_b128 %[b3], %[pb0] offset:6144\n\t" \
  "v_mfma_f32_16x16x32_bf16 %[c05], %[a10], %[b5], %[c05]\n\t" \
  "v_mfma_f32_16x16x32_bf16 %[c15], %[a11], %[b5], %[c15]\n\t" \
  "ds_read_b128 %[b4], %[pb0] offset:8192\n\t" \
  "ds_read_b128 %[b5], %[pb0] offset:10240\n\t" \
  "v_mfma_f32_16x16x32_bf16 %[c06], %[a10], %[b6], %[c06]\n\t" \
  "v_mfma_f32_16x16x32_bf16 %[c16], %[a11], %[b6], %[c16]\n\t" \
  "ds_read_b128 %[b6], %[pb0] offset:12288\n\t" \
  "s_add_u32 %[it], %[it], 1\n\t" \
  "v_mfma_f32_16x16x32_bf16 %[c07], %[a10], %[b7], %[c07]\n\t" \
  "v_mfma_f32_16x16x32_bf16 %[c17], %[a11], %[b7], %[c17]\n\t" \
  "ds_read_b128 %[b7], %[pb0] offset:14336\n\t" \
  "s_branch Lg1_%=\n\t" \
  "Lxe_%=:\n\t" \
  "ds_read_b64 %[xe00], %[xa0] offset:0\n\t" \
  "ds_read_b64 %[xe01], %[xa1] offset:0\n\t" \
  "ds_read_b64 %[xe02], %[xa2] offset:0\n\t" \
  "ds_read_b64 %[xe03], %[xa3] offset:0\n\t" \
  "ds_read_b64 %[xe10], %[xa0] offset:2048\n\t" \
  "ds_read_b64 %[xe11], %[xa1] offset:2048\n\t" \
  "ds_read_b64 %[xe12], %[xa2] offset:2048\n\t" \
  "ds_read_b64 %[xe13], %[xa3] offset:2048\n\t" \
  "ds_read_b32 %[tg], %[tg]\n\t" \
  "s_branch Lxb_%=\n\t" \
  "Lt1_%=:\n\t" \
  "v_mfma_f32_16x16x32_bf16 %[c03], %[a10], %[b3], %[c03]\n\t" \
  "v_mfma_f32_16x16x32_bf16 %[c13], %[a11], %[b3], %[c13]\n\t" \
  "v_mfma_f32_16x16x32_bf16 %[c04], %[a10], %[b4], %[c04]\n\t" \
  "v_mfma_f32_16x16x32_bf16 %[c14], %[a11], %[b4], %[c14]\n\t" \
  "v_mfma_f32_16x16x32_bf16 %[c05], %[a10], %[b5], %[c05]\n\t" \
  "v_mfma_f32_16x16x32_bf16 %[c15], %[a11], %[b5], %[c15]\n\t" \
  "v_mfma_f32_16x16x32_bf16 %[c06], %[a10], %[b6], %[c06]\n\t" \
  "v_mfma_f32_16x16x32_bf16 %[c16], %[a11], %[b6], %[c16]\n\t" \
  "v_mfma_f32_16x16x32_bf16 %[c07], %[a10], %[b7], %[c07]\n\t" \
  "v_mfma_f32_16x16x32_bf16 %[c17], %[a11], %[b7], %[c17]\n\t" \
  "s_nop 7\n\t"
#define ASM_GEMM2 \
  "s_nop 4\n\t" \
  "s_mov_b32 %[to], 0x40000\n\t" \
  "s_mov_b32 %[cn], 1\n\t" \
  "Lc0_%=:\n\t" \
  "ds_read_b128 v[252:255], %[fa]\n\t" \
  "s_cmp_eq_u32 %[to], 0\n\t" \
  "s_cbranch_scc1 Lc0_%=x\n\t" \
  "s_sub_u32 %[to], %[to], 1\n\t" \
  "s_waitcnt lgkmcnt(0)\n\t" \
  "v_readfirstlane_b32 %[s], v253\n\t" \
  "s_cmp_ge_u32 %[s], 2\n\t" \
  "s_cbranch_scc0 Lc0_%=\n\t" \
  "Lc0_%=x:\n\t" \
  "ds_read_b128 %[k00], %[pc0] offset:0\n\t" \
  "ds_read_b128 %[k01], %[pc0] offset:2048\n\t" \
  "ds_read_b128 %[k02], %[pc0] offset:4096\n\t" \
  "ds_read_b128 %[k03], %[pc0] offset:6144\n\t" \
  "ds_read_b128 %[k10], %[pc1] offset:0\n\t" \
  "ds_read_b128 %[k11], %[pc1] offset:2048\n\t" \
  "ds_read_b128 %[k12], %[pc1] offset:4096\n\t" \
  "ds_read_b128 %[k13], %[pc1] offset:6144\n\t" \
  "s_mov_b32 %[hn], 3\n\t" \
  "s_cmp_gt_u32 1, %[own]\n\t" \
  "s_addc_u32 %[hb0], 1, -1\n\t" \
  "s_lshl_b32 %[hb0], %[hb0], 15\n\t" \
  "s_or_b32 %[hb1], %[hb0], 0x2000\n\t" \
  "s_or_b32 %[hb2], %[hb0], 0x4000\n\t" \
  "s_or_b32 %[hb3], %[hb0], 0x6000\n\t" \
  "Lh1_%=:\n\t" \
  "ds_read_b128 v[252:255], %[fa]\n\t" \
  "s_cmp_eq_u32 %[to], 0\n\t" \
  "s_cbranch_scc1 Lh1_%=x\n\t" \
  "s_sub_u32 %[to], %[to], 1\n\t" \
  "s_waitcnt lgkmcnt(0)\n\t" \
  "v_readfirstlane_b32 %[s], v254\n\t" \
  "s_cmp_ge_u32 %[s], 1\n\t" \
  "s_cbranch_scc0 Lh1_%=\n\t" \
  "Lh1_%=x:\n\t" \
  "buffer_load_dwordx4 v[216:219], %[vo], %[srd], %[hb0] offen offset:0 sc1\n\t" \
  "buffer_load_dwordx4 v[220:223], %[vo], %[srd], %[hb0] offen offset:1024 sc1\n\t" \
  "buffer_load_dwordx4 v[224:227], %[vo], %[srd], %[hb1] offen offset:0 sc1\n\t" \
  "buffer_load_dwordx4 v[228:231], %[vo], %[srd], %[hb1] offen offset:1024 sc1\n\t" \
  "buffer_load_dwordx4 v[232:235], %[vo], %[srd], %[hb2] offen offset:0 sc1\n\t" \
  "buffer_load_dwordx4 v[236:239], %[vo], %[srd], %[hb2] offen offset:1024 sc1\n\t" \
  "buffer_load_dwordx4 v[240:243], %[vo], %[srd], %[hb3] offen offset:0 sc1\n\t" \
  "buffer_load_dwordx4 v[244:247], %[vo], %[srd], %[hb3] offen offset:1024 sc1\n\t" \
  "s_cmp_gt_u32 2, %[own]\n\t" \
  "s_addc_u32 %[hb0], 2, -1\n\t" \
  "s_lshl_b32 %[hb0], %[hb0], 15\n\t" \
  "s_or_b32 %[hb1], %[hb0], 0x2000\n\t" \
  "s_or_b32 %[hb2], %[hb0], 0x4000\n\t" \
  "s_or_b32 %[hb3], %[hb0], 0x6000\n\t" \
  "s_cmp_ge_u32 %[s], 2\n\t" \
  "s_cbranch_scc1 Lh2_%=x\n\t" \
  "Lh2_%=:\n\t" \
  "ds_read_b128 v[252:255], %[fa]\n\t" \
  "s_cmp_eq_u32 %[to], 0\n\t" \
  "s_cbranch_scc1 Lh2_%=x\n\t" \
  "s_sub_u32 %[to], %[to], 1\n\t" \
  "s_waitcnt lgkmcnt(0)\n\t" \
  "v_readfirstlane_b32 %[s], v254\n\t" \
  "s_cmp_ge_u32 %[s], 2\n\t" \
  "s_cbranch_scc0 Lh2_%=\n\t" \
  "Lh2_%=x:\n\t" \
  "buffer_load_dwordx4 v[184:187], %[vo], %[srd], %[hb0] offen offset:0 sc1\n\t" \
  "buffer_load_dwordx4 v[188:191], %[vo], %[srd], %[hb0] offen offset:1024 sc1\n\t" \
  "buffer_load_dwordx4 v[192:195], %[vo], %[srd], %[hb1] offen offset:0 sc1\n\t" \
  "buffer_load_dwordx4 v[196:199], %[vo], %[srd], %[hb1] offen offset:1024 sc1\n\t" \
  "buffer_load_dwordx4 v[200:203], %[vo], %[srd], %[hb2] offen offset:0 sc1\n\t" \
  "buffer_load_dwordx4 v[204:207], %[vo], %[srd], %[hb2] offen offset:1024 sc1\n\t" \
  "buffer_load_dwordx4 v[208:211], %[vo], %[srd], %[hb3] offen offset:0 sc1\n\t" \
  "buffer_load_dwordx4 v[212:215], %[vo], %[srd], %[hb3] offen offset:1024 sc1\n\t" \
  "s_cmp_ge_u32 %[s], 3\n\t" \
  "s_cbranch_scc1 Lh3_%=x\n\t" \
  "Lh3_%=:\n\t" \
  "ds_read_b128 v[252:255], %[fa]\n\t" \
  "s_cmp_eq_u32 %[to], 0\n\t" \
  "s_cbranch_scc1 Lh3_%=x\n\t" \
  "s_sub_u32 %[to], %[to], 1\n\t" \
  "s_waitcnt lgkmcnt(0)\n\t" \
  "v_readfirstlane_b32 %[s], v254\n\t" \
  "s_cmp_ge_u32 %[s], 3\n\t" \
  "s_cbranch_scc0 Lh3_%=\n\t" \
  "Lh3_%=x:\n\t" \
  "s_branch Lin_%=\n\t" \
  "Lg2_%=:\n\t" \
  "s_waitcnt vmcnt(14) lgkmcnt(7)\n\t" \
  "ds_read_b128 v[252:255], %[fa]\n\t" \
  "v_mfma_f32_16x16x32_bf16 %[c00], v[184:187], %[k00], %[c00]\n\t" \
  "s_cmp_gt_u32 %[hn], %[own]\n\t" \
  "s_addc_u32 %[hb0], %[hn], -1\n\t" \
  "v_mfma_f32_16x16x32_bf16 %[c10], v[188:191], %[k00], %[c10]\n\t" \
  "ds_read_b128 %[k00], %[pc2] offset:0\n\t" \
  "s_waitcnt lgkmcnt(8)\n\t" \
  "v_mfma_f32_16x16x32_bf16 %[c01], v[184:187], %[k01], %[c01]\n\t" \
  "s_lshl_b32 %[hb0], %[hb0], 15\n\t" \
  "v_mfma_f32_16x16x32_bf16 %[c11], v[188:191], %[k01], %[c11]\n\t" \
  "ds_read_b128 %[k01], %[pc2] offset:2048\n\t" \
  "s_waitcnt lgkmcnt(8)\n\t" \
  "v_mfma_f32_16x16x32_bf16 %[c02], v[184:187], %[k02], %[c02]\n\t" \
  "s_or_b32 %[hb1], %[hb0], 0x2000\n\t" \
  "v_mfma_f32_16x16x32_bf16 %[c12], v[188:191], %[k02], %[c12]\n\t" \
  "ds_read_b128 %[k02], %[pc2] offset:4096\n\t" \
  "s_waitcnt lgkmcnt(8)\n\t" \
  "v_mfma_f32_16x16x32_bf16 %[c03], v[184:187], %[k03], %[c03]\n\t" \
  "s_add_u32 %[cn], %[cn], 1\n\t" \
  "v_mfma_f32_16x16x32_bf16 %[c13], v[188:191], %[k03], %[c13]\n\t" \
  "ds_read_b128 %[k03], %[pc2] offset:6144\n\t" \
  "buffer_load_dwordx4 v[184:187], %[vo], %[srd], %[hb0] offen offset:0 sc1\n\t" \
  "s_waitcnt vmcnt(13) lgkmcnt(8)\n\t" \
  "v_mfma_f32_16x16x32_bf16 %[c00], v[192:195], %[k10], %[c00]\n\t" \
  "s_cmp_eq_u32 %[cn], 9\n\t" \
  "s_cselect_b32 %[dl], 0x20000, %[d16]\n\t" \
  "v_mfma_f32_16x16x32_bf16 %[c10], v[196:199], %[k10], %[c10]\n\t" \
  "s_bitcmp1_b32 %[wr], %[cn]\n\t" \
  "s_cselect_b32 %[dl], 0xfffe4000, %[dl]\n\t" \
  "ds_read_b128 %[k10], %[pc3] offset:0\n\t" \
  "s_waitcnt lgkmcnt(8)\n\t" \
  "v_mfma_f32_16x16x32_bf16 %[c01], v[192:195], %[k11], %[c01]\n\t" \
  "v_add_u32 %[pc0], %[dl], %[pc0]\n\t" \
  "v_mfma_f32_16x16x32_bf16 %[c11], v[196:199], %[k11], %[c11]\n\t" \
  "s_or_b32 %[hb2], %[hb0], 0x4000\n\t" \
  "ds_read_b128 %[k11], %[pc3] offset:2048\n\t" \
  "buffer_load_dwordx4 v[188:191], %[vo], %[srd], %[hb0] offen offset:1024 sc1\n\t" \
  "s_waitcnt lgkmcnt(8)\n\t" \
  "v_mfma_f32_16x16x32_bf16 %[c02], v[192:195], %[k12], %[c02]\n\t" \
  "v_add_u32 %[pc1], %[dl], %[pc1]\n\t" \
  "v_mfma_f32_16x16x32_bf16 %[c12], v[196:199], %[k12], %[c12]\n\t" \
  "v_add_u32 %[fv], 1, %[fv]\n\t" \
  "ds_read_b128 %[k12], %[pc3] offset:4096\n\t" \
  "s_waitcnt lgkmcnt(7)\n\t" \
  "v_mfma_f32_16x16x32_bf16 %[c03], v[192:195], %[k13], %[c03]\n\t" \
  "v_readfirstlane_b32 %[s], v253\n\t" \
  "v_mfma_f32_16x16x32_bf16 %[c13], v[196:199], %[k13], %[c13]\n\t" \
  "ds_read_b128 %[k13], %[pc3] offset:6144\n\t" \
  "ds_write_b32 %[fw], %[fv]\n\t" \
  "s_cmp_gt_u32 %[s], %[cn]\n\t" \
  "s_cbranch_scc0 Lcs0_%=\n\t" \
  "Lcb0_%=:\n\t" \
  "buffer_load_dwordx4 v[192:195], %[vo], %[srd], %[hb1] offen offset:0 sc1\n\t" \
  "s_waitcnt vmcnt(13) lgkmcnt(8)\n\t" \
  "ds_read_b128 v[252:255], %[fa]\n\t" \
  "v_mfma_f32_16x16x32_bf16 %[c00], v[200:203], %[k00], %[c00]\n\t" \
  "s_or_b32 %[hb3], %[hb0], 0x6000\n\t" \
  "v_mfma_f32_16x16x32_bf16 %[c10], v[204:207], %[k00], %[c10]\n\t" \
  "ds_read_b128 %[k00], %[pc0] offset:0\n\t" \
  "s_waitcnt lgkmcnt(9)\n\t" \
  "v_mfma_f32_16x16x32_bf16 %[c01], v[200:203], %[k01], %[c01]\n\t" \
  "v_mfma_f32_16x16x32_bf16 %[c11], v[204:207], %[k01], %[c11]\n\t" \
  "ds_read_b128 %[k01], %[pc0] offset:2048\n\t" \
  "buffer_load_dwordx4 v[196:199], %[vo], %[srd], %[hb1] offen offset:1024 sc1\n\t" \
  "s_waitcnt lgkmcnt(9)\n\t" \
  "v_mfma_f32_16x16x32_bf16 %[c02], v[200:203], %[k02], %[c02]\n\t" \
  "v_mfma_f32_16x16x32_bf16 %[c12], v[204:207], %[k02], %[c12]\n\t" \
  "ds_read_b128 %[k02], %[pc0] offset:4096\n\t" \
  "s_waitcnt lgkmcnt(9)\n\t" \
  "v_mfma_f32_16x16x32_bf16 %[c03], v[200:203], %[k03], %[c03]\n\t" \
  "v_mfma_f32_16x16x32_bf16 %[c13], v[204:207], %[k03], %[c13]\n\t" \
  "ds_read_b128 %[k03], %[pc0] offset:6144\n\t" \
  "buffer_load_dwordx4 v[200:203], %[vo], %[srd], %[hb2] offen offset:0 sc1\n\t" \
  "s_waitcnt vmcnt(13) lgkmcnt(9)\n\t" \
  "v_mfma_f32_16x16x32_bf16 %[c00], v[208:211], %[k10], %[c00]\n\t" \
  "v_add_u32 %[pc2], %[dl], %[pc2]\n\t" \
  "v_mfma_f32_16x16x32_bf16 %[c10], v[212:215], %[k10], %[c10]\n\t" \
  "ds_read_b128 %[k10], %[pc1] offset:0\n\t" \
  "s_waitcnt lgkmcnt(9)\n\t" \
  "v_mfma_f32_16x16x32_bf16 %[c01], v[208:211], %[k11], %[c01]\n\t" \
  "v_add_u32 %[pc3], %[dl], %[pc3]\n\t" \
  "v_mfma_f32_16x16x32_bf16 %[c11], v[212:215], %[k11], %[c11]\n\t" \
  "ds_read_b128 %[k11], %[pc1] offset:2048\n\t" \
  "buffer_load_dwordx4 v[204:207], %[vo], %[srd], %[hb2] offen offset:1024 sc1\n\t" \
  "s_waitcnt lgkmcnt(6)\n\t" \
  "v_mfma_f32_16x16x32_bf16 %[c02], v[208:211], %[k12], %[c02]\n\t" \
  "v_readfirstlane_b32 %[s], v254\n\t" \
  "v_mfma_f32_16x16x32_bf16 %[c12], v[212:215], %[k12], %[c12]\n\t" \
  "ds_read_b128 %[k12], %[pc1] offset:4096\n\t" \
  "v_mfma_f32_16x16x32_bf16 %[c03], v[208:211], %[k13], %[c03]\n\t" \
  "s_add_u32 %[hn], %[hn], 1\n\t" \
  "v_mfma_f32_16x16x32_bf16 %[c13], v[212:215], %[k13], %[c13]\n\t" \
  "ds_read_b128 %[k13], %[pc1] offset:6144\n\t" \
  "buffer_load_dwordx4 v[208:211], %[vo], %[srd], %[hb3] offen offset:0 sc1\n\t" \
  "buffer_load_dwordx4 v[212:215], %[vo], %[srd], %[hb3] offen offset:1024 sc1\n\t" \
  "s_cmp_ge_u32 %[s], %[hn]\n\t" \
  "s_cbranch_scc0 Lhs0_%=\n\t" \
  "Lhb0_%=:\n\t" \
  "Lin_%=:\n\t" \
  "s_waitcnt vmcnt(14) lgkmcnt(7)\n\t" \
  "ds_read_b128 v[252:255], %[fa]\n\t" \
  "v_mfma_f32_16x16x32_bf16 %[c00], v[216:219], %[k00], %[c00]\n\t" \
  "s_cmp_gt_u32 %[hn], %[own]\n\t" \
  "s_addc_u32 %[hb0], %[hn], -1\n\t" \
  "v_mfma_f32_16x16x32_bf16 %[c10], v[220:223], %[k00], %[c10]\n\t" \
  "ds_read_b128 %[k00], %[pc2] offset:0\n\t" \
  "s_waitcnt lgkmcnt(8)\n\t" \
  "v_mfma_f32_16x16x32_bf16 %[c01], v[216:219], %[k01], %[c01]\n\t" \
  "s_lshl_b32 %[hb0], %[hb0], 15\n\t" \
  "v_mfma_f32_16x16x32_bf16 %[c11], v[220:223], %[k01], %[c11]\n\t" \
  "ds_read_b128 %[k01], %[pc2] offset:2048\n\t" \
  "s_waitcnt lgkmcnt(8)\n\t" \
  "v_mfma_f32_16x16x32_bf16 %[c02], v[216:219], %[k02], %[c02]\n\t" \
  "s_or_b32 %[hb1], %[hb0], 0x2000\n\t" \
  "v_mfma_f32_16x16x32_bf16 %[c12], v[220:223], %[k02], %[c12]\n\t" \
  "ds_read_b128 %[k02], %[pc2] offset:4096\n\t" \
  "s_waitcnt lgkmcnt(8)\n\t" \
  "v_mfma_f32_16x16x32_bf16 %[c03], v[216:219], %[k03], %[c03]\n\t" \
  "s_add_u32 %[cn], %[cn], 1\n\t" \
  "v_mfma_f32_16x16x32_bf16 %[c13], v[220:223], %[k03], %[c13]\n\t" \
  "ds_read_b128 %[k03], %[pc2] offset:6144\n\t" \
  "buffer_load_dwordx4 v[216:219], %[vo], %[srd], %[hb0] offen offset:0 sc1\n\t" \
  "s_waitcnt vmcnt(13) lgkmcnt(8)\n\t" \
  "v_mfma_f32_16x16x32_bf16 %[c00], v[224:227], %[k10], %[c00]\n\t" \
  "s_cmp_eq_u32 %[cn], 9\n\t" \
  "s_cselect_b32 %[dl], 0x20000, %[d16]\n\t" \
  "v_mfma_f32_16x16x32_bf16 %[c10], v[228:231], %[k10], %[c10]\n\t" \
  "s_bitcmp1_b32 %[wr], %[cn]\n\t" \
  "s_cselect_b32 %[dl], 0xfffe4000, %[dl]\n\t" \
  "ds_read_b128 %[k10], %[pc3] offset:0\n\t" \
  "s_waitcnt lgkmcnt(8)\n\t" \
  "v_mfma_f32_16x16x32_bf16 %[c01], v[224:227], %[k11], %[c01]\n\t" \
  "v_add_u32 %[pc0], %[dl], %[pc0]\n\t" \
  "v_mfma_f32_16x16x32_bf16 %[c11], v[228:231], %[k11], %[c11]\n\t" \
  "s_or_b32 %[hb2], %[hb0], 0x4000\n\t" \
  "ds_read_b128 %[k11], %[pc3] offset:2048\n\t" \
  "buffer_load_dwordx4 v[220:223], %[vo], %[srd], %[hb0] offen offset:1024 sc1\n\t" \
  "s_waitcnt lgkmcnt(8)\n\t" \
  "v_mfma_f32_16x16x32_bf16 %[c02], v[224:227], %[k12], %[c02]\n\t" \
  "v_add_u32 %[pc1], %[dl], %[pc1]\n\t" \
  "v_mfma_f32_16x16x32_bf16 %[c12], v[228:231], %[k12], %[c12]\n\t" \
  "v_add_u32 %[fv], 1, %[fv]\n\t" \
  "ds_read_b128 %[k12], %[pc3] offset:4096\n\t" \
  "s_waitcnt lgkmcnt(7)\n\t" \
  "v_mfma_f32_16x16x32_bf16 %[c03], v[224:227], %[k13], %[c03]\n\t" \
  "v_readfirstlane_b32 %[s], v252\n\t" \
  "v_mfma_f32_16x16x32_bf16 %[c13], v[228:231], %[k13], %[c13]\n\t" \
  "ds_read_b128 %[k13], %[pc3] offset:6144\n\t" \
  "ds_write_b32 %[fw], %[fv]\n\t" \
  "s_cmp_gt_u32 %[s], %[cn]\n\t" \
  "s_cbranch_scc0 Lcs1_%=\n\t" \
  "Lcb1_%=:\n\t" \
  "buffer_load_dwordx4 v[224:227], %[vo], %[srd], %[hb1] offen offset:0 sc1\n\t" \
  "s_waitcnt vmcnt(13) lgkmcnt(8)\n\t" \
  "ds_read_b128 v[252:255], %[fa]\n\t" \
  "v_mfma_f32_16x16x32_bf16 %[c00], v[232:235], %[k00], %[c00]\n\t" \
  "s_or_b32 %[hb3], %[hb0], 0x6000\n\t" \
  "v_mfma_f32_16x16x32_bf16 %[c10], v[236:239], %[k00], %[c10]\n\t" \
  "ds_read_b128 %[k00], %[pc0] offset:0\n\t" \
  "s_waitcnt lgkmcnt(9)\n\t" \
  "v_mfma_f32_16x16x32_bf16 %[c01], v[232:235], %[k01], %[c01]\n\t" \
  "v_mfma_f32_16x16x32_bf16 %[c11], v[236:239], %[k01], %[c11]\n\t" \
  "ds_read_b128 %[k01], %[pc0] offset:2048\n\t" \
  "buffer_load_dwordx4 v[228:231], %[vo], %[srd], %[hb1] offen offset:1024 sc1\n\t" \
  "s_waitcnt lgkmcnt(9)\n\t" \
  "v_mfma_f32_16x16x32_bf16 %[c02], v[232:235], %[k02], %[c02]\n\t" \
  "v_mfma_f32_16x16x32_bf16 %[c12], v[236:239], %[k02], %[c12]\n\t" \
  "ds_read_b128 %[k02], %[pc0] offset:4096\n\t" \
  "s_waitcnt lgkmcnt(9)\n\t" \
  "v_mfma_f32_16x16x32_bf16 %[c03], v[232:235], %[k03], %[c03]\n\t" \
  "v_mfma_f32_16x16x32_bf16 %[c13], v[236:239], %[k03], %[c13]\n\t" \
  "ds_read_b128 %[k03], %[pc0] offset:6144\n\t" \
  "buffer_load_dwordx4 v[232:235], %[vo], %[srd], %[hb2] offen offset:0 sc1\n\t" \
  "s_waitcnt vmcnt(13) lgkmcnt(9)\n\t" \
  "v_mfma_f32_16x16x32_bf16 %[c00], v[240:243], %[k10], %[c00]\n\t" \
  "v_add_u32 %[pc2], %[dl], %[pc2]\n\t" \
  "v_mfma_f32_16x16x32_bf16 %[c10], v[244:247], %[k10], %[c10]\n\t" \
  "ds_read_b128 %[k10], %[pc1] offset:0\n\t" \
  "s_waitcnt lgkmcnt(9)\n\t" \
  "v_mfma_f32_16x16x32_bf16 %[c01], v[240:243], %[k11], %[c01]\n\t" \
  "v_add_u32 %[pc3], %[dl], %[pc3]\n\t" \
  "v_mfma_f32_16x16x32_bf16 %[c11], v[244:247], %[k11], %[c11]\n\t" \
  "ds_read_b128 %[k11], %[pc1] offset:2048\n\t" \
  "buffer_load_dwordx4 v[236:239], %[vo], %[srd], %[hb2] offen offset:1024 sc1\n\t" \
  "s_waitcnt lgkmcnt(6)\n\t" \
  "v_mfma_f32_16x16x32_bf16 %[c02], v[240:243], %[k12], %[c02]\n\t" \
  "v_readfirstlane_b32 %[s], v254\n\t" \
  "v_mfma_f32_16x16x32_bf16 %[c12], v[244:247], %[k12], %[c12]\n\t" \
  "ds_read_b128 %[k12], %[pc1] offset:4096\n\t" \
  "v_mfma_f32_16x16x32_bf16 %[c03], v[240:243], %[k13], %[c03]\n\t" \
  "s_add_u32 %[hn], %[hn], 1\n\t" \
  "v_mfma_f32_16x16x32_bf16 %[c13], v[244:247], %[k13], %[c13]\n\t" \
  "ds_read_b128 %[k13], %[pc1] offset:6144\n\t" \
  "buffer_load_dwordx4 v[240:243], %[vo], %[srd], %[hb3] offen offset:0 sc1\n\t" \
  "buffer_load_dwordx4 v[244:247], %[vo], %[srd], %[hb3] offen offset:1024 sc1\n\t" \
  "s_cmp_ge_u32 %[s], %[hn]\n\t" \
  "s_cbranch_scc0 Lhs1_%=\n\t" \
  "Lhb1_%=:\n\t" \
  "s_cmp_lt_u32 %[cn], 14\n\t" \
  "s_cbranch_scc1 Lg2_%=\n\t" \
  "s_waitcnt vmcnt(14) lgkmcnt(7)\n\t" \
  "ds_read_b128 v[252:255], %[fa]\n\t" \
  "v_mfma_f32_16x16x32_bf16 %[c00], v[184:187], %[k00], %[c00]\n\t" \
  "v_mfma_f32_16x16x32_bf16 %[c10], v[188:191], %[k00], %[c10]\n\t" \
  "ds_read_b128 %[k00], %[pc2] offset:0\n\t" \
  "s_waitcnt lgkmcnt(8)\n\t" \
  "v_mfma_f32_16x16x32_bf16 %[c01], v[184:187], %[k01], %[c01]\n\t" \
  "v_mfma_f32_16x16x32_bf16 %[c11], v[188:191], %[k01], %[c11]\n\t" \
  "ds_read_b128 %[k01], %[pc2] offset:2048\n\t" \
  "s_waitcnt lgkmcnt(8)\n\t" \
  "v_mfma_f32_16x16x32_bf16 %[c02], v[184:187], %[k02], %[c02]\n\t" \
  "v_mfma_f32_16x16x32_bf16 %[c12], v[188:191], %[k02], %[c12]\n\t" \
  "ds_read_b128 %[k02], %[pc2] offset:4096\n\t" \
  "s_waitcnt lgkmcnt(8)\n\t" \
  "v_mfma_f32_16x16x32_bf16 %[c03], v[184:187], %[k03], %[c03]\n\t" \
  "s_add_u32 %[cn], %[cn], 1\n\t" \
  "v_mfma_f32_16x16x32_bf16 %[c13], v[188:191], %[k03], %[c13]\n\t" \
  "ds_read_b128 %[k03], %[pc2] offset:6144\n\t" \
  "s_waitcnt vmcnt(12) lgkmcnt(8)\n\t" \
  "v_mfma_f32_16x16x32_bf16 %[c00], v[192:195], %[k10], %[c00]\n\t" \
  "s_cmp_eq_u32 %[cn], 9\n\t" \
  "s_cselect_b32 %[dl], 0x20000, %[d16]\n\t" \
  "v_mfma_f32_16x16x32_bf16 %[c10], v[196:199], %[k10], %[c10]\n\t" \
  "s_bitcmp1_b32 %[wr], %[cn]\n\t" \
  "s_cselect_b32 %[dl], 0xfffe4000, %[dl]\n\t" \
  "ds_read_b128 %[k10], %[pc3] offset:0\n\t" \
  "s_waitcnt lgkmcnt(8)\n\t" \
  "v_mfma_f32_16x16x32_bf16 %[c01], v[192:195], %[k11], %[c01]\n\t" \
  "v_add_u32 %[pc0], %[dl], %[pc0]\n\t" \
  "v_mfma_f32_16x16x32_bf16 %[c11], v[196:199], %[k11], %[c11]\n\t" \
  "ds_read_b128 %[k11], %[pc3] offset:2048\n\t" \
  "s_waitcnt lgkmcnt(8)\n\t" \
  "v_mfma_f32_16x16x32_bf16 %[c02], v[192:195], %[k12], %[c02]\n\t" \
  "v_add_u32 %[pc1], %[dl], %[pc1]\n\t" \
  "v_mfma_f32_16x16x32_bf16 %[c12], v[196:199], %[k12], %[c12]\n\t" \
  "ds_read_b128 %[k12], %[pc3] offset:4096\n\t" \
  "s_waitcnt lgkmcnt(7)\n\t" \
  "v_mfma_f32_16x16x32_bf16 %[c03], v[192:195], %[k13], %[c03]\n\t" \
  "v_readfirstlane_b32 %[s], v253\n\t" \
  "v_mfma_f32_16x16x32_bf16 %[c13], v[196:199], %[k13], %[c13]\n\t" \
  "ds_read_b128 %[k13], %[pc3] offset:6144\n\t" \
  "s_cmp_gt_u32 %[s], %[cn]\n\t" \
  "s_cbranch_scc0 Lcs0t_%=\n\t" \
  "Lcb0t_%=:\n\t" \
  "s_waitcnt vmcnt(10) lgkmcnt(7)\n\t" \
  "v_mfma_f32_16x16x32_bf16 %[c00], v[200:203], %[k00], %[c00]\n\t" \
  "v_mfma_f32_16x16x32_bf16 %[c10], v[204:207], %[k00], %[c10]\n\t" \
  "ds_read_b128 %[k00], %[pc0] offset:0\n\t" \
  "s_waitcnt lgkmcnt(7)\n\t" \
  "v_mfma_f32_16x16x32_bf16 %[c01], v[200:203], %[k01], %[c01]\n\t" \
  "v_mfma_f32_16x16x32_bf16 %[c11], v[204:207], %[k01], %[c11]\n\t" \
  "ds_read_b128 %[k01], %[pc0] offset:2048\n\t" \
  "s_waitcnt lgkmcnt(7)\n\t" \
  "v_mfma_f32_16x16x32_bf16 %[c02], v[200:203], %[k02], %[c02]\n\t" \
  "v_mfma_f32_16x16x32_bf16 %[c12], v[204:207], %[k02], %[c12]\n\t" \
  "ds_read_b128 %[k02], %[pc0] offset:4096\n\t" \
  "s_waitcnt lgkmcnt(7)\n\t" \
  "v_mfma_f32_16x16x32_bf16 %[c03], v[200:203], %[k03], %[c03]\n\t" \
  "v_mfma_f32_16x16x32_bf16 %[c13], v[204:207], %[k03], %[c13]\n\t" \
  "ds_read_b128 %[k03], %[pc0] offset:6144\n\t" \
  "s_waitcnt vmcnt(8) lgkmcnt(7)\n\t" \
  "v_mfma_f32_16x16x32_bf16 %[c00], v[208:211], %[k10], %[c00]\n\t" \
  "v_add_u32 %[pc2], %[dl], %[pc2]\n\t" \
  "v_mfma_f32_16x16x32_bf16 %[c10], v[212:215], %[k10], %[c10]\n\t" \
  "ds_read_b128 %[k10], %[pc1] offset:0\n\t" \
  "s_waitcnt lgkmcnt(7)\n\t" \
  "v_mfma_f32_16x16x32_bf16 %[c01], v[208:211], %[k11], %[c01]\n\t" \
  "v_add_u32 %[pc3], %[dl], %[pc3]\n\t" \
  "v_mfma_f32_16x16x32_bf16 %[c11], v[212:215], %[k11], %[c11]\n\t" \
  "ds_read_b128 %[k11], %[pc1] offset:2048\n\t" \
  "s_waitcnt lgkmcnt(7)\n\t" \
  "v_mfma_f32_16x16x32_bf16 %[c02], v[208:211], %[k12], %[c02]\n\t" \
  "v_mfma_f32_16x16x32_bf16 %[c12], v[212:215], %[k12], %[c12]\n\t" \
  "ds_read_b128 %[k12], %[pc1] offset:4096\n\t" \
  "s_waitcnt lgkmcnt(7)\n\t" \
  "v_mfma_f32_16x16x32_bf16 %[c03], v[208:211], %[k13], %[c03]\n\t" \
  "v_mfma_f32_16x16x32_bf16 %[c13], v[212:215], %[k13], %[c13]\n\t" \
  "ds_read_b128 %[k13], %[pc1] offset:6144\n\t" \
  "s_waitcnt vmcnt(6) lgkmcnt(7)\n\t" \
  "v_mfma_f32_16x16x32_bf16 %[c00], v[216:219], %[k00], %[c00]\n\t" \
  "v_mfma_f32_16x16x32_bf16 %[c10], v[220:223], %[k00], %[c10]\n\t" \
  "ds_read_b128 %[k00], %[pc2] offset:0\n\t" \
  "s_waitcnt lgkmcnt(7)\n\t" \
  "v_mfma_f32_16x16x32_bf16 %[c01], v[216:219], %[k01], %[c01]\n\t" \
  "v_mfma_f32_16x16x32_bf16 %[c11], v[220:223], %[k01], %[c11]\n\t" \
  "ds_read_b128 %[k01], %[pc2] offset:2048\n\t" \
  "s_waitcnt lgkmcnt(7)\n\t" \
  "v_mfma_f32_16x16x32_bf16 %[c02], v[216:219], %[k02], %[c02]\n\t" \
  "v_mfma_f32_16x16x32_bf16 %[c12], v[220:223], %[k02], %[c12]\n\t" \
  "ds_read_b128 %[k02], %[pc2] offset:4096\n\t" \
  "s_waitcnt lgkmcnt(7)\n\t" \
  "v_mfma_f32_16x16x32_bf16 %[c03], v[216:219], %[k03], %[c03]\n\t" \
  "v_mfma_f32_16x16x32_bf16 %[c13], v[220:223], %[k03], %[c13]\n\t" \
  "ds_read_b128 %[k03], %[pc2] offset:6144\n\t" \
  "s_waitcnt vmcnt(4) lgkmcnt(7)\n\t" \
  "v_mfma_f32_16x16x32_bf16 %[c00], v[224:227], %[k10], %[c00]\n\t" \
  "v_mfma_f32_16x16x32_bf16 %[c10], v[228:231], %[k10], %[c10]\n\t" \
  "ds_read_b128 %[k10], %[pc3] offset:0\n\t" \
  "s_waitcnt lgkmcnt(7)\n\t" \
  "v_mfma_f32_16x16x32_bf16 %[c01], v[224:227], %[k11], %[c01]\n\t" \
  "v_mfma_f32_16x16x32_bf16 %[c11], v[228:231], %[k11], %[c11]\n\t" \
  "ds_read_b128 %[k11], %[pc3] offset:2048\n\t" \
  "s_waitcnt lgkmcnt(7)\n\t" \
  "v_mfma_f32_16x16x32_bf16 %[c02], v[224:227], %[k12], %[c02]\n\t" \
  "v_mfma_f32_16x16x32_bf16 %[c12], v[228:231], %[k12], %[c12]\n\t" \
  "ds_read_b128 %[k12], %[pc3] offset:4096\n\t" \
  "s_waitcnt lgkmcnt(7)\n\t" \
  "v_mfma_f32_16x16x32_bf16 %[c03], v[224:227], %[k13], %[c03]\n\t" \
  "v_mfma_f32_16x16x32_bf16 %[c13], v[228:231], %[k13], %[c13]\n\t" \
  "ds_read_b128 %[k13], %[pc3] offset:6144\n\t" \
  "s_waitcnt vmcnt(2) lgkmcnt(7)\n\t" \
  "v_mfma_f32_16x16x32_bf16 %[c00], v[232:235], %[k00], %[c00]\n\t" \
  "v_mfma_f32_16x16x32_bf16 %[c10], v[236:239], %[k00], %[c10]\n\t" \
  "s_waitcnt lgkmcnt(6)\n\t" \
  "v_mfma_f32_16x16x32_bf16 %[c01], v[232:235], %[k01], %[c01]\n\t" \
  "v_mfma_f32_16x16x32_bf16 %[c11], v[236:239], %[k01], %[c11]\n\t" \
  "s_waitcnt lgkmcnt(5)\n\t" \
  "v_mfma_f32_16x16x32_bf16 %[c02], v[232:235], %[k02], %[c02]\n\t" \
  "v_mfma_f32_16x16x32_bf16 %[c12], v[236:239], %[k02], %[c12]\n\t" \
  "s_waitcnt lgkmcnt(4)\n\t" \
  "v_mfma_f32_16x16x32_bf16 %[c03], v[232:235], %[k03], %[c03]\n\t" \
  "v_mfma_f32_16x16x32_bf16 %[c13], v[236:239], %[k03], %[c13]\n\t" \
  "s_waitcnt vmcnt(0) lgkmcnt(3)\n\t" \
  "v_mfma_f32_16x16x32_bf16 %[c00], v[240:243], %[k10], %[c00]\n\t" \
  "v_mfma_f32_16x16x32_bf16 %[c10], v[244:247], %[k10], %[c10]\n\t" \
  "s_waitcnt lgkmcnt(2)\n\t" \
  "v_mfma_f32_16x16x32_bf16 %[c01], v[240:243], %[k11], %[c01]\n\t" \
  "v_mfma_f32_16x16x32_bf16 %[c11], v[244:247], %[k11], %[c11]\n\t" \
  "s_waitcnt lgkmcnt(1)\n\t" \
  "v_mfma_f32_16x16x32_bf16 %[c02], v[240:243], %[k12], %[c02]\n\t" \
  "v_mfma_f32_16x16x32_bf16 %[c12], v[244:247], %[k12], %[c12]\n\t" \
  "s_waitcnt lgkmcnt(0)\n\t" \
  "v_mfma_f32_16x16x32_bf16 %[c03], v[240:243], %[k13], %[c03]\n\t" \
  "v_mfma_f32_16x16x32_bf16 %[c13], v[244:247], %[k13], %[c13]\n\t" \
  "s_branch Lend_%=\n\t" \
  "Lcs0_%=:\n\t" \
  "s_add_u32 %[s2], %[cn], 1\n\t" \
  "Lcr0_%=:\n\t" \
  "ds_read_b128 v[252:255], %[fa]\n\t" \
  "s_cmp_eq_u32 %[to], 0\n\t" \
  "s_cbranch_scc1 Lcb0_%=\n\t" \
  "s_sub_u32 %[to], %[to], 1\n\t" \
  "s_waitcnt lgkmcnt(0)\n\t" \
  "v_readfirstlane_b32 %[s], v253\n\t" \
  "s_cmp_ge_u32 %[s], %[s2]\n\t" \
  "s_cbranch_scc0 Lcr0_%=\n\t" \
  "s_branch Lcb0_%=\n\t" \
  "Lhs0_%=:\n\t" \
  "ds_read_b128 v[252:255], %[fa]\n\t" \
  "s_cmp_eq_u32 %[to], 0\n\t" \
  "s_cbranch_scc1 Lhb0_%=\n\t" \
  "s_sub_u32 %[to], %[to], 1\n\t" \
  "s_waitcnt lgkmcnt(0)\n\t" \
  "v_readfirstlane_b32 %[s], v254\n\t" \
  "s_cmp_ge_u32 %[s], %[hn]\n\t" \
  "s_cbranch_scc0 Lhs0_%=\n\t" \
  "s_branch Lhb0_%=\n\t" \
  "Lcs1_%=:\n\t" \
  "s_add_u32 %[s2], %[cn], 1\n\t" \
  "Lcr1_%=:\n\t" \
  "ds_read_b128 v[252:255], %[fa]\n\t" \
  "s_cmp_eq_u32 %[to], 0\n\t" \
  "s_cbranch_scc1 Lcb1_%=\n\t" \
  "s_sub_u32 %[to], %[to], 1\n\t" \
  "s_waitcnt lgkmcnt(0)\n\t" \
  "v_readfirstlane_b32 %[s], v252\n\t" \
  "s_cmp_ge_u32 %[s], %[s2]\n\t" \
  "s_cbranch_scc0 Lcr1_%=\n\t" \
  "s_branch Lcb1_%=\n\t" \
  "Lhs1_%=:\n\t" \
  "ds_read_b128 v[252:255], %[fa]\n\t" \
  "s_cmp_eq_u32 %[to], 0\n\t" \
  "s_cbranch_scc1 Lhb1_%=\n\t" \
  "s_sub_u32 %[to], %[to], 1\n\t" \
  "s_waitcnt lgkmcnt(0)\n\t" \
  "v_readfirstlane_b32 %[s], v254\n\t" \
  "s_cmp_ge_u32 %[s], %[hn]\n\t" \
  "s_cbranch_scc0 Lhs1_%=\n\t" \
  "s_branch Lhb1_%=\n\t" \
  "Lcs0t_%=:\n\t" \
  "s_add_u32 %[s2], %[cn], 1\n\t" \
  "Lcr0t_%=:\n\t" \
  "ds_read_b128 v[252:255], %[fa]\n\t" \
  "s_cmp_eq_u32 %[to], 0\n\t" \
  "s_cbranch_scc1 Lcb0t_%=\n\t" \
  "s_sub_u32 %[to], %[to], 1\n\t" \
  "s_waitcnt lgkmcnt(0)\n\t" \
  "v_readfirstlane_b32 %[s], v253\n\t" \
  "s_cmp_ge_u32 %[s], %[s2]\n\t" \
  "s_cbranch_scc0 Lcr0t_%=\n\t" \
  "s_branch Lcb0t_%=\n\t" \
  "Lend_%=:\n\t" \
  "s_nop 7\n\t"
#define ASM_REQ \
  "s_cmp_gt_u32 1, %[own]\n\t" \
  "s_addc_u32 %[hb0], 1, -1\n\t" \
  "s_lshl_b32 %[hb0], %[hb0], 15\n\t" \
  "s_or_b32 %[hb1], %[hb0], 0x2000\n\t" \
  "s_or_b32 %[hb2], %[hb0], 0x4000\n\t" \
  "s_or_b32 %[hb3], %[hb0], 0x6000\n\t" \
  "buffer_load_dwordx4 v[216:219], %[vo], %[srd], %[hb0] offen offset:0 sc1\n\t" \
  "buffer_load_dwordx4 v[220:223], %[vo], %[srd], %[hb0] offen offset:1024 sc1\n\t" \
  "buffer_load_dwordx4 v[224:227], %[vo], %[srd], %[hb1] offen offset:0 sc1\n\t" \
  "buffer_load_dwordx4 v[228:231], %[vo], %[srd], %[hb1] offen offset:1024 sc1\n\t" \
  "buffer_load_dwordx4 v[232:235], %[vo], %[srd], %[hb2] offen offset:0 sc1\n\t" \
  "buffer_load_dwordx4 v[236:239], %[vo], %[srd], %[hb2] offen offset:1024 sc1\n\t" \
  "buffer_load_dwordx4 v[240:243], %[vo], %[srd], %[hb3] offen offset:0 sc1\n\t" \
  "buffer_load_dwordx4 v[244:247], %[vo], %[srd], %[hb3] offen offset:1024 sc1\n\t" \
  "s_cmp_gt_u32 2, %[own]\n\t" \
  "s_addc_u32 %[hb0], 2, -1\n\t" \
  "s_lshl_b32 %[hb0], %[hb0], 15\n\t" \
  "s_or_b32 %[hb1], %[hb0], 0x2000\n\t" \
  "s_or_b32 %[hb2], %[hb0], 0x4000\n\t" \
  "s_or_b32 %[hb3], %[hb0], 0x6000\n\t" \
  "buffer_load_dwordx4 v[184:187], %[vo], %[srd], %[hb0] offen offset:0 sc1\n\t" \
  "buffer_load_dwordx4 v[188:191], %[vo], %[srd], %[hb0] offen offset:1024 sc1\n\t" \
  "buffer_load_dwordx4 v[192:195], %[vo], %[srd], %[hb1] offen offset:0 sc1\n\t" \
  "buffer_load_dwordx4 v[196:199], %[vo], %[srd], %[hb1] offen offset:1024 sc1\n\t" \
  "buffer_load_dwordx4 v[200:203], %[vo], %[srd], %[hb2] offen offset:0 sc1\n\t" \
  "buffer_load_dwordx4 v[204:207], %[vo], %[srd], %[hb2] offen offset:1024 sc1\n\t" \
  "buffer_load_dwordx4 v[208:211], %[vo], %[srd], %[hb3] offen offset:0 sc1\n\t" \
  "buffer_load_dwordx4 v[212:215], %[vo], %[srd], %[hb3] offen offset:1024 sc1\n\t"
#define NS 0
#define TF 0

#define OST " sc1 nt"
#define XPRIO 2
#define XPRIO2 1
#define MPRIO2 2
#define HDLY 8
#define LPRIO 2
#define POLLN 8
#define YGATE 11
#define LTHR 16
#define CPK 9
#define W2P 0
#define SWP 0
#define MTOP 1
#define CROT 0
#define NRG0 3
#define NRG1 3
#define CSL 0
#define NSI 8
#define DSL 1
#define FSB 128
#define FSW 8
#define XP3 3
#define XP4 3
#define LZ 1
#define KROT 0
#define XGT 0
#define W1N 8
#define W1P 1
#define WIF 0
#define W2G 0
#define W2S 0
#define WNT " nt"
#define W1A 2
#define W1Bb 3
#define W1C 4
#define W1D 6
#define CT 0
#define MGP 1
#define RQD 0
#define XG2 2
#define NFW 28
#define SPINLOG 18
constexpr int D_ = 1024, F_ = 3584;
constexpr int L_XH = 131072;
constexpr int MISC = 155648;
constexpr int L_GT = MISC;
constexpr int L_FLG = L_GT + 512;
constexpr int L_JNK = L_FLG + 256;
constexpr int L_SEL = L_JNK + 256;
constexpr int L_BROW = L_SEL + 128;
constexpr int L_ACT = L_BROW + 2048;
constexpr int L_CAND = L_ACT + 1024;
constexpr int L_ZERO = L_CAND + 256;
constexpr int L_MAP = L_ZERO + 256;
constexpr int L_TAG = L_MAP + 512;
constexpr int L_RT = L_TAG + 16;
constexpr int LDS_BYTES = 163840;
static_assert(L_RT + 32 <= LDS_BYTES, "lds");
constexpr int FL_FULL = L_FLG, FL_FREE = L_FLG + 16, FL_C = L_FLG + 32  , FL_CFREE = L_FLG + 48, FL_TAG = L_FLG + 64  ,
              FL_XD = L_FLG + 80, FL_TK = L_FLG + 96, FL_SEL = L_FLG + 112, FL_G = L_FLG + 128, FL_W = L_FLG + 136, FL_HS = L_FLG + 144, FL_C8 = L_FLG + 160;
constexpr unsigned W_EPOCH = 0;
constexpr unsigned W_R2 = 256;
constexpr unsigned W_G2 = W_R2 + 8192;
constexpr unsigned W_HF = W_G2 + 8192;
constexpr unsigned W_AGG = W_HF + 256 * FSB;
constexpr unsigned W_SEL = W_AGG + 262144;
constexpr unsigned W_GU = W_SEL + 4096;
constexpr unsigned W_YP = W_GU + 56 * 16384;
constexpr unsigned W_H = 8u << 20;
constexpr unsigned W_DBG = 20u << 20;
constexpr unsigned W_END = 21u << 20;
static_assert(W_YP + 256 * 7168 <= W_H, "ws");
#define WS_BASE(n) (((n) - W_END) & ~(size_t)0xfffff)
constexpr int SPIN_MAX = 1 << SPINLOG;

DEV float bf2f(unsigned v) { return __uint_as_float(v << 16); }
DEV float bfhi(unsigned v) { return __uint_as_float(v & 0xffff0000u); }
DEV u16 f2bf(float f) { return __builtin_bit_cast(u16, (__bf16)f); }
DEV float rbf(float f) { return bf2f(f2bf(f)); }
typedef __bf16 bf16x2_t __attribute__((ext_vector_type(2)));
DEV unsigned pk2(float a, float b) { return __builtin_bit_cast(unsigned, __builtin_convertvector((f32x2){a, b}, bf16x2_t)); }
DEV float fma1(float a, float b, float c) { float d; asm("v_fma_f32 %0, %1, %2, %3" : "=v"(d) : "v"(a), "v"(b), "v"(c)); return d; }
DEV float fnma1(float a, float b, float c) { float d; asm("v_fma_f32 %0, -%1, %2, %3" : "=v"(d) : "v"(a), "v"(b), "v"(c)); return d; }
DEV float fnmn1(float a, float b, float c) { float d; asm("v_fma_f32 %0, -%1, %2, -%3" : "=v"(d) : "v"(a), "v"(b), "v"(c)); return d; }
DEV float mul1(float a, float b) { float d; asm("v_mul_f32 %0, %1, %2" : "=v"(d) : "v"(a), "v"(b)); return d; }
DEV f32x2 cmul(f32x2 a, f32x2 b) { return (f32x2){fnma1(a.y, b.y, mul1(a.x, b.x)), fma1(a.y, b.x, mul1(a.x, b.y))}; }
DEV f32x2 cfma(f32x2 a, f32x2 b, f32x2 c) { return (f32x2){fma1(a.x, b.x, fnma1(a.y, b.y, c.x)), fma1(a.x, b.y, fma1(a.y, b.x, c.y))}; }
template <int N> DEV void waitv() { asm volatile("s_waitcnt vmcnt(%0)" ::"n"(N) : "memory"); }
DEV void bar() { asm volatile("s_barrier" ::: "memory"); }
DEV unsigned lds_rd32(unsigned a) { unsigned v; asm volatile("ds_read_b32 %0, %1\n\ts_waitcnt lgkmcnt(0)" : "=v"(v) : "v"(a) : "memory"); return v; }
DEV u32x4 lds_rd128(unsigned a) { u32x4 v; asm volatile("ds_read_b128 %0, %1\n\ts_waitcnt lgkmcnt(0)" : "=v"(v) : "v"(a) : "memory"); return v; }
DEV void lds_wr32(unsigned a, unsigned v) { asm volatile("ds_write_b32 %0, %1" ::"v"(a), "v"(v) : "memory"); }
template <int S = 1> DEV void lds_wait_ge(unsigned a, unsigned need) {
#pragma unroll 1
  for (int it = 0; it < SPIN_MAX * 4; ++it) { if (__builtin_amdgcn_readfirstlane(lds_rd32(a)) >= need) break; __builtin_amdgcn_s_sleep(S); }
}
typedef __amdgpu_buffer_rsrc_t rsrc_t;
DEV u32x4 ld128(rsrc_t r, unsigned vo, unsigned so = 0) { return __builtin_amdgcn_raw_buffer_load_b128(r, vo, so, 0); }
#define FRESH() asm volatile("" ::: "memory")
DEV u32x4 ld128c(rsrc_t r, unsigned vo) { return __builtin_amdgcn_raw_buffer_load_b128(r, vo, 0, 16); }
DEV u32x2 ld64c(rsrc_t r, unsigned vo) { return __builtin_amdgcn_raw_buffer_load_b64(r, vo, 0, 16); }
DEV void st128c(rsrc_t r, unsigned vo, u32x4 v) {
  asm volatile("s_nop 4\n\tbuffer_store_dwordx4 %0, %1, %2, 0 offen sc1\n\ts_nop 7\n\ts_nop 7" ::"v"(v), "v"(vo), "s"(r) : "memory");
}
DEV void st64c(rsrc_t r, unsigned vo, u32x2 v) { __builtin_amdgcn_raw_buffer_store_b64(v, r, vo, 0, 16); }
DEV rsrc_t mk(const void* p, unsigned bytes) { return __builtin_amdgcn_make_buffer_rsrc((void*)p, 0, bytes, 0x00020000); }

DEV unsigned wave_maxu(unsigned v) {
  v = max(v, (unsigned)__builtin_amdgcn_update_dpp((int)v, (int)v, 0x111, 0xf, 0xf, false));
  v = max(v, (unsigned)__builtin_amdgcn_update_dpp((int)v, (int)v, 0x112, 0xf, 0xf, false));
  v = max(v, (unsigned)__builtin_amdgcn_update_dpp((int)v, (int)v, 0x114, 0xf, 0xf, false));
  v = max(v, (unsigned)__builtin_amdgcn_update_dpp((int)v, (int)v, 0x118, 0xf, 0xf, false));
  v = max(v, (unsigned)__builtin_amdgcn_update_dpp((int)v, (int)v, 0x142, 0xa, 0xf, false));
  v = max(v, (unsigned)__builtin_amdgcn_update_dpp((int)v, (int)v, 0x143, 0xc, 0xf, false));
  return (unsigned)__builtin_amdgcn_readlane((int)v, 63);
}
DEV float sum16_32(float v) {
  { auto p = __builtin_amdgcn_permlane16_swap(__float_as_uint(v), __float_as_uint(v), false, false); v = __uint_as_float(p[0]) + __uint_as_float(p[1]); }
  { auto p = __builtin_amdgcn_permlane32_swap(__float_as_uint(v), __float_as_uint(v), false, false); v = __uint_as_float(p[0]) + __uint_as_float(p[1]); }
  return v;
}
DEV float wave_sum(float v) {
  v += __uint_as_float((unsigned)__builtin_amdgcn_update_dpp(0, (int)__float_as_uint(v), 0x128, 0xf, 0xf, false));
  v += __uint_as_float((unsigned)__builtin_amdgcn_update_dpp(0, (int)__float_as_uint(v), 0x124, 0xf, 0xf, false));
  v += __uint_as_float((unsigned)__builtin_amdgcn_update_dpp(0, (int)__float_as_uint(v), 0x122, 0xf, 0xf, false));
  v += __uint_as_float((unsigned)__builtin_amdgcn_update_dpp(0, (int)__float_as_uint(v), 0x121, 0xf, 0xf, false));
  return sum16_32(v);
}
DEV unsigned route_key(float r, int idx) {
  unsigned u = __float_as_uint(r) >> 16;
  u = (u & 0x8000u) ? (~u & 0xffffu) : (u | 0x8000u);
  return (u << 16) | (unsigned)(2047 - idx);
}
DEV float key_val(unsigned key) {
  unsigned u = key >> 16;
  u = (u & 0x8000u) ? (u & 0x7fffu) : (~u & 0xffffu);
  return __uint_as_float(u << 16);
}

DEV void unit_trn(u32x4 (&w)[8]) {
  u32x4 b[8];
#pragma unroll
  for (int j = 0; j < 8; ++j)
#pragma unroll
    for (int t = 0; t < 4; ++t) b[j][t] = __builtin_amdgcn_perm(w[2 * t + 1][j >> 1], w[2 * t][j >> 1], (j & 1) ? 0x07060302u : 0x05040100u);
#pragma unroll
  for (int j = 0; j < 8; ++j) w[j] = b[j];
}
DEV f32x4 fma4(f32x4 a, f32x4 b, f32x4 c) { return __builtin_elementwise_fma(a, b, c); }
DEV f32x4 fnm4(f32x4 a, f32x4 b, f32x4 c) {
  const f32x2 l = __builtin_elementwise_fma(-a.lo, b.lo, c.lo), h = __builtin_elementwise_fma(-a.hi, b.hi, c.hi);
  return (f32x4){l.x, l.y, h.x, h.y};
}
DEV f32x4 nml4(f32x4 a, f32x4 b) { const f32x2 l = -a.lo * b.lo, h = -a.hi * b.hi; return (f32x4){l.x, l.y, h.x, h.y}; }
template <int CTRL> DEV float dpp1(float v) { return __uint_as_float((unsigned)__builtin_amdgcn_update_dpp(0, (int)__float_as_uint(v), CTRL, 0xf, 0xf, true)); }
template <int CTRL> DEV f32x4 dpp4(f32x4 v) { return (f32x4){dpp1<CTRL>(v.x), dpp1<CTRL>(v.y), dpp1<CTRL>(v.z), dpp1<CTRL>(v.w)}; }
DEV f32x4 splat4(float v) { f32x2 p = {v, v}; asm volatile("" : "+v"(p)); return (f32x4){p.x, p.y, p.x, p.y}; }
DEV void cexp4(f32x4 a2, f32x4 bv, f32x4& re, f32x4& im) {
  f32x4 m, cs, sn;
#pragma unroll
  for (int r = 0; r < 4; ++r) { const float ph = __builtin_amdgcn_fractf(bv[r]); m[r] = __builtin_amdgcn_exp2f(a2[r]); cs[r] = __builtin_amdgcn_cosf(ph); sn[r] = __builtin_amdgcn_sinf(ph); }
  re = m * cs; im = m * sn;
}
DEV f32x4 unpk4(u32x2 v) { return (f32x4){bf2f(v.x & 0xffffu), bfhi(v.x), bf2f(v.y & 0xffffu), bfhi(v.y)}; }
DEV void bcast8(int n, f32x4 v, f32x4& o) {
  switch (n) {
    case 0: o = dpp4<0x150>(v); break; case 1: o = dpp4<0x151>(v); break; case 2: o = dpp4<0x152>(v); break; case 3: o = dpp4<0x153>(v); break;
    case 4: o = dpp4<0x154>(v); break; case 5: o = dpp4<0x155>(v); break; case 6: o = dpp4<0x156>(v); break; default: o = dpp4<0x157>(v); break;
  }
}
DEV f32x4 lds4(LAS char* smem, int a) { return *(const LAS f32x4*)(smem + a); }
template <int S = 1> DEV void lds_wait_min4(unsigned a, unsigned need) {
#pragma unroll 1
  for (int it = 0; it < SPIN_MAX * 4; ++it) {
    const u32x4 f = lds_rd128(a);
    if (__builtin_amdgcn_readfirstlane(min(min(f.x, f.y), min(f.z, f.w))) >= need) break;
    __builtin_amdgcn_s_sleep(S);
  }
}

DEV void dma4(rsrc_t r0, rsrc_t r1, LAS char* la, const unsigned (&v)[4], unsigned so) {
  __builtin_amdgcn_raw_ptr_buffer_load_lds(r0, (LAS void*)la, 16, v[0], so, 0, 0);
  __builtin_amdgcn_raw_ptr_buffer_load_lds(r0, (LAS void*)la, 16, v[1], so, 1024, 0);
  __builtin_amdgcn_raw_ptr_buffer_load_lds(r1, (LAS void*)la, 16, v[2], so, 2048, 0);
  __builtin_amdgcn_raw_ptr_buffer_load_lds(r1, (LAS void*)la, 16, v[3], so, 3072, 0);
}

__global__ __launch_bounds__(512, 2) void fused(const u16*, const u16*, const u16*, const u16*, const u16*, const u16*, const u16*, const u16*, const u16*, const u16*,
                                                const u16*, const u16*, const u16*, const u16*, char*, float*) {
#if __HIP_DEVICE_COMPILE__
  unsigned kw0, kw1;
  asm volatile("s_load_dword %0, %2, 0x0\n\ts_load_dword %1, %2, 0x40" : "=&s"(kw0), "=&s"(kw1) : "s"(__builtin_amdgcn_kernarg_segment_ptr()));
  extern __shared__ __attribute__((aligned(16))) char smem_[];
  LAS char* smem = (LAS char*)smem_;
  const int tid = threadIdx.x, hw = __builtin_amdgcn_readfirstlane(tid >> 6), wid = SWP ? hw ^ ((hw >> 1) & 2) : hw;
  const int bid = blockIdx.x, xcd = bid & 7, loc = bid >> 3;
  const int c = (xcd >> 1) * 4 + (loc >> 3), j = (xcd & 1) * 8 + (loc & 7);
  int lane, fr, fq;
  auto relane = [&]() {
    asm volatile("v_mbcnt_lo_u32_b32 %0, -1, 0\n\tv_mbcnt_hi_u32_b32 %0, -1, %0" : "=v"(lane));
    __builtin_assume(lane >= 0 && lane < 64);
    fr = lane & 15; fq = lane >> 4;
  };
  const int crot = CROT == 0 ? c + 1 : CROT == 1 ? 4 * c + (c >> 2) : CROT == 2 ? (c & 1) + 4 * (c & 2) + (c >> 2) * 2 : CROT == 3 ? 2 * c : (c & 1) * 2 + 4 * (c & 2) + (c >> 2);
  const int pown = (j - crot) & 15;
  auto upos = [&](int u) { return u == 0 ? pown : u - 1 + (u - 1 >= pown ? 1 : 0); };
  u64 pc0;
  unsigned l0, l1, l2;
  asm volatile("s_getpc_b64 %0\n\tLk_pc:\n\ts_mov_b32 %1, Lk_m-Lk_pc\n\ts_mov_b32 %2, Lk_l-Lk_pc\n\ts_mov_b32 %3, Lk_x-Lk_pc" : "=s"(pc0), "=s"(l0), "=s"(l1), "=s"(l2));
  auto codepull = [&]() {
    const unsigned po = min((unsigned)(loc * 16 + (lane & 15)) * 128u, max(l0, max(l1, l2)) - 4u);
    if (lane < 16) __builtin_amdgcn_global_load_lds((const __attribute__((address_space(1))) void*)(pc0 + po), (LAS void*)(smem + L_JNK), 4, 0, 0);
  };
  u32x2 p0, p1, p2, dv;
  u32x4 xv[2][2], wv[2], gv[2];
#define MLOADS(A_, B_) \
    asm volatile("global_load_dwordx2 %0, %12, %16\n\tglobal_load_dwordx2 %1, %12, %17\n\tglobal_load_dwordx2 %2, %12, %18\n\t" \
                 "global_load_dwordx4 %4, %13, %19\n\tglobal_load_dwordx4 %5, %13, %19 offset:1024\n\tglobal_load_dwordx4 %6, %13, %19 offset:2048\n\tglobal_load_dwordx4 %7, %13, %19 offset:3072\n\t" \
                 "global_load_dwordx4 %8, %14, %20\n\tglobal_load_dwordx4 %9, %14, %20 offset:1024\n\tglobal_load_dwordx4 %10, %14, %21\n\tglobal_load_dwordx4 %11, %14, %21 offset:1024\n\t" \
                 "global_load_dwordx2 %3, %15, %22" \
                 : "=&v"(p0), "=&v"(p1), "=&v"(p2), "=&v"(dv), "=&v"(xv[0][0]), "=&v"(xv[0][1]), "=&v"(xv[1][0]), "=&v"(xv[1][1]), "=&v"(wv[0]), "=&v"(wv[1]), "=&v"(gv[0]), "=&v"(gv[1]) \
                 : "v"((j * 64 + wid * 16 + 4 * fq) * 2), "v"((c * 128 + j * 8 + wid * 2) * 2048 + lane * 16), "v"(lane * 16), "v"((j * 64 + 4 * fr) * 2), \
                   "s"(A_[1]), "s"(A_[2]), "s"(A_[7]), "s"(A_[0]), "s"(B_[1]), "s"(B_[5]), "s"(B_[0]) : "memory")
  if (wid < 4) {
    u64x8 ta, tb;
    if (MTOP) asm volatile("s_load_dwordx16 %0, %2, 0x0\n\ts_load_dwordx16 %1, %2, 0x40" : "=&s"(ta), "=&s"(tb) : "s"(__builtin_amdgcn_kernarg_segment_ptr()));
    relane();
    if (wid == 0) codepull();
    if (MTOP) {
      asm volatile("s_waitcnt lgkmcnt(0)" : "+s"(ta), "+s"(tb));
      MLOADS(ta, tb);
    }
  }
  const int l = wid & 1;
#define FFNDEF() int b_ = bid; asm volatile("" : "+s"(b_)); \
  const bool ffn = b_ < 8 * NFW; \
  const int sg = (b_ & 7) * 7 + (b_ >> 5), kq = (b_ >> 3) & 3, sg1 = (sg & ~1) + l, gu1 = sg & 1; \
  const unsigned gub = W_GU + ((sg1 * 4 + kq) * 2 + gu1) * 2048, ypb = W_YP + (kq * 4 + l * 2) * 16 * 7168 + sg * 128
  if (__builtin_expect(wid >= 4, 1)) {
  u64x8 ka, kb;
  asm volatile("s_load_dwordx16 %0, %2, 0x0\n\ts_load_dwordx16 %1, %2, 0x40" : "=&s"(ka), "=&s"(kb) : "s"(__builtin_amdgcn_kernarg_segment_ptr()));
  relane();
  unsigned vo1[4];
#pragma unroll
  for (int e = 0; e < 4; ++e)
    vo1[e] = (l == 0 ? c * 128 + 8 * (lane >> 3) + 64 * (e & 1) + (e >> 1) : j * 64 + (lane >> 3) + 8 * (e & 1)) * 2048 + (((lane & 7) ^ (lane >> 3)) * 16) + 4096 - e * 1024;
  const unsigned s4 = (l == 0 ? 2 : 16) * 2048, so1 = wid >= 6 ? 2 * s4 : 0, la1 = l * 16384 + (wid >= 6 ? 8192 : 0);
  const int s0 = (KROT == 0 ? c + j : KROT == 1 ? (c >> 1) + (j >> 1) + 4 * (c >> 2) : KROT == 2 ? c + (j >> 1) + 4 * (j >> 3) : KROT == 3 ? 2 * c + j : KROT == 4 ? 4 * c + 2 * j + (c >> 2) : KROT == 5 ? (c >> 1) + j : 3 * c + j) & 15;
  unsigned sl0 = ((s0 + (s0 == j ? 1 : 0)) & 15) * 128 + so1;
  asm volatile("" : "+v"(vo1[0]), "+v"(vo1[1]), "+v"(vo1[2]), "+v"(vo1[3]) : "s"(sl0), "s"(s4), "s"(la1));
  asm volatile("s_waitcnt lgkmcnt(0)" : "+s"(ka), "+s"(kb) : "s"(kw0), "s"(kw1));
  const char *x = (const char*)ka[0], *Bre = (const char*)ka[3], *Bim = (const char*)ka[4], *Cre = (const char*)ka[5], *Cim = (const char*)ka[6], *w_gate = (const char*)kb[2],
             *w_up = (const char*)kb[3], *w_down = (const char*)kb[4];
  char* ws = (char*)kb[6];
  const rsrc_t r0 = mk((l == 0 ? x : Bre) - 4096, (l == 0 ? 1 << 22 : 1 << 21) + 4096), r1 = mk((l == 0 ? x : Bim) - 4096, (l == 0 ? 1 << 22 : 1 << 21) + 4096);
  auto issue = [&](unsigned sk, unsigned slot) { dma4(r0, r1, smem + slot + la1, vo1, sk); dma4(r0, r1, smem + slot + la1 + 4096, vo1, sk + s4); };
  issue(sl0, 0);
  __builtin_amdgcn_sched_barrier(0);
  const rsrc_t rws = mk(ws, W_END);
  const rsrc_t rx = mk(x, 2048 * 1024 * 2);
  u64 tbl;
  {
    const int n = (j - s0) & 15;
    const u64 seq = 0xfedcba9876543210ull, rot = s0 ? (seq >> (4 * s0)) | (seq << (64 - 4 * s0)) : seq, lo = (1ull << (4 * n)) - 1;
    tbl = (rot & lo) | ((rot >> 4) & ~lo) | ((u64)j << 60);
  }
  auto slice = [&](int k) { return ((unsigned)(tbl >> (4 * k)) & 15u) * 128u; };
  __builtin_amdgcn_sched_barrier(0);

  auto flag = [&](int a, unsigned v) { lds_wr32(lane == 0 ? (unsigned)a : (unsigned)(L_JNK + lane * 4), v); };
  auto flagmax = [&](int a, unsigned v) { asm volatile("ds_max_u32 %0, %1" ::"v"(lane == 0 ? (unsigned)a : (unsigned)(L_JNK + lane * 4)), "v"(v) : "memory"); };
  auto hpoll = [&](unsigned tagA, unsigned tagB) {
    lds_wait_min4(FL_FREE, 9u);
    __builtin_amdgcn_s_sleep(HDLY);
#pragma unroll 1
    for (int it = 0; it < SPIN_MAX; ++it) {
      FRESH();
      const u32x2 f = ld64c(rws, W_HF + (c * 16 + upos(lane >> 2)) * FSB + (lane & 3) * FSW);
      const u64 m = ~__ballot(lane < 4 * NS + 4 || (f.x == tagA && f.y == tagB));
      if (!m) break;
      flag(FL_C + 8, ((unsigned)__builtin_ctzll(m) >> 2) - 1u);
    }
    flag(FL_C + 8, 99u);
  };
    const rsrc_t rcr = mk(Cre, WIF == 4 ? 0 : 1 << 21), rci = mk(Cim, WIF == 4 || WIF == 5 ? 0 : 1 << 21);
    const unsigned ce = (j * 64 + 4 * (lane >> 3)) * 2048 + (((lane & 7) ^ (lane >> 4)) * 16);
    const unsigned co = (j * 64 + 32 + 4 * (lane >> 3)) * 2048 + (((lane & 7) ^ (4 + (lane >> 4))) * 16);
    auto cfill = [&](int u, int slot, int q0, int q1) {
      LAS char* dst = smem + slot * 16384;
#pragma unroll
      for (int q = q0; q < q1; ++q)
        __builtin_amdgcn_raw_ptr_buffer_load_lds(q < 8 ? rcr : rci, (LAS void*)(dst + q * 1024), 16, (q & 1) ? co : ce, ((q & 7) >> 1) * 2048 + ((upos(u) + crot) & 15) * 128, 0, 0);
    };
  if (__builtin_expect(wid == 4 || wid == 5, 1)) {
    __builtin_amdgcn_s_setprio(LPRIO);
    issue(slice(1), 32768);
    issue(slice(2), 65536);
    constexpr int K0 = 0;
    {
      u64 t = tbl >> (4 * K0 + 12);
#pragma unroll 1
      for (int k = K0; k < 13; ++k) {
        const unsigned sk = ((unsigned)t & 15u) << 7;
        waitv<16>();
        bar();
        issue(sk, ((k + 3) & 3) * 32768);
        if (k == CPK && l == 1) codepull();
        t >>= 4;
      }
    }
    u32x4 cq[3][16];
    auto creg = [&](int g, int q0 = 0, int q1 = 16, bool thr = true) {
#pragma unroll
      for (int q = q0; q < q1; ++q) {
        const unsigned so = ((q & 7) >> 1) * 2048 + ((upos(10 + l + 2 * g) + crot) & 15) * 128;
        if (q < 8) asm volatile("buffer_load_dwordx4 %0, %1, %2, %3 offen" : "=v"(cq[g][q]) : "v"((q & 1) ? co : ce), "s"(rcr), "s"(so) : "memory");
        else asm volatile("buffer_load_dwordx4 %0, %1, %2, %3 offen" : "=v"(cq[g][q]) : "v"((q & 1) ? co : ce), "s"(rci), "s"(so) : "memory");
        if ((q & 7) == 7 && thr) waitv<LTHR>();
      }
    };
#define CTL(i) { _Pragma("unroll") for (int e = (i) * CT; e < (i) * CT + CT; ++e) creg(2 - (e >> 2), (e & 3) * 4, (e & 3) * 4 + 4, false); }
    CTL(0)
    waitv<16 + 4 * CT>(); bar();
    CTL(1)
    waitv<8 + 8 * CT>(); bar();
    CTL(2)
    const unsigned tagA = lds_rd32(L_TAG), tagB = tagA + 0x40000000u;
    waitv<12 * CT>(); bar();
    CTL(3)
    bar();
    lds_wait_ge(FL_W + l * 4, 1u);
    __builtin_amdgcn_s_setprio(1);
#pragma unroll
    for (int g = 0; g < (TF ? CT : 0); ++g) creg(2 - g);
#pragma unroll 1
    for (int u = l; u < 8; u += 2) { if (TF) lds_wait_min4(FL_FREE, (u >> 1) + 1); cfill(u, u, 0, 8); waitv<LTHR>(); cfill(u, u, 8, 16); waitv<LTHR>(); if (u >= 2) flag(FL_C + l * 4, (unsigned)(u - 1)); }
#pragma unroll
    for (int g = 0; g < 3; ++g) {
      if (g >= (l ? NRG1 : NRG0) - CT) break;
      waitv<16>();
      if (g == 1) flag(FL_C + l * 4, (unsigned)(7 + l));
      creg(g);
    }
    waitv<0>();
    if ((l ? NRG1 : NRG0) - CT < 2) flag(FL_C + l * 4, (unsigned)(7 + l));
    if (l == 0) {
      __builtin_amdgcn_s_setprio(3);
      hpoll(tagA, tagB);
    }
    else {
      lds_wait_min4(FL_CFREE, 1u);
      cfill(8, 0, 0, 16);
      cfill(9, 8, 0, 16);
      waitv<0>();
      flagmax(FL_C8, 1u);
      flag(FL_C + 4, 10u);
    }
    __builtin_amdgcn_s_setprio(0);
    if (l == 0) { lds_wait_ge(FL_C8, 1u); flag(FL_C, 9u); }
#pragma unroll
    for (int g = 0; g < 3; ++g) {
      asm volatile("" : "+v"(cq[g][0]), "+v"(cq[g][1]), "+v"(cq[g][2]), "+v"(cq[g][3]), "+v"(cq[g][4]), "+v"(cq[g][5]), "+v"(cq[g][6]), "+v"(cq[g][7]),
                   "+v"(cq[g][8]), "+v"(cq[g][9]), "+v"(cq[g][10]), "+v"(cq[g][11]), "+v"(cq[g][12]), "+v"(cq[g][13]), "+v"(cq[g][14]), "+v"(cq[g][15]));
      const int u = 10 + l + 2 * g;
      lds_wait_min4<LZ>(FL_CFREE, (unsigned)(u - 8));
      const int slot = u - 9;
      if (g < (l ? NRG1 : NRG0)) {
#pragma unroll
        for (int q = 0; q < 16; ++q)
          asm volatile("ds_write_b128 %0, %1 offset:%2" ::"v"(slot * 16384 + lane * 16), "v"(cq[g][q]), "n"(q * 1024) : "memory");
      } else {
#pragma unroll
        for (int q = 0; q < 16; q += 4) { cfill(u, slot, q, q + 4); if (CSL) __builtin_amdgcn_s_sleep(CSL); }
        waitv<0>();
      }
      flag(FL_C + l * 4, g == 2 ? 99u : (unsigned)(u + 1));
    }
    asm volatile("Lk_l:");
    return;
  }
  {
    const int xw = wid - 6;
    __builtin_amdgcn_s_setprio(XPRIO);
    u32x2 ep;
    unsigned tagA, tagB;
    if (xw == 0) lds_wr32(L_FLG + lane * 4, 0u);
    else asm volatile("ds_write_b64 %0, %1\n\tds_write_b64 %2, %3" ::"v"(L_CAND + lane * 8), "v"((u32x2){0u, 0u}), "v"(L_MAP + lane * 8), "v"((u32x2){L_ZERO, L_ZERO}) : "memory");
    FFNDEF();
    u32x4 w1[4][8], w2[2][8];
    const rsrc_t rw1 = mk(gu1 ? w_up : w_gate, ffn ? D_ * F_ * 2 : 0), r2 = mk(w_down, ffn ? D_ * F_ * 2 : 0);
    unsigned o1 = (kq * 256 + 32 * (fr >> 3) + 8 * fq) * (F_ * 2) + (sg1 * 64 + 8 * (fr & 7)) * 2;
    unsigned o2 = (sg * 64 + 32 * (fr >> 3) + 8 * fq) * 2048 + (kq * 256 + xw * 128 + 8 * (fr & 7)) * 2;
#define LDW1(b) { _Pragma("unroll") for (int i = 4 * ((b) & 1); i < 4 * ((b) & 1) + 4; ++i) \
      asm volatile("buffer_load_dwordx4 %0, %1, %2, %3 offen" WNT : "=v"(w1[(b) >> 1][i]) : "v"(o1), "s"(rw1), "s"((((b) >> 1) * 64 + i) * (F_ * 2)) : "memory"); }
#define LDW2(a, b) { _Pragma("unroll") for (int i = a; i < b; ++i) \
      asm volatile("buffer_load_dwordx4 %0, %1, %2, %3 offen" WNT : "=v"(w2[i >> 3][i & 7]) : "v"(o2), "s"(r2), "s"((i & 7) * 2048 + (i >> 3) * 128) : "memory"); }
#define TRN(w, n) { asm volatile("s_waitcnt vmcnt(%8)" : "+v"(w[0]), "+v"(w[1]), "+v"(w[2]), "+v"(w[3]), "+v"(w[4]), "+v"(w[5]), "+v"(w[6]), "+v"(w[7]) : "n"(n) : "memory"); \
      unit_trn(w); asm volatile("" : "+v"(w[0]), "+v"(w[1]), "+v"(w[2]), "+v"(w[3]), "+v"(w[4]), "+v"(w[5]), "+v"(w[6]), "+v"(w[7])); }
    unsigned key[16];
    unsigned rgo = W_R2 + l * 4096 + lane * 16, ggo = W_G2 + c * 512 + (lane & 31) * 16;
    u32x4 g[4], a;
    asm volatile("" : "+v"(o1), "+v"(o2), "+v"(rgo), "+v"(ggo) : "s"(rw1), "s"(r2));
    auto A_EP = [&]() { asm volatile("buffer_load_dwordx2 %0, off, %1, 0 sc1" : "=v"(ep) : "s"(rws) : "memory"); };
    auto A_TAG = [&]() {
      asm volatile("" : "+v"(ep));
      tagA = ep.x + 1u; tagB = tagA + 0x40000000u;
      if (xw == 0 && lane == 0) lds_wr32(L_TAG, tagA);
    };
    auto A_CLR = [&]() {
      asm volatile("s_nop 1\n\t"
                   "buffer_store_dwordx4 %0, %1, %3, 0 offen sc1\n\tbuffer_store_dwordx4 %0, %1, %3, 0 offen offset:1024 sc1\n\t"
                   "buffer_store_dwordx4 %0, %2, %3, 0 offen sc1\n\tbuffer_store_dwordx4 %0, %2, %3, %4 offen sc1\n\t"
                   "buffer_store_dwordx4 %0, %2, %3, %5 offen sc1\n\tbuffer_store_dwordx4 %0, %2, %3, %6 offen sc1\n\t"
                   "buffer_store_dwordx4 %0, %7, %3, %8 offen sc1\n\tbuffer_store_dwordx4 %0, %7, %3, %8 offen offset:1024 sc1\n\t"
                   "buffer_store_dwordx4 %0, %7, %3, %8 offen offset:2048 sc1\n\tbuffer_store_dwordx4 %0, %7, %3, %8 offen offset:3072 sc1\n\ts_nop 7"
                   ::"v"((u32x4){~0u, ~0u, ~0u, ~0u}), "v"(ffn ? gub + lane * 16 : 0xf0000000u), "v"(ffn ? ypb + (lane >> 3) * 7168 + (lane & 7) * 16 : 0xf0000000u), "s"(rws),
                   "s"(8 * 7168), "s"(16 * 7168), "s"(24 * 7168), "v"(NS ? lane * 16 : 0xf0000000u), "s"(W_H + c * 524288 + (j * 2 + xw) * 4096) : "memory");
    };
    auto A_PUB = [&]() {
      u32x2 rt;
      asm volatile("ds_read_b64 %0, %1\n\ts_waitcnt lgkmcnt(0)" : "=v"(rt) : "v"(L_RT + (lane & 1) * 16 + xw * 8) : "memory");
      st128c(rws, lane < 2 ? (lane ? W_G2 : W_R2) + (c * 128 + j * 8 + xw * 4) * 4 : 0xf0000000u, (u32x4){rt.x, tagA, rt.y, tagB});
    };
    auto A_GAT = [&]() {
#pragma unroll
      for (int q = 0; q < 4; ++q) asm volatile("buffer_load_dwordx4 %0, %1, %2, 0 offen offset:%3 sc1" : "=v"(g[q]) : "v"(rgo), "s"(rws), "n"(q * 1024) : "memory");
      asm volatile("buffer_load_dwordx4 %0, %1, %2, 0 offen sc1" : "=v"(a) : "v"(ggo), "s"(rws) : "memory");
    };
    static_assert(NS <= 3, "cleared blocks");
    A_EP();
    issue(slice(1) + so1, 32768);
    issue(slice(2) + so1, 65536);
    asm volatile("s_waitcnt vmcnt(17) lgkmcnt(0)" ::: "memory"); bar();
    issue(slice(3) + so1, 98304);
    waitv<24>(); A_TAG();
    A_CLR();
    asm volatile("s_waitcnt vmcnt(26) lgkmcnt(0)" ::: "memory"); bar();
    issue(slice(4) + so1, 0);
    asm volatile("s_waitcnt vmcnt(26) lgkmcnt(0)" ::: "memory"); bar();
    issue(slice(5) + so1, 32768);
    LDW2(0, 16)
    asm volatile("s_waitcnt vmcnt(42) lgkmcnt(0)" ::: "memory"); bar();
    issue(slice(6) + so1, 65536);
    waitv<40>(); A_PUB();
    asm volatile("s_waitcnt vmcnt(33) lgkmcnt(0)" ::: "memory"); bar();
    issue(slice(7) + so1, 98304);
    asm volatile("s_waitcnt vmcnt(33) lgkmcnt(0)" ::: "memory"); bar();
    issue(slice(8) + so1, 0);
    asm volatile("s_waitcnt vmcnt(17) lgkmcnt(0)" ::: "memory"); bar();
    issue(slice(9) + so1, 32768);
    asm volatile("s_waitcnt vmcnt(16) lgkmcnt(0)" ::: "memory"); bar();
    issue(slice(10) + so1, 65536);
    asm volatile("s_waitcnt vmcnt(16) lgkmcnt(0)" ::: "memory"); bar();
    issue(slice(11) + so1, 98304);
    A_GAT();
    asm volatile("s_waitcnt vmcnt(21) lgkmcnt(0)" ::: "memory"); bar();
    issue(slice(12) + so1, 0);
    asm volatile("s_waitcnt vmcnt(21) lgkmcnt(0)" ::: "memory"); bar();
    issue(slice(13) + so1, 32768);
    asm volatile("s_waitcnt vmcnt(21) lgkmcnt(0)" ::: "memory"); bar();
    issue(slice(14) + so1, 65536);
    constexpr int K0 = 12;
#pragma unroll
    for (int k = K0; k < 13; ++k) { waitv<16>(); bar(); issue(slice(k + 3) + so1, ((k + 3) & 3) * 32768); }
    asm volatile("" : "+v"(g[0]), "+v"(g[1]), "+v"(g[2]), "+v"(g[3]), "+v"(a));
#define LDW1R(a, b) { _Pragma("unroll") for (int b_ = (a); b_ < (b); ++b_) LDW1(b_) }
    LDW1R(0, W1A)
    waitv<16 + 4 * W1A>(); bar(); LDW1R(W1A, W1Bb)
    waitv<8 + 4 * W1Bb>(); bar(); LDW1R(W1Bb, W1C)
    waitv<4 * W1C>(); bar(); LDW1R(W1C, W1D)
    bar();
    __builtin_amdgcn_s_setprio(XPRIO2);
#define W1B() do { if (XGT == 1) lds_wait_min4(FL_FREE, 1u); if (XGT == 2) lds_wait_min4(FL_HS, 1u); if (XGT == 3) lds_wait_ge(FL_HS, 1u); \
      _Pragma("unroll") for (int b_ = W1N; b_ < 8; ++b_) LDW1(b_) } while (0)
#pragma unroll
    for (int b_ = W1D; b_ < W1N; ++b_) LDW1(b_)
    if (W2P == 10) LDW2(0, 16)
    if (lane == 0) lds_wr32(FL_W + xw * 4, 1u);
    {
#pragma unroll 1
      for (int it = 0; it < SPIN_MAX; ++it) {
        bool ok = true;
#pragma unroll
        for (int q = 0; q < 4; ++q) ok = ok && g[q].y == tagA && g[q].w == tagB;
        if (__all(ok && a.y == tagA && a.w == tagB)) break;
        __builtin_amdgcn_s_sleep(8);
        FRESH();
#pragma unroll
        for (int q = 0; q < 4; ++q) g[q] = ld128c(rws, rgo + q * 1024);
        a = ld128c(rws, ggo);
      }
      if (l == 0) {
        const unsigned gp = lane < 32 ? a.x : a.z, ga = L_GT + ((4 * (lane & 1) + 2 * (lane >> 5)) * 16 + ((lane & 31) >> 1)) * 4;
        lds_wr32(ga, gp << 16);
        lds_wr32(ga + 64, gp & 0xffff0000u);
      }
      asm volatile("ds_add_u32 %0, %1" ::"v"(lane == 0 ? (unsigned)FL_G : (unsigned)(L_JNK + lane * 4)), "v"(1u) : "memory");
      if (W1P == 1) W1B();
#pragma unroll
      for (int q = 0; q < 4; ++q) {
        const unsigned r0 = 2047 - (l * 1024 + q * 256 + lane * 4);
        key[4 * q] = (g[q].x << 16) + r0; key[4 * q + 1] = (g[q].x & 0xffff0000u) + (r0 - 1);
        key[4 * q + 2] = (g[q].z << 16) + (r0 - 2); key[4 * q + 3] = (g[q].z & 0xffff0000u) + (r0 - 3);
        asm volatile("" : "+v"(key[4 * q]), "+v"(key[4 * q + 1]), "+v"(key[4 * q + 2]), "+v"(key[4 * q + 3]));
      }
      unsigned m = key[0], m2 = 0, m3 = 0;
#pragma unroll
      for (int i = 1; i < 16; ++i) {
        asm("v_med3_u32 %0, %1, %0, %3\n\tv_med3_u32 %1, %2, %1, %3\n\tv_max_u32 %2, %2, %3" : "+v"(m3), "+v"(m2), "+v"(m) : "v"(key[i]));
      }
      asm volatile("" : "+v"(m), "+v"(m2), "+v"(m3));
      unsigned T = 0;
#pragma unroll
      for (int b = 31; b >= 16; --b) {
        const unsigned t2 = T | (1u << b);
        T = __popcll(__ballot(m >= t2)) >= 8 ? t2 : T;
      }
      asm volatile("" : "+s"(T));
      const u64 k1 = __ballot(m >= T), k2 = __ballot(m2 >= T);
      const int n1 = __popcll(k1);
      if (__ballot(m3 >= T) || n1 + __popcll(k2) > 16) {
        unsigned cand = 0;
#pragma unroll 1
        for (int s = 0; s < 8; ++s) {
          unsigned mm = key[0];
#pragma unroll
          for (int i = 1; i < 16; ++i) mm = max(mm, key[i]);
          mm = wave_maxu(mm);
          cand = lane == s ? mm : cand;
#pragma unroll
          for (int i = 0; i < 16; ++i) key[i] = key[i] == mm ? 0u : key[i];
        }
        if (lane < 8) lds_wr32(L_CAND + l * 64 + lane * 4, cand);
      } else {
        const unsigned p1 = __builtin_amdgcn_mbcnt_hi((unsigned)(k1 >> 32), __builtin_amdgcn_mbcnt_lo((unsigned)k1, 0u));
        const unsigned p2 = __builtin_amdgcn_mbcnt_hi((unsigned)(k2 >> 32), __builtin_amdgcn_mbcnt_lo((unsigned)k2, (unsigned)n1));
        lds_wr32(m >= T ? L_CAND + l * 64 + p1 * 4 : L_JNK + lane * 4, m);
        if (k2) lds_wr32(m2 >= T ? L_CAND + l * 64 + p2 * 4 : L_JNK + lane * 4, m2);
      }
      if (lane == 0) lds_wr32(FL_TK + 8 + l * 4, 1u);
      if (W1P == 2) W1B();
      lds_wait_ge(FL_TK + 8 + (l ^ 1) * 4, 1u);
      const unsigned ck = *(const volatile LAS unsigned*)(smem + L_CAND + (lane & 31) * 4);
      int rank = 0;
#pragma unroll
      for (int i = 0; i < 4; ++i) {
        const u32x4 o = *(const volatile LAS u32x4*)(smem + L_CAND + (lane >> 5) * 64 + i * 16);
        rank += (o.x > ck) + (o.y > ck) + (o.z > ck) + (o.w > ck);
      }
      { auto p = __builtin_amdgcn_permlane32_swap((unsigned)rank, (unsigned)rank, false, false); rank = (int)(p[0] + p[1]); }
      if (l == 0) {
        if (lane < 32 && ck != 0u && rank < 8) {
          const int t = 2047 - (int)(ck & 0xffffu);
          lds_wr32(L_SEL + rank * 4, ck);
          if ((t >> 7) == c) lds_wr32(L_MAP + ((t & 7) * 16 + ((t & 127) >> 3)) * 4, (unsigned)(L_BROW + rank * 256));
        }
        const unsigned any = __ballot(ck != 0u && rank < 8 && ((2047 - (int)(ck & 0xffffu)) >> 7) == c) ? 2u : 1u;
        flag(FL_SEL, any);
        if (bid == 0 && lane == 0) st64c(rws, W_EPOCH, (u32x2){tagA, tagB});
      }
    }
    if (W1P == 3) W1B();
    lds_wait_ge(FL_SEL, 1u);
    const unsigned mine = lds_rd32(L_SEL + (lane & 7) * 4);
    {
      {
        u32x4 xa[4];
#pragma unroll
        for (int u = 0; u < 4; ++u)
          asm volatile("buffer_load_dwordx4 %0, %1, %2, 0 offen offset:%3" : "=v"(xa[u]) : "v"((2047 - (mine & 0xffffu)) * 2048 + (kq * 256 + 32 * (fr >> 3) + 8 * fq) * 2), "s"(rx), "n"(u * 128) : "memory");
        TRN(w1[0], W2P == 10 ? 36 : 20) TRN(w1[1], W2P == 10 ? 36 : 20) TRN(w1[2], W2P == 10 ? 28 : 12) TRN(w1[3], 0)
        asm volatile("" : "+v"(xa[0]), "+v"(xa[1]), "+v"(xa[2]), "+v"(xa[3]));
        const bool val = (fq >> 1) == (fr >> 3), lo = lane < 32;
        auto publish = [&](f32x4 (&d)[8], unsigned o, unsigned sp) {
          u32x4 pv[4];
#pragma unroll
          for (int rr = 0; rr < 2; ++rr)
#pragma unroll
            for (int q = 0; q < 8; ++q) {
              auto p = __builtin_amdgcn_permlane32_swap(__float_as_uint(d[q][rr]), __float_as_uint(d[q][2 + rr]), false, false);
              const unsigned own = lo ? p[0] : p[1], oth = lo ? p[1] : p[0];
              pv[rr * 2 + (q >> 2)][q & 3] = min(__float_as_uint(__uint_as_float(own) + dpp1<0x128>(__uint_as_float(oth))), 0xfffffffeu);
            }
          asm volatile("s_nop 4\n\t"
                       "buffer_store_dwordx4 %0, %4, %5, 0 offen sc1\n\tbuffer_store_dwordx4 %1, %4, %5, %6 offen sc1\n\t"
                       "buffer_store_dwordx4 %2, %4, %5, %7 offen sc1\n\tbuffer_store_dwordx4 %3, %4, %5, %8 offen sc1\n\t"
                       "s_nop 7\n\ts_nop 7"
                       ::"v"(pv[0]), "v"(pv[1]), "v"(pv[2]), "v"(pv[3]), "v"(val && ffn ? o : 0xf0000000u), "s"(rws), "s"(sp), "s"(2 * sp), "s"(3 * sp) : "memory");
        };
        f32x4 d[8];
#pragma unroll
        for (int q = 0; q < 8; ++q) d[q] = (f32x4){0.f, 0.f, 0.f, 0.f};
#pragma unroll
        for (int u = 0; u < 4; ++u)
#pragma unroll
          for (int q = 0; q < 8; ++q) d[q] = __builtin_amdgcn_mfma_f32_16x16x32_bf16(__builtin_bit_cast(bf16x8, xa[u]), __builtin_bit_cast(bf16x8, w1[u][q]), d[q], 0, 0, 0);
        publish(d, gub + (16 * (fr >> 3) + 8 * (fq & 1) + (fr & 7)) * 16, 512);
        if (W2P < 11) { TRN(w2[0], 63) TRN(w2[1], 63) }
        lds_wait_ge<LZ>(FL_CFREE, XG2);
        __builtin_amdgcn_s_setprio(XP3);
        if (W2P == 11) LDW2(0, 16)
        if (ffn) {
          f32x4 G, U;
#pragma unroll 1
          for (int it = 0; it < SPIN_MAX; ++it) {
            FRESH();
            u32x4 a_[8];
#pragma unroll
            for (int k = 0; k < 8; ++k) a_[k] = __builtin_amdgcn_raw_buffer_load_b128(rws, W_GU + sg * 16384 + xw * 1024 + lane * 16, k * 2048, 16);
            unsigned m = 0;
#pragma unroll
            for (int k = 0; k < 8; ++k) m = max(max(m, max(a_[k].x, a_[k].y)), max(a_[k].z, a_[k].w));
            G = (__builtin_bit_cast(f32x4, a_[0]) + __builtin_bit_cast(f32x4, a_[2])) + (__builtin_bit_cast(f32x4, a_[4]) + __builtin_bit_cast(f32x4, a_[6]));
            U = (__builtin_bit_cast(f32x4, a_[1]) + __builtin_bit_cast(f32x4, a_[3])) + (__builtin_bit_cast(f32x4, a_[5]) + __builtin_bit_cast(f32x4, a_[7]));
            if (__all(m != ~0u)) break;
            __builtin_amdgcn_s_sleep(8);
          }
          if (W2P == 12) { lds_wait_ge<LZ>(FL_CFREE, W2G + W2S * (kq + 4 * xw) / 8); LDW2(0, 16) }
          float a4[4];
#pragma unroll
          for (int e = 0; e < 4; ++e) { const float g = rbf(G[e]); a4[e] = rbf(g * __builtin_amdgcn_rcpf(1.f + __expf(-g))) * rbf(U[e]); }
          asm volatile("s_nop 0\n\tds_write_b64 %0, %1" ::"v"(L_ACT + (4 * ((lane >> 3) & 1) + 2 * ((lane >> 4) & 1) + xw) * 128 + (lane & 7) * 16 + (lane >> 5) * 8),
                       "v"((u32x2){pk2(a4[0], a4[1]), pk2(a4[2], a4[3])}) : "memory");
          if (lane == 0) lds_wr32(FL_TK + xw * 4, 1u);
          lds_wait_ge(FL_TK + (xw ^ 1) * 4, 1u);
        }
        if (W2P >= 11) { TRN(w2[0], 8) TRN(w2[1], 0) }
        const u32x4 af = lds_rd128(L_ACT + ((fr & 7) * 8 + 4 * (fr >> 3) + fq) * 16);
#pragma unroll
        for (int u = 0; u < 2; ++u) {
#pragma unroll
          for (int q = 0; q < 8; ++q) d[q] = __builtin_amdgcn_mfma_f32_16x16x32_bf16(__builtin_bit_cast(bf16x8, af), __builtin_bit_cast(bf16x8, w2[u][q]), (f32x4){0.f, 0.f, 0.f, 0.f}, 0, 0, 0);
          publish(d, ypb + (u * 16 + 2 * (fr >> 3) + (fq & 1)) * 7168 + (fr & 7) * 16, 4 * 7168);
        }
      }
    }
    __builtin_amdgcn_s_setprio(XP4);
    unsigned rest = (unsigned)__ballot(((2047 - (int)(mine & 0xffffu)) >> 7) == c) & 0xffu;
    if (xw) rest &= rest - 1;
#pragma unroll 1
    for (; rest; rest &= rest - 1, rest &= rest - 1) {
      const int s = __builtin_ctz(rest);
      const unsigned ks = (unsigned)__builtin_amdgcn_readlane((int)mine, s);
      const int t = 2047 - (int)(ks & 0xffffu);
      const float rs = rbf(1.f / (1.f + __expf(-key_val(ks))));
      const unsigned yb = W_YP + (j * 16 + (s & 1) * 8 + 2 * ((s >> 1) & 1) + (s >> 2)) * 7168;
      const u32x2 xr = __builtin_amdgcn_raw_buffer_load_b64(rx, t * 2048 + (j * 64 + 8 * (lane & 7) + 4 * ((lane >> 3) & 1)) * 2, 0, 0);
      lds_wait_ge<LZ>(FL_CFREE, YGATE);
      const float omg = rbf(1.f - __uint_as_float(lds_rd32(L_GT + ((t & 7) * 16 + ((t & 127) >> 3)) * 4)));
      f32x4 y[2];
#pragma unroll 1
      for (int it = 0; it < SPIN_MAX; ++it) {
        FRESH();
        unsigned m = 0;
#pragma unroll
        for (int h = 0; h < 2; ++h) {
          y[h] = (f32x4){0.f, 0.f, 0.f, 0.f};
#pragma unroll
          for (int k = 0; k < 7; ++k) {
            const u32x4 a = __builtin_amdgcn_raw_buffer_load_b128(rws, yb + lane * 16, h * 4 * 7168 + k * 1024, 16);
            m = max(max(m, max(a.x, a.y)), max(a.z, a.w));
            y[h] += __builtin_bit_cast(f32x4, a);
          }
        }
        if (__all(m != ~0u)) break;
        __builtin_amdgcn_s_sleep(6);
      }
      const bool o8 = lane & 8;
      const f32x4 xf = unpk4(xr);
      f32x4 dl;
#pragma unroll
      for (int e = 0; e < 4; ++e) {
        const float v = sum16_32((o8 ? y[1][e] : y[0][e]) + dpp1<0x128>(o8 ? y[0][e] : y[1][e]));
        dl[e] = rbf(mul1(omg, rbf(xf[e] + rbf(mul1(rbf(v), rs))))) - rbf(mul1(omg, xf[e]));
      }
      if (lane < 16) asm volatile("s_nop 0\n\tds_write_b128 %0, %1\n\ts_nop 3" ::"v"(L_BROW + s * 256 + (lane & 7) * 32 + (lane >> 3) * 16), "v"(dl) : "memory");
    }
    if (lane == 0) lds_wr32(FL_XD + xw * 4, 1u);
    asm volatile("Lk_x:");
    return;
  }
  }

  __builtin_amdgcn_s_setprio(3);
  relane();
  u64x8 ma, mb;
  asm volatile("s_load_dwordx16 %0, %2, 0x0\n\ts_load_dwordx16 %1, %2, 0x40\n\ts_waitcnt lgkmcnt(0)" : "=&s"(ma), "=&s"(mb) : "s"(__builtin_amdgcn_kernarg_segment_ptr()), "s"(kw0), "s"(kw1));
  const char* out = (const char*)mb[7];
  char* ws = (char*)mb[6];
  const rsrc_t rws = mk(ws, W_END);
  if (!MTOP) MLOADS(ma, mb);
  f32x4 lr, li, cr, ci, Er, Ei, Fr, Fi, br_, bi_;
  u32x2 xe[2][4];
  {
    asm volatile("s_waitcnt vmcnt(%3)" : "+v"(p0), "+v"(p1), "+v"(p2) : "n"(9) : "memory");
    const f32x4 lre = unpk4(p0), lim = unpk4(p1), ld = unpk4(p2) * splat4(1.44269504f);
    f32x4 dt, id = fma4(lre, lre, lim * lim);
#pragma unroll
    for (int r = 0; r < 4; ++r) { dt[r] = rbf(__builtin_amdgcn_exp2f(ld[r])); id[r] = __builtin_amdgcn_rcpf(id[r]); }
    const f32x4 A2 = dt * lre * splat4(1.44269504f), Bv = dt * lim * splat4(0.159154943f);
    cexp4(A2, Bv, lr, li);
    const f32x4 nr = lr - splat4(1.f);
    cr = fma4(nr, lre, li * lim) * id; ci = fma4(li, lre, nml4(nr, lim)) * id;
    const f32x4 ne = splat4((float)(8 * fr)), nf = splat4((float)((fr & 7) + 1));
    cexp4(A2 * ne, Bv * ne, Er, Ei);
    f32x4 tr, ti;
    cexp4(A2 * nf, Bv * nf, tr, ti);
    Fr = fma4(cr, tr, nml4(ci, ti)); Fi = fma4(cr, ti, ci * tr);
    const float a1 = (fr & 2) ? ((fr & 1) ? A2.w : A2.z) : ((fr & 1) ? A2.y : A2.x), b1 = (fr & 2) ? ((fr & 1) ? Bv.w : Bv.z) : ((fr & 1) ? Bv.y : Bv.x);
    const int e0 = c - 1 - (fr >> 2);
    const f32x4 nl = {(float)(128 * max(e0, 0)), (float)(128 * max(e0 - 4, 0)), (float)(128 * max(e0 - 8, 0)), (float)(128 * max(e0 - 12, 0))};
    cexp4(splat4(a1) * nl, splat4(b1) * nl, br_, bi_);
    asm volatile("" : "+v"(lr), "+v"(li), "+v"(cr), "+v"(ci), "+v"(Er), "+v"(Ei), "+v"(Fr), "+v"(Fi), "+v"(br_), "+v"(bi_));
  }
  asm volatile("s_waitcnt vmcnt(%9)" : "+v"(dv), "+v"(xv[0][0]), "+v"(xv[0][1]), "+v"(xv[1][0]), "+v"(xv[1][1]), "+v"(wv[0]), "+v"(wv[1]), "+v"(gv[0]), "+v"(gv[1]) : "n"(0) : "memory");
  {
    float sv[4];
#pragma unroll
    for (int r = 0; r < 2; ++r) {
      float sr = 0.f, sg_ = 0.f;
#pragma unroll
      for (int h = 0; h < 2; ++h)
#pragma unroll
        for (int e = 0; e < 4; ++e) {
          const unsigned xx = xv[r][h][e], ww = wv[h][e], gw = gv[h][e];
          sr = __builtin_amdgcn_fdot2_f32_bf16(__builtin_bit_cast(bf16x2_t, xx), __builtin_bit_cast(bf16x2_t, ww), sr, false);
          sg_ = __builtin_amdgcn_fdot2_f32_bf16(__builtin_bit_cast(bf16x2_t, xx), __builtin_bit_cast(bf16x2_t, gw), sg_, false);
        }
      sv[r] = sr; sv[2 + r] = sg_;
    }
    const bool b1 = lane & 1, b2 = lane & 2;
    const float k0 = (b1 ? sv[1] : sv[0]) + dpp1<0xb1>(b1 ? sv[0] : sv[1]), k1 = (b1 ? sv[3] : sv[2]) + dpp1<0xb1>(b1 ? sv[2] : sv[3]);
    float k2 = (b2 ? k1 : k0) + dpp1<0x4e>(b2 ? k0 : k1);
    k2 += dpp1<0x124>(k2);
    k2 += dpp1<0x128>(k2);
    k2 = sum16_32(k2);
    const unsigned vb = b2 ? f2bf(1.f / (1.f + __expf(-rbf(k2)))) : route_key(rbf(k2), 0) >> 16;
    const unsigned p01 = vb | ((unsigned)__builtin_amdgcn_update_dpp(0, (int)vb, 0xb1, 0xf, 0xf, true) << 16);
    asm volatile("ds_write_b32 %0, %1\n\ts_waitcnt lgkmcnt(0)" ::"v"((lane & 61) == 0 ? (unsigned)(L_RT + (lane * 2 + wid) * 4) : (unsigned)(L_JNK + lane * 4)), "v"(p01) : "memory");
  }
  __builtin_amdgcn_s_setprio(2);
  unsigned tagA = L_TAG;
  f32x4 acc[2][8];
  const unsigned ago = fr < 4 ? W_AGG + ((j * 16 + c) * 64 + wid * 16 + 4 * fq + fr) * 16u : 0xf0000000u;
  {
#pragma unroll
    for (int m = 0; m < 2; ++m)
#pragma unroll
      for (int n = 0; n < 8; ++n) acc[m][n] = (f32x4){0.f, 0.f, 0.f, 0.f};
    const int x0 = (fq ^ (fr & 7)) * 16, x1 = ((4 + fq) ^ (fr & 7)) * 16;
    unsigned pa0 = 16384 + (wid * 32 + fr) * 128 + x0, pa1 = 16384 + (wid * 32 + fr) * 128 + x1, pb0 = fr * 128 + x0, pb1 = fr * 128 + x1;
    unsigned sit, xad[4], s1[3], fv1 = 0;
#pragma unroll
    for (int r = 0; r < 4; ++r) xad[r] = 98304 + (32 * wid + 4 * fq + r) * 128 + (((fr >> 1) ^ (4 * (fq & 1) + r)) * 16) + (fr & 1) * 8;
    bf16x8 fa[2][2], fb[8];
    asm volatile(ASM_GEMM1 : [c00] "+v"(acc[0][0]), [c01] "+v"(acc[0][1]), [c02] "+v"(acc[0][2]), [c03] "+v"(acc[0][3]), [c04] "+v"(acc[0][4]), [c05] "+v"(acc[0][5]), [c06] "+v"(acc[0][6]), [c07] "+v"(acc[0][7]), [c10] "+v"(acc[1][0]), [c11] "+v"(acc[1][1]), [c12] "+v"(acc[1][2]), [c13] "+v"(acc[1][3]), [c14] "+v"(acc[1][4]), [c15] "+v"(acc[1][5]), [c16] "+v"(acc[1][6]), [c17] "+v"(acc[1][7]), [a00] "=&v"(fa[0][0]), [a01] "=&v"(fa[0][1]), [a10] "=&v"(fa[1][0]), [a11] "=&v"(fa[1][1]), [b0] "=&v"(fb[0]), [b1] "=&v"(fb[1]), [b2] "=&v"(fb[2]), [b3] "=&v"(fb[3]), [b4] "=&v"(fb[4]), [b5] "=&v"(fb[5]), [b6] "=&v"(fb[6]), [b7] "=&v"(fb[7]), [pa0] "+v"(pa0), [pa1] "+v"(pa1), [pb0] "+v"(pb0), [pb1] "+v"(pb1), [it] "=&s"(sit), [tg] "+v"(tagA), [xe00] "=&v"(xe[0][0]), [xe01] "=&v"(xe[0][1]), [xe02] "=&v"(xe[0][2]), [xe03] "=&v"(xe[0][3]), [xe10] "=&v"(xe[1][0]), [xe11] "=&v"(xe[1][1]), [xe12] "=&v"(xe[1][2]), [xe13] "=&v"(xe[1][3]) : [xa0] "v"(xad[0]), [xa1] "v"(xad[1]), [xa2] "v"(xad[2]), [xa3] "v"(xad[3]) : "memory", "scc", "v252", "v253", "v254", "v255");
  }
  __builtin_amdgcn_s_setprio(3);
  const unsigned tagB = tagA + 0x40000000u;
  {
#pragma unroll
    for (int n = 1; n < 8; ++n) {
      const f32x4 pr = acc[0][n - 1], pi = acc[1][n - 1];
      acc[0][n] = fma4(lr, pr, fnm4(li, pi, acc[0][n]));
      acc[1][n] = fma4(lr, pi, fma4(li, pr, acc[1][n]));
    }
    const f32x4 wr = dpp4<0x140>(Er), wi = dpp4<0x140>(Ei);
    const f32x4 zr = fma4(wr, acc[0][7], nml4(wi, acc[1][7])), zi = fma4(wr, acc[1][7], wi * acc[0][7]);
    const bool o1 = fr & 1, o2 = fr & 2;
    const float r01 = (o1 ? zr.y : zr.x) + dpp1<0xb1>(o1 ? zr.x : zr.y), r23 = (o1 ? zr.w : zr.z) + dpp1<0xb1>(o1 ? zr.z : zr.w);
    const float i01 = (o1 ? zi.y : zi.x) + dpp1<0xb1>(o1 ? zi.x : zi.y), i23 = (o1 ? zi.w : zi.z) + dpp1<0xb1>(o1 ? zi.z : zi.w);
    float sr = (o2 ? r23 : r01) + dpp1<0x4e>(o2 ? r01 : r23), si = (o2 ? i23 : i01) + dpp1<0x4e>(o2 ? i01 : i23);
    sr += dpp1<0x124>(sr); si += dpp1<0x124>(si);
    sr += dpp1<0x128>(sr); si += dpp1<0x128>(si);
    u32x4 av = {__float_as_uint(sr), tagA, __float_as_uint(si), tagB};
    asm volatile("s_nop 1\n\tbuffer_store_dwordx4 %0, %1, %2, 0 offen sc1\n\ts_nop 7" : "+v"(av) : "v"(ago), "s"(rws) : "memory");
  }
  unsigned ao[4];
  u32x4 g[4];
#pragma unroll
  for (int i = 0; i < 4; ++i) {
    ao[i] = (fr >> 2) + 4 * i < c ? W_AGG + ((j * 16 + (fr >> 2) + 4 * i) * 64 + wid * 16 + 4 * fq + (fr & 3)) * 16u : 0xf0000000u;
    g[i] = (u32x4){0u, 0u, 0u, 0u};
  }
  if (POLLN < 0) {
    FRESH();
#pragma unroll
    for (int i = 0; i < 4; ++i)
      if (4 * i < c) g[i] = ld128c(rws, ao[i]);
    FRESH();
  }
  f32x4 Pr, Pi, qr[8], qi[8];
  {
    f32x4 yr = acc[0][7], yi = acc[1][7];
#define KS_STEP(S, CTRL) { const f32x4 mr = dpp4<0x150 + S>(Er), mi = dpp4<0x150 + S>(Ei); const f32x4 sr = dpp4<CTRL>(yr), si = dpp4<CTRL>(yi); \
      yr = fma4(mr, sr, fnm4(mi, si, yr)); yi = fma4(mr, si, fma4(mi, sr, yi)); }
    KS_STEP(1, 0x111) KS_STEP(2, 0x112) KS_STEP(4, 0x114) KS_STEP(8, 0x118)
    Pr = dpp4<0x111>(yr); Pi = dpp4<0x111>(yi);
    asm volatile("" : "+v"(Pr), "+v"(Pi));
#pragma unroll
    for (int n = 0; n < 8; ++n) {
      if (n == POLLN) {
        FRESH();
#pragma unroll
        for (int i = 0; i < 4; ++i)
          if (4 * i < c) g[i] = ld128c(rws, ao[i]);
        FRESH();
      }
      bcast8(n, Fr, qr[n]); bcast8(n, Fi, qi[n]);
      const f32x4 br = fma4(cr, acc[0][n], nml4(ci, acc[1][n])), bi = fnm4(cr, acc[1][n], nml4(ci, acc[0][n]));
      acc[0][n] = br; acc[1][n] = bi;
      asm volatile("" : "+v"(acc[0][n]), "+v"(acc[1][n]), "+v"(qr[n]), "+v"(qi[n]));
    }
  }
  f32x4 Xr = Pr, Xi = Pi;
  if (POLLN == 8) {
    FRESH();
#pragma unroll
    for (int i = 0; i < 4; ++i)
      if (4 * i < c) g[i] = ld128c(rws, ao[i]);
    FRESH();
  }
  u32x2 xd[2][4];
  lds_wait_ge(FL_G, 2u);
  {
    const f32x4 df = unpk4(dv) * splat4(0.5f);
#pragma unroll
    for (int m = 0; m < 2; ++m) {
      const f32x4 ge = __builtin_bit_cast(f32x4, lds_rd128(L_GT + ((2 * wid + m) * 16 + 4 * fq) * 4));
#pragma unroll
      for (int r = 0; r < 4; ++r) {
        const f32x4 xf = unpk4(xe[m][r]), p = xf * df, o = xf * splat4(rbf(1.f - ge[r]));
        xd[m][r] = (u32x2){pk2(p.x, p.y), pk2(p.z, p.w)};
        xe[m][r] = (u32x2){pk2(o.x, o.y), pk2(o.z, o.w)};
        asm volatile("" : "+v"(xd[m][r]), "+v"(xe[m][r]));
      }
    }
  }
  if (POLLN > 9) __builtin_amdgcn_s_sleep(POLLN > 9 ? POLLN - 9 : 0);
  if (c > 0) {
#pragma unroll 1
    for (int it = 0; it < SPIN_MAX; ++it) {
      bool ok = true;
#pragma unroll
      for (int i = 0; i < 4; ++i) ok = ok && (ao[i] == 0xf0000000u || (g[i].y == tagA && g[i].w == tagB));
      if (__all(ok) || WIF == 6) break;
      FRESH();
#pragma unroll
      for (int i = 0; i < 4; ++i)
        if (4 * i < c) g[i] = ld128c(rws, ao[i]);
    }
    const f32x4 sx = {__uint_as_float(g[0].x), __uint_as_float(g[1].x), __uint_as_float(g[2].x), __uint_as_float(g[3].x)};
    const f32x4 sy = {__uint_as_float(g[0].z), __uint_as_float(g[1].z), __uint_as_float(g[2].z), __uint_as_float(g[3].z)};
    const f32x4 tx = fma4(br_, sx, nml4(bi_, sy)), ty = fma4(br_, sy, bi_ * sx);
    float gx = (tx.x + tx.z) + (tx.y + tx.w), gy = (ty.x + ty.z) + (ty.y + ty.w);
    gx += dpp1<0x124>(gx); gy += dpp1<0x124>(gy);
    gx += dpp1<0x128>(gx); gy += dpp1<0x128>(gy);
    const f32x4 Gr = {dpp1<0x00>(gx), dpp1<0x55>(gx), dpp1<0xaa>(gx), dpp1<0xff>(gx)}, Gi = {dpp1<0x00>(gy), dpp1<0x55>(gy), dpp1<0xaa>(gy), dpp1<0xff>(gy)};
    Xr = fma4(Er, Gr, fnm4(Ei, Gi, Pr)); Xi = fma4(Er, Gi, fma4(Ei, Gr, Pi));
  }
  relane();
  const unsigned jnk = L_JNK + lane * 4;
  const unsigned ho = WIF == 3 ? 0xf0000000u : W_H + c * 524288 + (4 * pown + wid) * 8192 + lane * 16;
  u32x4 hv[8];
  __builtin_amdgcn_sched_barrier(0);
#define HST(t_) asm volatile("s_nop 1\n\tbuffer_store_dwordx4 %0, %1, %2, %3 offen offset:%4 sc1" : "+v"(hv[t_]) : "v"(ho), "s"(rws), "s"(((t_) >> 2) * 4096), "n"(((t_) & 3) * 1024) : "memory")
#pragma unroll
  for (int n = 0; n < 8; ++n) {
    const f32x4 hr = fma4(qr[n], Xr, fnm4(qi[n], Xi, acc[0][n])), hi = fnm4(qr[n], Xi, fnm4(qi[n], Xr, acc[1][n]));
    const auto p = __builtin_amdgcn_permlane16_swap(pk2(hr.x, hr.y), pk2(hi.x, hi.y), false, false);
    const auto q = __builtin_amdgcn_permlane16_swap(pk2(hr.z, hr.w), pk2(hi.z, hi.w), false, false);
    hv[n] = (u32x4){NS ? min(p[0], 0xfffffffeu) : p[0], q[0], NS ? min(p[1], 0xfffffffeu) : p[1], q[1]};
    if (NSI == 8) HST(n); else if (NSI == 4 ? (n & 1) : n < NSI) HST(NSI == 4 ? n >> 1 : n);
    __builtin_amdgcn_sched_barrier(0);
    if (!DSL && (n >> 1) != wid) asm volatile("ds_write_b128 %0, %1" ::"v"(L_XH + (((n >> 1) * 3 + wid - (wid > (n >> 1) ? 1 : 0)) * 2 + (n & 1)) * 1024 + lane * 16), "v"(hv[n]) : "memory");
  }
  if (DSL) {
#pragma unroll
    for (int w = 0; w < 4; ++w)
      if (w != wid) asm volatile("ds_write_b128 %0, %1\n\tds_write_b128 %0, %2 offset:1024" ::"v"(L_XH + (w * 3 + wid - (wid > w ? 1 : 0)) * 2048 + lane * 16), "v"(hv[2 * w]), "v"(hv[2 * w + 1]) : "memory");
  }
  if (lane == 0) lds_wr32(FL_HS + wid * 4, 1u);
  const rsrc_t rh = mk(ws + W_H + c * 524288, 524288);
  const unsigned vo = wid * 2048 + lane * 16;
  u32x32 hw0, hw1;
  unsigned st[12], pc[4];
  f32x4 ac2[2][4];
#define ACK() do { asm volatile("s_waitcnt vmcnt(0)" : "+v"(hv[0]), "+v"(hv[1]), "+v"(hv[2]), "+v"(hv[3]), "+v"(hv[4]), "+v"(hv[5]), "+v"(hv[6]), "+v"(hv[7])::"memory"); \
    asm volatile("buffer_store_dwordx2 %0, %1, %2, 0 offen sc1" ::"v"((u32x2){tagA, tagB}), "v"(lane == 0 ? W_HF + (c * 16 + pown) * FSB + wid * FSW : 0xf0000000u), "s"(rws) : "memory"); \
    lds_wr32(lane == 0 ? (unsigned)(FL_FREE + wid * 4) : jnk, 9u); \
    if (NS) { if (RQD) __builtin_amdgcn_s_sleep(RQD); asm volatile(ASM_REQ : "=&{v[184:215]}"(hw0), "=&{v[216:247]}"(hw1), [hb0] "=&s"(st[0]), [hb1] "=&s"(st[1]), [hb2] "=&s"(st[2]), [hb3] "=&s"(st[3]) : [vo] "v"(vo), [own] "s"(pown), [srd] "s"(rh) : "memory", "scc"); } } while (0)
#pragma unroll
  for (int sg = 0; sg < 4; ++sg) pc[sg] = (fq & 1) * 8192 + fr * 128 + (((2 * sg + (fq >> 1)) ^ ((fr >> 1) & 7)) * 16);
  {
    lds_wait_ge(FL_C, 1u);
    u32x4 k[4][4], h[4][2];
#pragma unroll
    for (int i = 0; i < 4; ++i) {
      const int sw = i == 0 ? wid : i - (i <= wid ? 1 : 0);
      asm volatile("ds_read_b128 %0, %4\n\tds_read_b128 %1, %4 offset:2048\n\tds_read_b128 %2, %4 offset:4096\n\tds_read_b128 %3, %4 offset:6144"
                   : "=&v"(k[i][0]), "=&v"(k[i][1]), "=&v"(k[i][2]), "=&v"(k[i][3]) : "v"(pc[0] ^ (sw * 32)) : "memory");
    }
#pragma unroll
    for (int m = 0; m < 2; ++m)
#pragma unroll
      for (int r = 0; r < 4; ++r) {
        const f32x4 t = unpk4(xd[m][r]);
#pragma unroll
        for (int n = 0; n < 4; ++n) ac2[m][n][r] = t[n];
      }
    h[0][0] = hv[0]; h[0][1] = hv[1];
    if (wid == 1) { h[0][0] = hv[2]; h[0][1] = hv[3]; }
    if (wid == 2) { h[0][0] = hv[4]; h[0][1] = hv[5]; }
    if (wid == 3) { h[0][0] = hv[6]; h[0][1] = hv[7]; }
    asm volatile("" : "+v"(ac2[0][0]), "+v"(ac2[0][1]), "+v"(ac2[0][2]), "+v"(ac2[0][3]), "+v"(ac2[1][0]), "+v"(ac2[1][1]), "+v"(ac2[1][2]), "+v"(ac2[1][3]), "+v"(h[0][0]), "+v"(h[0][1]));
    if (NSI < 8) HST(NSI);
    if (MGP == 1) ACK();
    lds_wait_min4(FL_HS, 1u);
    asm volatile("ds_read_b128 %0, %6\n\tds_read_b128 %1, %6 offset:1024\n\tds_read_b128 %2, %6 offset:2048\n\tds_read_b128 %3, %6 offset:3072\n\tds_read_b128 %4, %6 offset:4096\n\tds_read_b128 %5, %6 offset:5120"
                 : "=&v"(h[1][0]), "=&v"(h[1][1]), "=&v"(h[2][0]), "=&v"(h[2][1]), "=&v"(h[3][0]), "=&v"(h[3][1]) : "v"(L_XH + wid * 6144 + lane * 16) : "memory");
    lds_wr32(lane == 0 ? (unsigned)(FL_CFREE + wid * 4) : jnk, 1u);
#pragma unroll
    for (int i = 0; i < 4; ++i) {
      if (i == 1) asm volatile("s_waitcnt lgkmcnt(0)" : "+v"(h[1][0]), "+v"(h[1][1]), "+v"(h[2][0]), "+v"(h[2][1]), "+v"(h[3][0]), "+v"(h[3][1])::"memory");
      asm volatile("" : "+v"(k[i][0]), "+v"(k[i][1]), "+v"(k[i][2]), "+v"(k[i][3]));
#pragma unroll
      for (int n = 0; n < 4; ++n) {
        ac2[0][n] = __builtin_amdgcn_mfma_f32_16x16x32_bf16(__builtin_bit_cast(bf16x8, h[i][0]), __builtin_bit_cast(bf16x8, k[i][n]), ac2[0][n], 0, 0, 0);
        ac2[1][n] = __builtin_amdgcn_mfma_f32_16x16x32_bf16(__builtin_bit_cast(bf16x8, h[i][1]), __builtin_bit_cast(bf16x8, k[i][n]), ac2[1][n], 0, 0, 0);
      }
      asm volatile("" : "+v"(ac2[0][0]), "+v"(ac2[0][1]), "+v"(ac2[0][2]), "+v"(ac2[0][3]), "+v"(ac2[1][0]), "+v"(ac2[1][1]), "+v"(ac2[1][2]), "+v"(ac2[1][3])::"memory");
      if (NSI + 1 + i < 8) HST(NSI + 1 + i < 8 ? NSI + 1 + i : 0);
    }
    static_assert(NSI >= 3 && (MGP == 0 || NSI >= 7), "all stores");
  }
  if (MGP == 0) ACK();
  f32x4 ov[2][4];
  float g2[2][4];
#pragma unroll
  for (int m = 0; m < 2; ++m) {
    const f32x4 ge = lds4(smem, L_GT + ((2 * wid + m) * 16 + 4 * fq) * 4);
#pragma unroll
    for (int r = 0; r < 4; ++r) {
      ov[m][r] = unpk4(xe[m][r]);
      g2[m][r] = ge[r] + ge[r];
      asm volatile("" : "+v"(ov[m][r]), "+v"(g2[m][r]));
    }
  }
  unsigned ra[2][4];
  lds_wait_ge(FL_SEL, 1u);
  const unsigned routed = __builtin_amdgcn_readfirstlane(lds_rd32(FL_SEL));
#pragma unroll
  for (int m = 0; m < 2; ++m) {
    const u32x4 me = lds_rd128(L_MAP + ((2 * wid + m) * 16 + 4 * fq) * 4);
#pragma unroll
    for (int r = 0; r < 4; ++r) { ra[m][r] = me[r] + fr * 16; asm volatile("" : "+v"(ra[m][r])); }
  }
  const rsrc_t ro = mk(out, 2048 * 1024 * 4);
  unsigned oo = (c * 128 + 32 * fq + 2 * wid) * 4096 + (j * 64 + 4 * fr) * 4;
  if (WIF == 1) oo = 0xf0000000u;
  if (WIF == 2 && (fq & 1)) oo = 0xf0000000u;
  asm volatile("" : "+v"(oo) : "s"(ro));
  __builtin_amdgcn_s_setprio(MPRIO2);

  {
    const unsigned fad = FL_C, fwd = lane == 0 ? (unsigned)(FL_CFREE + wid * 4) : jnk;
    bf16x8 kb[2][4];
    unsigned fv = 1;
#pragma unroll
    for (int sg = 0; sg < 4; ++sg) pc[sg] += 16384;
    asm volatile(ASM_GEMM2 : [c00] "+v"(ac2[0][0]), [c01] "+v"(ac2[0][1]), [c02] "+v"(ac2[0][2]), [c03] "+v"(ac2[0][3]), [c10] "+v"(ac2[1][0]), [c11] "+v"(ac2[1][1]), [c12] "+v"(ac2[1][2]), [c13] "+v"(ac2[1][3]), "=&{v[184:215]}"(hw0), "=&{v[216:247]}"(hw1), [k00] "=&v"(kb[0][0]), [k01] "=&v"(kb[0][1]), [k02] "=&v"(kb[0][2]), [k03] "=&v"(kb[0][3]), [k10] "=&v"(kb[1][0]), [k11] "=&v"(kb[1][1]), [k12] "=&v"(kb[1][2]), [k13] "=&v"(kb[1][3]), [pc0] "+v"(pc[0]), [pc1] "+v"(pc[1]), [pc2] "+v"(pc[2]), [pc3] "+v"(pc[3]), [fv] "+v"(fv), [s] "=&s"(st[0]), [s2] "=&s"(st[1]), [to] "=&s"(st[2]), [hn] "=&s"(st[3]), [cn] "=&s"(st[4]), [dl] "=&s"(st[5]), [nf] "=&s"(st[6]), [hr] "=&s"(st[7]), [hb0] "=&s"(st[8]), [hb1] "=&s"(st[9]), [hb2] "=&s"(st[10]), [hb3] "=&s"(st[11]) : [vo] "v"(vo), [own] "s"(pown), [srd] "s"(rh), [fa] "v"(fad), [fw] "v"(fwd), [d16] "s"(0x4000), [wr] "s"(0x500) : "memory", "scc", "vcc", "v250", "v251", "v252", "v253", "v254", "v255");
  }
  if (routed > 1u) {
#pragma unroll 1
    for (int it = 0; it < SPIN_MAX * 4; ++it) {
      u32x2 f;
      f32x4 d[8];
      asm volatile("ds_read_b64 %8, %17\n\tds_read_b128 %0, %9\n\tds_read_b128 %1, %10\n\tds_read_b128 %2, %11\n\tds_read_b128 %3, %12\n\t"
                   "ds_read_b128 %4, %13\n\tds_read_b128 %5, %14\n\tds_read_b128 %6, %15\n\tds_read_b128 %7, %16\n\ts_waitcnt lgkmcnt(0)"
                   : "=&v"(d[0]), "=&v"(d[1]), "=&v"(d[2]), "=&v"(d[3]), "=&v"(d[4]), "=&v"(d[5]), "=&v"(d[6]), "=&v"(d[7]), "=&v"(f)
                   : "v"(ra[0][0]), "v"(ra[0][1]), "v"(ra[0][2]), "v"(ra[0][3]), "v"(ra[1][0]), "v"(ra[1][1]), "v"(ra[1][2]), "v"(ra[1][3]), "v"(FL_XD) : "memory");
      if (__builtin_amdgcn_readfirstlane(f.x & f.y)) {
#pragma unroll
        for (int e = 0; e < 8; ++e) ov[e >> 2][e & 3] += d[e];
        break;
      }
    }
  }
#pragma unroll
  for (int r = 0; r < 4; ++r)
#pragma unroll
    for (int m = 0; m < 2; ++m) {
#pragma unroll
      for (int n = 0; n < 4; ++n) ov[m][r][n] = fma1(g2[m][r], ac2[m][n][r], ov[m][r][n]);
      asm volatile("s_nop 1\n\tbuffer_store_dwordx4 %0, %1, %2, %3 offen" OST : "+v"(ov[m][r]) : "v"(oo), "s"(ro), "s"(r * 32768 + m * 4096) : "memory");
      __builtin_amdgcn_sched_barrier(0);
    }
  asm volatile("s_nop 7\n\ts_nop 7" ::"v"(ov[0][0]), "v"(ov[0][1]), "v"(ov[0][2]), "v"(ov[0][3]), "v"(ov[1][0]), "v"(ov[1][1]), "v"(ov[1][2]), "v"(ov[1][3]));
  asm volatile("Lk_m:");
#endif
}

extern "C" void kernel_launch(void* const* d_in, const int* in_sizes, int n_in, void* d_out, int out_size, void* d_ws, size_t ws_size,
                              hipStream_t stream) {
  (void)hipFuncSetAttribute((const void*)fused, hipFuncAttributeMaxDynamicSharedMemorySize, LDS_BYTES);
  fused<<<256, 512, LDS_BYTES, stream>>>((const u16*)d_in[0], (const u16*)d_in[1], (const u16*)d_in[2], (const u16*)d_in[3], (const u16*)d_in[4],
                                         (const u16*)d_in[5], (const u16*)d_in[6], (const u16*)d_in[7], (const u16*)d_in[8], (const u16*)d_in[10],
                                         (const u16*)d_in[11], (const u16*)d_in[12], (const u16*)d_in[13], (const u16*)d_in[14], (char*)d_ws + WS_BASE(ws_size),
                                         (float*)d_out);
}
```

```cpp
#include <hip/hip_runtime.h>
#include <hip/hip_bf16.h>
#include <stdint.h>

typedef __attribute__((ext_vector_type(8))) short bf16x8;
typedef __attribute__((ext_vector_type(4))) float f32x4;
typedef __attribute__((ext_vector_type(2))) float f32x2;
typedef __attribute__((ext_vector_type(4))) unsigned u32x4;
typedef __attribute__((ext_vector_type(2))) unsigned u32x2;
typedef __attribute__((ext_vector_type(32))) unsigned u32x32;
typedef unsigned short u16;
typedef unsigned long long u64;
typedef __attribute__((ext_vector_type(8))) unsigned long long u64x8;
#define LAS __attribute__((address_space(3)))
#define DEV __device__ __forceinline__

#define ASM_GEMM1 \
  "s_mov_b32 %[it], 0\n\t" \
  "s_barrier\n\t" \
  "ds_read_b128 %[a00], %[pa0] offset:0\n\t" \
  "ds_read_b128 %[a01], %[pa0] offset:2048\n\t" \
  "ds_read_b128 %[b0], %[pb0] offset:0\n\t" \
  "ds_read_b128 %[b1], %[pb0] offset:2048\n\t" \
  "ds_read_b128 %[b2], %[pb0] offset:4096\n\t" \
  "ds_read_b128 %[b3], %[pb0] offset:6144\n\t" \
  "ds_read_b128 %[b4], %[pb0] offset:8192\n\t" \
  "ds_read_b128 %[b5], %[pb0] offset:10240\n\t" \
  "ds_read_b128 %[b6], %[pb0] offset:12288\n\t" \
  "ds_read_b128 %[b7], %[pb0] offset:14336\n\t" \
  "Lg1_%=:\n\t" \
  "ds_read_b128 %[a10], %[pa1] offset:0\n\t" \
  "ds_read_b128 %[a11], %[pa1] offset:2048\n\t" \
  "s_waitcnt lgkmcnt(8)\n\t" \
  "v_mfma_f32_16x16x32_bf16 %[c00], %[a00], %[b0], %[c00]\n\t" \
  "v_mfma_f32_16x16x32_bf16 %[c10], %[a01], %[b0], %[c10]\n\t" \
  "ds_read_b128 %[b0], %[pb1] offset:0\n\t" \
  "v_mfma_f32_16x16x32_bf16 %[c01], %[a00], %[b1], %[c01]\n\t" \
  "v_mfma_f32_16x16x32_bf16 %[c11], %[a01], %[b1], %[c11]\n\t" \
  "ds_read_b128 %[b1], %[pb1] offset:2048\n\t" \
  "s_waitcnt lgkmcnt(8)\n\t" \
  "v_mfma_f32_16x16x32_bf16 %[c02], %[a00], %[b2], %[c02]\n\t" \
  "v_mfma_f32_16x16x32_bf16 %[c12], %[a01], %[b2], %[c12]\n\t" \
  "ds_read_b128 %[b2], %[pb1] offset:4096\n\t" \
  "v_mfma_f32_16x16x32_bf16 %[c03], %[a00], %[b3], %[c03]\n\t" \
  "v_mfma_f32_16x16x32_bf16 %[c13], %[a01], %[b3], %[c13]\n\t" \
  "ds_read_b128 %[b3], %[pb1] offset:6144\n\t" \
  "s_waitcnt lgkmcnt(8)\n\t" \
  "v_mfma_f32_16x16x32_bf16 %[c04], %[a00], %[b4], %[c04]\n\t" \
  "v_mfma_f32_16x16x32_bf16 %[c14], %[a01], %[b4], %[c14]\n\t" \
  "ds_read_b128 %[b4], %[pb1] offset:8192\n\t" \
  "v_mfma_f32_16x16x32_bf16 %[c05], %[a00], %[b5], %[c05]\n\t" \
  "v_mfma_f32_16x16x32_bf16 %[c15], %[a01], %[b5], %[c15]\n\t" \
  "ds_read_b128 %[b5], %[pb1] offset:10240\n\t" \
  "s_waitcnt lgkmcnt(8)\n\t" \
  "v_mfma_f32_16x16x32_bf16 %[c06], %[a00], %[b6], %[c06]\n\t" \
  "v_mfma_f32_16x16x32_bf16 %[c16], %[a01], %[b6], %[c16]\n\t" \
  "ds_read_b128 %[b6], %[pb1] offset:12288\n\t" \
  "v_mfma_f32_16x16x32_bf16 %[c07], %[a00], %[b7], %[c07]\n\t" \
  "v_mfma_f32_16x16x32_bf16 %[c17], %[a01], %[b7], %[c17]\n\t" \
  "ds_read_b128 %[b7], %[pb1] offset:14336\n\t" \
  "s_waitcnt lgkmcnt(6)\n\t" \
  "v_mfma_f32_16x16x32_bf16 %[c00], %[a10], %[b0], %[c00]\n\t" \
  "v_mfma_f32_16x16x32_bf16 %[c10], %[a11], %[b0], %[c10]\n\t" \
  "v_mfma_f32_16x16x32_bf16 %[c01], %[a10], %[b1], %[c01]\n\t" \
  "v_mfma_f32_16x16x32_bf16 %[c11], %[a11], %[b1], %[c11]\n\t" \
  "s_waitcnt lgkmcnt(4)\n\t" \
  "v_mfma_f32_16x16x32_bf16 %[c02], %[a10], %[b2], %[c02]\n\t" \
  "v_mfma_f32_16x16x32_bf16 %[c12], %[a11], %[b2], %[c12]\n\t" \
  "s_waitcnt lgkmcnt(0)\n\t" \
  "s_barrier\n\t" \
  "ds_read_b128 %[a00], %[pa0] offset:32768\n\t" \
  "ds_read_b128 %[a01], %[pa0] offset:34816\n\t" \
  "v_mfma_f32_16x16x32_bf16 %[c03], %[a10], %[b3], %[c03]\n\t" \
  "v_mfma_f32_16x16x32_bf16 %[c13], %[a11], %[b3], %[c13]\n\t" \
  "ds_read_b128 %[b0], %[pb0] offset:32768\n\t" \
  "ds_read_b128 %[b1], %[pb0] offset:34816\n\t" \
  "v_mfma_f32_16x16x32_bf16 %[c04], %[a10], %[b4], %[c04]\n\t" \
  "v_mfma_f32_16x16x32_bf16 %[c14], %[a11], %[b4], %[c14]\n\t" \
  "ds_read_b128 %[b2], %[pb0] offset:36864\n\t" \
  "ds_read_b128 %[b3], %[pb0] offset:38912\n\t" \
  "v_mfma_f32_16x16x32_bf16 %[c05], %[a10], %[b5], %[c05]\n\t" \
  "v_mfma_f32_16x16x32_bf16 %[c15], %[a11], %[b5], %[c15]\n\t" \
  "ds_read_b128 %[b4], %[pb0] offset:40960\n\t" \
  "ds_read_b128 %[b5], %[pb0] offset:43008\n\t" \
  "v_mfma_f32_16x16x32_bf16 %[c06], %[a10], %[b6], %[c06]\n\t" \
  "v_mfma_f32_16x16x32_bf16 %[c16], %[a11], %[b6], %[c16]\n\t" \
  "ds_read_b128 %[b6], %[pb0] offset:45056\n\t" \
  "v_mfma_f32_16x16x32_bf16 %[c07], %[a10], %[b7], %[c07]\n\t" \
  "v_mfma_f32_16x16x32_bf16 %[c17], %[a11], %[b7], %[c17]\n\t" \
  "ds_read_b128 %[b7], %[pb0] offset:47104\n\t" \
  "ds_read_b128 %[a10], %[pa1] offset:32768\n\t" \
  "ds_read_b128 %[a11], %[pa1] offset:34816\n\t" \
  "s_waitcnt lgkmcnt(8)\n\t" \
  "v_mfma_f32_16x16x32_bf16 %[c00], %[a00], %[b0], %[c00]\n\t" \
  "v_mfma_f32_16x16x32_bf16 %[c10], %[a01], %[b0], %[c10]\n\t" \
  "ds_read_b128 %[b0], %[pb1] offset:32768\n\t" \
  "v_mfma_f32_16x16x32_bf16 %[c01], %[a00], %[b1], %[c01]\n\t" \
  "v_mfma_f32_16x16x32_bf16 %[c11], %[a01], %[b1], %[c11]\n\t" \
  "ds_read_b128 %[b1], %[pb1] offset:34816\n\t" \
  "s_waitcnt lgkmcnt(8)\n\t" \
  "v_mfma_f32_16x16x32_bf16 %[c02], %[a00], %[b2], %[c02]\n\t" \
  "v_mfma_f32_16x16x32_bf16 %[c12], %[a01], %[b2], %[c12]\n\t" \
  "ds_read_b128 %[b2], %[pb1] offset:36864\n\t" \
  "v_mfma_f32_16x16x32_bf16 %[c03], %[a00], %[b3], %[c03]\n\t" \
  "v_mfma_f32_16x16x32_bf16 %[c13], %[a01], %[b3], %[c13]\n\t" \
  "ds_read_b128 %[b3], %[pb1] offset:38912\n\t" \
  "v_xor_b32 %[pa0], 0x10000, %[pa0]\n\t" \
  "s_waitcnt lgkmcnt(8)\n\t" \
  "v_mfma_f32_16x16x32_bf16 %[c04], %[a00], %[b4], %[c04]\n\t" \
  "v_mfma_f32_16x16x32_bf16 %[c14], %[a01], %[b4], %[c14]\n\t" \
  "ds_read_b128 %[b4], %[pb1] offset:40960\n\t" \
  "v_xor_b32 %[pb0], 0x10000, %[pb0]\n\t" \
  "v_mfma_f32_16x16x32_bf16 %[c05], %[a00], %[b5], %[c05]\n\t" \
  "v_mfma_f32_16x16x32_bf16 %[c15], %[a01], %[b5], %[c15]\n\t" \
  "ds_read_b128 %[b5], %[pb1] offset:43008\n\t" \
  "s_waitcnt lgkmcnt(8)\n\t" \
  "v_mfma_f32_16x16x32_bf16 %[c06], %[a00], %[b6], %[c06]\n\t" \
  "v_mfma_f32_16x16x32_bf16 %[c16], %[a01], %[b6], %[c16]\n\t" \
  "ds_read_b128 %[b6], %[pb1] offset:45056\n\t" \
  "s_cmp_eq_u32 %[it], 7\n\t" \
  "v_mfma_f32_16x16x32_bf16 %[c07], %[a00], %[b7], %[c07]\n\t" \
  "v_mfma_f32_16x16x32_bf16 %[c17], %[a01], %[b7], %[c17]\n\t" \
  "ds_read_b128 %[b7], %[pb1] offset:47104\n\t" \
  "s_cbranch_scc1 Lxe_%=\n\t" \
  "Lxb_%=:\n\t" \
  "s_waitcnt lgkmcnt(6)\n\t" \
  "v_mfma_f32_16x16x32_bf16 %[c00], %[a10], %[b0], %[c00]\n\t" \
  "v_mfma_f32_16x16x32_bf16 %[c10], %[a11], %[b0], %[c10]\n\t" \
  "v_mfma_f32_16x16x32_bf16 %[c01], %[a10], %[b1], %[c01]\n\t" \
  "v_mfma_f32_16x16x32_bf16 %[c11], %[a11], %[b1], %[c11]\n\t" \
  "v_xor_b32 %[pa1], 0x10000, %[pa1]\n\t" \
  "s_waitcnt lgkmcnt(4)\n\t" \
  "v_mfma_f32_16x16x32_bf16 %[c02], %[a10], %[b2], %[c02]\n\t" \
  "v_mfma_f32_16x16x32_bf16 %[c12], %[a11], %[b2], %[c12]\n\t" \
  "v_xor_b32 %[pb1], 0x10000, %[pb1]\n\t" \
  "s_waitcnt lgkmcnt(0)\n\t" \
  "s_barrier\n\t" \
  "s_cbranch_scc1 Lt1_%=\n\t" \
  "ds_read_b128 %[a00], %[pa0] offset:0\n\t" \
  "ds_read_b128 %[a01], %[pa0] offset:2048\n\t" \
  "v_mfma_f32_16x16x32_bf16 %[c03], %[a10], %[b3], %[c03]\n\t" \
  "v_mfma_f32_16x16x32_bf16 %[c13], %[a11], %[b3], %[c13]\n\t" \
  "ds_read_b128 %[b0], %[pb0] offset:0\n\t" \
  "ds_read_b128 %[b1], %[pb0] offset:2048\n\t" \
  "v_mfma_f32_16x16x32_bf16 %[c04], %[a10], %[b4], %[c04]\n\t" \
  "v_mfma_f32_16x16x32_bf16 %[c14], %[a11], %[b4], %[c14]\n\t" \
  "ds_read_b128 %[b2], %[pb0] offset:4096\n\t" \
  "ds_read_b128 %[b3], %[pb0] offset:6144\n\t" \
  "v_mfma_f32_16x16x32_bf16 %[c05], %[a10], %[b5], %[c05]\n\t" \
  "v_mfma_f32_16x16x32_bf16 %[c15], %[a11], %[b5], %[c15]\n\t" \
  "ds_read_b128 %[b4], %[pb0] offset:8192\n\t" \
  "ds_read_b128 %[b5], %[pb0] offset:10240\n\t" \
  "v_mfma_f32_16x16x32_bf16 %[c06], %[a10], %[b6], %[c06]\n\t" \
  "v_mfma_f32_16x16x32_bf16 %[c16], %[a11], %[b6], %[c16]\n\t" \
  "ds_read_b128 %[b6], %[pb0] offset:12288\n\t" \
  "s_add_u32 %[it], %[it], 1\n\t" \
  "v_mfma_f32_16x16x32_bf16 %[c07], %[a10], %[b7], %[c07]\n\t" \
  "v_mfma_f32_16x16x32_bf16 %[c17], %[a11], %[b7], %[c17]\n\t" \
  "ds_read_b128 %[b7], %[pb0] offset:14336\n\t" \
  "s_branch Lg1_%=\n\t" \
  "Lxe_%=:\n\t" \
  "ds_read_b64 %[xe00], %[xa0] offset:0\n\t" \
  "ds_read_b64 %[xe01], %[xa1] offset:0\n\t" \
  "ds_read_b64 %[xe02], %[xa2] offset:0\n\t" \
  "ds_read_b64 %[xe03], %[xa3] offset:0\n\t" \
  "ds_read_b64 %[xe10], %[xa0] offset:2048\n\t" \
  "ds_read_b64 %[xe11], %[xa1] offset:2048\n\t" \
  "ds_read_b64 %[xe12], %[xa2] offset:2048\n\t" \
  "ds_read_b64 %[xe13], %[xa3] offset:2048\n\t" \
  "ds_read_b32 %[tg], %[tg]\n\t" \
  "s_branch Lxb_%=\n\t" \
  "Lt1_%=:\n\t" \
  "v_mfma_f32_16x16x32_bf16 %[c03], %[a10], %[b3], %[c03]\n\t" \
  "v_mfma_f32_16x16x32_bf16 %[c13], %[a11], %[b3], %[c13]\n\t" \
  "v_mfma_f32_16x16x32_bf16 %[c04], %[a10], %[b4], %[c04]\n\t" \
  "v_mfma_f32_16x16x32_bf16 %[c14], %[a11], %[b4], %[c14]\n\t" \
  "v_mfma_f32_16x16x32_bf16 %[c05], %[a10], %[b5], %[c05]\n\t" \
  "v_mfma_f32_16x16x32_bf16 %[c15], %[a11], %[b5], %[c15]\n\t" \
  "v_mfma_f32_16x16x32_bf16 %[c06], %[a10], %[b6], %[c06]\n\t" \
  "v_mfma_f32_16x16x32_bf16 %[c16], %[a11], %[b6], %[c16]\n\t" \
  "v_mfma_f32_16x16x32_bf16 %[c07], %[a10], %[b7], %[c07]\n\t" \
  "v_mfma_f32_16x16x32_bf16 %[c17], %[a11], %[b7], %[c17]\n\t" \
  "s_nop 7\n\t"
#define ASM_GEMM2 \
  "s_nop 4\n\t" \
  "s_mov_b32 %[to], 0x40000\n\t" \
  "s_mov_b32 %[cn], 1\n\t" \
  "Lc0_%=:\n\t" \
  "ds_read_b128 v[252:255], %[fa]\n\t" \
  "s_cmp_eq_u32 %[to], 0\n\t" \
  "s_cbranch_scc1 Lc0_%=x\n\t" \
  "s_sub_u32 %[to], %[to], 1\n\t" \
  "s_waitcnt lgkmcnt(0)\n\t" \
  "v_readfirstlane_b32 %[s], v253\n\t" \
  "s_cmp_ge_u32 %[s], 2\n\t" \
  "s_cbranch_scc0 Lc0_%=\n\t" \
  "Lc0_%=x:\n\t" \
  "ds_read_b128 %[k00], %[pc0] offset:0\n\t" \
  "ds_read_b128 %[k01], %[pc0] offset:2048\n\t" \
  "ds_read_b128 %[k02], %[pc0] offset:4096\n\t" \
  "ds_read_b128 %[k03], %[pc0] offset:6144\n\t" \
  "ds_read_b128 %[k10], %[pc1] offset:0\n\t" \
  "ds_read_b128 %[k11], %[pc1] offset:2048\n\t" \
  "ds_read_b128 %[k12], %[pc1] offset:4096\n\t" \
  "ds_read_b128 %[k13], %[pc1] offset:6144\n\t" \
  "s_mov_b32 %[hn], 3\n\t" \
  "s_cmp_gt_u32 1, %[own]\n\t" \
  "s_addc_u32 %[hb0], 1, -1\n\t" \
  "s_lshl_b32 %[hb0], %[hb0], 15\n\t" \
  "s_or_b32 %[hb1], %[hb0], 0x2000\n\t" \
  "s_or_b32 %[hb2], %[hb0], 0x4000\n\t" \
  "s_or_b32 %[hb3], %[hb0], 0x6000\n\t" \
  "Lh1_%=:\n\t" \
  "ds_read_b128 v[252:255], %[fa]\n\t" \
  "s_cmp_eq_u32 %[to], 0\n\t" \
  "s_cbranch_scc1 Lh1_%=x\n\t" \
  "s_sub_u32 %[to], %[to], 1\n\t" \
  "s_waitcnt lgkmcnt(0)\n\t" \
  "v_readfirstlane_b32 %[s], v254\n\t" \
  "s_cmp_ge_u32 %[s], 1\n\t" \
  "s_cbranch_scc0 Lh1_%=\n\t" \
  "Lh1_%=x:\n\t" \
  "buffer_load_dwordx4 v[216:219], %[vo], %[srd], %[hb0] offen offset:0 sc1\n\t" \
  "buffer_load_dwordx4 v[220:223], %[vo], %[srd], %[hb0] offen offset:1024 sc1\n\t" \
  "buffer_load_dwordx4 v[224:227], %[vo], %[srd], %[hb1] offen offset:0 sc1\n\t" \
  "buffer_load_dwordx4 v[228:231], %[vo], %[srd], %[hb1] offen offset:1024 sc1\n\t" \
  "buffer_load_dwordx4 v[232:235], %[vo], %[srd], %[hb2] offen offset:0 sc1\n\t" \
  "buffer_load_dwordx4 v[236:239], %[vo], %[srd], %[hb2] offen offset:1024 sc1\n\t" \
  "buffer_load_dwordx4 v[240:243], %[vo], %[srd], %[hb3] offen offset:0 sc1\n\t" \
  "buffer_load_dwordx4 v[244:247], %[vo], %[srd], %[hb3] offen offset:1024 sc1\n\t" \
  "s_cmp_gt_u32 2, %[own]\n\t" \
  "s_addc_u32 %[hb0], 2, -1\n\t" \
  "s_lshl_b32 %[hb0], %[hb0], 15\n\t" \
  "s_or_b32 %[hb1], %[hb0], 0x2000\n\t" \
  "s_or_b32 %[hb2], %[hb0], 0x4000\n\t" \
  "s_or_b32 %[hb3], %[hb0], 0x6000\n\t" \
  "s_cmp_ge_u32 %[s], 2\n\t" \
  "s_cbranch_scc1 Lh2_%=x\n\t" \
  "Lh2_%=:\n\t" \
  "ds_read_b128 v[252:255], %[fa]\n\t" \
  "s_cmp_eq_u32 %[to], 0\n\t" \
  "s_cbranch_scc1 Lh2_%=x\n\t" \
  "s_sub_u32 %[to], %[to], 1\n\t" \
  "s_waitcnt lgkmcnt(0)\n\t" \
  "v_readfirstlane_b32 %[s], v254\n\t" \
  "s_cmp_ge_u32 %[s], 2\n\t" \
  "s_cbranch_scc0 Lh2_%=\n\t" \
  "Lh2_%=x:\n\t" \
  "buffer_load_dwordx4 v[184:187], %[vo], %[srd], %[hb0] offen offset:0 sc1\n\t" \
  "buffer_load_dwordx4 v[188:191], %[vo], %[srd], %[hb0] offen offset:1024 sc1\n\t" \
  "buffer_load_dwordx4 v[192:195], %[vo], %[srd], %[hb1] offen offset:0 sc1\n\t" \
  "buffer_load_dwordx4 v[196:199], %[vo], %[srd], %[hb1] offen offset:1024 sc1\n\t" \
  "buffer_load_dwordx4 v[200:203], %[vo], %[srd], %[hb2] offen offset:0 sc1\n\t" \
  "buffer_load_dwordx4 v[204:207], %[vo], %[srd], %[hb2] offen offset:1024 sc1\n\t" \
  "buffer_load_dwordx4 v[208:211], %[vo], %[srd], %[hb3] offen offset:0 sc1\n\t" \
  "buffer_load_dwordx4 v[212:215], %[vo], %[srd], %[hb3] offen offset:1024 sc1\n\t" \
  "s_cmp_ge_u32 %[s], 3\n\t" \
  "s_cbranch_scc1 Lh3_%=x\n\t" \
  "Lh3_%=:\n\t" \
  "ds_read_b128 v[252:255], %[fa]\n\t" \
  "s_cmp_eq_u32 %[to], 0\n\t" \
  "s_cbranch_scc1 Lh3_%=x\n\t" \
  "s_sub_u32 %[to], %[to], 1\n\t" \
  "s_waitcnt lgkmcnt(0)\n\t" \
  "v_readfirstlane_b32 %[s], v254\n\t" \
  "s_cmp_ge_u32 %[s], 3\n\t" \
  "s_cbranch_scc0 Lh3_%=\n\t" \
  "Lh3_%=x:\n\t" \
  "s_branch Lin_%=\n\t" \
  "Lg2_%=:\n\t" \
  "s_waitcnt vmcnt(14) lgkmcnt(7)\n\t" \
  "ds_read_b128 v[252:255], %[fa]\n\t" \
  "v_mfma_f32_16x16x32_bf16 %[c00], v[184:187], %[k00], %[c00]\n\t" \
  "s_cmp_gt_u32 %[hn], %[own]\n\t" \
  "s_addc_u32 %[hb0], %[hn], -1\n\t" \
  "v_mfma_f32_16x16x32_bf16 %[c10], v[188:191], %[k00], %[c10]\n\t" \
  "ds_read_b128 %[k00], %[pc2] offset:0\n\t" \
  "s_waitcnt lgkmcnt(8)\n\t" \
  "v_mfma_f32_16x16x32_bf16 %[c01], v[184:187], %[k01], %[c01]\n\t" \
  "s_lshl_b32 %[hb0], %[hb0], 15\n\t" \
  "v_mfma_f32_16x16x32_bf16 %[c11], v[188:191], %[k01], %[c11]\n\t" \
  "ds_read_b128 %[k01], %[pc2] offset:2048\n\t" \
  "s_waitcnt lgkmcnt(8)\n\t" \
  "v_mfma_f32_16x16x32_bf16 %[c02], v[184:187], %[k02], %[c02]\n\t" \
  "s_or_b32 %[hb1], %[hb0], 0x2000\n\t" \
  "v_mfma_f32_16x16x32_bf16 %[c12], v[188:191], %[k02], %[c12]\n\t" \
  "ds_read_b128 %[k02], %[pc2] offset:4096\n\t" \
  "s_waitcnt lgkmcnt(8)\n\t" \
  "v_mfma_f32_16x16x32_bf16 %[c03], v[184:187], %[k03], %[c03]\n\t" \
  "s_add_u32 %[cn], %[cn], 1\n\t" \
  "v_mfma_f32_16x16x32_bf16 %[c13], v[188:191], %[k03], %[c13]\n\t" \
  "ds_read_b128 %[k03], %[pc2] offset:6144\n\t" \
  "buffer_load_dwordx4 v[184:187], %[vo], %[srd], %[hb0] offen offset:0 sc1\n\t" \
  "s_waitcnt vmcnt(13) lgkmcnt(8)\n\t" \
  "v_mfma_f32_16x16x32_bf16 %[c00], v[192:195], %[k10], %[c00]\n\t" \
  "s_cmp_eq_u32 %[cn], 9\n\t" \
  "s_cselect_b32 %[dl], 0x20000, %[d16]\n\t" \
  "v_mfma_f32_16x16x32_bf16 %[c10], v[196:199], %[k10], %[c10]\n\t" \
  "s_bitcmp1_b32 %[wr], %[cn]\n\t" \
  "s_cselect_b32 %[dl], 0xfffe4000, %[dl]\n\t" \
  "ds_read_b128 %[k10], %[pc3] offset:0\n\t" \
  "s_waitcnt lgkmcnt(8)\n\t" \
  "v_mfma_f32_16x16x32_bf16 %[c01], v[192:195], %[k11], %[c01]\n\t" \
  "v_add_u32 %[pc0], %[dl], %[pc0]\n\t" \
  "v_mfma_f32_16x16x32_bf16 %[c11], v[196:199], %[k11], %[c11]\n\t" \
  "s_or_b32 %[hb2], %[hb0], 0x4000\n\t" \
  "ds_read_b128 %[k11], %[pc3] offset:2048\n\t" \
  "buffer_load_dwordx4 v[188:191], %[vo], %[srd], %[hb0] offen offset:1024 sc1\n\t" \
  "s_waitcnt lgkmcnt(8)\n\t" \
  "v_mfma_f32_16x16x32_bf16 %[c02], v[192:195], %[k12], %[c02]\n\t" \
  "v_add_u32 %[pc1], %[dl], %[pc1]\n\t" \
  "v_mfma_f32_16x16x32_bf16 %[c12], v[196:199], %[k12], %[c12]\n\t" \
  "v_add_u32 %[fv], 1, %[fv]\n\t" \
  "ds_read_b128 %[k12], %[pc3] offset:4096\n\t" \
  "s_waitcnt lgkmcnt(7)\n\t" \
  "v_mfma_f32_16x16x32_bf16 %[c03], v[192:195], %[k13], %[c03]\n\t" \
  "v_readfirstlane_b32 %[s], v253\n\t" \
  "v_mfma_f32_16x16x32_bf16 %[c13], v[196:199], %[k13], %[c13]\n\t" \
  "ds_read_b128 %[k13], %[pc3] offset:6144\n\t" \
  "ds_write_b32 %[fw], %[fv]\n\t" \
  "s_cmp_gt_u32 %[s], %[cn]\n\t" \
  "s_cbranch_scc0 Lcs0_%=\n\t" \
  "Lcb0_%=:\n\t" \
  "buffer_load_dwordx4 v[192:195], %[vo], %[srd], %[hb1] offen offset:0 sc1\n\t" \
  "s_waitcnt vmcnt(13) lgkmcnt(8)\n\t" \
  "ds_read_b128 v[252:255], %[fa]\n\t" \
  "v_mfma_f32_16x16x32_bf16 %[c00], v[200:203], %[k00], %[c00]\n\t" \
  "s_or_b32 %[hb3], %[hb0], 0x6000\n\t" \
  "v_mfma_f32_16x16x32_bf16 %[c10], v[204:207], %[k00], %[c10]\n\t" \
  "ds_read_b128 %[k00], %[pc0] offset:0\n\t" \
  "s_waitcnt lgkmcnt(9)\n\t" \
  "v_mfma_f32_16x16x32_bf16 %[c01], v[200:203], %[k01], %[c01]\n\t" \
  "v_mfma_f32_16x16x32_bf16 %[c11], v[204:207], %[k01], %[c11]\n\t" \
  "ds_read_b128 %[k01], %[pc0] offset:2048\n\t" \
  "buffer_load_dwordx4 v[196:199], %[vo], %[srd], %[hb1] offen offset:1024 sc1\n\t" \
  "s_waitcnt lgkmcnt(9)\n\t" \
  "v_mfma_f32_16x16x32_bf16 %[c02], v[200:203], %[k02], %[c02]\n\t" \
  "v_mfma_f32_16x16x32_bf16 %[c12], v[204:207], %[k02], %[c12]\n\t" \
  "ds_read_b128 %[k02], %[pc0] offset:4096\n\t" \
  "s_waitcnt lgkmcnt(9)\n\t" \
  "v_mfma_f32_16x16x32_bf16 %[c03], v[200:203], %[k03], %[c03]\n\t" \
  "v_mfma_f32_16x16x32_bf16 %[c13], v[204:207], %[k03], %[c13]\n\t" \
  "ds_read_b128 %[k03], %[pc0] offset:6144\n\t" \
  "buffer_load_dwordx4 v[200:203], %[vo], %[srd], %[hb2] offen offset:0 sc1\n\t" \
  "s_waitcnt vmcnt(13) lgkmcnt(9)\n\t" \
  "v_mfma_f32_16x16x32_bf16 %[c00], v[208:211], %[k10], %[c00]\n\t" \
  "v_add_u32 %[pc2], %[dl], %[pc2]\n\t" \
  "v_mfma_f32_16x16x32_bf16 %[c10], v[212:215], %[k10], %[c10]\n\t" \
  "ds_read_b128 %[k10], %[pc1] offset:0\n\t" \
  "s_waitcnt lgkmcnt(9)\n\t" \
  "v_mfma_f32_16x16x32_bf16 %[c01], v[208:211], %[k11], %[c01]\n\t" \
  "v_add_u32 %[pc3], %[dl], %[pc3]\n\t" \
  "v_mfma_f32_16x16x32_bf16 %[c11], v[212:215], %[k11], %[c11]\n\t" \
  "ds_read_b128 %[k11], %[pc1] offset:2048\n\t" \
  "buffer_load_dwordx4 v[204:207], %[vo], %[srd], %[hb2] offen offset:1024 sc1\n\t" \
  "s_waitcnt lgkmcnt(6)\n\t" \
  "v_mfma_f32_16x16x32_bf16 %[c02], v[208:211], %[k12], %[c02]\n\t" \
  "v_readfirstlane_b32 %[s], v254\n\t" \
  "v_mfma_f32_16x16x32_bf16 %[c12], v[212:215], %[k12], %[c12]\n\t" \
  "ds_read_b128 %[k12], %[pc1] offset:4096\n\t" \
  "v_mfma_f32_16x16x32_bf16 %[c03], v[208:211], %[k13], %[c03]\n\t" \
  "s_add_u32 %[hn], %[hn], 1\n\t" \
  "v_mfma_f32_16x16x32_bf16 %[c13], v[212:215], %[k13], %[c13]\n\t" \
  "ds_read_b128 %[k13], %[pc1] offset:6144\n\t" \
  "buffer_load_dwordx4 v[208:211], %[vo], %[srd], %[hb3] offen offset:0 sc1\n\t" \
  "buffer_load_dwordx4 v[212:215], %[vo], %[srd], %[hb3] offen offset:1024 sc1\n\t" \
  "s_cmp_ge_u32 %[s], %[hn]\n\t" \
  "s_cbranch_scc0 Lhs0_%=\n\t" \
  "Lhb0_%=:\n\t" \
  "Lin_%=:\n\t" \
  "s_waitcnt vmcnt(14) lgkmcnt(7)\n\t" \
  "ds_read_b128 v[252:255], %[fa]\n\t" \
  "v_mfma_f32_16x16x32_bf16 %[c00], v[216:219], %[k00], %[c00]\n\t" \
  "s_cmp_gt_u32 %[hn], %[own]\n\t" \
  "s_addc_u32 %[hb0], %[hn], -1\n\t" \
  "v_mfma_f32_16x16x32_bf16 %[c10], v[220:223], %[k00], %[c10]\n\t" \
  "ds_read_b128 %[k00], %[pc2] offset:0\n\t" \
  "s_waitcnt lgkmcnt(8)\n\t" \
  "v_mfma_f32_16x16x32_bf16 %[c01], v[216:219], %[k01], %[c01]\n\t" \
  "s_lshl_b32 %[hb0], %[hb0], 15\n\t" \
  "v_mfma_f32_16x16x32_bf16 %[c11], v[220:223], %[k01], %[c11]\n\t" \
  "ds_read_b128 %[k01], %[pc2] offset:2048\n\t" \
  "s_waitcnt lgkmcnt(8)\n\t" \
  "v_mfma_f32_16x16x32_bf16 %[c02], v[216:219], %[k02], %[c02]\n\t" \
  "s_or_b32 %[hb1], %[hb0], 0x2000\n\t" \
  "v_mfma_f32_16x16x32_bf16 %[c12], v[220:223], %[k02], %[c12]\n\t" \
  "ds_read_b128 %[k02], %[pc2] offset:4096\n\t" \
  "s_waitcnt lgkmcnt(8)\n\t" \
  "v_mfma_f32_16x16x32_bf16 %[c03], v[216:219], %[k03], %[c03]\n\t" \
  "s_add_u32 %[cn], %[cn], 1\n\t" \
  "v_mfma_f32_16x16x32_bf16 %[c13], v[220:223], %[k03], %[c13]\n\t" \
  "ds_read_b128 %[k03], %[pc2] offset:6144\n\t" \
  "buffer_load_dwordx4 v[216:219], %[vo], %[srd], %[hb0] offen offset:0 sc1\n\t" \
  "s_waitcnt vmcnt(13) lgkmcnt(8)\n\t" \
  "v_mfma_f32_16x16x32_bf16 %[c00], v[224:227], %[k10], %[c00]\n\t" \
  "s_cmp_eq_u32 %[cn], 9\n\t" \
  "s_cselect_b32 %[dl], 0x20000, %[d16]\n\t" \
  "v_mfma_f32_16x16x32_bf16 %[c10], v[228:231], %[k10], %[c10]\n\t" \
  "s_bitcmp1_b32 %[wr], %[cn]\n\t" \
  "s_cselect_b32 %[dl], 0xfffe4000, %[dl]\n\t" \
  "ds_read_b128 %[k10], %[pc3] offset:0\n\t" \
  "s_waitcnt lgkmcnt(8)\n\t" \
  "v_mfma_f32_16x16x32_bf16 %[c01], v[224:227], %[k11], %[c01]\n\t" \
  "v_add_u32 %[pc0], %[dl], %[pc0]\n\t" \
  "v_mfma_f32_16x16x32_bf16 %[c11], v[228:231], %[k11], %[c11]\n\t" \
  "s_or_b32 %[hb2], %[hb0], 0x4000\n\t" \
  "ds_read_b128 %[k11], %[pc3] offset:2048\n\t" \
  "buffer_load_dwordx4 v[220:223], %[vo], %[srd], %[hb0] offen offset:1024 sc1\n\t" \
  "s_waitcnt lgkmcnt(8)\n\t" \
  "v_mfma_f32_16x16x32_bf16 %[c02], v[224:227], %[k12], %[c02]\n\t" \
  "v_add_u32 %[pc1], %[dl], %[pc1]\n\t" \
  "v_mfma_f32_16x16x32_bf16 %[c12], v[228:231], %[k12], %[c12]\n\t" \
  "v_add_u32 %[fv], 1, %[fv]\n\t" \
  "ds_read_b128 %[k12], %[pc3] offset:4096\n\t" \
  "s_waitcnt lgkmcnt(7)\n\t" \
  "v_mfma_f32_16x16x32_bf16 %[c03], v[224:227], %[k13], %[c03]\n\t" \
  "v_readfirstlane_b32 %[s], v252\n\t" \
  "v_mfma_f32_16x16x32_bf16 %[c13], v[228:231], %[k13], %[c13]\n\t" \
  "ds_read_b128 %[k13], %[pc3] offset:6144\n\t" \
  "ds_write_b32 %[fw], %[fv]\n\t" \
  "s_cmp_gt_u32 %[s], %[cn]\n\t" \
  "s_cbranch_scc0 Lcs1_%=\n\t" \
  "Lcb1_%=:\n\t" \
  "buffer_load_dwordx4 v[224:227], %[vo], %[srd], %[hb1] offen offset:0 sc1\n\t" \
  "s_waitcnt vmcnt(13) lgkmcnt(8)\n\t" \
  "ds_read_b128 v[252:255], %[fa]\n\t" \
  "v_mfma_f32_16x16x32_bf16 %[c00], v[232:235], %[k00], %[c00]\n\t" \
  "s_or_b32 %[hb3], %[hb0], 0x6000\n\t" \
  "v_mfma_f32_16x16x32_bf16 %[c10], v[236:239], %[k00], %[c10]\n\t" \
  "ds_read_b128 %[k00], %[pc0] offset:0\n\t" \
  "s_waitcnt lgkmcnt(9)\n\t" \
  "v_mfma_f32_16x16x32_bf16 %[c01], v[232:235], %[k01], %[c01]\n\t" \
  "v_mfma_f32_16x16x32_bf16 %[c11], v[236:239], %[k01], %[c11]\n\t" \
  "ds_read_b128 %[k01], %[pc0] offset:2048\n\t" \
  "buffer_load_dwordx4 v[228:231], %[vo], %[srd], %[hb1] offen offset:1024 sc1\n\t" \
  "s_waitcnt lgkmcnt(9)\n\t" \
  "v_mfma_f32_16x16x32_bf16 %[c02], v[232:235], %[k02], %[c02]\n\t" \
  "v_mfma_f32_16x16x32_bf16 %[c12], v[236:239], %[k02], %[c12]\n\t" \
  "ds_read_b128 %[k02], %[pc0] offset:4096\n\t" \
  "s_waitcnt lgkmcnt(9)\n\t" \
  "v_mfma_f32_16x16x32_bf16 %[c03], v[232:235], %[k03], %[c03]\n\t" \
  "v_mfma_f32_16x16x32_bf16 %[c13], v[236:239], %[k03], %[c13]\n\t" \
  "ds_read_b128 %[k03], %[pc0] offset:6144\n\t" \
  "buffer_load_dwordx4 v[232:235], %[vo], %[srd], %[hb2] offen offset:0 sc1\n\t" \
  "s_waitcnt vmcnt(13) lgkmcnt(9)\n\t" \
  "v_mfma_f32_16x16x32_bf16 %[c00], v[240:243], %[k10], %[c00]\n\t" \
  "v_add_u32 %[pc2], %[dl], %[pc2]\n\t" \
  "v_mfma_f32_16x16x32_bf16 %[c10], v[244:247], %[k10], %[c10]\n\t" \
  "ds_read_b128 %[k10], %[pc1] offset:0\n\t" \
  "s_waitcnt lgkmcnt(9)\n\t" \
  "v_mfma_f32_16x16x32_bf16 %[c01], v[240:243], %[k11], %[c01]\n\t" \
  "v_add_u32 %[pc3], %[dl], %[pc3]\n\t" \
  "v_mfma_f32_16x16x32_bf16 %[c11], v[244:247], %[k11], %[c11]\n\t" \
  "ds_read_b128 %[k11], %[pc1] offset:2048\n\t" \
  "buffer_load_dwordx4 v[236:239], %[vo], %[srd], %[hb2] offen offset:1024 sc1\n\t" \
  "s_waitcnt lgkmcnt(6)\n\t" \
  "v_mfma_f32_16x16x32_bf16 %[c02], v[240:243], %[k12], %[c02]\n\t" \
  "v_readfirstlane_b32 %[s], v254\n\t" \
  "v_mfma_f32_16x16x32_bf16 %[c12], v[244:247], %[k12], %[c12]\n\t" \
  "ds_read_b128 %[k12], %[pc1] offset:4096\n\t" \
  "v_mfma_f32_16x16x32_bf16 %[c03], v[240:243], %[k13], %[c03]\n\t" \
  "s_add_u32 %[hn], %[hn], 1\n\t" \
  "v_mfma_f32_16x16x32_bf16 %[c13], v[244:247], %[k13], %[c13]\n\t" \
  "ds_read_b128 %[k13], %[pc1] offset:6144\n\t" \
  "buffer_load_dwordx4 v[240:243], %[vo], %[srd], %[hb3] offen offset:0 sc1\n\t" \
  "buffer_load_dwordx4 v[244:247], %[vo], %[srd], %[hb3] offen offset:1024 sc1\n\t" \
  "s_cmp_ge_u32 %[s], %[hn]\n\t" \
  "s_cbranch_scc0 Lhs1_%=\n\t" \
  "Lhb1_%=:\n\t" \
  "s_cmp_lt_u32 %[cn], 14\n\t" \
  "s_cbranch_scc1 Lg2_%=\n\t" \
  "s_waitcnt vmcnt(14) lgkmcnt(7)\n\t" \
  "ds_read_b128 v[252:255], %[fa]\n\t" \
  "v_mfma_f32_16x16x32_bf16 %[c00], v[184:187], %[k00], %[c00]\n\t" \
  "v_mfma_f32_16x16x32_bf16 %[c10], v[188:191], %[k00], %[c10]\n\t" \
  "ds_read_b128 %[k00], %[pc2] offset:0\n\t" \
  "s_waitcnt lgkmcnt(8)\n\t" \
  "v_mfma_f32_16x16x32_bf16 %[c01], v[184:187], %[k01], %[c01]\n\t" \
  "v_mfma_f32_16x16x32_bf16 %[c11], v[188:191], %[k01], %[c11]\n\t" \
  "ds_read_b128 %[k01], %[pc2] offset:2048\n\t" \
  "s_waitcnt lgkmcnt(8)\n\t" \
  "v_mfma_f32_16x16x32_bf16 %[c02], v[184:187], %[k02], %[c02]\n\t" \
  "v_mfma_f32_16x16x32_bf16 %[c12], v[188:191], %[k02], %[c12]\n\t" \
  "ds_read_b128 %[k02], %[pc2] offset:4096\n\t" \
  "s_waitcnt lgkmcnt(8)\n\t" \
  "v_mfma_f32_16x16x32_bf16 %[c03], v[184:187], %[k03], %[c03]\n\t" \
  "s_add_u32 %[cn], %[cn], 1\n\t" \
  "v_mfma_f32_16x16x32_bf16 %[c13], v[188:191], %[k03], %[c13]\n\t" \
  "ds_read_b128 %[k03], %[pc2] offset:6144\n\t" \
  "s_waitcnt vmcnt(12) lgkmcnt(8)\n\t" \
  "v_mfma_f32_16x16x32_bf16 %[c00], v[192:195], %[k10], %[c00]\n\t" \
  "s_cmp_eq_u32 %[cn], 9\n\t" \
  "s_cselect_b32 %[dl], 0x20000, %[d16]\n\t" \
  "v_mfma_f32_16x16x32_bf16 %[c10], v[196:199], %[k10], %[c10]\n\t" \
  "s_bitcmp1_b32 %[wr], %[cn]\n\t" \
  "s_cselect_b32 %[dl], 0xfffe4000, %[dl]\n\t" \
  "ds_read_b128 %[k10], %[pc3] offset:0\n\t" \
  "s_waitcnt lgkmcnt(8)\n\t" \
  "v_mfma_f32_16x16x32_bf16 %[c01], v[192:195], %[k11], %[c01]\n\t" \
  "v_add_u32 %[pc0], %[dl], %[pc0]\n\t" \
  "v_mfma_f32_16x16x32_bf16 %[c11], v[196:199], %[k11], %[c11]\n\t" \
  "ds_read_b128 %[k11], %[pc3] offset:2048\n\t" \
  "s_waitcnt lgkmcnt(8)\n\t" \
  "v_mfma_f32_16x16x32_bf16 %[c02], v[192:195], %[k12], %[c02]\n\t" \
  "v_add_u32 %[pc1], %[dl], %[pc1]\n\t" \
  "v_mfma_f32_16x16x32_bf16 %[c12], v[196:199], %[k12], %[c12]\n\t" \
  "ds_read_b128 %[k12], %[pc3] offset:4096\n\t" \
  "s_waitcnt lgkmcnt(7)\n\t" \
  "v_mfma_f32_16x16x32_bf16 %[c03], v[192:195], %[k13], %[c03]\n\t" \
  "v_readfirstlane_b32 %[s], v253\n\t" \
  "v_mfma_f32_16x16x32_bf16 %[c13], v[196:199], %[k13], %[c13]\n\t" \
  "ds_read_b128 %[k13], %[pc3] offset:6144\n\t" \
  "s_cmp_gt_u32 %[s], %[cn]\n\t" \
  "s_cbranch_scc0 Lcs0t_%=\n\t" \
  "Lcb0t_%=:\n\t" \
  "s_waitcnt vmcnt(10) lgkmcnt(7)\n\t" \
  "v_mfma_f32_16x16x32_bf16 %[c00], v[200:203], %[k00], %[c00]\n\t" \
  "v_mfma_f32_16x16x32_bf16 %[c10], v[204:207], %[k00], %[c10]\n\t" \
  "ds_read_b128 %[k00], %[pc0] offset:0\n\t" \
  "s_waitcnt lgkmcnt(7)\n\t" \
  "v_mfma_f32_16x16x32_bf16 %[c01], v[200:203], %[k01], %[c01]\n\t" \
  "v_mfma_f32_16x16x32_bf16 %[c11], v[204:207], %[k01], %[c11]\n\t" \
  "ds_read_b128 %[k01], %[pc0] offset:2048\n\t" \
  "s_waitcnt lgkmcnt(7)\n\t" \
  "v_mfma_f32_16x16x32_bf16 %[c02], v[200:203], %[k02], %[c02]\n\t" \
  "v_mfma_f32_16x16x32_bf16 %[c12], v[204:207], %[k02], %[c12]\n\t" \
  "ds_read_b128 %[k02], %[pc0] offset:4096\n\t" \
  "s_waitcnt lgkmcnt(7)\n\t" \
  "v_mfma_f32_16x16x32_bf16 %[c03], v[200:203], %[k03], %[c03]\n\t" \
  "v_mfma_f32_16x16x32_bf16 %[c13], v[204:207], %[k03], %[c13]\n\t" \
  "ds_read_b128 %[k03], %[pc0] offset:6144\n\t" \
  "s_waitcnt vmcnt(8) lgkmcnt(7)\n\t" \
  "v_mfma_f32_16x16x32_bf16 %[c00], v[208:211], %[k10], %[c00]\n\t" \
  "v_add_u32 %[pc2], %[dl], %[pc2]\n\t" \
  "v_mfma_f32_16x16x32_bf16 %[c10], v[212:215], %[k10], %[c10]\n\t" \
  "ds_read_b128 %[k10], %[pc1] offset:0\n\t" \
  "s_waitcnt lgkmcnt(7)\n\t" \
  "v_mfma_f32_16x16x32_bf16 %[c01], v[208:211], %[k11], %[c01]\n\t" \
  "v_add_u32 %[pc3], %[dl], %[pc3]\n\t" \
  "v_mfma_f32_16x16x32_bf16 %[c11], v[212:215], %[k11], %[c11]\n\t" \
  "ds_read_b128 %[k11], %[pc1] offset:2048\n\t" \
  "s_waitcnt lgkmcnt(7)\n\t" \
  "v_mfma_f32_16x16x32_bf16 %[c02], v[208:211], %[k12], %[c02]\n\t" \
  "v_mfma_f32_16x16x32_bf16 %[c12], v[212:215], %[k12], %[c12]\n\t" \
  "ds_read_b128 %[k12], %[pc1] offset:4096\n\t" \
  "s_waitcnt lgkmcnt(7)\n\t" \
  "v_mfma_f32_16x16x32_bf16 %[c03], v[208:211], %[k13], %[c03]\n\t" \
  "v_mfma_f32_16x16x32_bf16 %[c13], v[212:215], %[k13], %[c13]\n\t" \
  "ds_read_b128 %[k13], %[pc1] offset:6144\n\t" \
  "s_waitcnt vmcnt(6) lgkmcnt(7)\n\t" \
  "v_mfma_f32_16x16x32_bf16 %[c00], v[216:219], %[k00], %[c00]\n\t" \
  "v_mfma_f32_16x16x32_bf16 %[c10], v[220:223], %[k00], %[c10]\n\t" \
  "ds_read_b128 %[k00], %[pc2] offset:0\n\t" \
  "s_waitcnt lgkmcnt(7)\n\t" \
  "v_mfma_f32_16x16x32_bf16 %[c01], v[216:219], %[k01], %[c01]\n\t" \
  "v_mfma_f32_16x16x32_bf16 %[c11], v[220:223], %[k01], %[c11]\n\t" \
  "ds_read_b128 %[k01], %[pc2] offset:2048\n\t" \
  "s_waitcnt lgkmcnt(7)\n\t" \
  "v_mfma_f32_16x16x32_bf16 %[c02], v[216:219], %[k02], %[c02]\n\t" \
  "v_mfma_f32_16x16x32_bf16 %[c12], v[220:223], %[k02], %[c12]\n\t" \
  "ds_read_b128 %[k02], %[pc2] offset:4096\n\t" \
  "s_waitcnt lgkmcnt(7)\n\t" \
  "v_mfma_f32_16x16x32_bf16 %[c03], v[216:219], %[k03], %[c03]\n\t" \
  "v_mfma_f32_16x16x32_bf16 %[c13], v[220:223], %[k03], %[c13]\n\t" \
  "ds_read_b128 %[k03], %[pc2] offset:6144\n\t" \
  "s_waitcnt vmcnt(4) lgkmcnt(7)\n\t" \
  "v_mfma_f32_16x16x32_bf16 %[c00], v[224:227], %[k10], %[c00]\n\t" \
  "v_mfma_f32_16x16x32_bf16 %[c10], v[228:231], %[k10], %[c10]\n\t" \
  "ds_read_b128 %[k10], %[pc3] offset:0\n\t" \
  "s_waitcnt lgkmcnt(7)\n\t" \
  "v_mfma_f32_16x16x32_bf16 %[c01], v[224:227], %[k11], %[c01]\n\t" \
  "v_mfma_f32_16x16x32_bf16 %[c11], v[228:231], %[k11], %[c11]\n\t" \
  "ds_read_b128 %[k11], %[pc3] offset:2048\n\t" \
  "s_waitcnt lgkmcnt(7)\n\t" \
  "v_mfma_f32_16x16x32_bf16 %[c02], v[224:227], %[k12], %[c02]\n\t" \
  "v_mfma_f32_16x16x32_bf16 %[c12], v[228:231], %[k12], %[c12]\n\t" \
  "ds_read_b128 %[k12], %[pc3] offset:4096\n\t" \
  "s_waitcnt lgkmcnt(7)\n\t" \
  "v_mfma_f32_16x16x32_bf16 %[c03], v[224:227], %[k13], %[c03]\n\t" \
  "v_mfma_f32_16x16x32_bf16 %[c13], v[228:231], %[k13], %[c13]\n\t" \
  "ds_read_b128 %[k13], %[pc3] offset:6144\n\t" \
  "s_waitcnt vmcnt(2) lgkmcnt(7)\n\t" \
  "v_mfma_f32_16x16x32_bf16 %[c00], v[232:235], %[k00], %[c00]\n\t" \
  "v_mfma_f32_16x16x32_bf16 %[c10], v[236:239], %[k00], %[c10]\n\t" \
  "s_waitcnt lgkmcnt(6)\n\t" \
  "v_mfma_f32_16x16x32_bf16 %[c01], v[232:235], %[k01], %[c01]\n\t" \
  "v_mfma_f32_16x16x32_bf16 %[c11], v[236:239], %[k01], %[c11]\n\t" \
  "s_waitcnt lgkmcnt(5)\n\t" \
  "v_mfma_f32_16x16x32_bf16 %[c02], v[232:235], %[k02], %[c02]\n\t" \
  "v_mfma_f32_16x16x32_bf16 %[c12], v[236:239], %[k02], %[c12]\n\t" \
  "s_waitcnt lgkmcnt(4)\n\t" \
  "v_mfma_f32_16x16x32_bf16 %[c03], v[232:235], %[k03], %[c03]\n\t" \
  "v_mfma_f32_16x16x32_bf16 %[c13], v[236:239], %[k03], %[c13]\n\t" \
  "s_waitcnt vmcnt(0) lgkmcnt(3)\n\t" \
  "v_mfma_f32_16x16x32_bf16 %[c00], v[240:243], %[k10], %[c00]\n\t" \
  "v_mfma_f32_16x16x32_bf16 %[c10], v[244:247], %[k10], %[c10]\n\t" \
  "s_waitcnt lgkmcnt(2)\n\t" \
  "v_mfma_f32_16x16x32_bf16 %[c01], v[240:243], %[k11], %[c01]\n\t" \
  "v_mfma_f32_16x16x32_bf16 %[c11], v[244:247], %[k11], %[c11]\n\t" \
  "s_waitcnt lgkmcnt(1)\n\t" \
  "v_mfma_f32_16x16x32_bf16 %[c02], v[240:243], %[k12], %[c02]\n\t" \
  "v_mfma_f32_16x16x32_bf16 %[c12], v[244:247], %[k12], %[c12]\n\t" \
  "s_waitcnt lgkmcnt(0)\n\t" \
  "v_mfma_f32_16x16x32_bf16 %[c03], v[240:243], %[k13], %[c03]\n\t" \
  "v_mfma_f32_16x16x32_bf16 %[c13], v[244:247], %[k13], %[c13]\n\t" \
  "s_branch Lend_%=\n\t" \
  "Lcs0_%=:\n\t" \
  "s_add_u32 %[s2], %[cn], 1\n\t" \
  "Lcr0_%=:\n\t" \
  "ds_read_b128 v[252:255], %[fa]\n\t" \
  "s_cmp_eq_u32 %[to], 0\n\t" \
  "s_cbranch_scc1 Lcb0_%=\n\t" \
  "s_sub_u32 %[to], %[to], 1\n\t" \
  "s_waitcnt lgkmcnt(0)\n\t" \
  "v_readfirstlane_b32 %[s], v253\n\t" \
  "s_cmp_ge_u32 %[s], %[s2]\n\t" \
  "s_cbranch_scc0 Lcr0_%=\n\t" \
  "s_branch Lcb0_%=\n\t" \
  "Lhs0_%=:\n\t" \
  "ds_read_b128 v[252:255], %[fa]\n\t" \
  "s_cmp_eq_u32 %[to], 0\n\t" \
  "s_cbranch_scc1 Lhb0_%=\n\t" \
  "s_sub_u32 %[to], %[to], 1\n\t" \
  "s_waitcnt lgkmcnt(0)\n\t" \
  "v_readfirstlane_b32 %[s], v254\n\t" \
  "s_cmp_ge_u32 %[s], %[hn]\n\t" \
  "s_cbranch_scc0 Lhs0_%=\n\t" \
  "s_branch Lhb0_%=\n\t" \
  "Lcs1_%=:\n\t" \
  "s_add_u32 %[s2], %[cn], 1\n\t" \
  "Lcr1_%=:\n\t" \
  "ds_read_b128 v[252:255], %[fa]\n\t" \
  "s_cmp_eq_u32 %[to], 0\n\t" \
  "s_cbranch_scc1 Lcb1_%=\n\t" \
  "s_sub_u32 %[to], %[to], 1\n\t" \
  "s_waitcnt lgkmcnt(0)\n\t" \
  "v_readfirstlane_b32 %[s], v252\n\t" \
  "s_cmp_ge_u32 %[s], %[s2]\n\t" \
  "s_cbranch_scc0 Lcr1_%=\n\t" \
  "s_branch Lcb1_%=\n\t" \
  "Lhs1_%=:\n\t" \
  "ds_read_b128 v[252:255], %[fa]\n\t" \
  "s_cmp_eq_u32 %[to], 0\n\t" \
  "s_cbranch_scc1 Lhb1_%=\n\t" \
  "s_sub_u32 %[to], %[to], 1\n\t" \
  "s_waitcnt lgkmcnt(0)\n\t" \
  "v_readfirstlane_b32 %[s], v254\n\t" \
  "s_cmp_ge_u32 %[s], %[hn]\n\t" \
  "s_cbranch_scc0 Lhs1_%=\n\t" \
  "s_branch Lhb1_%=\n\t" \
  "Lcs0t_%=:\n\t" \
  "s_add_u32 %[s2], %[cn], 1\n\t" \
  "Lcr0t_%=:\n\t" \
  "ds_read_b128 v[252:255], %[fa]\n\t" \
  "s_cmp_eq_u32 %[to], 0\n\t" \
  "s_cbranch_scc1 Lcb0t_%=\n\t" \
  "s_sub_u32 %[to], %[to], 1\n\t" \
  "s_waitcnt lgkmcnt(0)\n\t" \
  "v_readfirstlane_b32 %[s], v253\n\t" \
  "s_cmp_ge_u32 %[s], %[s2]\n\t" \
  "s_cbranch_scc0 Lcr0t_%=\n\t" \
  "s_branch Lcb0t_%=\n\t" \
  "Lend_%=:\n\t" \
  "s_nop 7\n\t"
#define ASM_REQ \
  "s_cmp_gt_u32 1, %[own]\n\t" \
  "s_addc_u32 %[hb0], 1, -1\n\t" \
  "s_lshl_b32 %[hb0], %[hb0], 15\n\t" \
  "s_or_b32 %[hb1], %[hb0], 0x2000\n\t" \
  "s_or_b32 %[hb2], %[hb0], 0x4000\n\t" \
  "s_or_b32 %[hb3], %[hb0], 0x6000\n\t" \
  "buffer_load_dwordx4 v[216:219], %[vo], %[srd], %[hb0] offen offset:0 sc1\n\t" \
  "buffer_load_dwordx4 v[220:223], %[vo], %[srd], %[hb0] offen offset:1024 sc1\n\t" \
  "buffer_load_dwordx4 v[224:227], %[vo], %[srd], %[hb1] offen offset:0 sc1\n\t" \
  "buffer_load_dwordx4 v[228:231], %[vo], %[srd], %[hb1] offen offset:1024 sc1\n\t" \
  "buffer_load_dwordx4 v[232:235], %[vo], %[srd], %[hb2] offen offset:0 sc1\n\t" \
  "buffer_load_dwordx4 v[236:239], %[vo], %[srd], %[hb2] offen offset:1024 sc1\n\t" \
  "buffer_load_dwordx4 v[240:243], %[vo], %[srd], %[hb3] offen offset:0 sc1\n\t" \
  "buffer_load_dwordx4 v[244:247], %[vo], %[srd], %[hb3] offen offset:1024 sc1\n\t" \
  "s_cmp_gt_u32 2, %[own]\n\t" \
  "s_addc_u32 %[hb0], 2, -1\n\t" \
  "s_lshl_b32 %[hb0], %[hb0], 15\n\t" \
  "s_or_b32 %[hb1], %[hb0], 0x2000\n\t" \
  "s_or_b32 %[hb2], %[hb0], 0x4000\n\t" \
  "s_or_b32 %[hb3], %[hb0], 0x6000\n\t" \
  "buffer_load_dwordx4 v[184:187], %[vo], %[srd], %[hb0] offen offset:0 sc1\n\t" \
  "buffer_load_dwordx4 v[188:191], %[vo], %[srd], %[hb0] offen offset:1024 sc1\n\t" \
  "buffer_load_dwordx4 v[192:195], %[vo], %[srd], %[hb1] offen offset:0 sc1\n\t" \
  "buffer_load_dwordx4 v[196:199], %[vo], %[srd], %[hb1] offen offset:1024 sc1\n\t" \
  "buffer_load_dwordx4 v[200:203], %[vo], %[srd], %[hb2] offen offset:0 sc1\n\t" \
  "buffer_load_dwordx4 v[204:207], %[vo], %[srd], %[hb2] offen offset:1024 sc1\n\t" \
  "buffer_load_dwordx4 v[208:211], %[vo], %[srd], %[hb3] offen offset:0 sc1\n\t" \
  "buffer_load_dwordx4 v[212:215], %[vo], %[srd], %[hb3] offen offset:1024 sc1\n\t"
#define NS 0
#define TF 0

#define OST " sc1 nt"
#define XPRIO 2
#define XPRIO2 1
#define MPRIO2 2
#define HDLY 0
#define LPRIO 2
#define POLLN 8
#define YGATE 11
#define LTHR 16
#define CPK 9
#define W2P 0
#define SWP 0
#define GK 12
#define MTOP 1
#define CROT 2
#define NRG0 0
#define NRG1 2
#define CSL 0
#define NSI 8
#define DSL 1
#define FSB 128
#define FSW 8
#define XP3 3
#define XP4 3
#define LZ 1
#define KROT 0
#define XGT 0
#define W1N 8
#define W1P 1
#define WIF 0
#define W2G 0
#define W2S 0
#define WNT " nt"
#define W1A 2
#define W1Bb 3
#define W1C 4
#define W1D 6
#define CT 0
#define STN 6
#define ST2 0
#define PK 99
#define STA 1
#define STB 1
#define STX 0
#define PLK 4
#define MPW 0
#define MPV 0
#define TKL 0
#define MGP 1
#define RQD 0
#define XG2 2
#define NFW 28
#define SPINLOG 18
constexpr int D_ = 1024, F_ = 3584;
constexpr int L_XH = 131072;
constexpr int MISC = 155648;
constexpr int L_GT = MISC;
constexpr int L_FLG = L_GT + 512;
constexpr int L_JNK = L_FLG + 256;
constexpr int L_SEL = L_JNK + 256;
constexpr int L_BROW = L_SEL + 128;
constexpr int L_ACT = L_BROW + 2048;
constexpr int L_CAND = L_ACT + 1024;
constexpr int L_ZERO = L_CAND + 256;
constexpr int L_MAP = L_ZERO + 256;
constexpr int L_TAG = L_MAP + 512;
constexpr int L_RT = L_TAG + 16;
constexpr int LDS_BYTES = 163840;
static_assert(L_RT + 32 <= LDS_BYTES, "lds");
constexpr int FL_FULL = L_FLG, FL_FREE = L_FLG + 16, FL_C = L_FLG + 32  , FL_CFREE = L_FLG + 48, FL_TAG = L_FLG + 64  ,
              FL_XD = L_FLG + 80, FL_TK = L_FLG + 96, FL_SEL = L_FLG + 112, FL_G = L_FLG + 128, FL_W = L_FLG + 136, FL_HS = L_FLG + 144, FL_C8 = L_FLG + 160;
constexpr unsigned W_EPOCH = 0;
constexpr unsigned W_R2 = 256;
constexpr unsigned W_G2 = W_R2 + 8192;
constexpr unsigned W_HF = W_G2 + 8192;
constexpr unsigned W_AGG = W_HF + 256 * FSB;
constexpr unsigned W_SEL = W_AGG + 262144;
constexpr unsigned W_GU = W_SEL + 4096;
constexpr unsigned W_YP = W_GU + 56 * 16384;
constexpr unsigned W_H = 8u << 20;
constexpr unsigned W_DBG = 20u << 20;
constexpr unsigned W_END = 21u << 20;
static_assert(W_YP + 256 * 7168 <= W_H, "ws");
#define WS_BASE(n) (((n) - W_END) & ~(size_t)0xfffff)
constexpr int SPIN_MAX = 1 << SPINLOG;

DEV float bf2f(unsigned v) { return __uint_as_float(v << 16); }
DEV float bfhi(unsigned v) { return __uint_as_float(v & 0xffff0000u); }
DEV u16 f2bf(float f) { return __builtin_bit_cast(u16, (__bf16)f); }
DEV float rbf(float f) { return bf2f(f2bf(f)); }
typedef __bf16 bf16x2_t __attribute__((ext_vector_type(2)));
DEV unsigned pk2(float a, float b) { return __builtin_bit_cast(unsigned, __builtin_convertvector((f32x2){a, b}, bf16x2_t)); }
DEV float fma1(float a, float b, float c) { float d; asm("v_fma_f32 %0, %1, %2, %3" : "=v"(d) : "v"(a), "v"(b), "v"(c)); return d; }
DEV float fnma1(float a, float b, float c) { float d; asm("v_fma_f32 %0, -%1, %2, %3" : "=v"(d) : "v"(a), "v"(b), "v"(c)); return d; }
DEV float fnmn1(float a, float b, float c) { float d; asm("v_fma_f32 %0, -%1, %2, -%3" : "=v"(d) : "v"(a), "v"(b), "v"(c)); return d; }
DEV float mul1(float a, float b) { float d; asm("v_mul_f32 %0, %1, %2" : "=v"(d) : "v"(a), "v"(b)); return d; }
DEV f32x2 cmul(f32x2 a, f32x2 b) { return (f32x2){fnma1(a.y, b.y, mul1(a.x, b.x)), fma1(a.y, b.x, mul1(a.x, b.y))}; }
DEV f32x2 cfma(f32x2 a, f32x2 b, f32x2 c) { return (f32x2){fma1(a.x, b.x, fnma1(a.y, b.y, c.x)), fma1(a.x, b.y, fma1(a.y, b.x, c.y))}; }
template <int N> DEV void waitv() { asm volatile("s_waitcnt vmcnt(%0)" ::"n"(N) : "memory"); }
DEV void bar() { asm volatile("s_barrier" ::: "memory"); }
DEV unsigned lds_rd32(unsigned a) { unsigned v; asm volatile("ds_read_b32 %0, %1\n\ts_waitcnt lgkmcnt(0)" : "=v"(v) : "v"(a) : "memory"); return v; }
DEV u32x4 lds_rd128(unsigned a) { u32x4 v; asm volatile("ds_read_b128 %0, %1\n\ts_waitcnt lgkmcnt(0)" : "=v"(v) : "v"(a) : "memory"); return v; }
DEV void lds_wr32(unsigned a, unsigned v) { asm volatile("ds_write_b32 %0, %1" ::"v"(a), "v"(v) : "memory"); }
template <int S = 1> DEV void lds_wait_ge(unsigned a, unsigned need) {
#pragma unroll 1
  for (int it = 0; it < SPIN_MAX * 4; ++it) { if (__builtin_amdgcn_readfirstlane(lds_rd32(a)) >= need) break; __builtin_amdgcn_s_sleep(S); }
}
typedef __amdgpu_buffer_rsrc_t rsrc_t;
DEV u32x4 ld128(rsrc_t r, unsigned vo, unsigned so = 0) { return __builtin_amdgcn_raw_buffer_load_b128(r, vo, so, 0); }
#define FRESH() asm volatile("" ::: "memory")
DEV u32x4 ld128c(rsrc_t r, unsigned vo) { return __builtin_amdgcn_raw_buffer_load_b128(r, vo, 0, 16); }
DEV u32x2 ld64c(rsrc_t r, unsigned vo) { return __builtin_amdgcn_raw_buffer_load_b64(r, vo, 0, 16); }
DEV void st128c(rsrc_t r, unsigned vo, u32x4 v) {
  asm volatile("s_nop 4\n\tbuffer_store_dwordx4 %0, %1, %2, 0 offen sc1\n\ts_nop 7\n\ts_nop 7" ::"v"(v), "v"(vo), "s"(r) : "memory");
}
DEV void st64c(rsrc_t r, unsigned vo, u32x2 v) { __builtin_amdgcn_raw_buffer_store_b64(v, r, vo, 0, 16); }
DEV rsrc_t mk(const void* p, unsigned bytes) { return __builtin_amdgcn_make_buffer_rsrc((void*)p, 0, bytes, 0x00020000); }

DEV unsigned wave_maxu(unsigned v) {
  v = max(v, (unsigned)__builtin_amdgcn_update_dpp((int)v, (int)v, 0x111, 0xf, 0xf, false));
  v = max(v, (unsigned)__builtin_amdgcn_update_dpp((int)v, (int)v, 0x112, 0xf, 0xf, false));
  v = max(v, (unsigned)__builtin_amdgcn_update_dpp((int)v, (int)v, 0x114, 0xf, 0xf, false));
  v = max(v, (unsigned)__builtin_amdgcn_update_dpp((int)v, (int)v, 0x118, 0xf, 0xf, false));
  v = max(v, (unsigned)__builtin_amdgcn_update_dpp((int)v, (int)v, 0x142, 0xa, 0xf, false));
  v = max(v, (unsigned)__builtin_amdgcn_update_dpp((int)v, (int)v, 0x143, 0xc, 0xf, false));
  return (unsigned)__builtin_amdgcn_readlane((int)v, 63);
}
DEV float sum16_32(float v) {
  { auto p = __builtin_amdgcn_permlane16_swap(__float_as_uint(v), __float_as_uint(v), false, false); v = __uint_as_float(p[0]) + __uint_as_float(p[1]); }
  { auto p = __builtin_amdgcn_permlane32_swap(__float_as_uint(v), __float_as_uint(v), false, false); v = __uint_as_float(p[0]) + __uint_as_float(p[1]); }
  return v;
}
DEV float wave_sum(float v) {
  v += __uint_as_float((unsigned)__builtin_amdgcn_update_dpp(0, (int)__float_as_uint(v), 0x128, 0xf, 0xf, false));
  v += __uint_as_float((unsigned)__builtin_amdgcn_update_dpp(0, (int)__float_as_uint(v), 0x124, 0xf, 0xf, false));
  v += __uint_as_float((unsigned)__builtin_amdgcn_update_dpp(0, (int)__float_as_uint(v), 0x122, 0xf, 0xf, false));
  v += __uint_as_float((unsigned)__builtin_amdgcn_update_dpp(0, (int)__float_as_uint(v), 0x121, 0xf, 0xf, false));
  return sum16_32(v);
}
DEV unsigned route_key(float r, int idx) {
  unsigned u = __float_as_uint(r) >> 16;
  u = (u & 0x8000u) ? (~u & 0xffffu) : (u | 0x8000u);
  return (u << 16) | (unsigned)(2047 - idx);
}
DEV float key_val(unsigned key) {
  unsigned u = key >> 16;
  u = (u & 0x8000u) ? (u & 0x7fffu) : (~u & 0xffffu);
  return __uint_as_float(u << 16);
}

DEV void unit_trn(u32x4 (&w)[8]) {
  u32x4 b[8];
#pragma unroll
  for (int j = 0; j < 8; ++j)
#pragma unroll
    for (int t = 0; t < 4; ++t) b[j][t] = __builtin_amdgcn_perm(w[2 * t + 1][j >> 1], w[2 * t][j >> 1], (j & 1) ? 0x07060302u : 0x05040100u);
#pragma unroll
  for (int j = 0; j < 8; ++j) w[j] = b[j];
}
DEV f32x4 fma4(f32x4 a, f32x4 b, f32x4 c) { return __builtin_elementwise_fma(a, b, c); }
DEV f32x4 fnm4(f32x4 a, f32x4 b, f32x4 c) {
  const f32x2 l = __builtin_elementwise_fma(-a.lo, b.lo, c.lo), h = __builtin_elementwise_fma(-a.hi, b.hi, c.hi);
  return (f32x4){l.x, l.y, h.x, h.y};
}
DEV f32x4 nml4(f32x4 a, f32x4 b) { const f32x2 l = -a.lo * b.lo, h = -a.hi * b.hi; return (f32x4){l.x, l.y, h.x, h.y}; }
template <int CTRL> DEV float dpp1(float v) { return __uint_as_float((unsigned)__builtin_amdgcn_update_dpp(0, (int)__float_as_uint(v), CTRL, 0xf, 0xf, true)); }
template <int CTRL> DEV f32x4 dpp4(f32x4 v) { return (f32x4){dpp1<CTRL>(v.x), dpp1<CTRL>(v.y), dpp1<CTRL>(v.z), dpp1<CTRL>(v.w)}; }
DEV f32x4 splat4(float v) { f32x2 p = {v, v}; asm volatile("" : "+v"(p)); return (f32x4){p.x, p.y, p.x, p.y}; }
DEV void cexp4(f32x4 a2, f32x4 bv, f32x4& re, f32x4& im) {
  f32x4 m, cs, sn;
#pragma unroll
  for (int r = 0; r < 4; ++r) { const float ph = __builtin_amdgcn_fractf(bv[r]); m[r] = __builtin_amdgcn_exp2f(a2[r]); cs[r] = __builtin_amdgcn_cosf(ph); sn[r] = __builtin_amdgcn_sinf(ph); }
  re = m * cs; im = m * sn;
}
DEV f32x4 unpk4(u32x2 v) { return (f32x4){bf2f(v.x & 0xffffu), bfhi(v.x), bf2f(v.y & 0xffffu), bfhi(v.y)}; }
DEV void bcast8(int n, f32x4 v, f32x4& o) {
  switch (n) {
    case 0: o = dpp4<0x150>(v); break; case 1: o = dpp4<0x151>(v); break; case 2: o = dpp4<0x152>(v); break; case 3: o = dpp4<0x153>(v); break;
    case 4: o = dpp4<0x154>(v); break; case 5: o = dpp4<0x155>(v); break; case 6: o = dpp4<0x156>(v); break; default: o = dpp4<0x157>(v); break;
  }
}
DEV f32x4 lds4(LAS char* smem, int a) { return *(const LAS f32x4*)(smem + a); }
template <int S = 1> DEV void lds_wait_min4(unsigned a, unsigned need) {
#pragma unroll 1
  for (int it = 0; it < SPIN_MAX * 4; ++it) {
    const u32x4 f = lds_rd128(a);
    if (__builtin_amdgcn_readfirstlane(min(min(f.x, f.y), min(f.z, f.w))) >= need) break;
    __builtin_amdgcn_s_sleep(S);
  }
}

DEV void dma4(rsrc_t r0, rsrc_t r1, LAS char* la, const unsigned (&v)[4], unsigned so) {
  __builtin_amdgcn_raw_ptr_buffer_load_lds(r0, (LAS void*)la, 16, v[0], so, 0, 0);
  __builtin_amdgcn_raw_ptr_buffer_load_lds(r0, (LAS void*)la, 16, v[1], so, 1024, 0);
  __builtin_amdgcn_raw_ptr_buffer_load_lds(r1, (LAS void*)la, 16, v[2], so, 2048, 0);
  __builtin_amdgcn_raw_ptr_buffer_load_lds(r1, (LAS void*)la, 16, v[3], so, 3072, 0);
}

__global__ __launch_bounds__(512, 2) void fused(const u16*, const u16*, const u16*, const u16*, const u16*, const u16*, const u16*, const u16*, const u16*, const u16*,
                                                const u16*, const u16*, const u16*, const u16*, char*, float*) {
#if __HIP_DEVICE_COMPILE__
  unsigned kw0, kw1;
  asm volatile("s_load_dword %0, %2, 0x0\n\ts_load_dword %1, %2, 0x40" : "=&s"(kw0), "=&s"(kw1) : "s"(__builtin_amdgcn_kernarg_segment_ptr()));
  extern __shared__ __attribute__((aligned(16))) char smem_[];
  LAS char* smem = (LAS char*)smem_;
  const int tid = threadIdx.x, hw = __builtin_amdgcn_readfirstlane(tid >> 6), wid = SWP ? hw ^ ((hw >> 1) & 2) : hw;
  const int bid = blockIdx.x, xcd = bid & 7, loc = bid >> 3;
  const int c = (xcd >> 1) * 4 + (loc >> 3), j = (xcd & 1) * 8 + (loc & 7);
  int lane, fr, fq;
  auto relane = [&]() {
    asm volatile("v_mbcnt_lo_u32_b32 %0, -1, 0\n\tv_mbcnt_hi_u32_b32 %0, -1, %0" : "=v"(lane));
    __builtin_assume(lane >= 0 && lane < 64);
    fr = lane & 15; fq = lane >> 4;
  };
  const int crot = CROT == 0 ? c + 1 : CROT == 1 ? 4 * c + (c >> 2) : CROT == 2 ? (c & 1) + 4 * (c & 2) + (c >> 2) * 2 : CROT == 3 ? 2 * c : (c & 1) * 2 + 4 * (c & 2) + (c >> 2);
  const int pown = (j - crot) & 15;
  auto upos = [&](int u) { return u == 0 ? pown : u - 1 + (u - 1 >= pown ? 1 : 0); };
  u64 pc0;
  unsigned l0, l1, l2;
  asm volatile("s_getpc_b64 %0\n\tLk_pc:\n\ts_mov_b32 %1, Lk_m-Lk_pc\n\ts_mov_b32 %2, Lk_l-Lk_pc\n\ts_mov_b32 %3, Lk_x-Lk_pc" : "=s"(pc0), "=s"(l0), "=s"(l1), "=s"(l2));
  auto codepull = [&]() {
    const unsigned po = min((unsigned)(loc * 16 + (lane & 15)) * 128u, max(l0, max(l1, l2)) - 4u);
    if (lane < 16) __builtin_amdgcn_global_load_lds((const __attribute__((address_space(1))) void*)(pc0 + po), (LAS void*)(smem + L_JNK), 4, 0, 0);
  };
  u32x2 p0, p1, p2, dv;
  u32x4 xv[2][2], wv[2], gv[2];
#define MLOADS(A_, B_) \
    asm volatile("global_load_dwordx2 %0, %12, %16\n\tglobal_load_dwordx2 %1, %12, %17\n\tglobal_load_dwordx2 %2, %12, %18\n\t" \
                 "global_load_dwordx4 %4, %13, %19\n\tglobal_load_dwordx4 %5, %13, %19 offset:1024\n\tglobal_load_dwordx4 %6, %13, %19 offset:2048\n\tglobal_load_dwordx4 %7, %13, %19 offset:3072\n\t" \
                 "global_load_dwordx4 %8, %14, %20\n\tglobal_load_dwordx4 %9, %14, %20 offset:1024\n\tglobal_load_dwordx4 %10, %14, %21\n\tglobal_load_dwordx4 %11, %14, %21 offset:1024\n\t" \
                 "global_load_dwordx2 %3, %15, %22" \
                 : "=&v"(p0), "=&v"(p1), "=&v"(p2), "=&v"(dv), "=&v"(xv[0][0]), "=&v"(xv[0][1]), "=&v"(xv[1][0]), "=&v"(xv[1][1]), "=&v"(wv[0]), "=&v"(wv[1]), "=&v"(gv[0]), "=&v"(gv[1]) \
                 : "v"((j * 64 + wid * 16 + 4 * fq) * 2), "v"((c * 128 + j * 8 + wid * 2) * 2048 + lane * 16), "v"(lane * 16), "v"((j * 64 + 4 * fr) * 2), \
                   "s"(A_[1]), "s"(A_[2]), "s"(A_[7]), "s"(A_[0]), "s"(B_[1]), "s"(B_[5]), "s"(B_[0]) : "memory")
  if (wid < 4) {
    u64x8 ta, tb;
    if (MTOP) asm volatile("s_load_dwordx16 %0, %2, 0x0\n\ts_load_dwordx16 %1, %2, 0x40" : "=&s"(ta), "=&s"(tb) : "s"(__builtin_amdgcn_kernarg_segment_ptr()));
    relane();
    if (wid == 0) codepull();
    if (MTOP) {
      asm volatile("s_waitcnt lgkmcnt(0)" : "+s"(ta), "+s"(tb));
      MLOADS(ta, tb);
    }
  }
  const int l = wid & 1;
#define FFNDEF() int b_ = bid; asm volatile("" : "+s"(b_)); \
  const bool ffn = b_ < 8 * NFW; \
  const int sg = (b_ & 7) * 7 + (b_ >> 5), kq = (b_ >> 3) & 3, sg1 = (sg & ~1) + l, gu1 = sg & 1; \
  const unsigned gub = W_GU + ((sg1 * 4 + kq) * 2 + gu1) * 2048, ypb = W_YP + (kq * 4 + l * 2) * 16 * 7168 + sg * 128
  if (__builtin_expect(wid >= 4, 1)) {
  u64x8 ka, kb;
  asm volatile("s_load_dwordx16 %0, %2, 0x0\n\ts_load_dwordx16 %1, %2, 0x40" : "=&s"(ka), "=&s"(kb) : "s"(__builtin_amdgcn_kernarg_segment_ptr()));
  relane();
  unsigned vo1[4];
#pragma unroll
  for (int e = 0; e < 4; ++e)
    vo1[e] = (l == 0 ? c * 128 + 8 * (lane >> 3) + 64 * (e & 1) + (e >> 1) : j * 64 + (lane >> 3) + 8 * (e & 1)) * 2048 + (((lane & 7) ^ (lane >> 3)) * 16) + 4096 - e * 1024;
  const unsigned s4 = (l == 0 ? 2 : 16) * 2048, so1 = wid >= 6 ? 2 * s4 : 0, la1 = l * 16384 + (wid >= 6 ? 8192 : 0);
  const int s0 = (KROT == 0 ? c + j : KROT == 1 ? (c >> 1) + (j >> 1) + 4 * (c >> 2) : KROT == 2 ? c + (j >> 1) + 4 * (j >> 3) : KROT == 3 ? 2 * c + j : KROT == 4 ? 4 * c + 2 * j + (c >> 2) : KROT == 5 ? (c >> 1) + j : 3 * c + j) & 15;
  unsigned sl0 = ((s0 + (s0 == j ? 1 : 0)) & 15) * 128 + so1;
  asm volatile("" : "+v"(vo1[0]), "+v"(vo1[1]), "+v"(vo1[2]), "+v"(vo1[3]) : "s"(sl0), "s"(s4), "s"(la1));
  asm volatile("s_waitcnt lgkmcnt(0)" : "+s"(ka), "+s"(kb) : "s"(kw0), "s"(kw1));
  const char *x = (const char*)ka[0], *Bre = (const char*)ka[3], *Bim = (const char*)ka[4], *Cre = (const char*)ka[5], *Cim = (const char*)ka[6], *w_gate = (const char*)kb[2],
             *w_up = (const char*)kb[3], *w_down = (const char*)kb[4];
  char* ws = (char*)kb[6];
  const rsrc_t r0 = mk((l == 0 ? x : Bre) - 4096, (l == 0 ? 1 << 22 : 1 << 21) + 4096), r1 = mk((l == 0 ? x : Bim) - 4096, (l == 0 ? 1 << 22 : 1 << 21) + 4096);
  auto issue = [&](unsigned sk, unsigned slot) { dma4(r0, r1, smem + slot + la1, vo1, sk); dma4(r0, r1, smem + slot + la1 + 4096, vo1, sk + s4); };
  issue(sl0, 0);
  __builtin_amdgcn_sched_barrier(0);
  const rsrc_t rws = mk(ws, W_END);
  const rsrc_t rx = mk(x, 2048 * 1024 * 2);
  u64 tbl;
  {
    const int n = (j - s0) & 15;
    const u64 seq = 0xfedcba9876543210ull, rot = s0 ? (seq >> (4 * s0)) | (seq << (64 - 4 * s0)) : seq, lo = (1ull << (4 * n)) - 1;
    tbl = (rot & lo) | ((rot >> 4) & ~lo) | ((u64)j << 60);
  }
  auto slice = [&](int k) { return ((unsigned)(tbl >> (4 * k)) & 15u) * 128u; };
  __builtin_amdgcn_sched_barrier(0);

  auto flag = [&](int a, unsigned v) { lds_wr32(lane == 0 ? (unsigned)a : (unsigned)(L_JNK + lane * 4), v); };
  auto flagmax = [&](int a, unsigned v) { asm volatile("ds_max_u32 %0, %1" ::"v"(lane == 0 ? (unsigned)a : (unsigned)(L_JNK + lane * 4)), "v"(v) : "memory"); };
  auto hpoll = [&](unsigned tagA, unsigned tagB) {
    lds_wait_min4(FL_FREE, 9u);
    __builtin_amdgcn_s_sleep(HDLY);
#pragma unroll 1
    for (int it = 0; it < SPIN_MAX; ++it) {
      FRESH();
      u32x2 f = {0u, 0u};
      const unsigned fo = W_HF + (c * 16 + upos(lane >> 2)) * FSB + (lane & 3) * FSW;
      if (PLK == 0) f = ld64c(rws, fo);
      else if (PLK == 4) asm volatile("buffer_atomic_or_x2 %0, %1, %2, 0 offen sc0 sc1\n\ts_waitcnt vmcnt(0)" : "+v"(f) : "v"(fo), "s"(rws) : "memory");
      else f = __builtin_amdgcn_raw_buffer_load_b64(rws, fo, 0, PLK == 1 ? 17 : PLK == 2 ? 18 : 19);
      const u64 m = ~__ballot(lane < 4 * NS + 4 || (f.x == tagA && f.y == tagB));
      if (!m) break;
      flagmax(FL_C + 8, ((unsigned)__builtin_ctzll(m) >> 2) - 1u);
    }
    flagmax(FL_C + 8, 99u);
  };
    const rsrc_t rcr = mk(Cre, WIF == 4 ? 0 : 1 << 21), rci = mk(Cim, WIF == 4 || WIF == 5 ? 0 : 1 << 21);
    const unsigned ce = (j * 64 + 4 * (lane >> 3)) * 2048 + (((lane & 7) ^ (lane >> 4)) * 16);
    const unsigned co = (j * 64 + 32 + 4 * (lane >> 3)) * 2048 + (((lane & 7) ^ (4 + (lane >> 4))) * 16);
    auto cfill = [&](int u, int slot, int q0, int q1) {
      LAS char* dst = smem + slot * 16384;
#pragma unroll
      for (int q = q0; q < q1; ++q)
        __builtin_amdgcn_raw_ptr_buffer_load_lds(q < 8 ? rcr : rci, (LAS void*)(dst + q * 1024), 16, (q & 1) ? co : ce, ((q & 7) >> 1) * 2048 + ((upos(u) + crot) & 15) * 128, 0, 0);
    };
  if (__builtin_expect(wid == 4 || wid == 5, 1)) {
    __builtin_amdgcn_s_setprio(LPRIO);
    issue(slice(1), 32768);
    issue(slice(2), 65536);
    constexpr int K0 = 0, W10 = 0;
    {
      u64 t = tbl >> (4 * K0 + 12);
#pragma unroll 1
      for (int k = K0; k < 13; ++k) {
        const unsigned sk = ((unsigned)t & 15u) << 7;
        waitv<16>();
        bar();
        issue(sk, ((k + 3) & 3) * 32768);
        if (k == CPK && l == 1) codepull();
        t >>= 4;
      }
    }
    u32x4 cq[3][16];
    auto creg = [&](int g, int q0 = 0, int q1 = 16, bool thr = true) {
#pragma unroll
      for (int q = q0; q < q1; ++q) {
        const unsigned so = ((q & 7) >> 1) * 2048 + ((upos(10 + l + 2 * g) + crot) & 15) * 128;
        if (q < 8) asm volatile("buffer_load_dwordx4 %0, %1, %2, %3 offen" : "=v"(cq[g][q]) : "v"((q & 1) ? co : ce), "s"(rcr), "s"(so) : "memory");
        else asm volatile("buffer_load_dwordx4 %0, %1, %2, %3 offen" : "=v"(cq[g][q]) : "v"((q & 1) ? co : ce), "s"(rci), "s"(so) : "memory");
        if ((q & 7) == 7 && thr) waitv<LTHR>();
      }
    };
#define CTL(i) { _Pragma("unroll") for (int e = (i) * CT; e < (i) * CT + CT; ++e) creg(2 - (e >> 2), (e & 3) * 4, (e & 3) * 4 + 4, false); }
    CTL(0)
    waitv<16 + 4 * CT>(); bar();
    CTL(1)
    waitv<8 + 8 * CT>(); bar();
    CTL(2)
    const unsigned tagA = lds_rd32(L_TAG), tagB = tagA + 0x40000000u;
    waitv<12 * CT>(); bar();
    CTL(3)
    bar();
    lds_wait_ge(FL_W + l * 4, 1u);
    __builtin_amdgcn_s_setprio(1);
#pragma unroll
    for (int g = 0; g < (TF ? CT : 0); ++g) creg(2 - g);
#pragma unroll 1
    for (int u = l; u < 8; u += 2) { if (TF) lds_wait_min4(FL_FREE, (u >> 1) + 1); cfill(u, u, 0, 8); waitv<LTHR>(); cfill(u, u, 8, 16); waitv<LTHR>(); if (u >= 2) flag(FL_C + l * 4, (unsigned)(u - 1)); }
#pragma unroll
    for (int g = 0; g < 3; ++g) {
      if (g >= (l ? NRG1 : NRG0) - CT) break;
      waitv<16>();
      if (g == 1) flag(FL_C + l * 4, (unsigned)(7 + l));
      creg(g);
    }
    waitv<0>();
    if ((l ? NRG1 : NRG0) - CT < 2) flag(FL_C + l * 4, (unsigned)(7 + l));
    if (l == 0) {
      __builtin_amdgcn_s_setprio(3);
      hpoll(tagA, tagB);
    }
    else {
      lds_wait_min4(FL_CFREE, 1u);
      cfill(8, 0, 0, 16);
      cfill(9, 8, 0, 16);
      waitv<0>();
      flagmax(FL_C8, 1u);
      flag(FL_C + 4, 10u);
    }
    __builtin_amdgcn_s_setprio(0);
    if (l == 0) { lds_wait_ge(FL_C8, 1u); flag(FL_C, 9u); }
#pragma unroll
    for (int g = 0; g < 3; ++g) {
      asm volatile("" : "+v"(cq[g][0]), "+v"(cq[g][1]), "+v"(cq[g][2]), "+v"(cq[g][3]), "+v"(cq[g][4]), "+v"(cq[g][5]), "+v"(cq[g][6]), "+v"(cq[g][7]),
                   "+v"(cq[g][8]), "+v"(cq[g][9]), "+v"(cq[g][10]), "+v"(cq[g][11]), "+v"(cq[g][12]), "+v"(cq[g][13]), "+v"(cq[g][14]), "+v"(cq[g][15]));
      const int u = 10 + l + 2 * g;
      lds_wait_min4<LZ>(FL_CFREE, (unsigned)(u - 8));
      const int slot = u - 9;
      if (g < (l ? NRG1 : NRG0)) {
#pragma unroll
        for (int q = 0; q < 16; ++q)
          asm volatile("ds_write_b128 %0, %1 offset:%2" ::"v"(slot * 16384 + lane * 16), "v"(cq[g][q]), "n"(q * 1024) : "memory");
      } else {
#pragma unroll
        for (int q = 0; q < 16; q += 4) { cfill(u, slot, q, q + 4); if (CSL) __builtin_amdgcn_s_sleep(CSL); }
        waitv<0>();
      }
      flag(FL_C + l * 4, g == 2 ? 99u : (unsigned)(u + 1));
    }
    asm volatile("Lk_l:");
    return;
  }
  {
    const int xw = wid - 6;
    __builtin_amdgcn_s_setprio(XPRIO);
    u32x2 ep;
    unsigned tagA, tagB;
    if (xw == 0) lds_wr32(L_FLG + lane * 4, 0u);
    else asm volatile("ds_write_b64 %0, %1\n\tds_write_b64 %2, %3" ::"v"(L_CAND + lane * 8), "v"((u32x2){0u, 0u}), "v"(L_MAP + lane * 8), "v"((u32x2){L_ZERO, L_ZERO}) : "memory");
    FFNDEF();
    u32x4 w1[4][8], w2[2][8];
    const rsrc_t rw1 = mk(gu1 ? w_up : w_gate, ffn ? D_ * F_ * 2 : 0), r2 = mk(w_down, ffn ? D_ * F_ * 2 : 0);
    unsigned o1 = (kq * 256 + 32 * (fr >> 3) + 8 * fq) * (F_ * 2) + (sg1 * 64 + 8 * (fr & 7)) * 2;
    unsigned o2 = (sg * 64 + 32 * (fr >> 3) + 8 * fq) * 2048 + (kq * 256 + xw * 128 + 8 * (fr & 7)) * 2;
#define LDW1(b) { _Pragma("unroll") for (int i = 4 * ((b) & 1); i < 4 * ((b) & 1) + 4; ++i) \
      asm volatile("buffer_load_dwordx4 %0, %1, %2, %3 offen" WNT : "=v"(w1[(b) >> 1][i]) : "v"(o1), "s"(rw1), "s"((((b) >> 1) * 64 + i) * (F_ * 2)) : "memory"); }
#define LDW2(a, b) { _Pragma("unroll") for (int i = a; i < b; ++i) \
      asm volatile("buffer_load_dwordx4 %0, %1, %2, %3 offen" WNT : "=v"(w2[i >> 3][i & 7]) : "v"(o2), "s"(r2), "s"((i & 7) * 2048 + (i >> 3) * 128) : "memory"); }
#define TRN(w, n) { asm volatile("s_waitcnt vmcnt(%8)" : "+v"(w[0]), "+v"(w[1]), "+v"(w[2]), "+v"(w[3]), "+v"(w[4]), "+v"(w[5]), "+v"(w[6]), "+v"(w[7]) : "n"(n) : "memory"); \
      unit_trn(w); asm volatile("" : "+v"(w[0]), "+v"(w[1]), "+v"(w[2]), "+v"(w[3]), "+v"(w[4]), "+v"(w[5]), "+v"(w[6]), "+v"(w[7])); }
    unsigned key[16];
    unsigned rgo = W_R2 + l * 4096 + lane * 16, ggo = W_G2 + c * 512 + (lane & 31) * 16;
    u32x4 g[4], a;
    asm volatile("" : "+v"(o1), "+v"(o2), "+v"(rgo), "+v"(ggo) : "s"(rw1), "s"(r2));
    auto A_EP = [&]() { asm volatile("buffer_load_dwordx2 %0, off, %1, 0 sc1" : "=v"(ep) : "s"(rws) : "memory"); };
    auto A_TAG = [&]() {
      asm volatile("" : "+v"(ep));
      tagA = ep.x + 1u; tagB = tagA + 0x40000000u;
      if (xw == 0 && lane == 0) lds_wr32(L_TAG, tagA);
    };
    auto A_CLR = [&]() {
      asm volatile("s_nop 1\n\t"
                   "buffer_store_dwordx4 %0, %1, %3, 0 offen sc1\n\tbuffer_store_dwordx4 %0, %1, %3, 0 offen offset:1024 sc1\n\t"
                   "buffer_store_dwordx4 %0, %2, %3, 0 offen sc1\n\tbuffer_store_dwordx4 %0, %2, %3, %4 offen sc1\n\t"
                   "buffer_store_dwordx4 %0, %2, %3, %5 offen sc1\n\tbuffer_store_dwordx4 %0, %2, %3, %6 offen sc1\n\t"
                   "buffer_store_dwordx4 %0, %7, %3, %8 offen sc1\n\tbuffer_store_dwordx4 %0, %7, %3, %8 offen offset:1024 sc1\n\t"
                   "buffer_store_dwordx4 %0, %7, %3, %8 offen offset:2048 sc1\n\tbuffer_store_dwordx4 %0, %7, %3, %8 offen offset:3072 sc1\n\ts_nop 7"
                   ::"v"((u32x4){~0u, ~0u, ~0u, ~0u}), "v"(ffn ? gub + lane * 16 : 0xf0000000u), "v"(ffn ? ypb + (lane >> 3) * 7168 + (lane & 7) * 16 : 0xf0000000u), "s"(rws),
                   "s"(8 * 7168), "s"(16 * 7168), "s"(24 * 7168), "v"(NS ? lane * 16 : 0xf0000000u), "s"(W_H + c * 524288 + (j * 2 + xw) * 4096) : "memory");
    };
    auto A_PUB = [&]() {
      u32x2 rt;
      asm volatile("ds_read_b64 %0, %1\n\ts_waitcnt lgkmcnt(0)" : "=v"(rt) : "v"(L_RT + (lane & 1) * 16 + xw * 8) : "memory");
      st128c(rws, lane < 2 ? (lane ? W_G2 : W_R2) + (c * 128 + j * 8 + xw * 4) * 4 : 0xf0000000u, (u32x4){rt.x, tagA, rt.y, tagB});
    };
    auto A_GAT = [&]() {
#pragma unroll
      for (int q = 0; q < 4; ++q) asm volatile("buffer_load_dwordx4 %0, %1, %2, 0 offen offset:%3 sc1" : "=v"(g[q]) : "v"(rgo), "s"(rws), "n"(q * 1024) : "memory");
      asm volatile("buffer_load_dwordx4 %0, %1, %2, 0 offen sc1" : "=v"(a) : "v"(ggo), "s"(rws) : "memory");
    };
    static_assert(NS <= 3, "cleared blocks");
    A_EP();
    issue(slice(1) + so1, 32768);
    issue(slice(2) + so1, 65536);
    asm volatile("s_waitcnt vmcnt(17) lgkmcnt(0)" ::: "memory"); bar();
    issue(slice(3) + so1, 98304);
    waitv<24>(); A_TAG();
    A_CLR();
    asm volatile("s_waitcnt vmcnt(26) lgkmcnt(0)" ::: "memory"); bar();
    issue(slice(4) + so1, 0);
    asm volatile("s_waitcnt vmcnt(26) lgkmcnt(0)" ::: "memory"); bar();
    issue(slice(5) + so1, 32768);
    asm volatile("s_waitcnt vmcnt(26) lgkmcnt(0)" ::: "memory"); bar();
    issue(slice(6) + so1, 65536);
    waitv<24>(); A_PUB();
    constexpr int K0 = 4, W10 = 0;
    {
      const int m0 = ((b_ >> 3) * STA + (b_ & 7) * STB + xw * STX) % STN, ms = K0 + m0, m2 = ST2 ? K0 + (m0 + STN / 2) % STN : 99;
#pragma unroll
      for (int k = K0; k < 13; ++k) {
        const int e = k > GK && k <= GK + 3 ? 5 : 0;
        const bool i1 = (unsigned)(k - ms - 1) < 3u, i2 = (unsigned)(k - m2 - 1) < 3u;
        if (i1 && i2) asm volatile("s_waitcnt vmcnt(%0)" ::"n"(32 + 4 * ST2 + e) : "memory");
        else if (i1) asm volatile("s_waitcnt vmcnt(%0)" ::"n"(32 + e) : "memory");
        else if (i2) asm volatile("s_waitcnt vmcnt(%0)" ::"n"(16 + 4 * ST2 + e) : "memory");
        else asm volatile("s_waitcnt vmcnt(%0)" ::"n"(16 + e) : "memory");
        bar();
        issue(slice(k + 3) + so1, ((k + 3) & 3) * 32768);
        if (k == PK) A_PUB();
        if (k == GK) A_GAT();
        if (k < K0 + STN && k == ms) LDW2(0, 16)
        if (k < K0 + STN && k == m2) { _Pragma("unroll") for (int b2 = 0; b2 < ST2; ++b2) LDW1(b2) }
      }
      static_assert(K0 + STN <= 13 && PK >= 4, "order");
    }
#define LDW1R(a, b) { _Pragma("unroll") for (int b_ = (a); b_ < (b); ++b_) LDW1(b_) }
    LDW1R((W10 + ST2), W1A)
    waitv<16 + 4 * (W1A - (W10 + ST2)) + (GK >= 10 ? 5 : 0)>(); bar(); LDW1R(W1A, W1Bb)
    waitv<8 + 4 * (W1Bb - (W10 + ST2)) + (GK >= 11 ? 5 : 0)>(); bar(); LDW1R(W1Bb, W1C)
    waitv<4 * (W1C - (W10 + ST2)) + (GK >= 12 ? 5 : 0)>(); bar(); LDW1R(W1C, W1D)
    bar();
    __builtin_amdgcn_s_setprio(XPRIO2);
#define W1B() do { if (XGT == 1) lds_wait_min4(FL_FREE, 1u); if (XGT == 2) lds_wait_min4(FL_HS, 1u); if (XGT == 3) lds_wait_ge(FL_HS, 1u); \
      _Pragma("unroll") for (int b_ = W1N; b_ < 8; ++b_) LDW1(b_) } while (0)
#pragma unroll
    for (int b_ = W1D; b_ < W1N; ++b_) LDW1(b_)
    if (W2P == 10) LDW2(0, 16)
    if (lane == 0) lds_wr32(FL_W + xw * 4, 1u);
    asm volatile("s_waitcnt vmcnt(%5)" : "+v"(g[0]), "+v"(g[1]), "+v"(g[2]), "+v"(g[3]), "+v"(a) : "n"(TKL == 2 ? 0 : 4 * (W1N - (W10 + ST2))) : "memory");
    {
#pragma unroll 1
      for (int it = 0; it < SPIN_MAX; ++it) {
        bool ok = true;
#pragma unroll
        for (int q = 0; q < 4; ++q) ok = ok && g[q].y == tagA && g[q].w == tagB;
        if (__all(ok && a.y == tagA && a.w == tagB)) break;
        __builtin_amdgcn_s_sleep(8);
        A_GAT();
        asm volatile("s_waitcnt vmcnt(0)" : "+v"(g[0]), "+v"(g[1]), "+v"(g[2]), "+v"(g[3]), "+v"(a)::"memory");
      }
      if (l == 0) {
        const unsigned gp = lane < 32 ? a.x : a.z, ga = L_GT + ((4 * (lane & 1) + 2 * (lane >> 5)) * 16 + ((lane & 31) >> 1)) * 4;
        lds_wr32(ga, gp << 16);
        lds_wr32(ga + 64, gp & 0xffff0000u);
      }
      asm volatile("ds_add_u32 %0, %1" ::"v"(lane == 0 ? (unsigned)FL_G : (unsigned)(L_JNK + lane * 4)), "v"(1u) : "memory");
      if (W1P == 1) W1B();
      if (TKL == 1) waitv<0>();
#pragma unroll
      for (int q = 0; q < 4; ++q) {
        const unsigned r0 = 2047 - (l * 1024 + q * 256 + lane * 4);
        key[4 * q] = (g[q].x << 16) + r0; key[4 * q + 1] = (g[q].x & 0xffff0000u) + (r0 - 1);
        key[4 * q + 2] = (g[q].z << 16) + (r0 - 2); key[4 * q + 3] = (g[q].z & 0xffff0000u) + (r0 - 3);
        asm volatile("" : "+v"(key[4 * q]), "+v"(key[4 * q + 1]), "+v"(key[4 * q + 2]), "+v"(key[4 * q + 3]));
      }
      unsigned m = key[0], m2 = 0, m3 = 0;
#pragma unroll
      for (int i = 1; i < 16; ++i) {
        asm("v_med3_u32 %0, %1, %0, %3\n\tv_med3_u32 %1, %2, %1, %3\n\tv_max_u32 %2, %2, %3" : "+v"(m3), "+v"(m2), "+v"(m) : "v"(key[i]));
      }
      asm volatile("" : "+v"(m), "+v"(m2), "+v"(m3));
      unsigned T = 0;
#pragma unroll
      for (int b = 31; b >= 16; --b) {
        const unsigned t2 = T | (1u << b);
        T = __popcll(__ballot(m >= t2)) >= 8 ? t2 : T;
      }
      asm volatile("" : "+s"(T));
      const u64 k1 = __ballot(m >= T), k2 = __ballot(m2 >= T);
      const int n1 = __popcll(k1);
      if (__ballot(m3 >= T) || n1 + __popcll(k2) > 16) {
        unsigned cand = 0;
#pragma unroll 1
        for (int s = 0; s < 8; ++s) {
          unsigned mm = key[0];
#pragma unroll
          for (int i = 1; i < 16; ++i) mm = max(mm, key[i]);
          mm = wave_maxu(mm);
          cand = lane == s ? mm : cand;
#pragma unroll
          for (int i = 0; i < 16; ++i) key[i] = key[i] == mm ? 0u : key[i];
        }
        if (lane < 8) lds_wr32(L_CAND + l * 64 + lane * 4, cand);
      } else {
        const unsigned p1 = __builtin_amdgcn_mbcnt_hi((unsigned)(k1 >> 32), __builtin_amdgcn_mbcnt_lo((unsigned)k1, 0u));
        const unsigned p2 = __builtin_amdgcn_mbcnt_hi((unsigned)(k2 >> 32), __builtin_amdgcn_mbcnt_lo((unsigned)k2, (unsigned)n1));
        lds_wr32(m >= T ? L_CAND + l * 64 + p1 * 4 : L_JNK + lane * 4, m);
        if (k2) lds_wr32(m2 >= T ? L_CAND + l * 64 + p2 * 4 : L_JNK + lane * 4, m2);
      }
      if (lane == 0) lds_wr32(FL_TK + 8 + l * 4, 1u);
      if (W1P == 2) W1B();
      lds_wait_ge(FL_TK + 8 + (l ^ 1) * 4, 1u);
      const unsigned ck = *(const volatile LAS unsigned*)(smem + L_CAND + (lane & 31) * 4);
      int rank = 0;
#pragma unroll
      for (int i = 0; i < 4; ++i) {
        const u32x4 o = *(const volatile LAS u32x4*)(smem + L_CAND + (lane >> 5) * 64 + i * 16);
        rank += (o.x > ck) + (o.y > ck) + (o.z > ck) + (o.w > ck);
      }
      { auto p = __builtin_amdgcn_permlane32_swap((unsigned)rank, (unsigned)rank, false, false); rank = (int)(p[0] + p[1]); }
      if (l == 0) {
        if (lane < 32 && ck != 0u && rank < 8) {
          const int t = 2047 - (int)(ck & 0xffffu);
          lds_wr32(L_SEL + rank * 4, ck);
          if ((t >> 7) == c) lds_wr32(L_MAP + ((t & 7) * 16 + ((t & 127) >> 3)) * 4, (unsigned)(L_BROW + rank * 256));
        }
        const unsigned any = __ballot(ck != 0u && rank < 8 && ((2047 - (int)(ck & 0xffffu)) >> 7) == c) ? 2u : 1u;
        flag(FL_SEL, any);
        if (bid == 0 && lane == 0) st64c(rws, W_EPOCH, (u32x2){tagA, tagB});
      }
    }
    if (W1P == 3) W1B();
    lds_wait_ge(FL_SEL, 1u);
    const unsigned mine = lds_rd32(L_SEL + (lane & 7) * 4);
    {
      {
        u32x4 xa[4];
#pragma unroll
        for (int u = 0; u < 4; ++u)
          asm volatile("buffer_load_dwordx4 %0, %1, %2, 0 offen offset:%3" : "=v"(xa[u]) : "v"((2047 - (mine & 0xffffu)) * 2048 + (kq * 256 + 32 * (fr >> 3) + 8 * fq) * 2), "s"(rx), "n"(u * 128) : "memory");
        TRN(w1[0], W2P == 10 ? 36 : 20) TRN(w1[1], W2P == 10 ? 36 : 20) TRN(w1[2], W2P == 10 ? 28 : 12) TRN(w1[3], 0)
        asm volatile("" : "+v"(xa[0]), "+v"(xa[1]), "+v"(xa[2]), "+v"(xa[3]));
        const bool val = (fq >> 1) == (fr >> 3), lo = lane < 32;
        auto publish = [&](f32x4 (&d)[8], unsigned o, unsigned sp) {
          u32x4 pv[4];
#pragma unroll
          for (int rr = 0; rr < 2; ++rr)
#pragma unroll
            for (int q = 0; q < 8; ++q) {
              auto p = __builtin_amdgcn_permlane32_swap(__float_as_uint(d[q][rr]), __float_as_uint(d[q][2 + rr]), false, false);
              const unsigned own = lo ? p[0] : p[1], oth = lo ? p[1] : p[0];
              pv[rr * 2 + (q >> 2)][q & 3] = min(__float_as_uint(__uint_as_float(own) + dpp1<0x128>(__uint_as_float(oth))), 0xfffffffeu);
            }
          asm volatile("s_nop 4\n\t"
                       "buffer_store_dwordx4 %0, %4, %5, 0 offen sc1\n\tbuffer_store_dwordx4 %1, %4, %5, %6 offen sc1\n\t"
                       "buffer_store_dwordx4 %2, %4, %5, %7 offen sc1\n\tbuffer_store_dwordx4 %3, %4, %5, %8 offen sc1\n\t"
                       "s_nop 7\n\ts_nop 7"
                       ::"v"(pv[0]), "v"(pv[1]), "v"(pv[2]), "v"(pv[3]), "v"(val && ffn ? o : 0xf0000000u), "s"(rws), "s"(sp), "s"(2 * sp), "s"(3 * sp) : "memory");
        };
        f32x4 d[8];
#pragma unroll
        for (int q = 0; q < 8; ++q) d[q] = (f32x4){0.f, 0.f, 0.f, 0.f};
#pragma unroll
        for (int u = 0; u < 4; ++u)
#pragma unroll
          for (int q = 0; q < 8; ++q) d[q] = __builtin_amdgcn_mfma_f32_16x16x32_bf16(__builtin_bit_cast(bf16x8, xa[u]), __builtin_bit_cast(bf16x8, w1[u][q]), d[q], 0, 0, 0);
        publish(d, gub + (16 * (fr >> 3) + 8 * (fq & 1) + (fr & 7)) * 16, 512);
        if (W2P < 11) { TRN(w2[0], 63) TRN(w2[1], 63) }
        lds_wait_ge<LZ>(FL_CFREE, XG2);
        __builtin_amdgcn_s_setprio(XP3);
        if (W2P == 11) LDW2(0, 16)
        if (ffn) {
          f32x4 G, U;
#pragma unroll 1
          for (int it = 0; it < SPIN_MAX; ++it) {
            FRESH();
            u32x4 a_[8];
#pragma unroll
            for (int k = 0; k < 8; ++k) a_[k] = __builtin_amdgcn_raw_buffer_load_b128(rws, W_GU + sg * 16384 + xw * 1024 + lane * 16, k * 2048, 16);
            unsigned m = 0;
#pragma unroll
            for (int k = 0; k < 8; ++k) m = max(max(m, max(a_[k].x, a_[k].y)), max(a_[k].z, a_[k].w));
            G = (__builtin_bit_cast(f32x4, a_[0]) + __builtin_bit_cast(f32x4, a_[2])) + (__builtin_bit_cast(f32x4, a_[4]) + __builtin_bit_cast(f32x4, a_[6]));
            U = (__builtin_bit_cast(f32x4, a_[1]) + __builtin_bit_cast(f32x4, a_[3])) + (__builtin_bit_cast(f32x4, a_[5]) + __builtin_bit_cast(f32x4, a_[7]));
            if (__all(m != ~0u)) break;
            __builtin_amdgcn_s_sleep(8);
          }
          if (W2P == 12) { lds_wait_ge<LZ>(FL_CFREE, W2G + W2S * (kq + 4 * xw) / 8); LDW2(0, 16) }
          float a4[4];
#pragma unroll
          for (int e = 0; e < 4; ++e) { const float g = rbf(G[e]); a4[e] = rbf(g * __builtin_amdgcn_rcpf(1.f + __expf(-g))) * rbf(U[e]); }
          asm volatile("s_nop 0\n\tds_write_b64 %0, %1" ::"v"(L_ACT + (4 * ((lane >> 3) & 1) + 2 * ((lane >> 4) & 1) + xw) * 128 + (lane & 7) * 16 + (lane >> 5) * 8),
                       "v"((u32x2){pk2(a4[0], a4[1]), pk2(a4[2], a4[3])}) : "memory");
          if (lane == 0) lds_wr32(FL_TK + xw * 4, 1u);
          lds_wait_ge(FL_TK + (xw ^ 1) * 4, 1u);
        }
        if (W2P >= 11) { TRN(w2[0], 8) TRN(w2[1], 0) }
        const u32x4 af = lds_rd128(L_ACT + ((fr & 7) * 8 + 4 * (fr >> 3) + fq) * 16);
#pragma unroll
        for (int u = 0; u < 2; ++u) {
#pragma unroll
          for (int q = 0; q < 8; ++q) d[q] = __builtin_amdgcn_mfma_f32_16x16x32_bf16(__builtin_bit_cast(bf16x8, af), __builtin_bit_cast(bf16x8, w2[u][q]), (f32x4){0.f, 0.f, 0.f, 0.f}, 0, 0, 0);
          publish(d, ypb + (u * 16 + 2 * (fr >> 3) + (fq & 1)) * 7168 + (fr & 7) * 16, 4 * 7168);
        }
      }
    }
    __builtin_amdgcn_s_setprio(XP4);
    unsigned rest = (unsigned)__ballot(((2047 - (int)(mine & 0xffffu)) >> 7) == c) & 0xffu;
    if (xw) rest &= rest - 1;
#pragma unroll 1
    for (; rest; rest &= rest - 1, rest &= rest - 1) {
      const int s = __builtin_ctz(rest);
      const unsigned ks = (unsigned)__builtin_amdgcn_readlane((int)mine, s);
      const int t = 2047 - (int)(ks & 0xffffu);
      const float rs = rbf(1.f / (1.f + __expf(-key_val(ks))));
      const unsigned yb = W_YP + (j * 16 + (s & 1) * 8 + 2 * ((s >> 1) & 1) + (s >> 2)) * 7168;
      const u32x2 xr = __builtin_amdgcn_raw_buffer_load_b64(rx, t * 2048 + (j * 64 + 8 * (lane & 7) + 4 * ((lane >> 3) & 1)) * 2, 0, 0);
      lds_wait_ge<LZ>(FL_CFREE, YGATE);
      const float omg = rbf(1.f - __uint_as_float(lds_rd32(L_GT + ((t & 7) * 16 + ((t & 127) >> 3)) * 4)));
      f32x4 y[2];
#pragma unroll 1
      for (int it = 0; it < SPIN_MAX; ++it) {
        FRESH();
        unsigned m = 0;
#pragma unroll
        for (int h = 0; h < 2; ++h) {
          y[h] = (f32x4){0.f, 0.f, 0.f, 0.f};
#pragma unroll
          for (int k = 0; k < 7; ++k) {
            const u32x4 a = __builtin_amdgcn_raw_buffer_load_b128(rws, yb + lane * 16, h * 4 * 7168 + k * 1024, 16);
            m = max(max(m, max(a.x, a.y)), max(a.z, a.w));
            y[h] += __builtin_bit_cast(f32x4, a);
          }
        }
        if (__all(m != ~0u)) break;
        __builtin_amdgcn_s_sleep(6);
      }
      const bool o8 = lane & 8;
      const f32x4 xf = unpk4(xr);
      f32x4 dl;
#pragma unroll
      for (int e = 0; e < 4; ++e) {
        const float v = sum16_32((o8 ? y[1][e] : y[0][e]) + dpp1<0x128>(o8 ? y[0][e] : y[1][e]));
        dl[e] = rbf(mul1(omg, rbf(xf[e] + rbf(mul1(rbf(v), rs))))) - rbf(mul1(omg, xf[e]));
      }
      if (lane < 16) asm volatile("s_nop 0\n\tds_write_b128 %0, %1\n\ts_nop 3" ::"v"(L_BROW + s * 256 + (lane & 7) * 32 + (lane >> 3) * 16), "v"(dl) : "memory");
    }
    if (lane == 0) lds_wr32(FL_XD + xw * 4, 1u);
    asm volatile("Lk_x:");
    return;
  }
  }

  __builtin_amdgcn_s_setprio(3);
  relane();
  u64x8 ma, mb;
  asm volatile("s_load_dwordx16 %0, %2, 0x0\n\ts_load_dwordx16 %1, %2, 0x40\n\ts_waitcnt lgkmcnt(0)" : "=&s"(ma), "=&s"(mb) : "s"(__builtin_amdgcn_kernarg_segment_ptr()), "s"(kw0), "s"(kw1));
  const char* out = (const char*)mb[7];
  char* ws = (char*)mb[6];
  const rsrc_t rws = mk(ws, W_END);
  if (!MTOP) MLOADS(ma, mb);
  f32x4 lr, li, cr, ci, Er, Ei, Fr, Fi, br_, bi_;
  u32x2 xe[2][4];
  {
    asm volatile("s_waitcnt vmcnt(%3)" : "+v"(p0), "+v"(p1), "+v"(p2) : "n"(9) : "memory");
    const f32x4 lre = unpk4(p0), lim = unpk4(p1), ld = unpk4(p2) * splat4(1.44269504f);
    f32x4 dt, id = fma4(lre, lre, lim * lim);
#pragma unroll
    for (int r = 0; r < 4; ++r) { dt[r] = rbf(__builtin_amdgcn_exp2f(ld[r])); id[r] = __builtin_amdgcn_rcpf(id[r]); }
    const f32x4 A2 = dt * lre * splat4(1.44269504f), Bv = dt * lim * splat4(0.159154943f);
    cexp4(A2, Bv, lr, li);
    const f32x4 nr = lr - splat4(1.f);
    cr = fma4(nr, lre, li * lim) * id; ci = fma4(li, lre, nml4(nr, lim)) * id;
    const f32x4 ne = splat4((float)(8 * fr)), nf = splat4((float)((fr & 7) + 1));
    cexp4(A2 * ne, Bv * ne, Er, Ei);
    f32x4 tr, ti;
    cexp4(A2 * nf, Bv * nf, tr, ti);
    Fr = fma4(cr, tr, nml4(ci, ti)); Fi = fma4(cr, ti, ci * tr);
    const float a1 = (fr & 2) ? ((fr & 1) ? A2.w : A2.z) : ((fr & 1) ? A2.y : A2.x), b1 = (fr & 2) ? ((fr & 1) ? Bv.w : Bv.z) : ((fr & 1) ? Bv.y : Bv.x);
    const int e0 = c - 1 - (fr >> 2);
    const f32x4 nl = {(float)(128 * max(e0, 0)), (float)(128 * max(e0 - 4, 0)), (float)(128 * max(e0 - 8, 0)), (float)(128 * max(e0 - 12, 0))};
    cexp4(splat4(a1) * nl, splat4(b1) * nl, br_, bi_);
    asm volatile("" : "+v"(lr), "+v"(li), "+v"(cr), "+v"(ci), "+v"(Er), "+v"(Ei), "+v"(Fr), "+v"(Fi), "+v"(br_), "+v"(bi_));
  }
  asm volatile("s_waitcnt vmcnt(%9)" : "+v"(dv), "+v"(xv[0][0]), "+v"(xv[0][1]), "+v"(xv[1][0]), "+v"(xv[1][1]), "+v"(wv[0]), "+v"(wv[1]), "+v"(gv[0]), "+v"(gv[1]) : "n"(0) : "memory");
  {
    float sv[4];
#pragma unroll
    for (int r = 0; r < 2; ++r) {
      float sr = 0.f, sg_ = 0.f;
#pragma unroll
      for (int h = 0; h < 2; ++h)
#pragma unroll
        for (int e = 0; e < 4; ++e) {
          const unsigned xx = xv[r][h][e], ww = wv[h][e], gw = gv[h][e];
          sr = __builtin_amdgcn_fdot2_f32_bf16(__builtin_bit_cast(bf16x2_t, xx), __builtin_bit_cast(bf16x2_t, ww), sr, false);
          sg_ = __builtin_amdgcn_fdot2_f32_bf16(__builtin_bit_cast(bf16x2_t, xx), __builtin_bit_cast(bf16x2_t, gw), sg_, false);
        }
      sv[r] = sr; sv[2 + r] = sg_;
    }
    const bool b1 = lane & 1, b2 = lane & 2;
    const float k0 = (b1 ? sv[1] : sv[0]) + dpp1<0xb1>(b1 ? sv[0] : sv[1]), k1 = (b1 ? sv[3] : sv[2]) + dpp1<0xb1>(b1 ? sv[2] : sv[3]);
    float k2 = (b2 ? k1 : k0) + dpp1<0x4e>(b2 ? k0 : k1);
    k2 += dpp1<0x124>(k2);
    k2 += dpp1<0x128>(k2);
    k2 = sum16_32(k2);
    const unsigned vb = b2 ? f2bf(1.f / (1.f + __expf(-rbf(k2)))) : route_key(rbf(k2), 0) >> 16;
    const unsigned p01 = vb | ((unsigned)__builtin_amdgcn_update_dpp(0, (int)vb, 0xb1, 0xf, 0xf, true) << 16);
    asm volatile("ds_write_b32 %0, %1\n\ts_waitcnt lgkmcnt(0)" ::"v"((lane & 61) == 0 ? (unsigned)(L_RT + (lane * 2 + wid) * 4) : (unsigned)(L_JNK + lane * 4)), "v"(p01) : "memory");
  }
  __builtin_amdgcn_s_setprio(2);
  unsigned tagA = L_TAG;
  f32x4 acc[2][8];
  const unsigned ago = fr < 4 ? W_AGG + ((j * 16 + c) * 64 + wid * 16 + 4 * fq + fr) * 16u : 0xf0000000u;
  {
#pragma unroll
    for (int m = 0; m < 2; ++m)
#pragma unroll
      for (int n = 0; n < 8; ++n) acc[m][n] = (f32x4){0.f, 0.f, 0.f, 0.f};
    const int x0 = (fq ^ (fr & 7)) * 16, x1 = ((4 + fq) ^ (fr & 7)) * 16;
    unsigned pa0 = 16384 + (wid * 32 + fr) * 128 + x0, pa1 = 16384 + (wid * 32 + fr) * 128 + x1, pb0 = fr * 128 + x0, pb1 = fr * 128 + x1;
    unsigned sit, xad[4], s1[3], fv1 = 0;
#pragma unroll
    for (int r = 0; r < 4; ++r) xad[r] = 98304 + (32 * wid + 4 * fq + r) * 128 + (((fr >> 1) ^ (4 * (fq & 1) + r)) * 16) + (fr & 1) * 8;
    bf16x8 fa[2][2], fb[8];
    asm volatile(ASM_GEMM1 : [c00] "+v"(acc[0][0]), [c01] "+v"(acc[0][1]), [c02] "+v"(acc[0][2]), [c03] "+v"(acc[0][3]), [c04] "+v"(acc[0][4]), [c05] "+v"(acc[0][5]), [c06] "+v"(acc[0][6]), [c07] "+v"(acc[0][7]), [c10] "+v"(acc[1][0]), [c11] "+v"(acc[1][1]), [c12] "+v"(acc[1][2]), [c13] "+v"(acc[1][3]), [c14] "+v"(acc[1][4]), [c15] "+v"(acc[1][5]), [c16] "+v"(acc[1][6]), [c17] "+v"(acc[1][7]), [a00] "=&v"(fa[0][0]), [a01] "=&v"(fa[0][1]), [a10] "=&v"(fa[1][0]), [a11] "=&v"(fa[1][1]), [b0] "=&v"(fb[0]), [b1] "=&v"(fb[1]), [b2] "=&v"(fb[2]), [b3] "=&v"(fb[3]), [b4] "=&v"(fb[4]), [b5] "=&v"(fb[5]), [b6] "=&v"(fb[6]), [b7] "=&v"(fb[7]), [pa0] "+v"(pa0), [pa1] "+v"(pa1), [pb0] "+v"(pb0), [pb1] "+v"(pb1), [it] "=&s"(sit), [tg] "+v"(tagA), [xe00] "=&v"(xe[0][0]), [xe01] "=&v"(xe[0][1]), [xe02] "=&v"(xe[0][2]), [xe03] "=&v"(xe[0][3]), [xe10] "=&v"(xe[1][0]), [xe11] "=&v"(xe[1][1]), [xe12] "=&v"(xe[1][2]), [xe13] "=&v"(xe[1][3]) : [xa0] "v"(xad[0]), [xa1] "v"(xad[1]), [xa2] "v"(xad[2]), [xa3] "v"(xad[3]) : "memory", "scc", "v252", "v253", "v254", "v255");
  }
  __builtin_amdgcn_s_setprio(3);
  const unsigned tagB = tagA + 0x40000000u;
  {
#pragma unroll
    for (int n = 1; n < 8; ++n) {
      const f32x4 pr = acc[0][n - 1], pi = acc[1][n - 1];
      acc[0][n] = fma4(lr, pr, fnm4(li, pi, acc[0][n]));
      acc[1][n] = fma4(lr, pi, fma4(li, pr, acc[1][n]));
    }
    const f32x4 wr = dpp4<0x140>(Er), wi = dpp4<0x140>(Ei);
    const f32x4 zr = fma4(wr, acc[0][7], nml4(wi, acc[1][7])), zi = fma4(wr, acc[1][7], wi * acc[0][7]);
    const bool o1 = fr & 1, o2 = fr & 2;
    const float r01 = (o1 ? zr.y : zr.x) + dpp1<0xb1>(o1 ? zr.x : zr.y), r23 = (o1 ? zr.w : zr.z) + dpp1<0xb1>(o1 ? zr.z : zr.w);
    const float i01 = (o1 ? zi.y : zi.x) + dpp1<0xb1>(o1 ? zi.x : zi.y), i23 = (o1 ? zi.w : zi.z) + dpp1<0xb1>(o1 ? zi.z : zi.w);
    float sr = (o2 ? r23 : r01) + dpp1<0x4e>(o2 ? r01 : r23), si = (o2 ? i23 : i01) + dpp1<0x4e>(o2 ? i01 : i23);
    sr += dpp1<0x124>(sr); si += dpp1<0x124>(si);
    sr += dpp1<0x128>(sr); si += dpp1<0x128>(si);
    u32x4 av = {__float_as_uint(sr), tagA, __float_as_uint(si), tagB};
    asm volatile("s_nop 1\n\tbuffer_store_dwordx4 %0, %1, %2, 0 offen sc1\n\ts_nop 7" : "+v"(av) : "v"(ago), "s"(rws) : "memory");
  }
  unsigned ao[4];
  u32x4 g[4];
#pragma unroll
  for (int i = 0; i < 4; ++i) {
    ao[i] = (fr >> 2) + 4 * i < c ? W_AGG + ((j * 16 + (fr >> 2) + 4 * i) * 64 + wid * 16 + 4 * fq + (fr & 3)) * 16u : 0xf0000000u;
    g[i] = (u32x4){0u, 0u, 0u, 0u};
  }
  if (POLLN < 0) {
    FRESH();
#pragma unroll
    for (int i = 0; i < 4; ++i)
      if (4 * i < c) g[i] = ld128c(rws, ao[i]);
    FRESH();
  }
  f32x4 Pr, Pi, qr[8], qi[8];
  {
    f32x4 yr = acc[0][7], yi = acc[1][7];
#define KS_STEP(S, CTRL) { const f32x4 mr = dpp4<0x150 + S>(Er), mi = dpp4<0x150 + S>(Ei); const f32x4 sr = dpp4<CTRL>(yr), si = dpp4<CTRL>(yi); \
      yr = fma4(mr, sr, fnm4(mi, si, yr)); yi = fma4(mr, si, fma4(mi, sr, yi)); }
    KS_STEP(1, 0x111) KS_STEP(2, 0x112) KS_STEP(4, 0x114) KS_STEP(8, 0x118)
    Pr = dpp4<0x111>(yr); Pi = dpp4<0x111>(yi);
    asm volatile("" : "+v"(Pr), "+v"(Pi));
#pragma unroll
    for (int n = 0; n < 8; ++n) {
      if (n == POLLN) {
        FRESH();
#pragma unroll
        for (int i = 0; i < 4; ++i)
          if (4 * i < c) g[i] = ld128c(rws, ao[i]);
        FRESH();
      }
      bcast8(n, Fr, qr[n]); bcast8(n, Fi, qi[n]);
      const f32x4 br = fma4(cr, acc[0][n], nml4(ci, acc[1][n])), bi = fnm4(cr, acc[1][n], nml4(ci, acc[0][n]));
      acc[0][n] = br; acc[1][n] = bi;
      asm volatile("" : "+v"(acc[0][n]), "+v"(acc[1][n]), "+v"(qr[n]), "+v"(qi[n]));
    }
  }
  f32x4 Xr = Pr, Xi = Pi;
  if (POLLN == 8) {
    FRESH();
#pragma unroll
    for (int i = 0; i < 4; ++i)
      if (4 * i < c) g[i] = ld128c(rws, ao[i]);
    FRESH();
  }
  u32x2 xd[2][4];
  lds_wait_ge(FL_G, 2u);
  {
    const f32x4 df = unpk4(dv) * splat4(0.5f);
#pragma unroll
    for (int m = 0; m < 2; ++m) {
      const f32x4 ge = __builtin_bit_cast(f32x4, lds_rd128(L_GT + ((2 * wid + m) * 16 + 4 * fq) * 4));
#pragma unroll
      for (int r = 0; r < 4; ++r) {
        const f32x4 xf = unpk4(xe[m][r]), p = xf * df, o = xf * splat4(rbf(1.f - ge[r]));
        xd[m][r] = (u32x2){pk2(p.x, p.y), pk2(p.z, p.w)};
        xe[m][r] = (u32x2){pk2(o.x, o.y), pk2(o.z, o.w)};
        asm volatile("" : "+v"(xd[m][r]), "+v"(xe[m][r]));
      }
    }
  }
  if (POLLN > 9) __builtin_amdgcn_s_sleep(POLLN > 9 ? POLLN - 9 : 0);
  if (c > 0) {
#pragma unroll 1
    for (int it = 0; it < SPIN_MAX; ++it) {
      unsigned bad = 0;
#pragma unroll
      for (int i = 0; i < 4; ++i) bad |= ((g[i].y ^ tagA) | (g[i].w ^ tagB)) & ~(unsigned)((int)ao[i] >> 31);
      if (!__ballot(bad != 0u)) break;
      FRESH();
#pragma unroll
      for (int i = 0; i < 4; ++i)
        if (4 * i < c) g[i] = ld128c(rws, ao[i]);
    }
    const f32x4 sx = {__uint_as_float(g[0].x), __uint_as_float(g[1].x), __uint_as_float(g[2].x), __uint_as_float(g[3].x)};
    const f32x4 sy = {__uint_as_float(g[0].z), __uint_as_float(g[1].z), __uint_as_float(g[2].z), __uint_as_float(g[3].z)};
    const f32x4 tx = fma4(br_, sx, nml4(bi_, sy)), ty = fma4(br_, sy, bi_ * sx);
    float gx = (tx.x + tx.z) + (tx.y + tx.w), gy = (ty.x + ty.z) + (ty.y + ty.w);
    gx += dpp1<0x124>(gx); gy += dpp1<0x124>(gy);
    gx += dpp1<0x128>(gx); gy += dpp1<0x128>(gy);
    const f32x4 Gr = {dpp1<0x00>(gx), dpp1<0x55>(gx), dpp1<0xaa>(gx), dpp1<0xff>(gx)}, Gi = {dpp1<0x00>(gy), dpp1<0x55>(gy), dpp1<0xaa>(gy), dpp1<0xff>(gy)};
    Xr = fma4(Er, Gr, fnm4(Ei, Gi, Pr)); Xi = fma4(Er, Gi, fma4(Ei, Gr, Pi));
  }
  relane();
  const unsigned jnk = L_JNK + lane * 4;
  const unsigned ho = WIF == 3 ? 0xf0000000u : W_H + c * 524288 + (4 * pown + wid) * 8192 + lane * 16;
  u32x4 hv[8];
  __builtin_amdgcn_sched_barrier(0);
#define HST(t_) asm volatile("s_nop 1\n\tbuffer_store_dwordx4 %0, %1, %2, %3 offen offset:%4 sc1" : "+v"(hv[t_]) : "v"(ho), "s"(rws), "s"(((t_) >> 2) * 4096), "n"(((t_) & 3) * 1024) : "memory")
#pragma unroll
  for (int n = 0; n < 8; ++n) {
    const f32x4 hr = fma4(qr[n], Xr, fnm4(qi[n], Xi, acc[0][n])), hi = fnm4(qr[n], Xi, fnm4(qi[n], Xr, acc[1][n]));
    const auto p = __builtin_amdgcn_permlane16_swap(pk2(hr.x, hr.y), pk2(hi.x, hi.y), false, false);
    const auto q = __builtin_amdgcn_permlane16_swap(pk2(hr.z, hr.w), pk2(hi.z, hi.w), false, false);
    hv[n] = (u32x4){NS ? min(p[0], 0xfffffffeu) : p[0], q[0], NS ? min(p[1], 0xfffffffeu) : p[1], q[1]};
    if (NSI == 8) HST(n); else if (NSI == 4 ? (n & 1) : n < NSI) HST(NSI == 4 ? n >> 1 : n);
    __builtin_amdgcn_sched_barrier(0);
    if (!DSL && (n >> 1) != wid) asm volatile("ds_write_b128 %0, %1" ::"v"(L_XH + (((n >> 1) * 3 + wid - (wid > (n >> 1) ? 1 : 0)) * 2 + (n & 1)) * 1024 + lane * 16), "v"(hv[n]) : "memory");
  }
  if (DSL) {
#pragma unroll
    for (int w = 0; w < 4; ++w)
      if (w != wid) asm volatile("ds_write_b128 %0, %1\n\tds_write_b128 %0, %2 offset:1024" ::"v"(L_XH + (w * 3 + wid - (wid > w ? 1 : 0)) * 2048 + lane * 16), "v"(hv[2 * w]), "v"(hv[2 * w + 1]) : "memory");
  }
  if (lane == 0) lds_wr32(FL_HS + wid * 4, 1u);
  const rsrc_t rh = mk(ws + W_H + c * 524288, 524288);
  const unsigned vo = wid * 2048 + lane * 16;
  u32x32 hw0, hw1;
  unsigned st[12], pc[4];
  f32x4 ac2[2][4];
#define ACK() do { asm volatile("s_waitcnt vmcnt(0)" : "+v"(hv[0]), "+v"(hv[1]), "+v"(hv[2]), "+v"(hv[3]), "+v"(hv[4]), "+v"(hv[5]), "+v"(hv[6]), "+v"(hv[7])::"memory"); \
    asm volatile("buffer_store_dwordx2 %0, %1, %2, 0 offen sc1" ::"v"((u32x2){tagA, tagB}), "v"(lane == 0 ? W_HF + (c * 16 + pown) * FSB + wid * FSW : 0xf0000000u), "s"(rws) : "memory"); \
    lds_wr32(lane == 0 ? (unsigned)(FL_FREE + wid * 4) : jnk, 9u); \
    if (NS) { if (RQD) __builtin_amdgcn_s_sleep(RQD); asm volatile(ASM_REQ : "=&{v[184:215]}"(hw0), "=&{v[216:247]}"(hw1), [hb0] "=&s"(st[0]), [hb1] "=&s"(st[1]), [hb2] "=&s"(st[2]), [hb3] "=&s"(st[3]) : [vo] "v"(vo), [own] "s"(pown), [srd] "s"(rh) : "memory", "scc"); } } while (0)
#pragma unroll
  for (int sg = 0; sg < 4; ++sg) pc[sg] = (fq & 1) * 8192 + fr * 128 + (((2 * sg + (fq >> 1)) ^ ((fr >> 1) & 7)) * 16);
  {
    lds_wait_ge(FL_C, 1u);
    u32x4 k[4][4], h[4][2];
#pragma unroll
    for (int i = 0; i < 4; ++i) {
      const int sw = i == 0 ? wid : i - (i <= wid ? 1 : 0);
      asm volatile("ds_read_b128 %0, %4\n\tds_read_b128 %1, %4 offset:2048\n\tds_read_b128 %2, %4 offset:4096\n\tds_read_b128 %3, %4 offset:6144"
                   : "=&v"(k[i][0]), "=&v"(k[i][1]), "=&v"(k[i][2]), "=&v"(k[i][3]) : "v"(pc[0] ^ (sw * 32)) : "memory");
    }
#pragma unroll
    for (int m = 0; m < 2; ++m)
#pragma unroll
      for (int r = 0; r < 4; ++r) {
        const f32x4 t = unpk4(xd[m][r]);
#pragma unroll
        for (int n = 0; n < 4; ++n) ac2[m][n][r] = t[n];
      }
    h[0][0] = hv[0]; h[0][1] = hv[1];
    if (wid == 1) { h[0][0] = hv[2]; h[0][1] = hv[3]; }
    if (wid == 2) { h[0][0] = hv[4]; h[0][1] = hv[5]; }
    if (wid == 3) { h[0][0] = hv[6]; h[0][1] = hv[7]; }
    asm volatile("" : "+v"(ac2[0][0]), "+v"(ac2[0][1]), "+v"(ac2[0][2]), "+v"(ac2[0][3]), "+v"(ac2[1][0]), "+v"(ac2[1][1]), "+v"(ac2[1][2]), "+v"(ac2[1][3]), "+v"(h[0][0]), "+v"(h[0][1]));
    if (NSI < 8) HST(NSI);
    if (MGP == 1) ACK();
    lds_wait_min4(FL_HS, 1u);
    asm volatile("ds_read_b128 %0, %6\n\tds_read_b128 %1, %6 offset:1024\n\tds_read_b128 %2, %6 offset:2048\n\tds_read_b128 %3, %6 offset:3072\n\tds_read_b128 %4, %6 offset:4096\n\tds_read_b128 %5, %6 offset:5120"
                 : "=&v"(h[1][0]), "=&v"(h[1][1]), "=&v"(h[2][0]), "=&v"(h[2][1]), "=&v"(h[3][0]), "=&v"(h[3][1]) : "v"(L_XH + wid * 6144 + lane * 16) : "memory");
    lds_wr32(lane == 0 ? (unsigned)(FL_CFREE + wid * 4) : jnk, 1u);
#pragma unroll
    for (int i = 0; i < 4; ++i) {
      if (i == 1) asm volatile("s_waitcnt lgkmcnt(0)" : "+v"(h[1][0]), "+v"(h[1][1]), "+v"(h[2][0]), "+v"(h[2][1]), "+v"(h[3][0]), "+v"(h[3][1])::"memory");
      asm volatile("" : "+v"(k[i][0]), "+v"(k[i][1]), "+v"(k[i][2]), "+v"(k[i][3]));
#pragma unroll
      for (int n = 0; n < 4; ++n) {
        ac2[0][n] = __builtin_amdgcn_mfma_f32_16x16x32_bf16(__builtin_bit_cast(bf16x8, h[i][0]), __builtin_bit_cast(bf16x8, k[i][n]), ac2[0][n], 0, 0, 0);
        ac2[1][n] = __builtin_amdgcn_mfma_f32_16x16x32_bf16(__builtin_bit_cast(bf16x8, h[i][1]), __builtin_bit_cast(bf16x8, k[i][n]), ac2[1][n], 0, 0, 0);
      }
      asm volatile("" : "+v"(ac2[0][0]), "+v"(ac2[0][1]), "+v"(ac2[0][2]), "+v"(ac2[0][3]), "+v"(ac2[1][0]), "+v"(ac2[1][1]), "+v"(ac2[1][2]), "+v"(ac2[1][3])::"memory");
      if (NSI + 1 + i < 8) HST(NSI + 1 + i < 8 ? NSI + 1 + i : 0);
    }
    static_assert(NSI >= 3 && (MGP == 0 || NSI >= 7), "all stores");
  }
  if (MGP == 0) ACK();
  f32x4 ov[2][4];
  float g2[2][4];
#pragma unroll
  for (int m = 0; m < 2; ++m) {
    const f32x4 ge = lds4(smem, L_GT + ((2 * wid + m) * 16 + 4 * fq) * 4);
#pragma unroll
    for (int r = 0; r < 4; ++r) {
      ov[m][r] = unpk4(xe[m][r]);
      g2[m][r] = ge[r] + ge[r];
      asm volatile("" : "+v"(ov[m][r]), "+v"(g2[m][r]));
    }
  }
  unsigned ra[2][4];
  lds_wait_ge(FL_SEL, 1u);
  const unsigned routed = __builtin_amdgcn_readfirstlane(lds_rd32(FL_SEL));
#pragma unroll
  for (int m = 0; m < 2; ++m) {
    const u32x4 me = lds_rd128(L_MAP + ((2 * wid + m) * 16 + 4 * fq) * 4);
#pragma unroll
    for (int r = 0; r < 4; ++r) { ra[m][r] = me[r] + fr * 16; asm volatile("" : "+v"(ra[m][r])); }
  }
  const rsrc_t ro = mk(out, 2048 * 1024 * 4);
  unsigned oo = (c * 128 + 32 * fq + 2 * wid) * 4096 + (j * 64 + 4 * fr) * 4;
  if (WIF == 1) oo = 0xf0000000u;
  if (WIF == 2 && (fq & 1)) oo = 0xf0000000u;
  asm volatile("" : "+v"(oo) : "s"(ro));
  if (MPW && wid == MPV) {
#pragma unroll 1
    for (int it = 0; it < SPIN_MAX; ++it) {
      u32x2 f = {0u, 0u};
      asm volatile("buffer_atomic_or_x2 %0, %1, %2, 0 offen sc0 sc1\n\ts_waitcnt vmcnt(0)" : "+v"(f) : "v"(W_HF + (c * 16 + upos(lane >> 2)) * FSB + (lane & 3) * FSW), "s"(rws) : "memory");
      const u64 m = ~__ballot(lane < 4 || (f.x == tagA && f.y == tagB));
      const unsigned n = m ? ((unsigned)__builtin_ctzll(m) >> 2) - 1u : 99u;
      asm volatile("ds_max_u32 %0, %1" ::"v"(lane == 0 ? (unsigned)(FL_C + 8) : jnk), "v"(n) : "memory");
      if (n >= MPW) break;
    }
  }
  __builtin_amdgcn_s_setprio(MPRIO2);

  {
    const unsigned fad = FL_C, fwd = lane == 0 ? (unsigned)(FL_CFREE + wid * 4) : jnk;
    bf16x8 kb[2][4];
    unsigned fv = 1;
#pragma unroll
    for (int sg = 0; sg < 4; ++sg) pc[sg] += 16384;
    asm volatile(ASM_GEMM2 : [c00] "+v"(ac2[0][0]), [c01] "+v"(ac2[0][1]), [c02] "+v"(ac2[0][2]), [c03] "+v"(ac2[0][3]), [c10] "+v"(ac2[1][0]), [c11] "+v"(ac2[1][1]), [c12] "+v"(ac2[1][2]), [c13] "+v"(ac2[1][3]), "=&{v[184:215]}"(hw0), "=&{v[216:247]}"(hw1), [k00] "=&v"(kb[0][0]), [k01] "=&v"(kb[0][1]), [k02] "=&v"(kb[0][2]), [k03] "=&v"(kb[0][3]), [k10] "=&v"(kb[1][0]), [k11] "=&v"(kb[1][1]), [k12] "=&v"(kb[1][2]), [k13] "=&v"(kb[1][3]), [pc0] "+v"(pc[0]), [pc1] "+v"(pc[1]), [pc2] "+v"(pc[2]), [pc3] "+v"(pc[3]), [fv] "+v"(fv), [s] "=&s"(st[0]), [s2] "=&s"(st[1]), [to] "=&s"(st[2]), [hn] "=&s"(st[3]), [cn] "=&s"(st[4]), [dl] "=&s"(st[5]), [nf] "=&s"(st[6]), [hr] "=&s"(st[7]), [hb0] "=&s"(st[8]), [hb1] "=&s"(st[9]), [hb2] "=&s"(st[10]), [hb3] "=&s"(st[11]) : [vo] "v"(vo), [own] "s"(pown), [srd] "s"(rh), [fa] "v"(fad), [fw] "v"(fwd), [d16] "s"(0x4000), [wr] "s"(0x500) : "memory", "scc", "vcc", "v250", "v251", "v252", "v253", "v254", "v255");
  }
  if (routed > 1u) {
#pragma unroll 1
    for (int it = 0; it < SPIN_MAX * 4; ++it) {
      u32x2 f;
      f32x4 d[8];
      asm volatile("ds_read_b64 %8, %17\n\tds_read_b128 %0, %9\n\tds_read_b128 %1, %10\n\tds_read_b128 %2, %11\n\tds_read_b128 %3, %12\n\t"
                   "ds_read_b128 %4, %13\n\tds_read_b128 %5, %14\n\tds_read_b128 %6, %15\n\tds_read_b128 %7, %16\n\ts_waitcnt lgkmcnt(0)"
                   : "=&v"(d[0]), "=&v"(d[1]), "=&v"(d[2]), "=&v"(d[3]), "=&v"(d[4]), "=&v"(d[5]), "=&v"(d[6]), "=&v"(d[7]), "=&v"(f)
                   : "v"(ra[0][0]), "v"(ra[0][1]), "v"(ra[0][2]), "v"(ra[0][3]), "v"(ra[1][0]), "v"(ra[1][1]), "v"(ra[1][2]), "v"(ra[1][3]), "v"(FL_XD) : "memory");
      if (__builtin_amdgcn_readfirstlane(f.x & f.y)) {
#pragma unroll
        for (int e = 0; e < 8; ++e) ov[e >> 2][e & 3] += d[e];
        break;
      }
    }
  }
#pragma unroll
  for (int r = 0; r < 4; ++r)
#pragma unroll
    for (int m = 0; m < 2; ++m) {
#pragma unroll
      for (int n = 0; n < 4; ++n) ov[m][r][n] = fma1(g2[m][r], ac2[m][n][r], ov[m][r][n]);
      asm volatile("s_nop 1\n\tbuffer_store_dwordx4 %0, %1, %2, %3 offen" OST : "+v"(ov[m][r]) : "v"(oo), "s"(ro), "s"(r * 32768 + m * 4096) : "memory");
      __builtin_amdgcn_sched_barrier(0);
    }
  asm volatile("s_nop 7\n\ts_nop 7" ::"v"(ov[0][0]), "v"(ov[0][1]), "v"(ov[0][2]), "v"(ov[0][3]), "v"(ov[1][0]), "v"(ov[1][1]), "v"(ov[1][2]), "v"(ov[1][3]));
  asm volatile("Lk_m:");
#endif
}

extern "C" void kernel_launch(void* const* d_in, const int* in_sizes, int n_in, void* d_out, int out_size, void* d_ws, size_t ws_size,
                              hipStream_t stream) {
  (void)hipFuncSetAttribute((const void*)fused, hipFuncAttributeMaxDynamicSharedMemorySize, LDS_BYTES);
  fused<<<256, 512, LDS_BYTES, stream>>>((const u16*)d_in[0], (const u16*)d_in[1], (const u16*)d_in[2], (const u16*)d_in[3], (const u16*)d_in[4],
                                         (const u16*)d_in[5], (const u16*)d_in[6], (const u16*)d_in[7], (const u16*)d_in[8], (const u16*)d_in[10],
                                         (const u16*)d_in[11], (const u16*)d_in[12], (const u16*)d_in[13], (const u16*)d_in[14], (char*)d_ws + WS_BASE(ws_size),
                                         (float*)d_out);
}
```

```cpp
#include <hip/hip_runtime.h>
#include <hip/hip_bf16.h>
#include <stdint.h>

typedef __attribute__((ext_vector_type(8))) short bf16x8;
typedef __attribute__((ext_vector_type(4))) float f32x4;
typedef __attribute__((ext_vector_type(2))) float f32x2;
typedef __attribute__((ext_vector_type(4))) unsigned u32x4;
typedef __attribute__((ext_vector_type(2))) unsigned u32x2;
typedef __attribute__((ext_vector_type(32))) unsigned u32x32;
typedef unsigned short u16;
typedef unsigned long long u64;
typedef __attribute__((ext_vector_type(8))) unsigned long long u64x8;
#define LAS __attribute__((address_space(3)))
#define DEV __device__ __forceinline__

#define ASM_GEMM1 \
  "s_mov_b32 %[it], 0\n\t" \
  "s_barrier\n\t" \
  "ds_read_b128 %[a00], %[pa0] offset:0\n\t" \
  "ds_read_b128 %[a01], %[pa0] offset:2048\n\t" \
  "ds_read_b128 %[b0], %[pb0] offset:0\n\t" \
  "ds_read_b128 %[b1], %[pb0] offset:2048\n\t" \
  "ds_read_b128 %[b2], %[pb0] offset:4096\n\t" \
  "ds_read_b128 %[b3], %[pb0] offset:6144\n\t" \
  "ds_read_b128 %[b4], %[pb0] offset:8192\n\t" \
  "ds_read_b128 %[b5], %[pb0] offset:10240\n\t" \
  "ds_read_b128 %[b6], %[pb0] offset:12288\n\t" \
  "ds_read_b128 %[b7], %[pb0] offset:14336\n\t" \
  "Lg1_%=:\n\t" \
  "ds_read_b128 %[a10], %[pa1] offset:0\n\t" \
  "ds_read_b128 %[a11], %[pa1] offset:2048\n\t" \
  "s_waitcnt lgkmcnt(8)\n\t" \
  "v_mfma_f32_16x16x32_bf16 %[c00], %[a00], %[b0], %[c00]\n\t" \
  "v_mfma_f32_16x16x32_bf16 %[c10], %[a01], %[b0], %[c10]\n\t" \
  "ds_read_b128 %[b0], %[pb1] offset:0\n\t" \
  "v_mfma_f32_16x16x32_bf16 %[c01], %[a00], %[b1], %[c01]\n\t" \
  "v_mfma_f32_16x16x32_bf16 %[c11], %[a01], %[b1], %[c11]\n\t" \
  "ds_read_b128 %[b1], %[pb1] offset:2048\n\t" \
  "s_waitcnt lgkmcnt(8)\n\t" \
  "v_mfma_f32_16x16x32_bf16 %[c02], %[a00], %[b2], %[c02]\n\t" \
  "v_mfma_f32_16x16x32_bf16 %[c12], %[a01], %[b2], %[c12]\n\t" \
  "ds_read_b128 %[b2], %[pb1] offset:4096\n\t" \
  "v_mfma_f32_16x16x32_bf16 %[c03], %[a00], %[b3], %[c03]\n\t" \
  "v_mfma_f32_16x16x32_bf16 %[c13], %[a01], %[b3], %[c13]\n\t" \
  "ds_read_b128 %[b3], %[pb1] offset:6144\n\t" \
  "s_waitcnt lgkmcnt(8)\n\t" \
  "v_mfma_f32_16x16x32_bf16 %[c04], %[a00], %[b4], %[c04]\n\t" \
  "v_mfma_f32_16x16x32_bf16 %[c14], %[a01], %[b4], %[c14]\n\t" \
  "ds_read_b128 %[b4], %[pb1] offset:8192\n\t" \
  "v_mfma_f32_16x16x32_bf16 %[c05], %[a00], %[b5], %[c05]\n\t" \
  "v_mfma_f32_16x16x32_bf16 %[c15], %[a01], %[b5], %[c15]\n\t" \
  "ds_read_b128 %[b5], %[pb1] offset:10240\n\t" \
  "s_waitcnt lgkmcnt(8)\n\t" \
  "v_mfma_f32_16x16x32_bf16 %[c06], %[a00], %[b6], %[c06]\n\t" \
  "v_mfma_f32_16x16x32_bf16 %[c16], %[a01], %[b6], %[c16]\n\t" \
  "ds_read_b128 %[b6], %[pb1] offset:12288\n\t" \
  "v_mfma_f32_16x16x32_bf16 %[c07], %[a00], %[b7], %[c07]\n\t" \
  "v_mfma_f32_16x16x32_bf16 %[c17], %[a01], %[b7], %[c17]\n\t" \
  "ds_read_b128 %[b7], %[pb1] offset:14336\n\t" \
  "s_waitcnt lgkmcnt(6)\n\t" \
  "v_mfma_f32_16x16x32_bf16 %[c00], %[a10], %[b0], %[c00]\n\t" \
  "v_mfma_f32_16x16x32_bf16 %[c10], %[a11], %[b0], %[c10]\n\t" \
  "v_mfma_f32_16x16x32_bf16 %[c01], %[a10], %[b1], %[c01]\n\t" \
  "v_mfma_f32_16x16x32_bf16 %[c11], %[a11], %[b1], %[c11]\n\t" \
  "s_waitcnt lgkmcnt(4)\n\t" \
  "v_mfma_f32_16x16x32_bf16 %[c02], %[a10], %[b2], %[c02]\n\t" \
  "v_mfma_f32_16x16x32_bf16 %[c12], %[a11], %[b2], %[c12]\n\t" \
  "s_waitcnt lgkmcnt(0)\n\t" \
  "s_barrier\n\t" \
  "ds_read_b128 %[a00], %[pa0] offset:32768\n\t" \
  "ds_read_b128 %[a01], %[pa0] offset:34816\n\t" \
  "v_mfma_f32_16x16x32_bf16 %[c03], %[a10], %[b3], %[c03]\n\t" \
  "v_mfma_f32_16x16x32_bf16 %[c13], %[a11], %[b3], %[c13]\n\t" \
  "ds_read_b128 %[b0], %[pb0] offset:32768\n\t" \
  "ds_read_b128 %[b1], %[pb0] offset:34816\n\t" \
  "v_mfma_f32_16x16x32_bf16 %[c04], %[a10], %[b4], %[c04]\n\t" \
  "v_mfma_f32_16x16x32_bf16 %[c14], %[a11], %[b4], %[c14]\n\t" \
  "ds_read_b128 %[b2], %[pb0] offset:36864\n\t" \
  "ds_read_b128 %[b3], %[pb0] offset:38912\n\t" \
  "v_mfma_f32_16x16x32_bf16 %[c05], %[a10], %[b5], %[c05]\n\t" \
  "v_mfma_f32_16x16x32_bf16 %[c15], %[a11], %[b5], %[c15]\n\t" \
  "ds_read_b128 %[b4], %[pb0] offset:40960\n\t" \
  "ds_read_b128 %[b5], %[pb0] offset:43008\n\t" \
  "v_mfma_f32_16x16x32_bf16 %[c06], %[a10], %[b6], %[c06]\n\t" \
  "v_mfma_f32_16x16x32_bf16 %[c16], %[a11], %[b6], %[c16]\n\t" \
  "ds_read_b128 %[b6], %[pb0] offset:45056\n\t" \
  "v_mfma_f32_16x16x32_bf16 %[c07], %[a10], %[b7], %[c07]\n\t" \
  "v_mfma_f32_16x16x32_bf16 %[c17], %[a11], %[b7], %[c17]\n\t" \
  "ds_read_b128 %[b7], %[pb0] offset:47104\n\t" \
  "ds_read_b128 %[a10], %[pa1] offset:32768\n\t" \
  "ds_read_b128 %[a11], %[pa1] offset:34816\n\t" \
  "s_waitcnt lgkmcnt(8)\n\t" \
  "v_mfma_f32_16x16x32_bf16 %[c00], %[a00], %[b0], %[c00]\n\t" \
  "v_mfma_f32_16x16x32_bf16 %[c10], %[a01], %[b0], %[c10]\n\t" \
  "ds_read_b128 %[b0], %[pb1] offset:32768\n\t" \
  "v_mfma_f32_16x16x32_bf16 %[c01], %[a00], %[b1], %[c01]\n\t" \
  "v_mfma_f32_16x16x32_bf16 %[c11], %[a01], %[b1], %[c11]\n\t" \
  "ds_read_b128 %[b1], %[pb1] offset:34816\n\t" \
  "s_waitcnt lgkmcnt(8)\n\t" \
  "v_mfma_f32_16x16x32_bf16 %[c02], %[a00], %[b2], %[c02]\n\t" \
  "v_mfma_f32_16x16x32_bf16 %[c12], %[a01], %[b2], %[c12]\n\t" \
  "ds_read_b128 %[b2], %[pb1] offset:36864\n\t" \
  "v_mfma_f32_16x16x32_bf16 %[c03], %[a00], %[b3], %[c03]\n\t" \
  "v_mfma_f32_16x16x32_bf16 %[c13], %[a01], %[b3], %[c13]\n\t" \
  "ds_read_b128 %[b3], %[pb1] offset:38912\n\t" \
  "v_xor_b32 %[pa0], 0x10000, %[pa0]\n\t" \
  "s_waitcnt lgkmcnt(8)\n\t" \
  "v_mfma_f32_16x16x32_bf16 %[c04], %[a00], %[b4], %[c04]\n\t" \
  "v_mfma_f32_16x16x32_bf16 %[c14], %[a01], %[b4], %[c14]\n\t" \
  "ds_read_b128 %[b4], %[pb1] offset:40960\n\t" \
  "v_xor_b32 %[pb0], 0x10000, %[pb0]\n\t" \
  "v_mfma_f32_16x16x32_bf16 %[c05], %[a00], %[b5], %[c05]\n\t" \
  "v_mfma_f32_16x16x32_bf16 %[c15], %[a01], %[b5], %[c15]\n\t" \
  "ds_read_b128 %[b5], %[pb1] offset:43008\n\t" \
  "s_waitcnt lgkmcnt(8)\n\t" \
  "v_mfma_f32_16x16x32_bf16 %[c06], %[a00], %[b6], %[c06]\n\t" \
  "v_mfma_f32_16x16x32_bf16 %[c16], %[a01], %[b6], %[c16]\n\t" \
  "ds_read_b128 %[b6], %[pb1] offset:45056\n\t" \
  "s_cmp_eq_u32 %[it], 7\n\t" \
  "v_mfma_f32_16x16x32_bf16 %[c07], %[a00], %[b7], %[c07]\n\t" \
  "v_mfma_f32_16x16x32_bf16 %[c17], %[a01], %[b7], %[c17]\n\t" \
  "ds_read_b128 %[b7], %[pb1] offset:47104\n\t" \
  "s_cbranch_scc1 Lxe_%=\n\t" \
  "Lxb_%=:\n\t" \
  "s_waitcnt lgkmcnt(6)\n\t" \
  "v_mfma_f32_16x16x32_bf16 %[c00], %[a10], %[b0], %[c00]\n\t" \
  "v_mfma_f32_16x16x32_bf16 %[c10], %[a11], %[b0], %[c10]\n\t" \
  "v_mfma_f32_16x16x32_bf16 %[c01], %[a10], %[b1], %[c01]\n\t" \
  "v_mfma_f32_16x16x32_bf16 %[c11], %[a11], %[b1], %[c11]\n\t" \
  "v_xor_b32 %[pa1], 0x10000, %[pa1]\n\t" \
  "s_waitcnt lgkmcnt(4)\n\t" \
  "v_mfma_f32_16x16x32_bf16 %[c02], %[a10], %[b2], %[c02]\n\t" \
  "v_mfma_f32_16x16x32_bf16 %[c12], %[a11], %[b2], %[c12]\n\t" \
  "v_xor_b32 %[pb1], 0x10000, %[pb1]\n\t" \
  "s_waitcnt lgkmcnt(0)\n\t" \
  "s_barrier\n\t" \
  "s_cbranch_scc1 Lt1_%=\n\t" \
  "ds_read_b128 %[a00], %[pa0] offset:0\n\t" \
  "ds_read_b128 %[a01], %[pa0] offset:2048\n\t" \
  "v_mfma_f32_16x16x32_bf16 %[c03], %[a10], %[b3], %[c03]\n\t" \
  "v_mfma_f32_16x16x32_bf16 %[c13], %[a11], %[b3], %[c13]\n\t" \
  "ds_read_b128 %[b0], %[pb0] offset:0\n\t" \
  "ds_read_b128 %[b1], %[pb0] offset:2048\n\t" \
  "v_mfma_f32_16x16x32_bf16 %[c04], %[a10], %[b4], %[c04]\n\t" \
  "v_mfma_f32_16x16x32_bf16 %[c14], %[a11], %[b4], %[c14]\n\t" \
  "ds_read_b128 %[b2], %[pb0] offset:4096\n\t" \
  "ds_read_b128 %[b3], %[pb0] offset:6144\n\t" \
  "v_mfma_f32_16x16x32_bf16 %[c05], %[a10], %[b5], %[c05]\n\t" \
  "v_mfma_f32_16x16x32_bf16 %[c15], %[a11], %[b5], %[c15]\n\t" \
  "ds_read_b128 %[b4], %[pb0] offset:8192\n\t" \
  "ds_read_b128 %[b5], %[pb0] offset:10240\n\t" \
  "v_mfma_f32_16x16x32_bf16 %[c06], %[a10], %[b6], %[c06]\n\t" \
  "v_mfma_f32_16x16x32_bf16 %[c16], %[a11], %[b6], %[c16]\n\t" \
  "ds_read_b128 %[b6], %[pb0] offset:12288\n\t" \
  "s_add_u32 %[it], %[it], 1\n\t" \
  "v_mfma_f32_16x16x32_bf16 %[c07], %[a10], %[b7], %[c07]\n\t" \
  "v_mfma_f32_16x16x32_bf16 %[c17], %[a11], %[b7], %[c17]\n\t" \
  "ds_read_b128 %[b7], %[pb0] offset:14336\n\t" \
  "s_branch Lg1_%=\n\t" \
  "Lxe_%=:\n\t" \
  "ds_read_b64 %[xe00], %[xa0] offset:0\n\t" \
  "ds_read_b64 %[xe01], %[xa1] offset:0\n\t" \
  "ds_read_b64 %[xe02], %[xa2] offset:0\n\t" \
  "ds_read_b64 %[xe03], %[xa3] offset:0\n\t" \
  "ds_read_b64 %[xe10], %[xa0] offset:2048\n\t" \
  "ds_read_b64 %[xe11], %[xa1] offset:2048\n\t" \
  "ds_read_b64 %[xe12], %[xa2] offset:2048\n\t" \
  "ds_read_b64 %[xe13], %[xa3] offset:2048\n\t" \
  "ds_read_b32 %[tg], %[tg]\n\t" \
  "s_branch Lxb_%=\n\t" \
  "Lt1_%=:\n\t" \
  "v_mfma_f32_16x16x32_bf16 %[c03], %[a10], %[b3], %[c03]\n\t" \
  "v_mfma_f32_16x16x32_bf16 %[c13], %[a11], %[b3], %[c13]\n\t" \
  "v_mfma_f32_16x16x32_bf16 %[c04], %[a10], %[b4], %[c04]\n\t" \
  "v_mfma_f32_16x16x32_bf16 %[c14], %[a11], %[b4], %[c14]\n\t" \
  "v_mfma_f32_16x16x32_bf16 %[c05], %[a10], %[b5], %[c05]\n\t" \
  "v_mfma_f32_16x16x32_bf16 %[c15], %[a11], %[b5], %[c15]\n\t" \
  "v_mfma_f32_16x16x32_bf16 %[c06], %[a10], %[b6], %[c06]\n\t" \
  "v_mfma_f32_16x16x32_bf16 %[c16], %[a11], %[b6], %[c16]\n\t" \
  "v_mfma_f32_16x16x32_bf16 %[c07], %[a10], %[b7], %[c07]\n\t" \
  "v_mfma_f32_16x16x32_bf16 %[c17], %[a11], %[b7], %[c17]\n\t" \
  "s_nop 7\n\t"
#define ASM_GEMM2 \
  "s_nop 4\n\t" \
  "s_mov_b32 %[to], 0x40000\n\t" \
  "s_mov_b32 %[cn], 1\n\t" \
  "Lc0_%=:\n\t" \
  "ds_read_b128 v[252:255], %[fa]\n\t" \
  "s_cmp_eq_u32 %[to], 0\n\t" \
  "s_cbranch_scc1 Lc0_%=x\n\t" \
  "s_sub_u32 %[to], %[to], 1\n\t" \
  "s_waitcnt lgkmcnt(0)\n\t" \
  "v_readfirstlane_b32 %[s], v253\n\t" \
  "s_cmp_ge_u32 %[s], 2\n\t" \
  "s_cbranch_scc0 Lc0_%=\n\t" \
  "Lc0_%=x:\n\t" \
  "ds_read_b128 %[k00], %[pc0] offset:0\n\t" \
  "ds_read_b128 %[k01], %[pc0] offset:2048\n\t" \
  "ds_read_b128 %[k02], %[pc0] offset:4096\n\t" \
  "ds_read_b128 %[k03], %[pc0] offset:6144\n\t" \
  "ds_read_b128 %[k10], %[pc1] offset:0\n\t" \
  "ds_read_b128 %[k11], %[pc1] offset:2048\n\t" \
  "ds_read_b128 %[k12], %[pc1] offset:4096\n\t" \
  "ds_read_b128 %[k13], %[pc1] offset:6144\n\t" \
  "s_mov_b32 %[hn], 3\n\t" \
  "s_cmp_gt_u32 1, %[own]\n\t" \
  "s_addc_u32 %[hb0], 1, -1\n\t" \
  "s_lshl_b32 %[hb0], %[hb0], 15\n\t" \
  "s_or_b32 %[hb1], %[hb0], 0x2000\n\t" \
  "s_or_b32 %[hb2], %[hb0], 0x4000\n\t" \
  "s_or_b32 %[hb3], %[hb0], 0x6000\n\t" \
  "Lh1_%=:\n\t" \
  "ds_read_b128 v[252:255], %[fa]\n\t" \
  "s_cmp_eq_u32 %[to], 0\n\t" \
  "s_cbranch_scc1 Lh1_%=x\n\t" \
  "s_sub_u32 %[to], %[to], 1\n\t" \
  "s_waitcnt lgkmcnt(0)\n\t" \
  "v_readfirstlane_b32 %[s], v254\n\t" \
  "s_cmp_ge_u32 %[s], 1\n\t" \
  "s_cbranch_scc0 Lh1_%=\n\t" \
  "Lh1_%=x:\n\t" \
  "buffer_load_dwordx4 v[216:219], %[vo], %[srd], %[hb0] offen offset:0 sc1\n\t" \
  "buffer_load_dwordx4 v[220:223], %[vo], %[srd], %[hb0] offen offset:1024 sc1\n\t" \
  "buffer_load_dwordx4 v[224:227], %[vo], %[srd], %[hb1] offen offset:0 sc1\n\t" \
  "buffer_load_dwordx4 v[228:231], %[vo], %[srd], %[hb1] offen offset:1024 sc1\n\t" \
  "buffer_load_dwordx4 v[232:235], %[vo], %[srd], %[hb2] offen offset:0 sc1\n\t" \
  "buffer_load_dwordx4 v[236:239], %[vo], %[srd], %[hb2] offen offset:1024 sc1\n\t" \
  "buffer_load_dwordx4 v[240:243], %[vo], %[srd], %[hb3] offen offset:0 sc1\n\t" \
  "buffer_load_dwordx4 v[244:247], %[vo], %[srd], %[hb3] offen offset:1024 sc1\n\t" \
  "s_cmp_gt_u32 2, %[own]\n\t" \
  "s_addc_u32 %[hb0], 2, -1\n\t" \
  "s_lshl_b32 %[hb0], %[hb0], 15\n\t" \
  "s_or_b32 %[hb1], %[hb0], 0x2000\n\t" \
  "s_or_b32 %[hb2], %[hb0], 0x4000\n\t" \
  "s_or_b32 %[hb3], %[hb0], 0x6000\n\t" \
  "s_cmp_ge_u32 %[s], 2\n\t" \
  "s_cbranch_scc1 Lh2_%=x\n\t" \
  "Lh2_%=:\n\t" \
  "ds_read_b128 v[252:255], %[fa]\n\t" \
  "s_cmp_eq_u32 %[to], 0\n\t" \
  "s_cbranch_scc1 Lh2_%=x\n\t" \
  "s_sub_u32 %[to], %[to], 1\n\t" \
  "s_waitcnt lgkmcnt(0)\n\t" \
  "v_readfirstlane_b32 %[s], v254\n\t" \
  "s_cmp_ge_u32 %[s], 2\n\t" \
  "s_cbranch_scc0 Lh2_%=\n\t" \
  "Lh2_%=x:\n\t" \
  "buffer_load_dwordx4 v[184:187], %[vo], %[srd], %[hb0] offen offset:0 sc1\n\t" \
  "buffer_load_dwordx4 v[188:191], %[vo], %[srd], %[hb0] offen offset:1024 sc1\n\t" \
  "buffer_load_dwordx4 v[192:195], %[vo], %[srd], %[hb1] offen offset:0 sc1\n\t" \
  "buffer_load_dwordx4 v[196:199], %[vo], %[srd], %[hb1] offen offset:1024 sc1\n\t" \
  "buffer_load_dwordx4 v[200:203], %[vo], %[srd], %[hb2] offen offset:0 sc1\n\t" \
  "buffer_load_dwordx4 v[204:207], %[vo], %[srd], %[hb2] offen offset:1024 sc1\n\t" \
  "buffer_load_dwordx4 v[208:211], %[vo], %[srd], %[hb3] offen offset:0 sc1\n\t" \
  "buffer_load_dwordx4 v[212:215], %[vo], %[srd], %[hb3] offen offset:1024 sc1\n\t" \
  "s_cmp_ge_u32 %[s], 3\n\t" \
  "s_cbranch_scc1 Lh3_%=x\n\t" \
  "Lh3_%=:\n\t" \
  "ds_read_b128 v[252:255], %[fa]\n\t" \
  "s_cmp_eq_u32 %[to], 0\n\t" \
  "s_cbranch_scc1 Lh3_%=x\n\t" \
  "s_sub_u32 %[to], %[to], 1\n\t" \
  "s_waitcnt lgkmcnt(0)\n\t" \
  "v_readfirstlane_b32 %[s], v254\n\t" \
  "s_cmp_ge_u32 %[s], 3\n\t" \
  "s_cbranch_scc0 Lh3_%=\n\t" \
  "Lh3_%=x:\n\t" \
  "s_branch Lin_%=\n\t" \
  "Lg2_%=:\n\t" \
  "s_waitcnt vmcnt(14) lgkmcnt(7)\n\t" \
  "ds_read_b128 v[252:255], %[fa]\n\t" \
  "v_mfma_f32_16x16x32_bf16 %[c00], v[184:187], %[k00], %[c00]\n\t" \
  "s_cmp_gt_u32 %[hn], %[own]\n\t" \
  "s_addc_u32 %[hb0], %[hn], -1\n\t" \
  "v_mfma_f32_16x16x32_bf16 %[c10], v[188:191], %[k00], %[c10]\n\t" \
  "ds_read_b128 %[k00], %[pc2] offset:0\n\t" \
  "s_waitcnt lgkmcnt(8)\n\t" \
  "v_mfma_f32_16x16x32_bf16 %[c01], v[184:187], %[k01], %[c01]\n\t" \
  "s_lshl_b32 %[hb0], %[hb0], 15\n\t" \
  "v_mfma_f32_16x16x32_bf16 %[c11], v[188:191], %[k01], %[c11]\n\t" \
  "ds_read_b128 %[k01], %[pc2] offset:2048\n\t" \
  "s_waitcnt lgkmcnt(8)\n\t" \
  "v_mfma_f32_16x16x32_bf16 %[c02], v[184:187], %[k02], %[c02]\n\t" \
  "s_or_b32 %[hb1], %[hb0], 0x2000\n\t" \
  "v_mfma_f32_16x16x32_bf16 %[c12], v[188:191], %[k02], %[c12]\n\t" \
  "ds_read_b128 %[k02], %[pc2] offset:4096\n\t" \
  "s_waitcnt lgkmcnt(8)\n\t" \
  "v_mfma_f32_16x16x32_bf16 %[c03], v[184:187], %[k03], %[c03]\n\t" \
  "s_add_u32 %[cn], %[cn], 1\n\t" \
  "v_mfma_f32_16x16x32_bf16 %[c13], v[188:191], %[k03], %[c13]\n\t" \
  "ds_read_b128 %[k03], %[pc2] offset:6144\n\t" \
  "buffer_load_dwordx4 v[184:187], %[vo], %[srd], %[hb0] offen offset:0 sc1\n\t" \
  "s_waitcnt vmcnt(13) lgkmcnt(8)\n\t" \
  "v_mfma_f32_16x16x32_bf16 %[c00], v[192:195], %[k10], %[c00]\n\t" \
  "s_cmp_eq_u32 %[cn], 9\n\t" \
  "s_cselect_b32 %[dl], 0x20000, %[d16]\n\t" \
  "v_mfma_f32_16x16x32_bf16 %[c10], v[196:199], %[k10], %[c10]\n\t" \
  "s_bitcmp1_b32 %[wr], %[cn]\n\t" \
  "s_cselect_b32 %[dl], 0xfffe4000, %[dl]\n\t" \
  "ds_read_b128 %[k10], %[pc3] offset:0\n\t" \
  "s_waitcnt lgkmcnt(8)\n\t" \
  "v_mfma_f32_16x16x32_bf16 %[c01], v[192:195], %[k11], %[c01]\n\t" \
  "v_add_u32 %[pc0], %[dl], %[pc0]\n\t" \
  "v_mfma_f32_16x16x32_bf16 %[c11], v[196:199], %[k11], %[c11]\n\t" \
  "s_or_b32 %[hb2], %[hb0], 0x4000\n\t" \
  "ds_read_b128 %[k11], %[pc3] offset:2048\n\t" \
  "buffer_load_dwordx4 v[188:191], %[vo], %[srd], %[hb0] offen offset:1024 sc1\n\t" \
  "s_waitcnt lgkmcnt(8)\n\t" \
  "v_mfma_f32_16x16x32_bf16 %[c02], v[192:195], %[k12], %[c02]\n\t" \
  "v_add_u32 %[pc1], %[dl], %[pc1]\n\t" \
  "v_mfma_f32_16x16x32_bf16 %[c12], v[196:199], %[k12], %[c12]\n\t" \
  "v_add_u32 %[fv], 1, %[fv]\n\t" \
  "ds_read_b128 %[k12], %[pc3] offset:4096\n\t" \
  "s_waitcnt lgkmcnt(7)\n\t" \
  "v_mfma_f32_16x16x32_bf16 %[c03], v[192:195], %[k13], %[c03]\n\t" \
  "v_readfirstlane_b32 %[s], v253\n\t" \
  "v_mfma_f32_16x16x32_bf16 %[c13], v[196:199], %[k13], %[c13]\n\t" \
  "ds_read_b128 %[k13], %[pc3] offset:6144\n\t" \
  "ds_write_b32 %[fw], %[fv]\n\t" \
  "s_cmp_gt_u32 %[s], %[cn]\n\t" \
  "s_cbranch_scc0 Lcs0_%=\n\t" \
  "Lcb0_%=:\n\t" \
  "buffer_load_dwordx4 v[192:195], %[vo], %[srd], %[hb1] offen offset:0 sc1\n\t" \
  "s_waitcnt vmcnt(13) lgkmcnt(8)\n\t" \
  "ds_read_b128 v[252:255], %[fa]\n\t" \
  "v_mfma_f32_16x16x32_bf16 %[c00], v[200:203], %[k00], %[c00]\n\t" \
  "s_or_b32 %[hb3], %[hb0], 0x6000\n\t" \
  "v_mfma_f32_16x16x32_bf16 %[c10], v[204:207], %[k00], %[c10]\n\t" \
  "ds_read_b128 %[k00], %[pc0] offset:0\n\t" \
  "s_waitcnt lgkmcnt(9)\n\t" \
  "v_mfma_f32_16x16x32_bf16 %[c01], v[200:203], %[k01], %[c01]\n\t" \
  "v_mfma_f32_16x16x32_bf16 %[c11], v[204:207], %[k01], %[c11]\n\t" \
  "ds_read_b128 %[k01], %[pc0] offset:2048\n\t" \
  "buffer_load_dwordx4 v[196:199], %[vo], %[srd], %[hb1] offen offset:1024 sc1\n\t" \
  "s_waitcnt lgkmcnt(9)\n\t" \
  "v_mfma_f32_16x16x32_bf16 %[c02], v[200:203], %[k02], %[c02]\n\t" \
  "v_mfma_f32_16x16x32_bf16 %[c12], v[204:207], %[k02], %[c12]\n\t" \
  "ds_read_b128 %[k02], %[pc0] offset:4096\n\t" \
  "s_waitcnt lgkmcnt(9)\n\t" \
  "v_mfma_f32_16x16x32_bf16 %[c03], v[200:203], %[k03], %[c03]\n\t" \
  "v_mfma_f32_16x16x32_bf16 %[c13], v[204:207], %[k03], %[c13]\n\t" \
  "ds_read_b128 %[k03], %[pc0] offset:6144\n\t" \
  "buffer_load_dwordx4 v[200:203], %[vo], %[srd], %[hb2] offen offset:0 sc1\n\t" \
  "s_waitcnt vmcnt(13) lgkmcnt(9)\n\t" \
  "v_mfma_f32_16x16x32_bf16 %[c00], v[208:211], %[k10], %[c00]\n\t" \
  "v_add_u32 %[pc2], %[dl], %[pc2]\n\t" \
  "v_mfma_f32_16x16x32_bf16 %[c10], v[212:215], %[k10], %[c10]\n\t" \
  "ds_read_b128 %[k10], %[pc1] offset:0\n\t" \
  "s_waitcnt lgkmcnt(9)\n\t" \
  "v_mfma_f32_16x16x32_bf16 %[c01], v[208:211], %[k11], %[c01]\n\t" \
  "v_add_u32 %[pc3], %[dl], %[pc3]\n\t" \
  "v_mfma_f32_16x16x32_bf16 %[c11], v[212:215], %[k11], %[c11]\n\t" \
  "ds_read_b128 %[k11], %[pc1] offset:2048\n\t" \
  "buffer_load_dwordx4 v[204:207], %[vo], %[srd], %[hb2] offen offset:1024 sc1\n\t" \
  "s_waitcnt lgkmcnt(6)\n\t" \
  "v_mfma_f32_16x16x32_bf16 %[c02], v[208:211], %[k12], %[c02]\n\t" \
  "v_readfirstlane_b32 %[s], v254\n\t" \
  "v_mfma_f32_16x16x32_bf16 %[c12], v[212:215], %[k12], %[c12]\n\t" \
  "ds_read_b128 %[k12], %[pc1] offset:4096\n\t" \
  "v_mfma_f32_16x16x32_bf16 %[c03], v[208:211], %[k13], %[c03]\n\t" \
  "s_add_u32 %[hn], %[hn], 1\n\t" \
  "v_mfma_f32_16x16x32_bf16 %[c13], v[212:215], %[k13], %[c13]\n\t" \
  "ds_read_b128 %[k13], %[pc1] offset:6144\n\t" \
  "buffer_load_dwordx4 v[208:211], %[vo], %[srd], %[hb3] offen offset:0 sc1\n\t" \
  "buffer_load_dwordx4 v[212:215], %[vo], %[srd], %[hb3] offen offset:1024 sc1\n\t" \
  "s_cmp_ge_u32 %[s], %[hn]\n\t" \
  "s_cbranch_scc0 Lhs0_%=\n\t" \
  "Lhb0_%=:\n\t" \
  "Lin_%=:\n\t" \
  "s_waitcnt vmcnt(14) lgkmcnt(7)\n\t" \
  "ds_read_b128 v[252:255], %[fa]\n\t" \
  "v_mfma_f32_16x16x32_bf16 %[c00], v[216:219], %[k00], %[c00]\n\t" \
  "s_cmp_gt_u32 %[hn], %[own]\n\t" \
  "s_addc_u32 %[hb0], %[hn], -1\n\t" \
  "v_mfma_f32_16x16x32_bf16 %[c10], v[220:223], %[k00], %[c10]\n\t" \
  "ds_read_b128 %[k00], %[pc2] offset:0\n\t" \
  "s_waitcnt lgkmcnt(8)\n\t" \
  "v_mfma_f32_16x16x32_bf16 %[c01], v[216:219], %[k01], %[c01]\n\t" \
  "s_lshl_b32 %[hb0], %[hb0], 15\n\t" \
  "v_mfma_f32_16x16x32_bf16 %[c11], v[220:223], %[k01], %[c11]\n\t" \
  "ds_read_b128 %[k01], %[pc2] offset:2048\n\t" \
  "s_waitcnt lgkmcnt(8)\n\t" \
  "v_mfma_f32_16x16x32_bf16 %[c02], v[216:219], %[k02], %[c02]\n\t" \
  "s_or_b32 %[hb1], %[hb0], 0x2000\n\t" \
  "v_mfma_f32_16x16x32_bf16 %[c12], v[220:223], %[k02], %[c12]\n\t" \
  "ds_read_b128 %[k02], %[pc2] offset:4096\n\t" \
  "s_waitcnt lgkmcnt(8)\n\t" \
  "v_mfma_f32_16x16x32_bf16 %[c03], v[216:219], %[k03], %[c03]\n\t" \
  "s_add_u32 %[cn], %[cn], 1\n\t" \
  "v_mfma_f32_16x16x32_bf16 %[c13], v[220:223], %[k03], %[c13]\n\t" \
  "ds_read_b128 %[k03], %[pc2] offset:6144\n\t" \
  "buffer_load_dwordx4 v[216:219], %[vo], %[srd], %[hb0] offen offset:0 sc1\n\t" \
  "s_waitcnt vmcnt(13) lgkmcnt(8)\n\t" \
  "v_mfma_f32_16x16x32_bf16 %[c00], v[224:227], %[k10], %[c00]\n\t" \
  "s_cmp_eq_u32 %[cn], 9\n\t" \
  "s_cselect_b32 %[dl], 0x20000, %[d16]\n\t" \
  "v_mfma_f32_16x16x32_bf16 %[c10], v[228:231], %[k10], %[c10]\n\t" \
  "s_bitcmp1_b32 %[wr], %[cn]\n\t" \
  "s_cselect_b32 %[dl], 0xfffe4000, %[dl]\n\t" \
  "ds_read_b128 %[k10], %[pc3] offset:0\n\t" \
  "s_waitcnt lgkmcnt(8)\n\t" \
  "v_mfma_f32_16x16x32_bf16 %[c01], v[224:227], %[k11], %[c01]\n\t" \
  "v_add_u32 %[pc0], %[dl], %[pc0]\n\t" \
  "v_mfma_f32_16x16x32_bf16 %[c11], v[228:231], %[k11], %[c11]\n\t" \
  "s_or_b32 %[hb2], %[hb0], 0x4000\n\t" \
  "ds_read_b128 %[k11], %[pc3] offset:2048\n\t" \
  "buffer_load_dwordx4 v[220:223], %[vo], %[srd], %[hb0] offen offset:1024 sc1\n\t" \
  "s_waitcnt lgkmcnt(8)\n\t" \
  "v_mfma_f32_16x16x32_bf16 %[c02], v[224:227], %[k12], %[c02]\n\t" \
  "v_add_u32 %[pc1], %[dl], %[pc1]\n\t" \
  "v_mfma_f32_16x16x32_bf16 %[c12], v[228:231], %[k12], %[c12]\n\t" \
  "v_add_u32 %[fv], 1, %[fv]\n\t" \
  "ds_read_b128 %[k12], %[pc3] offset:4096\n\t" \
  "s_waitcnt lgkmcnt(7)\n\t" \
  "v_mfma_f32_16x16x32_bf16 %[c03], v[224:227], %[k13], %[c03]\n\t" \
  "v_readfirstlane_b32 %[s], v252\n\t" \
  "v_mfma_f32_16x16x32_bf16 %[c13], v[228:231], %[k13], %[c13]\n\t" \
  "ds_read_b128 %[k13], %[pc3] offset:6144\n\t" \
  "ds_write_b32 %[fw], %[fv]\n\t" \
  "s_cmp_gt_u32 %[s], %[cn]\n\t" \
  "s_cbranch_scc0 Lcs1_%=\n\t" \
  "Lcb1_%=:\n\t" \
  "buffer_load_dwordx4 v[224:227], %[vo], %[srd], %[hb1] offen offset:0 sc1\n\t" \
  "s_waitcnt vmcnt(13) lgkmcnt(8)\n\t" \
  "ds_read_b128 v[252:255], %[fa]\n\t" \
  "v_mfma_f32_16x16x32_bf16 %[c00], v[232:235], %[k00], %[c00]\n\t" \
  "s_or_b32 %[hb3], %[hb0], 0x6000\n\t" \
  "v_mfma_f32_16x16x32_bf16 %[c10], v[236:239], %[k00], %[c10]\n\t" \
  "ds_read_b128 %[k00], %[pc0] offset:0\n\t" \
  "s_waitcnt lgkmcnt(9)\n\t" \
  "v_mfma_f32_16x16x32_bf16 %[c01], v[232:235], %[k01], %[c01]\n\t" \
  "v_mfma_f32_16x16x32_bf16 %[c11], v[236:239], %[k01], %[c11]\n\t" \
  "ds_read_b128 %[k01], %[pc0] offset:2048\n\t" \
  "buffer_load_dwordx4 v[228:231], %[vo], %[srd], %[hb1] offen offset:1024 sc1\n\t" \
  "s_waitcnt lgkmcnt(9)\n\t" \
  "v_mfma_f32_16x16x32_bf16 %[c02], v[232:235], %[k02], %[c02]\n\t" \
  "v_mfma_f32_16x16x32_bf16 %[c12], v[236:239], %[k02], %[c12]\n\t" \
  "ds_read_b128 %[k02], %[pc0] offset:4096\n\t" \
  "s_waitcnt lgkmcnt(9)\n\t" \
  "v_mfma_f32_16x16x32_bf16 %[c03], v[232:235], %[k03], %[c03]\n\t" \
  "v_mfma_f32_16x16x32_bf16 %[c13], v[236:239], %[k03], %[c13]\n\t" \
  "ds_read_b128 %[k03], %[pc0] offset:6144\n\t" \
  "buffer_load_dwordx4 v[232:235], %[vo], %[srd], %[hb2] offen offset:0 sc1\n\t" \
  "s_waitcnt vmcnt(13) lgkmcnt(9)\n\t" \
  "v_mfma_f32_16x16x32_bf16 %[c00], v[240:243], %[k10], %[c00]\n\t" \
  "v_add_u32 %[pc2], %[dl], %[pc2]\n\t" \
  "v_mfma_f32_16x16x32_bf16 %[c10], v[244:247], %[k10], %[c10]\n\t" \
  "ds_read_b128 %[k10], %[pc1] offset:0\n\t" \
  "s_waitcnt lgkmcnt(9)\n\t" \
  "v_mfma_f32_16x16x32_bf16 %[c01], v[240:243], %[k11], %[c01]\n\t" \
  "v_add_u32 %[pc3], %[dl], %[pc3]\n\t" \
  "v_mfma_f32_16x16x32_bf16 %[c11], v[244:247], %[k11], %[c11]\n\t" \
  "ds_read_b128 %[k11], %[pc1] offset:2048\n\t" \
  "buffer_load_dwordx4 v[236:239], %[vo], %[srd], %[hb2] offen offset:1024 sc1\n\t" \
  "s_waitcnt lgkmcnt(6)\n\t" \
  "v_mfma_f32_16x16x32_bf16 %[c02], v[240:243], %[k12], %[c02]\n\t" \
  "v_readfirstlane_b32 %[s], v254\n\t" \
  "v_mfma_f32_16x16x32_bf16 %[c12], v[244:247], %[k12], %[c12]\n\t" \
  "ds_read_b128 %[k12], %[pc1] offset:4096\n\t" \
  "v_mfma_f32_16x16x32_bf16 %[c03], v[240:243], %[k13], %[c03]\n\t" \
  "s_add_u32 %[hn], %[hn], 1\n\t" \
  "v_mfma_f32_16x16x32_bf16 %[c13], v[244:247], %[k13], %[c13]\n\t" \
  "ds_read_b128 %[k13], %[pc1] offset:6144\n\t" \
  "buffer_load_dwordx4 v[240:243], %[vo], %[srd], %[hb3] offen offset:0 sc1\n\t" \
  "buffer_load_dwordx4 v[244:247], %[vo], %[srd], %[hb3] offen offset:1024 sc1\n\t" \
  "s_cmp_ge_u32 %[s], %[hn]\n\t" \
  "s_cbranch_scc0 Lhs1_%=\n\t" \
  "Lhb1_%=:\n\t" \
  "s_cmp_lt_u32 %[cn], 14\n\t" \
  "s_cbranch_scc1 Lg2_%=\n\t" \
  "s_waitcnt vmcnt(14) lgkmcnt(7)\n\t" \
  "ds_read_b128 v[252:255], %[fa]\n\t" \
  "v_mfma_f32_16x16x32_bf16 %[c00], v[184:187], %[k00], %[c00]\n\t" \
  "v_mfma_f32_16x16x32_bf16 %[c10], v[188:191], %[k00], %[c10]\n\t" \
  "ds_read_b128 %[k00], %[pc2] offset:0\n\t" \
  "s_waitcnt lgkmcnt(8)\n\t" \
  "v_mfma_f32_16x16x32_bf16 %[c01], v[184:187], %[k01], %[c01]\n\t" \
  "v_mfma_f32_16x16x32_bf16 %[c11], v[188:191], %[k01], %[c11]\n\t" \
  "ds_read_b128 %[k01], %[pc2] offset:2048\n\t" \
  "s_waitcnt lgkmcnt(8)\n\t" \
  "v_mfma_f32_16x16x32_bf16 %[c02], v[184:187], %[k02], %[c02]\n\t" \
  "v_mfma_f32_16x16x32_bf16 %[c12], v[188:191], %[k02], %[c12]\n\t" \
  "ds_read_b128 %[k02], %[pc2] offset:4096\n\t" \
  "s_waitcnt lgkmcnt(8)\n\t" \
  "v_mfma_f32_16x16x32_bf16 %[c03], v[184:187], %[k03], %[c03]\n\t" \
  "s_add_u32 %[cn], %[cn], 1\n\t" \
  "v_mfma_f32_16x16x32_bf16 %[c13], v[188:191], %[k03], %[c13]\n\t" \
  "ds_read_b128 %[k03], %[pc2] offset:6144\n\t" \
  "s_waitcnt vmcnt(12) lgkmcnt(8)\n\t" \
  "v_mfma_f32_16x16x32_bf16 %[c00], v[192:195], %[k10], %[c00]\n\t" \
  "s_cmp_eq_u32 %[cn], 9\n\t" \
  "s_cselect_b32 %[dl], 0x20000, %[d16]\n\t" \
  "v_mfma_f32_16x16x32_bf16 %[c10], v[196:199], %[k10], %[c10]\n\t" \
  "s_bitcmp1_b32 %[wr], %[cn]\n\t" \
  "s_cselect_b32 %[dl], 0xfffe4000, %[dl]\n\t" \
  "ds_read_b128 %[k10], %[pc3] offset:0\n\t" \
  "s_waitcnt lgkmcnt(8)\n\t" \
  "v_mfma_f32_16x16x32_bf16 %[c01], v[192:195], %[k11], %[c01]\n\t" \
  "v_add_u32 %[pc0], %[dl], %[pc0]\n\t" \
  "v_mfma_f32_16x16x32_bf16 %[c11], v[196:199], %[k11], %[c11]\n\t" \
  "ds_read_b128 %[k11], %[pc3] offset:2048\n\t" \
  "s_waitcnt lgkmcnt(8)\n\t" \
  "v_mfma_f32_16x16x32_bf16 %[c02], v[192:195], %[k12], %[c02]\n\t" \
  "v_add_u32 %[pc1], %[dl], %[pc1]\n\t" \
  "v_mfma_f32_16x16x32_bf16 %[c12], v[196:199], %[k12], %[c12]\n\t" \
  "ds_read_b128 %[k12], %[pc3] offset:4096\n\t" \
  "s_waitcnt lgkmcnt(7)\n\t" \
  "v_mfma_f32_16x16x32_bf16 %[c03], v[192:195], %[k13], %[c03]\n\t" \
  "v_readfirstlane_b32 %[s], v253\n\t" \
  "v_mfma_f32_16x16x32_bf16 %[c13], v[196:199], %[k13], %[c13]\n\t" \
  "ds_read_b128 %[k13], %[pc3] offset:6144\n\t" \
  "s_cmp_gt_u32 %[s], %[cn]\n\t" \
  "s_cbranch_scc0 Lcs0t_%=\n\t" \
  "Lcb0t_%=:\n\t" \
  "s_waitcnt vmcnt(10) lgkmcnt(7)\n\t" \
  "v_mfma_f32_16x16x32_bf16 %[c00], v[200:203], %[k00], %[c00]\n\t" \
  "v_mfma_f32_16x16x32_bf16 %[c10], v[204:207], %[k00], %[c10]\n\t" \
  "ds_read_b128 %[k00], %[pc0] offset:0\n\t" \
  "s_waitcnt lgkmcnt(7)\n\t" \
  "v_mfma_f32_16x16x32_bf16 %[c01], v[200:203], %[k01], %[c01]\n\t" \
  "v_mfma_f32_16x16x32_bf16 %[c11], v[204:207], %[k01], %[c11]\n\t" \
  "ds_read_b128 %[k01], %[pc0] offset:2048\n\t" \
  "s_waitcnt lgkmcnt(7)\n\t" \
  "v_mfma_f32_16x16x32_bf16 %[c02], v[200:203], %[k02], %[c02]\n\t" \
  "v_mfma_f32_16x16x32_bf16 %[c12], v[204:207], %[k02], %[c12]\n\t" \
  "ds_read_b128 %[k02], %[pc0] offset:4096\n\t" \
  "s_waitcnt lgkmcnt(7)\n\t" \
  "v_mfma_f32_16x16x32_bf16 %[c03], v[200:203], %[k03], %[c03]\n\t" \
  "v_mfma_f32_16x16x32_bf16 %[c13], v[204:207], %[k03], %[c13]\n\t" \
  "ds_read_b128 %[k03], %[pc0] offset:6144\n\t" \
  "s_waitcnt vmcnt(8) lgkmcnt(7)\n\t" \
  "v_mfma_f32_16x16x32_bf16 %[c00], v[208:211], %[k10], %[c00]\n\t" \
  "v_add_u32 %[pc2], %[dl], %[pc2]\n\t" \
  "v_mfma_f32_16x16x32_bf16 %[c10], v[212:215], %[k10], %[c10]\n\t" \
  "ds_read_b128 %[k10], %[pc1] offset:0\n\t" \
  "s_waitcnt lgkmcnt(7)\n\t" \
  "v_mfma_f32_16x16x32_bf16 %[c01], v[208:211], %[k11], %[c01]\n\t" \
  "v_add_u32 %[pc3], %[dl], %[pc3]\n\t" \
  "v_mfma_f32_16x16x32_bf16 %[c11], v[212:215], %[k11], %[c11]\n\t" \
  "ds_read_b128 %[k11], %[pc1] offset:2048\n\t" \
  "s_waitcnt lgkmcnt(7)\n\t" \
  "v_mfma_f32_16x16x32_bf16 %[c02], v[208:211], %[k12], %[c02]\n\t" \
  "v_mfma_f32_16x16x32_bf16 %[c12], v[212:215], %[k12], %[c12]\n\t" \
  "ds_read_b128 %[k12], %[pc1] offset:4096\n\t" \
  "s_waitcnt lgkmcnt(7)\n\t" \
  "v_mfma_f32_16x16x32_bf16 %[c03], v[208:211], %[k13], %[c03]\n\t" \
  "v_mfma_f32_16x16x32_bf16 %[c13], v[212:215], %[k13], %[c13]\n\t" \
  "ds_read_b128 %[k13], %[pc1] offset:6144\n\t" \
  "s_waitcnt vmcnt(6) lgkmcnt(7)\n\t" \
  "v_mfma_f32_16x16x32_bf16 %[c00], v[216:219], %[k00], %[c00]\n\t" \
  "v_mfma_f32_16x16x32_bf16 %[c10], v[220:223], %[k00], %[c10]\n\t" \
  "ds_read_b128 %[k00], %[pc2] offset:0\n\t" \
  "s_waitcnt lgkmcnt(7)\n\t" \
  "v_mfma_f32_16x16x32_bf16 %[c01], v[216:219], %[k01], %[c01]\n\t" \
  "v_mfma_f32_16x16x32_bf16 %[c11], v[220:223], %[k01], %[c11]\n\t" \
  "ds_read_b128 %[k01], %[pc2] offset:2048\n\t" \
  "s_waitcnt lgkmcnt(7)\n\t" \
  "v_mfma_f32_16x16x32_bf16 %[c02], v[216:219], %[k02], %[c02]\n\t" \
  "v_mfma_f32_16x16x32_bf16 %[c12], v[220:223], %[k02], %[c12]\n\t" \
  "ds_read_b128 %[k02], %[pc2] offset:4096\n\t" \
  "s_waitcnt lgkmcnt(7)\n\t" \
  "v_mfma_f32_16x16x32_bf16 %[c03], v[216:219], %[k03], %[c03]\n\t" \
  "v_mfma_f32_16x16x32_bf16 %[c13], v[220:223], %[k03], %[c13]\n\t" \
  "ds_read_b128 %[k03], %[pc2] offset:6144\n\t" \
  "s_waitcnt vmcnt(4) lgkmcnt(7)\n\t" \
  "v_mfma_f32_16x16x32_bf16 %[c00], v[224:227], %[k10], %[c00]\n\t" \
  "v_mfma_f32_16x16x32_bf16 %[c10], v[228:231], %[k10], %[c10]\n\t" \
  "ds_read_b128 %[k10], %[pc3] offset:0\n\t" \
  "s_waitcnt lgkmcnt(7)\n\t" \
  "v_mfma_f32_16x16x32_bf16 %[c01], v[224:227], %[k11], %[c01]\n\t" \
  "v_mfma_f32_16x16x32_bf16 %[c11], v[228:231], %[k11], %[c11]\n\t" \
  "ds_read_b128 %[k11], %[pc3] offset:2048\n\t" \
  "s_waitcnt lgkmcnt(7)\n\t" \
  "v_mfma_f32_16x16x32_bf16 %[c02], v[224:227], %[k12], %[c02]\n\t" \
  "v_mfma_f32_16x16x32_bf16 %[c12], v[228:231], %[k12], %[c12]\n\t" \
  "ds_read_b128 %[k12], %[pc3] offset:4096\n\t" \
  "s_waitcnt lgkmcnt(7)\n\t" \
  "v_mfma_f32_16x16x32_bf16 %[c03], v[224:227], %[k13], %[c03]\n\t" \
  "v_mfma_f32_16x16x32_bf16 %[c13], v[228:231], %[k13], %[c13]\n\t" \
  "ds_read_b128 %[k13], %[pc3] offset:6144\n\t" \
  "s_waitcnt vmcnt(2) lgkmcnt(7)\n\t" \
  "v_mfma_f32_16x16x32_bf16 %[c00], v[232:235], %[k00], %[c00]\n\t" \
  "v_mfma_f32_16x16x32_bf16 %[c10], v[236:239], %[k00], %[c10]\n\t" \
  "s_waitcnt lgkmcnt(6)\n\t" \
  "v_mfma_f32_16x16x32_bf16 %[c01], v[232:235], %[k01], %[c01]\n\t" \
  "v_mfma_f32_16x16x32_bf16 %[c11], v[236:239], %[k01], %[c11]\n\t" \
  "s_waitcnt lgkmcnt(5)\n\t" \
  "v_mfma_f32_16x16x32_bf16 %[c02], v[232:235], %[k02], %[c02]\n\t" \
  "v_mfma_f32_16x16x32_bf16 %[c12], v[236:239], %[k02], %[c12]\n\t" \
  "s_waitcnt lgkmcnt(4)\n\t" \
  "v_mfma_f32_16x16x32_bf16 %[c03], v[232:235], %[k03], %[c03]\n\t" \
  "v_mfma_f32_16x16x32_bf16 %[c13], v[236:239], %[k03], %[c13]\n\t" \
  "s_waitcnt vmcnt(0) lgkmcnt(3)\n\t" \
  "v_mfma_f32_16x16x32_bf16 %[c00], v[240:243], %[k10], %[c00]\n\t" \
  "v_mfma_f32_16x16x32_bf16 %[c10], v[244:247], %[k10], %[c10]\n\t" \
  "s_waitcnt lgkmcnt(2)\n\t" \
  "v_mfma_f32_16x16x32_bf16 %[c01], v[240:243], %[k11], %[c01]\n\t" \
  "v_mfma_f32_16x16x32_bf16 %[c11], v[244:247], %[k11], %[c11]\n\t" \
  "s_waitcnt lgkmcnt(1)\n\t" \
  "v_mfma_f32_16x16x32_bf16 %[c02], v[240:243], %[k12], %[c02]\n\t" \
  "v_mfma_f32_16x16x32_bf16 %[c12], v[244:247], %[k12], %[c12]\n\t" \
  "s_waitcnt lgkmcnt(0)\n\t" \
  "v_mfma_f32_16x16x32_bf16 %[c03], v[240:243], %[k13], %[c03]\n\t" \
  "v_mfma_f32_16x16x32_bf16 %[c13], v[244:247], %[k13], %[c13]\n\t" \
  "s_branch Lend_%=\n\t" \
  "Lcs0_%=:\n\t" \
  "s_add_u32 %[s2], %[cn], 1\n\t" \
  "Lcr0_%=:\n\t" \
  "ds_read_b128 v[252:255], %[fa]\n\t" \
  "s_cmp_eq_u32 %[to], 0\n\t" \
  "s_cbranch_scc1 Lcb0_%=\n\t" \
  "s_sub_u32 %[to], %[to], 1\n\t" \
  "s_waitcnt lgkmcnt(0)\n\t" \
  "v_readfirstlane_b32 %[s], v253\n\t" \
  "s_cmp_ge_u32 %[s], %[s2]\n\t" \
  "s_cbranch_scc0 Lcr0_%=\n\t" \
  "s_branch Lcb0_%=\n\t" \
  "Lhs0_%=:\n\t" \
  "ds_read_b128 v[252:255], %[fa]\n\t" \
  "s_cmp_eq_u32 %[to], 0\n\t" \
  "s_cbranch_scc1 Lhb0_%=\n\t" \
  "s_sub_u32 %[to], %[to], 1\n\t" \
  "s_waitcnt lgkmcnt(0)\n\t" \
  "v_readfirstlane_b32 %[s], v254\n\t" \
  "s_cmp_ge_u32 %[s], %[hn]\n\t" \
  "s_cbranch_scc0 Lhs0_%=\n\t" \
  "s_branch Lhb0_%=\n\t" \
  "Lcs1_%=:\n\t" \
  "s_add_u32 %[s2], %[cn], 1\n\t" \
  "Lcr1_%=:\n\t" \
  "ds_read_b128 v[252:255], %[fa]\n\t" \
  "s_cmp_eq_u32 %[to], 0\n\t" \
  "s_cbranch_scc1 Lcb1_%=\n\t" \
  "s_sub_u32 %[to], %[to], 1\n\t" \
  "s_waitcnt lgkmcnt(0)\n\t" \
  "v_readfirstlane_b32 %[s], v252\n\t" \
  "s_cmp_ge_u32 %[s], %[s2]\n\t" \
  "s_cbranch_scc0 Lcr1_%=\n\t" \
  "s_branch Lcb1_%=\n\t" \
  "Lhs1_%=:\n\t" \
  "ds_read_b128 v[252:255], %[fa]\n\t" \
  "s_cmp_eq_u32 %[to], 0\n\t" \
  "s_cbranch_scc1 Lhb1_%=\n\t" \
  "s_sub_u32 %[to], %[to], 1\n\t" \
  "s_waitcnt lgkmcnt(0)\n\t" \
  "v_readfirstlane_b32 %[s], v254\n\t" \
  "s_cmp_ge_u32 %[s], %[hn]\n\t" \
  "s_cbranch_scc0 Lhs1_%=\n\t" \
  "s_branch Lhb1_%=\n\t" \
  "Lcs0t_%=:\n\t" \
  "s_add_u32 %[s2], %[cn], 1\n\t" \
  "Lcr0t_%=:\n\t" \
  "ds_read_b128 v[252:255], %[fa]\n\t" \
  "s_cmp_eq_u32 %[to], 0\n\t" \
  "s_cbranch_scc1 Lcb0t_%=\n\t" \
  "s_sub_u32 %[to], %[to], 1\n\t" \
  "s_waitcnt lgkmcnt(0)\n\t" \
  "v_readfirstlane_b32 %[s], v253\n\t" \
  "s_cmp_ge_u32 %[s], %[s2]\n\t" \
  "s_cbranch_scc0 Lcr0t_%=\n\t" \
  "s_branch Lcb0t_%=\n\t" \
  "Lend_%=:\n\t" \
  "s_nop 7\n\t"
#define ASM_REQ \
  "s_cmp_gt_u32 1, %[own]\n\t" \
  "s_addc_u32 %[hb0], 1, -1\n\t" \
  "s_lshl_b32 %[hb0], %[hb0], 15\n\t" \
  "s_or_b32 %[hb1], %[hb0], 0x2000\n\t" \
  "s_or_b32 %[hb2], %[hb0], 0x4000\n\t" \
  "s_or_b32 %[hb3], %[hb0], 0x6000\n\t" \
  "buffer_load_dwordx4 v[216:219], %[vo], %[srd], %[hb0] offen offset:0 sc1\n\t" \
  "buffer_load_dwordx4 v[220:223], %[vo], %[srd], %[hb0] offen offset:1024 sc1\n\t" \
  "buffer_load_dwordx4 v[224:227], %[vo], %[srd], %[hb1] offen offset:0 sc1\n\t" \
  "buffer_load_dwordx4 v[228:231], %[vo], %[srd], %[hb1] offen offset:1024 sc1\n\t" \
  "buffer_load_dwordx4 v[232:235], %[vo], %[srd], %[hb2] offen offset:0 sc1\n\t" \
  "buffer_load_dwordx4 v[236:239], %[vo], %[srd], %[hb2] offen offset:1024 sc1\n\t" \
  "buffer_load_dwordx4 v[240:243], %[vo], %[srd], %[hb3] offen offset:0 sc1\n\t" \
  "buffer_load_dwordx4 v[244:247], %[vo], %[srd], %[hb3] offen offset:1024 sc1\n\t" \
  "s_cmp_gt_u32 2, %[own]\n\t" \
  "s_addc_u32 %[hb0], 2, -1\n\t" \
  "s_lshl_b32 %[hb0], %[hb0], 15\n\t" \
  "s_or_b32 %[hb1], %[hb0], 0x2000\n\t" \
  "s_or_b32 %[hb2], %[hb0], 0x4000\n\t" \
  "s_or_b32 %[hb3], %[hb0], 0x6000\n\t" \
  "buffer_load_dwordx4 v[184:187], %[vo], %[srd], %[hb0] offen offset:0 sc1\n\t" \
  "buffer_load_dwordx4 v[188:191], %[vo], %[srd], %[hb0] offen offset:1024 sc1\n\t" \
  "buffer_load_dwordx4 v[192:195], %[vo], %[srd], %[hb1] offen offset:0 sc1\n\t" \
  "buffer_load_dwordx4 v[196:199], %[vo], %[srd], %[hb1] offen offset:1024 sc1\n\t" \
  "buffer_load_dwordx4 v[200:203], %[vo], %[srd], %[hb2] offen offset:0 sc1\n\t" \
  "buffer_load_dwordx4 v[204:207], %[vo], %[srd], %[hb2] offen offset:1024 sc1\n\t" \
  "buffer_load_dwordx4 v[208:211], %[vo], %[srd], %[hb3] offen offset:0 sc1\n\t" \
  "buffer_load_dwordx4 v[212:215], %[vo], %[srd], %[hb3] offen offset:1024 sc1\n\t"
#define NS 0
#define TF 0
#define T16 0

#define OST " sc1 nt"
#define XPRIO 2
#define XPRIO2 1
#define MPRIO2 2
#define HDLY 0
#define LPRIO 2
#define POLLN 8
#define YGATE 9
#define LTHR 16
#define CPK 9
#define W2P 0
#define SWP 0
#define GK 12
#define MTOP 1
#define CROT 2
#define NRG0 0
#define NRG1 2
#define CSL 0
#define NSI 8
#define DSL 1
#define FSB 128
#define FSW 8
#define XP3 3
#define XP4 3
#define LZ 1
#define KROT 0
#define XGT 0
#define W1N 8
#define W1P 1
#define WIF 0
#define W2G 0
#define W2S 0
#define WNT " nt"
#define W1A 2
#define W1Bb 3
#define W1C 4
#define W1D 6
#define CT 0
#define STN 6
#define ST2 0
#define PK 99
#define STA 1
#define STB 1
#define STX 0
#define MPW 0
#define MPV 0
#define TKL 0
#define PRB 0
#define PRS 8
#define LBP 0
#define LG2 0
#define BM 1
#define MPS 4
#define MGP 1
#define RQD 0
#define XG2 2
#define NFW 28
#define SPINLOG 18
constexpr int D_ = 1024, F_ = 3584;
constexpr int L_XH = 131072;
constexpr int MISC = 155648;
constexpr int L_GT = MISC;
constexpr int L_FLG = L_GT + 512;
constexpr int L_JNK = L_FLG + 256;
constexpr int L_SEL = L_JNK + 256;
constexpr int L_BROW = L_SEL + 128;
constexpr int L_ACT = L_BROW + 2048;
constexpr int L_CAND = L_ACT + 1024;
constexpr int L_ZERO = L_CAND + 256;
constexpr int L_MAP = L_ZERO + 256;
constexpr int L_TAG = L_MAP + 512;
constexpr int L_RT = L_TAG + 16;
constexpr int LDS_BYTES = 163840;
static_assert(L_RT + 32 <= LDS_BYTES, "lds");
constexpr int FL_FULL = L_FLG, FL_FREE = L_FLG + 16, FL_C = L_FLG + 32  , FL_CFREE = L_FLG + 48, FL_TAG = L_FLG + 64  ,
              FL_XD = L_FLG + 80, FL_TK = L_FLG + 96, FL_SEL = L_FLG + 112, FL_G = L_FLG + 128, FL_W = L_FLG + 136, FL_HS = L_FLG + 144, FL_C8 = L_FLG + 160, FL_P = L_FLG + 164;
constexpr unsigned W_EPOCH = 0;
constexpr unsigned W_R2 = 256;
constexpr unsigned W_G2 = W_R2 + 8192;
constexpr unsigned W_HF = W_G2 + 8192;
constexpr unsigned W_AGG = W_HF + 256 * FSB;
constexpr unsigned W_SEL = W_AGG + 262144;
constexpr unsigned W_GU = W_SEL + 4096;
constexpr unsigned W_YP = W_GU + 56 * 16384;
constexpr unsigned YTILE = 56 * 256, YTOK = 16 * YTILE;
constexpr unsigned W_H = 8u << 20;
constexpr unsigned W_DBG = 20u << 20;
constexpr unsigned W_END = 21u << 20;
static_assert(W_YP + 256 * 7168 <= W_H, "ws");
#define WS_BASE(n) (((n) - W_END) & ~(size_t)0xfffff)
constexpr int SPIN_MAX = 1 << SPINLOG;

DEV float bf2f(unsigned v) { return __uint_as_float(v << 16); }
DEV float bfhi(unsigned v) { return __uint_as_float(v & 0xffff0000u); }
DEV u16 f2bf(float f) { return __builtin_bit_cast(u16, (__bf16)f); }
DEV float rbf(float f) { return bf2f(f2bf(f)); }
typedef __bf16 bf16x2_t __attribute__((ext_vector_type(2)));
DEV unsigned pk2(float a, float b) { return __builtin_bit_cast(unsigned, __builtin_convertvector((f32x2){a, b}, bf16x2_t)); }
DEV float fma1(float a, float b, float c) { float d; asm("v_fma_f32 %0, %1, %2, %3" : "=v"(d) : "v"(a), "v"(b), "v"(c)); return d; }
DEV float fnma1(float a, float b, float c) { float d; asm("v_fma_f32 %0, -%1, %2, %3" : "=v"(d) : "v"(a), "v"(b), "v"(c)); return d; }
DEV float fnmn1(float a, float b, float c) { float d; asm("v_fma_f32 %0, -%1, %2, -%3" : "=v"(d) : "v"(a), "v"(b), "v"(c)); return d; }
DEV float mul1(float a, float b) { float d; asm("v_mul_f32 %0, %1, %2" : "=v"(d) : "v"(a), "v"(b)); return d; }
DEV f32x2 cmul(f32x2 a, f32x2 b) { return (f32x2){fnma1(a.y, b.y, mul1(a.x, b.x)), fma1(a.y, b.x, mul1(a.x, b.y))}; }
DEV f32x2 cfma(f32x2 a, f32x2 b, f32x2 c) { return (f32x2){fma1(a.x, b.x, fnma1(a.y, b.y, c.x)), fma1(a.x, b.y, fma1(a.y, b.x, c.y))}; }
template <int N> DEV void waitv() { asm volatile("s_waitcnt vmcnt(%0)" ::"n"(N) : "memory"); }
DEV void bar() { asm volatile("s_barrier" ::: "memory"); }
DEV unsigned lds_rd32(unsigned a) { unsigned v; asm volatile("ds_read_b32 %0, %1\n\ts_waitcnt lgkmcnt(0)" : "=v"(v) : "v"(a) : "memory"); return v; }
DEV u32x4 lds_rd128(unsigned a) { u32x4 v; asm volatile("ds_read_b128 %0, %1\n\ts_waitcnt lgkmcnt(0)" : "=v"(v) : "v"(a) : "memory"); return v; }
DEV void lds_wr32(unsigned a, unsigned v) { asm volatile("ds_write_b32 %0, %1" ::"v"(a), "v"(v) : "memory"); }
template <int S = 1> DEV void lds_wait_ge(unsigned a, unsigned need) {
#pragma unroll 1
  for (int it = 0; it < SPIN_MAX * 4; ++it) { if (__builtin_amdgcn_readfirstlane(lds_rd32(a)) >= need) break; __builtin_amdgcn_s_sleep(S); }
}
typedef __amdgpu_buffer_rsrc_t rsrc_t;
DEV u32x4 ld128(rsrc_t r, unsigned vo, unsigned so = 0) { return __builtin_amdgcn_raw_buffer_load_b128(r, vo, so, 0); }
#define FRESH() asm volatile("" ::: "memory")
DEV u32x4 ld128c(rsrc_t r, unsigned vo) { return __builtin_amdgcn_raw_buffer_load_b128(r, vo, 0, 16); }
DEV u32x2 ld64c(rsrc_t r, unsigned vo) { return __builtin_amdgcn_raw_buffer_load_b64(r, vo, 0, 16); }
DEV void st128c(rsrc_t r, unsigned vo, u32x4 v) {
  asm volatile("s_nop 4\n\tbuffer_store_dwordx4 %0, %1, %2, 0 offen sc1\n\ts_nop 7\n\ts_nop 7" ::"v"(v), "v"(vo), "s"(r) : "memory");
}
DEV void st64c(rsrc_t r, unsigned vo, u32x2 v) { __builtin_amdgcn_raw_buffer_store_b64(v, r, vo, 0, 16); }
DEV rsrc_t mk(const void* p, unsigned bytes) { return __builtin_amdgcn_make_buffer_rsrc((void*)p, 0, bytes, 0x00020000); }

DEV unsigned wave_maxu(unsigned v) {
  v = max(v, (unsigned)__builtin_amdgcn_update_dpp((int)v, (int)v, 0x111, 0xf, 0xf, false));
  v = max(v, (unsigned)__builtin_amdgcn_update_dpp((int)v, (int)v, 0x112, 0xf, 0xf, false));
  v = max(v, (unsigned)__builtin_amdgcn_update_dpp((int)v, (int)v, 0x114, 0xf, 0xf, false));
  v = max(v, (unsigned)__builtin_amdgcn_update_dpp((int)v, (int)v, 0x118, 0xf, 0xf, false));
  v = max(v, (unsigned)__builtin_amdgcn_update_dpp((int)v, (int)v, 0x142, 0xa, 0xf, false));
  v = max(v, (unsigned)__builtin_amdgcn_update_dpp((int)v, (int)v, 0x143, 0xc, 0xf, false));
  return (unsigned)__builtin_amdgcn_readlane((int)v, 63);
}
DEV float sum16_32(float v) {
  { auto p = __builtin_amdgcn_permlane16_swap(__float_as_uint(v), __float_as_uint(v), false, false); v = __uint_as_float(p[0]) + __uint_as_float(p[1]); }
  { auto p = __builtin_amdgcn_permlane32_swap(__float_as_uint(v), __float_as_uint(v), false, false); v = __uint_as_float(p[0]) + __uint_as_float(p[1]); }
  return v;
}
DEV float wave_sum(float v) {
  v += __uint_as_float((unsigned)__builtin_amdgcn_update_dpp(0, (int)__float_as_uint(v), 0x128, 0xf, 0xf, false));
  v += __uint_as_float((unsigned)__builtin_amdgcn_update_dpp(0, (int)__float_as_uint(v), 0x124, 0xf, 0xf, false));
  v += __uint_as_float((unsigned)__builtin_amdgcn_update_dpp(0, (int)__float_as_uint(v), 0x122, 0xf, 0xf, false));
  v += __uint_as_float((unsigned)__builtin_amdgcn_update_dpp(0, (int)__float_as_uint(v), 0x121, 0xf, 0xf, false));
  return sum16_32(v);
}
DEV unsigned route_key(float r, int idx) {
  unsigned u = __float_as_uint(r) >> 16;
  u = (u & 0x8000u) ? (~u & 0xffffu) : (u | 0x8000u);
  return (u << 16) | (unsigned)(2047 - idx);
}
DEV float key_val(unsigned key) {
  unsigned u = key >> 16;
  u = (u & 0x8000u) ? (u & 0x7fffu) : (~u & 0xffffu);
  return __uint_as_float(u << 16);
}

DEV void unit_trn(u32x4 (&w)[8]) {
  u32x4 b[8];
#pragma unroll
  for (int j = 0; j < 8; ++j)
#pragma unroll
    for (int t = 0; t < 4; ++t) b[j][t] = __builtin_amdgcn_perm(w[2 * t + 1][j >> 1], w[2 * t][j >> 1], (j & 1) ? 0x07060302u : 0x05040100u);
#pragma unroll
  for (int j = 0; j < 8; ++j) w[j] = b[j];
}
DEV f32x4 fma4(f32x4 a, f32x4 b, f32x4 c) { return __builtin_elementwise_fma(a, b, c); }
DEV f32x4 fnm4(f32x4 a, f32x4 b, f32x4 c) {
  const f32x2 l = __builtin_elementwise_fma(-a.lo, b.lo, c.lo), h = __builtin_elementwise_fma(-a.hi, b.hi, c.hi);
  return (f32x4){l.x, l.y, h.x, h.y};
}
DEV f32x4 nml4(f32x4 a, f32x4 b) { const f32x2 l = -a.lo * b.lo, h = -a.hi * b.hi; return (f32x4){l.x, l.y, h.x, h.y}; }
template <int CTRL> DEV float dpp1(float v) { return __uint_as_float((unsigned)__builtin_amdgcn_update_dpp(0, (int)__float_as_uint(v), CTRL, 0xf, 0xf, true)); }
template <int CTRL> DEV f32x4 dpp4(f32x4 v) { return (f32x4){dpp1<CTRL>(v.x), dpp1<CTRL>(v.y), dpp1<CTRL>(v.z), dpp1<CTRL>(v.w)}; }
DEV f32x4 splat4(float v) { f32x2 p = {v, v}; asm volatile("" : "+v"(p)); return (f32x4){p.x, p.y, p.x, p.y}; }
DEV void cexp4(f32x4 a2, f32x4 bv, f32x4& re, f32x4& im) {
  f32x4 m, cs, sn;
#pragma unroll
  for (int r = 0; r < 4; ++r) { const float ph = __builtin_amdgcn_fractf(bv[r]); m[r] = __builtin_amdgcn_exp2f(a2[r]); cs[r] = __builtin_amdgcn_cosf(ph); sn[r] = __builtin_amdgcn_sinf(ph); }
  re = m * cs; im = m * sn;
}
DEV f32x4 unpk4(u32x2 v) { return (f32x4){bf2f(v.x & 0xffffu), bfhi(v.x), bf2f(v.y & 0xffffu), bfhi(v.y)}; }
DEV void bcast8(int n, f32x4 v, f32x4& o) {
  switch (n) {
    case 0: o = dpp4<0x150>(v); break; case 1: o = dpp4<0x151>(v); break; case 2: o = dpp4<0x152>(v); break; case 3: o = dpp4<0x153>(v); break;
    case 4: o = dpp4<0x154>(v); break; case 5: o = dpp4<0x155>(v); break; case 6: o = dpp4<0x156>(v); break; default: o = dpp4<0x157>(v); break;
  }
}
DEV f32x4 lds4(LAS char* smem, int a) { return *(const LAS f32x4*)(smem + a); }
template <int S = 1> DEV void lds_wait_min4(unsigned a, unsigned need) {
#pragma unroll 1
  for (int it = 0; it < SPIN_MAX * 4; ++it) {
    const u32x4 f = lds_rd128(a);
    if (__builtin_amdgcn_readfirstlane(min(min(f.x, f.y), min(f.z, f.w))) >= need) break;
    __builtin_amdgcn_s_sleep(S);
  }
}

DEV void dma4(rsrc_t r0, rsrc_t r1, LAS char* la, const unsigned (&v)[4], unsigned so) {
  __builtin_amdgcn_raw_ptr_buffer_load_lds(r0, (LAS void*)la, 16, v[0], so, 0, 0);
  __builtin_amdgcn_raw_ptr_buffer_load_lds(r0, (LAS void*)la, 16, v[1], so, 1024, 0);
  __builtin_amdgcn_raw_ptr_buffer_load_lds(r1, (LAS void*)la, 16, v[2], so, 2048, 0);
  __builtin_amdgcn_raw_ptr_buffer_load_lds(r1, (LAS void*)la, 16, v[3], so, 3072, 0);
}

__global__ __launch_bounds__(512, 2) void fused(const u16*, const u16*, const u16*, const u16*, const u16*, const u16*, const u16*, const u16*, const u16*, const u16*,
                                                const u16*, const u16*, const u16*, const u16*, char*, float*) {
#if __HIP_DEVICE_COMPILE__
  unsigned kw0, kw1;
  asm volatile("s_load_dword %0, %2, 0x0\n\ts_load_dword %1, %2, 0x40" : "=&s"(kw0), "=&s"(kw1) : "s"(__builtin_amdgcn_kernarg_segment_ptr()));
  extern __shared__ __attribute__((aligned(16))) char smem_[];
  LAS char* smem = (LAS char*)smem_;
  const int tid = threadIdx.x, hw = __builtin_amdgcn_readfirstlane(tid >> 6), wid = SWP ? hw ^ ((hw >> 1) & 2) : hw;
  const int bid = blockIdx.x, xcd = bid & 7, loc = bid >> 3;
  const int c = (xcd >> 1) * 4 + (loc >> 3), j = (xcd & 1) * 8 + (loc & 7);
  int lane, fr, fq;
  auto relane = [&]() {
    asm volatile("v_mbcnt_lo_u32_b32 %0, -1, 0\n\tv_mbcnt_hi_u32_b32 %0, -1, %0" : "=v"(lane));
    __builtin_assume(lane >= 0 && lane < 64);
    fr = lane & 15; fq = lane >> 4;
  };
  const int crot = CROT == 0 ? c + 1 : CROT == 1 ? 4 * c + (c >> 2) : CROT == 2 ? (c & 1) + 4 * (c & 2) + (c >> 2) * 2 : CROT == 3 ? 2 * c : (c & 1) * 2 + 4 * (c & 2) + (c >> 2);
  const int pown = (j - crot) & 15;
  auto upos = [&](int u) { return u == 0 ? pown : u - 1 + (u - 1 >= pown ? 1 : 0); };
  auto lead = [&](u32x2 f) {
    const u64 m = ~(((u64)(unsigned)__builtin_amdgcn_readfirstlane(f.y) << 32 | (unsigned)__builtin_amdgcn_readfirstlane(f.x)) | 15ull << (4 * pown));
    const int p = (int)__builtin_ctzll(m) >> 2;
    return m ? (unsigned)(p - (p > pown ? 1 : 0)) : 99u;
  };
  u64 pc0;
  unsigned l0, l1, l2;
  asm volatile("s_getpc_b64 %0\n\tLk_pc:\n\ts_mov_b32 %1, Lk_m-Lk_pc\n\ts_mov_b32 %2, Lk_l-Lk_pc\n\ts_mov_b32 %3, Lk_x-Lk_pc" : "=s"(pc0), "=s"(l0), "=s"(l1), "=s"(l2));
  auto codepull = [&]() {
    const unsigned po = min((unsigned)(loc * 16 + (lane & 15)) * 128u, max(l0, max(l1, l2)) - 4u);
    if (lane < 16) __builtin_amdgcn_global_load_lds((const __attribute__((address_space(1))) void*)(pc0 + po), (LAS void*)(smem + L_JNK), 4, 0, 0);
  };
  u32x2 p0, p1, p2, dv;
  u32x4 xv[2][2], wv[2], gv[2];
#define MLOADS(A_, B_) \
    asm volatile("global_load_dwordx2 %0, %12, %16\n\tglobal_load_dwordx2 %1, %12, %17\n\tglobal_load_dwordx2 %2, %12, %18\n\t" \
                 "global_load_dwordx4 %4, %13, %19\n\tglobal_load_dwordx4 %5, %13, %19 offset:1024\n\tglobal_load_dwordx4 %6, %13, %19 offset:2048\n\tglobal_load_dwordx4 %7, %13, %19 offset:3072\n\t" \
                 "global_load_dwordx4 %8, %14, %20\n\tglobal_load_dwordx4 %9, %14, %20 offset:1024\n\tglobal_load_dwordx4 %10, %14, %21\n\tglobal_load_dwordx4 %11, %14, %21 offset:1024\n\t" \
                 "global_load_dwordx2 %3, %15, %22" \
                 : "=&v"(p0), "=&v"(p1), "=&v"(p2), "=&v"(dv), "=&v"(xv[0][0]), "=&v"(xv[0][1]), "=&v"(xv[1][0]), "=&v"(xv[1][1]), "=&v"(wv[0]), "=&v"(wv[1]), "=&v"(gv[0]), "=&v"(gv[1]) \
                 : "v"((j * 64 + wid * 16 + 4 * fq) * 2), "v"((c * 128 + j * 8 + wid * 2) * 2048 + lane * 16), "v"(lane * 16), "v"((j * 64 + 4 * fr) * 2), \
                   "s"(A_[1]), "s"(A_[2]), "s"(A_[7]), "s"(A_[0]), "s"(B_[1]), "s"(B_[5]), "s"(B_[0]) : "memory")
  if (wid < 4) {
    u64x8 ta, tb;
    if (MTOP) asm volatile("s_load_dwordx16 %0, %2, 0x0\n\ts_load_dwordx16 %1, %2, 0x40" : "=&s"(ta), "=&s"(tb) : "s"(__builtin_amdgcn_kernarg_segment_ptr()));
    relane();
    if (wid == 0) codepull();
    if (MTOP) {
      asm volatile("s_waitcnt lgkmcnt(0)" : "+s"(ta), "+s"(tb));
      MLOADS(ta, tb);
    }
  }
  const int l = wid & 1;
#define FFNDEF() int b_ = bid; asm volatile("" : "+s"(b_)); \
  const bool ffn = b_ < 8 * NFW; \
  const int sg = (b_ & 7) * 7 + (b_ >> 5), kq = (b_ >> 3) & 3, sg1 = (sg & ~1) + l, gu1 = sg & 1; \
  const unsigned gub = W_GU + ((sg1 * 4 + kq) * 2 + gu1) * 2048, ypb = W_YP + (kq * 4 + l * 2) * YTILE + sg * 256
  if (__builtin_expect(wid >= 4, 1)) {
  u64x8 ka, kb;
  asm volatile("s_load_dwordx16 %0, %2, 0x0\n\ts_load_dwordx16 %1, %2, 0x40" : "=&s"(ka), "=&s"(kb) : "s"(__builtin_amdgcn_kernarg_segment_ptr()));
  relane();
  unsigned vo1[4];
#pragma unroll
  for (int e = 0; e < 4; ++e)
    vo1[e] = (l == 0 ? c * 128 + 8 * (lane >> 3) + 64 * (e & 1) + (e >> 1) : j * 64 + (lane >> 3) + 8 * (e & 1)) * 2048 + (((lane & 7) ^ (lane >> 3)) * 16) + 4096 - e * 1024;
  const unsigned s4 = (l == 0 ? 2 : 16) * 2048, so1 = wid >= 6 ? 2 * s4 : 0, la1 = l * 16384 + (wid >= 6 ? 8192 : 0);
  const int s0 = (KROT == 0 ? c + j : KROT == 1 ? (c >> 1) + (j >> 1) + 4 * (c >> 2) : KROT == 2 ? c + (j >> 1) + 4 * (j >> 3) : KROT == 3 ? 2 * c + j : KROT == 4 ? 4 * c + 2 * j + (c >> 2) : KROT == 5 ? (c >> 1) + j : 3 * c + j) & 15;
  unsigned sl0 = ((s0 + (s0 == j ? 1 : 0)) & 15) * 128 + so1;
  asm volatile("" : "+v"(vo1[0]), "+v"(vo1[1]), "+v"(vo1[2]), "+v"(vo1[3]) : "s"(sl0), "s"(s4), "s"(la1));
  asm volatile("s_waitcnt lgkmcnt(0)" : "+s"(ka), "+s"(kb) : "s"(kw0), "s"(kw1));
  const char *x = (const char*)ka[0], *Bre = (const char*)ka[3], *Bim = (const char*)ka[4], *Cre = (const char*)ka[5], *Cim = (const char*)ka[6], *w_gate = (const char*)kb[2],
             *w_up = (const char*)kb[3], *w_down = (const char*)kb[4];
  char* ws = (char*)kb[6];
  const rsrc_t r0 = mk((l == 0 ? x : Bre) - 4096, (l == 0 ? 1 << 22 : 1 << 21) + 4096), r1 = mk((l == 0 ? x : Bim) - 4096, (l == 0 ? 1 << 22 : 1 << 21) + 4096);
  auto issue = [&](unsigned sk, unsigned slot) { dma4(r0, r1, smem + slot + la1, vo1, sk); dma4(r0, r1, smem + slot + la1 + 4096, vo1, sk + s4); };
  issue(sl0, 0);
  __builtin_amdgcn_sched_barrier(0);
  const rsrc_t rws = mk(ws, W_END);
  const rsrc_t rx = mk(x, 2048 * 1024 * 2);
  u64 tbl;
  {
    const int n = (j - s0) & 15;
    const u64 seq = 0xfedcba9876543210ull, rot = s0 ? (seq >> (4 * s0)) | (seq << (64 - 4 * s0)) : seq, lo = (1ull << (4 * n)) - 1;
    tbl = (rot & lo) | ((rot >> 4) & ~lo) | ((u64)j << 60);
  }
  auto slice = [&](int k) { return ((unsigned)(tbl >> (4 * k)) & 15u) * 128u; };
  __builtin_amdgcn_sched_barrier(0);

  auto flag = [&](int a, unsigned v) { lds_wr32(lane == 0 ? (unsigned)a : (unsigned)(L_JNK + lane * 4), v); };
  auto flagmax = [&](int a, unsigned v) { asm volatile("ds_max_u32 %0, %1" ::"v"(lane == 0 ? (unsigned)a : (unsigned)(L_JNK + lane * 4)), "v"(v) : "memory"); };
  auto hpoll = [&](unsigned tagA, unsigned tagB) {
    lds_wait_min4(FL_FREE, 9u);
    __builtin_amdgcn_s_sleep(HDLY);
#pragma unroll 1
    for (int it = 0; it < SPIN_MAX; ++it) {
      if ((MPW || BM) && __builtin_amdgcn_readfirstlane(lds_rd32(FL_C + 8)) == 99u) return;
      FRESH();
      u32x2 f = {0u, 0u};
      asm volatile("buffer_atomic_or_x2 %0, %1, %2, 0 offen sc0 sc1\n\ts_waitcnt vmcnt(0)" : "+v"(f) : "v"(lane == 0 ? W_HF + c * 128 : 0xf0000000u), "s"(rws) : "memory");
      const unsigned n = lead(f);
      const u64 m = 1;
      if (n == 99u) break;
      flagmax(FL_C + 8, n);
    }
    flagmax(FL_C + 8, 99u);
  };
    const rsrc_t rcr = mk(Cre, WIF == 4 ? 0 : 1 << 21), rci = mk(Cim, WIF == 4 || WIF == 5 ? 0 : 1 << 21);
    const unsigned ce = (j * 64 + 4 * (lane >> 3)) * 2048 + (((lane & 7) ^ (lane >> 4)) * 16);
    const unsigned co = (j * 64 + 32 + 4 * (lane >> 3)) * 2048 + (((lane & 7) ^ (4 + (lane >> 4))) * 16);
    auto cfill = [&](int u, int slot, int q0, int q1) {
      LAS char* dst = smem + slot * 16384;
#pragma unroll
      for (int q = q0; q < q1; ++q)
        __builtin_amdgcn_raw_ptr_buffer_load_lds(q < 8 ? rcr : rci, (LAS void*)(dst + q * 1024), 16, (q & 1) ? co : ce, ((q & 7) >> 1) * 2048 + ((upos(u) + crot) & 15) * 128, 0, 0);
    };
  if (__builtin_expect(wid == 4 || wid == 5, 1)) {
    __builtin_amdgcn_s_setprio(LPRIO);
    issue(slice(1), 32768);
    issue(slice(2), 65536);
    constexpr int K0 = 0, W10 = 0;
    {
      u64 t = tbl >> (4 * K0 + 12);
#pragma unroll 1
      for (int k = K0; k < 13; ++k) {
        const unsigned sk = ((unsigned)t & 15u) << 7;
        waitv<16>();
        bar();
        issue(sk, ((k + 3) & 3) * 32768);
        if (k == CPK && l == 1) codepull();
        t >>= 4;
      }
    }
    auto hold = [&]() { if (LG2 && __builtin_amdgcn_readfirstlane(lds_rd32(FL_P))) { if (LG2 == 1) lds_wait_min4(FL_HS, 1u); else lds_wait_min4(FL_FREE, 9u); } };
    u32x4 cq[3][16];
    auto creg = [&](int g, int q0 = 0, int q1 = 16, bool thr = true) {
#pragma unroll
      for (int q = q0; q < q1; ++q) {
        if ((q & 7) == 0) hold();
        const unsigned so = ((q & 7) >> 1) * 2048 + ((upos(10 + l + 2 * g) + crot) & 15) * 128;
        if (q < 8) asm volatile("buffer_load_dwordx4 %0, %1, %2, %3 offen" : "=v"(cq[g][q]) : "v"((q & 1) ? co : ce), "s"(rcr), "s"(so) : "memory");
        else asm volatile("buffer_load_dwordx4 %0, %1, %2, %3 offen" : "=v"(cq[g][q]) : "v"((q & 1) ? co : ce), "s"(rci), "s"(so) : "memory");
        if ((q & 7) == 7 && thr) waitv<LTHR>();
      }
    };
#define CTL(i) { _Pragma("unroll") for (int e = (i) * CT; e < (i) * CT + CT; ++e) creg(2 - (e >> 2), (e & 3) * 4, (e & 3) * 4 + 4, false); }
    CTL(0)
    waitv<16 + 4 * CT>(); bar();
    CTL(1)
    waitv<8 + 8 * CT>(); bar();
    CTL(2)
    const unsigned tagA = lds_rd32(L_TAG), tagB = tagA + 0x40000000u;
    waitv<12 * CT>(); bar();
    CTL(3)
    if (T16) lds_wait_min4(FL_FREE, 1u); else bar();
    lds_wait_ge(FL_W + l * 4, 1u);
    __builtin_amdgcn_s_setprio(1);
#pragma unroll
    for (int g = 0; g < (TF ? CT : 0); ++g) creg(2 - g);
#pragma unroll 1
    for (int u = l; u < 8; u += 2) { if (TF) lds_wait_min4(FL_FREE, (u >> 1) + 1); hold(); cfill(u, u, 0, 8); waitv<LTHR>(); hold(); cfill(u, u, 8, 16); waitv<LTHR>(); if (u >= 2) flag(FL_C + l * 4, (unsigned)(u - 1)); }
#pragma unroll
    for (int g = 0; g < 3; ++g) {
      if (g >= (l ? NRG1 : NRG0) - CT) break;
      waitv<16>();
      if (g == 1) flag(FL_C + l * 4, (unsigned)(7 + l));
      creg(g);
    }
    waitv<0>();
    if ((l ? NRG1 : NRG0) - CT < 2) flag(FL_C + l * 4, (unsigned)(7 + l));
    if (l == 0) {
      __builtin_amdgcn_s_setprio(3);
      hpoll(tagA, tagB);
    }
    else {
      lds_wait_min4(FL_CFREE, 1u);
      cfill(8, 0, 0, 16);
      cfill(9, 8, 0, 16);
      waitv<0>();
      flagmax(FL_C8, 1u);
      flag(FL_C + 4, 10u);
    }
    __builtin_amdgcn_s_setprio(0);
    if (l == 0) { lds_wait_ge(FL_C8, 1u); flag(FL_C, 9u); }
#pragma unroll
    for (int g = 0; g < 3; ++g) {
      asm volatile("" : "+v"(cq[g][0]), "+v"(cq[g][1]), "+v"(cq[g][2]), "+v"(cq[g][3]), "+v"(cq[g][4]), "+v"(cq[g][5]), "+v"(cq[g][6]), "+v"(cq[g][7]),
                   "+v"(cq[g][8]), "+v"(cq[g][9]), "+v"(cq[g][10]), "+v"(cq[g][11]), "+v"(cq[g][12]), "+v"(cq[g][13]), "+v"(cq[g][14]), "+v"(cq[g][15]));
      const int u = 10 + l + 2 * g;
      lds_wait_min4<LZ>(FL_CFREE, (unsigned)(u - 8));
      const int slot = u - 9;
      if (g < (l ? NRG1 : NRG0)) {
#pragma unroll
        for (int q = 0; q < 16; ++q)
          asm volatile("ds_write_b128 %0, %1 offset:%2" ::"v"(slot * 16384 + lane * 16), "v"(cq[g][q]), "n"(q * 1024) : "memory");
      } else {
#pragma unroll
        for (int q = 0; q < 16; q += 4) { cfill(u, slot, q, q + 4); if (CSL) __builtin_amdgcn_s_sleep(CSL); }
        waitv<0>();
      }
      flag(FL_C + l * 4, g == 2 ? 99u : (unsigned)(u + 1));
    }
    asm volatile("Lk_l:");
    return;
  }
  {
    const int xw = wid - 6;
    __builtin_amdgcn_s_setprio(XPRIO);
    u32x2 ep;
    unsigned tagA, tagB;
    if (xw == 0) lds_wr32(L_FLG + lane * 4, 0u);
    else asm volatile("ds_write_b64 %0, %1\n\tds_write_b64 %2, %3" ::"v"(L_CAND + lane * 8), "v"((u32x2){0u, 0u}), "v"(L_MAP + lane * 8), "v"((u32x2){L_ZERO, L_ZERO}) : "memory");
    FFNDEF();
    u32x4 w1[4][8], w2[2][8];
    const rsrc_t rw1 = mk(gu1 ? w_up : w_gate, ffn ? D_ * F_ * 2 : 0), r2 = mk(w_down, ffn ? D_ * F_ * 2 : 0);
    unsigned o1 = (kq * 256 + 32 * (fr >> 3) + 8 * fq) * (F_ * 2) + (sg1 * 64 + 8 * (fr & 7)) * 2;
    unsigned o2 = (sg * 64 + 8 * fq) * 2048 + (kq * 256 + xw * 128 + 8 * fr) * 2;
#define LDW1(b) { _Pragma("unroll") for (int i = 4 * ((b) & 1); i < 4 * ((b) & 1) + 4; ++i) \
      asm volatile("buffer_load_dwordx4 %0, %1, %2, %3 offen" WNT : "=v"(w1[(b) >> 1][i]) : "v"(o1), "s"(rw1), "s"((((b) >> 1) * 64 + i) * (F_ * 2)) : "memory"); }
#define LDW2(a, b) { _Pragma("unroll") for (int i = a; i < b; ++i) \
      asm volatile("buffer_load_dwordx4 %0, %1, %2, %3 offen" WNT : "=v"(w2[i >> 3][i & 7]) : "v"(o2), "s"(r2), "s"(((i & 7) + (i >> 3) * 32) * 2048) : "memory"); }
#define TRN(w, n) { asm volatile("s_waitcnt vmcnt(%8)" : "+v"(w[0]), "+v"(w[1]), "+v"(w[2]), "+v"(w[3]), "+v"(w[4]), "+v"(w[5]), "+v"(w[6]), "+v"(w[7]) : "n"(n) : "memory"); \
      unit_trn(w); asm volatile("" : "+v"(w[0]), "+v"(w[1]), "+v"(w[2]), "+v"(w[3]), "+v"(w[4]), "+v"(w[5]), "+v"(w[6]), "+v"(w[7])); }
    unsigned key[16];
    unsigned rgo = W_R2 + l * 4096 + lane * 16, ggo = W_G2 + c * 512 + (lane & 31) * 16;
    u32x4 g[4], a;
    asm volatile("" : "+v"(o1), "+v"(o2), "+v"(rgo), "+v"(ggo) : "s"(rw1), "s"(r2));
    auto A_EP = [&]() { asm volatile("buffer_load_dwordx2 %0, off, %1, 0 sc1" : "=v"(ep) : "s"(rws) : "memory"); };
    auto A_TAG = [&]() {
      asm volatile("" : "+v"(ep));
      tagA = ep.x + 1u; tagB = tagA + 0x40000000u;
      if (xw == 0 && lane == 0) lds_wr32(L_TAG, tagA);
    };
    auto A_CLR = [&]() {
      asm volatile("buffer_store_dwordx2 %0, %1, %2, 0 offen sc1" ::"v"((u32x2){0u, 0u}), "v"(BM && j == 0 && xw == 0 && lane == 0 ? W_HF + c * 128 : 0xf0000000u), "s"(rws) : "memory");
      asm volatile("s_nop 1\n\t"
                   "buffer_store_dwordx4 %0, %1, %3, 0 offen sc1\n\tbuffer_store_dwordx4 %0, %1, %3, 0 offen offset:1024 sc1\n\t"
                   "buffer_store_dwordx4 %0, %2, %3, 0 offen sc1\n\tbuffer_store_dwordx4 %0, %2, %3, %4 offen sc1\n\t"
                   "buffer_store_dwordx4 %0, %2, %3, %5 offen sc1\n\tbuffer_store_dwordx4 %0, %2, %3, %6 offen sc1\n\t"
                   "buffer_store_dwordx4 %0, %7, %3, %8 offen sc1\n\tbuffer_store_dwordx4 %0, %7, %3, %8 offen offset:1024 sc1\n\t"
                   "buffer_store_dwordx4 %0, %7, %3, %8 offen offset:2048 sc1\n\tbuffer_store_dwordx4 %0, %7, %3, %8 offen offset:3072 sc1\n\ts_nop 7"
                   ::"v"((u32x4){~0u, ~0u, ~0u, ~0u}), "v"(ffn ? gub + lane * 16 : 0xf0000000u), "v"(ffn ? ypb + (lane >> 5) * YTOK + ((lane >> 4) & 1) * YTILE + fr * 16 : 0xf0000000u), "s"(rws),
                   "s"(2 * YTOK), "s"(4 * YTOK), "s"(6 * YTOK), "v"(NS ? lane * 16 : 0xf0000000u), "s"(W_H + c * 524288 + (j * 2 + xw) * 4096) : "memory");
    };
    auto A_PUB = [&]() {
      u32x2 rt;
      asm volatile("ds_read_b64 %0, %1\n\ts_waitcnt lgkmcnt(0)" : "=v"(rt) : "v"(L_RT + (lane & 1) * 16 + xw * 8) : "memory");
      st128c(rws, lane < 2 ? (lane ? W_G2 : W_R2) + (c * 128 + j * 8 + xw * 4) * 4 : 0xf0000000u, (u32x4){rt.x, tagA, rt.y, tagB});
    };
    auto A_GAT = [&]() {
#pragma unroll
      for (int q = 0; q < 4; ++q) asm volatile("buffer_load_dwordx4 %0, %1, %2, 0 offen offset:%3 sc1" : "=v"(g[q]) : "v"(rgo), "s"(rws), "n"(q * 1024) : "memory");
      asm volatile("buffer_load_dwordx4 %0, %1, %2, 0 offen sc1" : "=v"(a) : "v"(ggo), "s"(rws) : "memory");
    };
    static_assert(NS <= 3, "cleared blocks");
    A_EP();
    issue(slice(1) + so1, 32768);
    issue(slice(2) + so1, 65536);
    asm volatile("s_waitcnt vmcnt(17) lgkmcnt(0)" ::: "memory"); bar();
    issue(slice(3) + so1, 98304);
    waitv<24>(); A_TAG();
    A_CLR();
    asm volatile("s_waitcnt vmcnt(27) lgkmcnt(0)" ::: "memory"); bar();
    issue(slice(4) + so1, 0);
    asm volatile("s_waitcnt vmcnt(27) lgkmcnt(0)" ::: "memory"); bar();
    issue(slice(5) + so1, 32768);
    asm volatile("s_waitcnt vmcnt(27) lgkmcnt(0)" ::: "memory"); bar();
    issue(slice(6) + so1, 65536);
    waitv<24>(); A_PUB();
    constexpr int K0 = 4, W10 = 0;
    {
      const int m0 = ((b_ >> 3) * STA + (b_ & 7) * STB + xw * STX) % STN, ms = K0 + m0, m2 = ST2 ? K0 + (m0 + STN / 2) % STN : 99;
#pragma unroll
      for (int k = K0; k < 13; ++k) {
        const int e = k > GK && k <= GK + 3 ? 5 : 0;
        const bool i1 = (unsigned)(k - ms - 1) < 3u, i2 = (unsigned)(k - m2 - 1) < 3u;
        if (i1 && i2) asm volatile("s_waitcnt vmcnt(%0)" ::"n"(32 + 4 * ST2 + e) : "memory");
        else if (i1) asm volatile("s_waitcnt vmcnt(%0)" ::"n"(32 + e) : "memory");
        else if (i2) asm volatile("s_waitcnt vmcnt(%0)" ::"n"(16 + 4 * ST2 + e) : "memory");
        else asm volatile("s_waitcnt vmcnt(%0)" ::"n"(16 + e) : "memory");
        bar();
        issue(slice(k + 3) + so1, ((k + 3) & 3) * 32768);
        if (k == PK) A_PUB();
        if (k == GK) A_GAT();
        if (k < K0 + STN && k == ms) LDW2(0, 16)
        if (k < K0 + STN && k == m2) { _Pragma("unroll") for (int b2 = 0; b2 < ST2; ++b2) LDW1(b2) }
      }
      static_assert(K0 + STN <= 13 && PK >= 4, "order");
    }
#define LDW1R(a, b) { _Pragma("unroll") for (int b_ = (a); b_ < (b); ++b_) LDW1(b_) }
    LDW1R((W10 + ST2), W1A)
    waitv<16 + 4 * (W1A - (W10 + ST2)) + (GK >= 10 ? 5 : 0)>(); bar(); LDW1R(W1A, W1Bb)
    waitv<8 + 4 * (W1Bb - (W10 + ST2)) + (GK >= 11 ? 5 : 0)>(); bar(); LDW1R(W1Bb, W1C)
    waitv<4 * (W1C - (W10 + ST2)) + (GK >= 12 ? 5 : 0)>(); bar(); LDW1R(W1C, W1D)
    if (!T16) bar(); else if (W1D < 8) lds_wait_ge(FL_FREE, 1u);
    __builtin_amdgcn_s_setprio(XPRIO2);
#define W1B() do { if (XGT == 1) lds_wait_min4(FL_FREE, 1u); if (XGT == 2) lds_wait_min4(FL_HS, 1u); if (XGT == 3) lds_wait_ge(FL_HS, 1u); \
      _Pragma("unroll") for (int b_ = W1N; b_ < 8; ++b_) LDW1(b_) } while (0)
#pragma unroll
    for (int b_ = W1D; b_ < W1N; ++b_) LDW1(b_)
    if (W2P == 10) LDW2(0, 16)
    if (lane == 0) lds_wr32(FL_W + xw * 4, 1u);
    asm volatile("s_waitcnt vmcnt(%5)" : "+v"(g[0]), "+v"(g[1]), "+v"(g[2]), "+v"(g[3]), "+v"(a) : "n"(TKL == 2 ? 0 : 4 * (W1N - (W10 + ST2))) : "memory");
    {
#pragma unroll 1
      for (int it = 0; it < SPIN_MAX; ++it) {
        bool ok = true;
#pragma unroll
        for (int q = 0; q < 4; ++q) ok = ok && g[q].y == tagA && g[q].w == tagB;
        if (__all(ok && a.y == tagA && a.w == tagB)) break;
        __builtin_amdgcn_s_sleep(8);
        A_GAT();
        asm volatile("s_waitcnt vmcnt(0)" : "+v"(g[0]), "+v"(g[1]), "+v"(g[2]), "+v"(g[3]), "+v"(a)::"memory");
      }
      if (l == 0) {
        const unsigned gp = lane < 32 ? a.x : a.z, ga = L_GT + ((4 * (lane & 1) + 2 * (lane >> 5)) * 16 + ((lane & 31) >> 1)) * 4;
        lds_wr32(ga, gp << 16);
        lds_wr32(ga + 64, gp & 0xffff0000u);
      }
      asm volatile("ds_add_u32 %0, %1" ::"v"(lane == 0 ? (unsigned)FL_G : (unsigned)(L_JNK + lane * 4)), "v"(1u) : "memory");
      if (W1P == 1) W1B();
      if (TKL == 1) waitv<0>();
#pragma unroll
      for (int q = 0; q < 4; ++q) {
        const unsigned r0 = 2047 - (l * 1024 + q * 256 + lane * 4);
        key[4 * q] = (g[q].x << 16) + r0; key[4 * q + 1] = (g[q].x & 0xffff0000u) + (r0 - 1);
        key[4 * q + 2] = (g[q].z << 16) + (r0 - 2); key[4 * q + 3] = (g[q].z & 0xffff0000u) + (r0 - 3);
        asm volatile("" : "+v"(key[4 * q]), "+v"(key[4 * q + 1]), "+v"(key[4 * q + 2]), "+v"(key[4 * q + 3]));
      }
      unsigned m = key[0], m2 = 0, m3 = 0;
#pragma unroll
      for (int i = 1; i < 16; ++i) {
        asm("v_med3_u32 %0, %1, %0, %3\n\tv_med3_u32 %1, %2, %1, %3\n\tv_max_u32 %2, %2, %3" : "+v"(m3), "+v"(m2), "+v"(m) : "v"(key[i]));
      }
      asm volatile("" : "+v"(m), "+v"(m2), "+v"(m3));
      unsigned T = 0;
#pragma unroll
      for (int b = 31; b >= 16; --b) {
        const unsigned t2 = T | (1u << b);
        T = __popcll(__ballot(m >= t2)) >= 8 ? t2 : T;
      }
      asm volatile("" : "+s"(T));
      const u64 k1 = __ballot(m >= T), k2 = __ballot(m2 >= T);
      const int n1 = __popcll(k1);
      if (__ballot(m3 >= T) || n1 + __popcll(k2) > 16) {
        unsigned cand = 0;
#pragma unroll 1
        for (int s = 0; s < 8; ++s) {
          unsigned mm = key[0];
#pragma unroll
          for (int i = 1; i < 16; ++i) mm = max(mm, key[i]);
          mm = wave_maxu(mm);
          cand = lane == s ? mm : cand;
#pragma unroll
          for (int i = 0; i < 16; ++i) key[i] = key[i] == mm ? 0u : key[i];
        }
        if (lane < 8) lds_wr32(L_CAND + l * 64 + lane * 4, cand);
      } else {
        const unsigned p1 = __builtin_amdgcn_mbcnt_hi((unsigned)(k1 >> 32), __builtin_amdgcn_mbcnt_lo((unsigned)k1, 0u));
        const unsigned p2 = __builtin_amdgcn_mbcnt_hi((unsigned)(k2 >> 32), __builtin_amdgcn_mbcnt_lo((unsigned)k2, (unsigned)n1));
        lds_wr32(m >= T ? L_CAND + l * 64 + p1 * 4 : L_JNK + lane * 4, m);
        if (k2) lds_wr32(m2 >= T ? L_CAND + l * 64 + p2 * 4 : L_JNK + lane * 4, m2);
      }
      if (lane == 0) lds_wr32(FL_TK + 8 + l * 4, 1u);
      if (W1P == 2) W1B();
      lds_wait_ge(FL_TK + 8 + (l ^ 1) * 4, 1u);
      const unsigned ck = *(const volatile LAS unsigned*)(smem + L_CAND + (lane & 31) * 4);
      int rank = 0;
#pragma unroll
      for (int i = 0; i < 4; ++i) {
        const u32x4 o = *(const volatile LAS u32x4*)(smem + L_CAND + (lane >> 5) * 64 + i * 16);
        rank += (o.x > ck) + (o.y > ck) + (o.z > ck) + (o.w > ck);
      }
      { auto p = __builtin_amdgcn_permlane32_swap((unsigned)rank, (unsigned)rank, false, false); rank = (int)(p[0] + p[1]); }
      if (l == 0) {
        if (lane < 32 && ck != 0u && rank < 8) {
          const int t = 2047 - (int)(ck & 0xffffu);
          lds_wr32(L_SEL + rank * 4, ck);
          if ((t >> 7) == c) lds_wr32(L_MAP + ((t & 7) * 16 + ((t & 127) >> 3)) * 4, (unsigned)(L_BROW + rank * 256));
        }
        const unsigned any = __ballot(ck != 0u && rank < 8 && ((2047 - (int)(ck & 0xffffu)) >> 7) == c) ? 2u : 1u;
        flag(FL_SEL, any);
        if (bid == 0 && lane == 0) st64c(rws, W_EPOCH, (u32x2){tagA, tagB});
      }
    }
    if (W1P == 3) W1B();
    lds_wait_ge(FL_SEL, 1u);
    auto probe = [&](unsigned o) {
#pragma unroll 1
      for (int it = 0; it < SPIN_MAX; ++it) {
        unsigned v = 0;
        asm volatile("buffer_atomic_or %0, %1, %2, 0 offen sc0 sc1\n\ts_waitcnt vmcnt(0)" : "+v"(v) : "v"(o), "s"(rws) : "memory");
        if (!__ballot(v == ~0u)) break;
        __builtin_amdgcn_s_sleep(PRS);
      }
    };
    const unsigned mine = lds_rd32(L_SEL + (lane & 7) * 4);
    {
      {
        u32x4 xa[4];
#pragma unroll
        for (int u = 0; u < 4; ++u)
          asm volatile("buffer_load_dwordx4 %0, %1, %2, 0 offen offset:%3" : "=v"(xa[u]) : "v"((2047 - (mine & 0xffffu)) * 2048 + (kq * 256 + 32 * (fr >> 3) + 8 * fq) * 2), "s"(rx), "n"(u * 128) : "memory");
        TRN(w1[0], W2P == 10 ? 36 : 20) TRN(w1[1], W2P == 10 ? 36 : 20) TRN(w1[2], W2P == 10 ? 28 : 12) TRN(w1[3], 0)
        asm volatile("" : "+v"(xa[0]), "+v"(xa[1]), "+v"(xa[2]), "+v"(xa[3]));
        const bool val = (fq >> 1) == (fr >> 3), lo = lane < 32;
        auto publish = [&](f32x4 (&d)[8], unsigned o, unsigned sp) {
          u32x4 pv[4];
#pragma unroll
          for (int rr = 0; rr < 2; ++rr)
#pragma unroll
            for (int q = 0; q < 8; ++q) {
              auto p = __builtin_amdgcn_permlane32_swap(__float_as_uint(d[q][rr]), __float_as_uint(d[q][2 + rr]), false, false);
              const unsigned own = lo ? p[0] : p[1], oth = lo ? p[1] : p[0];
              pv[rr * 2 + (q >> 2)][q & 3] = min(__float_as_uint(__uint_as_float(own) + dpp1<0x128>(__uint_as_float(oth))), 0xfffffffeu);
            }
          asm volatile("s_nop 4\n\t"
                       "buffer_store_dwordx4 %0, %4, %5, 0 offen sc1\n\tbuffer_store_dwordx4 %1, %4, %5, %6 offen sc1\n\t"
                       "buffer_store_dwordx4 %2, %4, %5, %7 offen sc1\n\tbuffer_store_dwordx4 %3, %4, %5, %8 offen sc1\n\t"
                       "s_nop 7\n\ts_nop 7"
                       ::"v"(pv[0]), "v"(pv[1]), "v"(pv[2]), "v"(pv[3]), "v"(val && ffn ? o : 0xf0000000u), "s"(rws), "s"(sp), "s"(2 * sp), "s"(3 * sp) : "memory");
        };
        f32x4 d[8];
#pragma unroll
        for (int q = 0; q < 8; ++q) d[q] = (f32x4){0.f, 0.f, 0.f, 0.f};
#pragma unroll
        for (int u = 0; u < 4; ++u)
#pragma unroll
          for (int q = 0; q < 8; ++q) d[q] = __builtin_amdgcn_mfma_f32_16x16x32_bf16(__builtin_bit_cast(bf16x8, xa[u]), __builtin_bit_cast(bf16x8, w1[u][q]), d[q], 0, 0, 0);
        publish(d, gub + (16 * (fr >> 3) + 8 * (fq & 1) + (fr & 7)) * 16, 512);
        if (W2P < 11) { TRN(w2[0], 63) TRN(w2[1], 63) }
        lds_wait_ge<LZ>(FL_CFREE, XG2);
        __builtin_amdgcn_s_setprio(XP3);
        if (W2P == 11) LDW2(0, 16)
        if (ffn) {
          f32x4 G, U;
          if (PRB & 1) probe(lane < 8 ? W_GU + sg * 16384 + lane * 2048 + xw * 1024 + 1020 : 0xf0000000u);
#pragma unroll 1
          for (int it = 0; it < SPIN_MAX; ++it) {
            FRESH();
            u32x4 a_[8];
#pragma unroll
            for (int k = 0; k < 8; ++k) a_[k] = __builtin_amdgcn_raw_buffer_load_b128(rws, W_GU + sg * 16384 + xw * 1024 + lane * 16, k * 2048, 16);
            unsigned m = 0;
#pragma unroll
            for (int k = 0; k < 8; ++k) m = max(max(m, max(a_[k].x, a_[k].y)), max(a_[k].z, a_[k].w));
            G = (__builtin_bit_cast(f32x4, a_[0]) + __builtin_bit_cast(f32x4, a_[2])) + (__builtin_bit_cast(f32x4, a_[4]) + __builtin_bit_cast(f32x4, a_[6]));
            U = (__builtin_bit_cast(f32x4, a_[1]) + __builtin_bit_cast(f32x4, a_[3])) + (__builtin_bit_cast(f32x4, a_[5]) + __builtin_bit_cast(f32x4, a_[7]));
            if (__all(m != ~0u)) break;
            if (PRB & 4) probe(lane < 8 ? W_GU + sg * 16384 + lane * 2048 + xw * 1024 + 1020 : 0xf0000000u); else __builtin_amdgcn_s_sleep(8);
          }
          if (W2P == 12) { lds_wait_ge<LZ>(FL_CFREE, W2G + W2S * (kq + 4 * xw) / 8); LDW2(0, 16) }
          float a4[4];
#pragma unroll
          for (int e = 0; e < 4; ++e) { const float g = rbf(G[e]); a4[e] = rbf(g * __builtin_amdgcn_rcpf(1.f + __expf(-g))) * rbf(U[e]); }
          asm volatile("s_nop 0\n\tds_write_b64 %0, %1" ::"v"(L_ACT + (4 * ((lane >> 3) & 1) + 2 * ((lane >> 4) & 1) + xw) * 128 + (lane & 7) * 16 + (lane >> 5) * 8),
                       "v"((u32x2){pk2(a4[0], a4[1]), pk2(a4[2], a4[3])}) : "memory");
          if (lane == 0) lds_wr32(FL_TK + xw * 4, 1u);
          lds_wait_ge(FL_TK + (xw ^ 1) * 4, 1u);
        }
        if (W2P >= 11) { TRN(w2[0], 8) TRN(w2[1], 0) }
        const u32x4 af0 = lds_rd128(L_ACT + ((fr & 7) * 8 + fq) * 16), af1 = lds_rd128(L_ACT + ((fr & 7) * 8 + 4 + fq) * 16);
#pragma unroll
        for (int q = 0; q < 8; ++q) d[q] = __builtin_amdgcn_mfma_f32_16x16x32_bf16(__builtin_bit_cast(bf16x8, af0), __builtin_bit_cast(bf16x8, w2[0][q]), (f32x4){0.f, 0.f, 0.f, 0.f}, 0, 0, 0);
#pragma unroll
        for (int q = 0; q < 8; ++q) d[q] = __builtin_amdgcn_mfma_f32_16x16x32_bf16(__builtin_bit_cast(bf16x8, af1), __builtin_bit_cast(bf16x8, w2[1][q]), d[q], 0, 0, 0);
        {
          u32x4 pv[4];
#pragma unroll
          for (int r = 0; r < 4; ++r)
#pragma unroll
            for (int q = 0; q < 4; ++q) pv[r][q] = min(__float_as_uint(lo ? d[q][r] : d[4 + q][r]), 0xfffffffeu);
          asm volatile("s_nop 4\n\t"
                       "buffer_store_dwordx4 %0, %4, %5, 0 offen sc1\n\tbuffer_store_dwordx4 %1, %4, %5, %6 offen sc1\n\t"
                       "buffer_store_dwordx4 %2, %4, %5, %7 offen sc1\n\tbuffer_store_dwordx4 %3, %4, %5, %8 offen sc1\n\t"
                       "s_nop 7\n\ts_nop 7"
                       ::"v"(pv[0]), "v"(pv[1]), "v"(pv[2]), "v"(pv[3]), "v"(ffn ? ypb + (fq & 1) * 4 * YTOK + (fr >> 3) * YTILE + (lane >> 5) * 128 + (fr & 7) * 16 : 0xf0000000u),
                       "s"(rws), "s"(YTOK), "s"(2 * YTOK), "s"(3 * YTOK) : "memory");
        }
      }
    }
    __builtin_amdgcn_s_setprio(XP4);
    unsigned rest = (unsigned)__ballot(((2047 - (int)(mine & 0xffffu)) >> 7) == c) & 0xffu;
    if (WIF == 7) rest = 0;
    if (xw) rest &= rest - 1;
#pragma unroll 1
    for (; rest; rest &= rest - 1, rest &= rest - 1) {
      const int s = __builtin_ctz(rest);
      const unsigned ks = (unsigned)__builtin_amdgcn_readlane((int)mine, s);
      const int t = 2047 - (int)(ks & 0xffffu);
      const float rs = rbf(1.f / (1.f + __expf(-key_val(ks))));
      const unsigned yb = W_YP + s * YTOK + j * YTILE;
      const u32x2 xr = __builtin_amdgcn_raw_buffer_load_b64(rx, t * 2048 + (j * 64 + 8 * (lane & 7) + 4 * ((lane >> 3) & 1)) * 2, 0, 0);
      lds_wait_ge<LZ>(FL_CFREE, YGATE);
      const float omg = rbf(1.f - __uint_as_float(lds_rd32(L_GT + ((t & 7) * 16 + ((t & 127) >> 3)) * 4)));
      f32x4 y;
#pragma unroll 1
      for (int it = 0; it < SPIN_MAX; ++it) {
        FRESH();
        unsigned m = 0;
        f32x4 y2[2] = {{0.f, 0.f, 0.f, 0.f}, {0.f, 0.f, 0.f, 0.f}};
#pragma unroll
        for (int k = 0; k < 14; ++k) {
          const u32x4 a = __builtin_amdgcn_raw_buffer_load_b128(rws, yb + lane * 16, k * 1024, 16);
          m = max(max(m, max(a.x, a.y)), max(a.z, a.w));
          y2[k & 1] += __builtin_bit_cast(f32x4, a);
        }
        y = y2[0] + y2[1];
        if (__all(m != ~0u)) break;
        __builtin_amdgcn_s_sleep(6);
      }
      const f32x4 xf = unpk4(xr);
      f32x4 dl;
#pragma unroll
      for (int e = 0; e < 4; ++e) {
        const float v = sum16_32(y[e]);
        dl[e] = rbf(mul1(omg, rbf(xf[e] + rbf(mul1(rbf(v), rs))))) - rbf(mul1(omg, xf[e]));
      }
      if (lane < 16) asm volatile("s_nop 0\n\tds_write_b128 %0, %1\n\ts_nop 3" ::"v"(L_BROW + s * 256 + (lane & 7) * 32 + (lane >> 3) * 16), "v"(dl) : "memory");
    }
    if (lane == 0) lds_wr32(FL_XD + xw * 4, 1u);
    asm volatile("Lk_x:");
    return;
  }
  }

  __builtin_amdgcn_s_setprio(3);
  relane();
  u64x8 ma, mb;
  asm volatile("s_load_dwordx16 %0, %2, 0x0\n\ts_load_dwordx16 %1, %2, 0x40\n\ts_waitcnt lgkmcnt(0)" : "=&s"(ma), "=&s"(mb) : "s"(__builtin_amdgcn_kernarg_segment_ptr()), "s"(kw0), "s"(kw1));
  const char* out = (const char*)mb[7];
  char* ws = (char*)mb[6];
  const rsrc_t rws = mk(ws, W_END);
  if (!MTOP) MLOADS(ma, mb);
  f32x4 lr, li, cr, ci, Er, Ei, Fr, Fi, br_, bi_;
  u32x2 xe[2][4];
  {
    asm volatile("s_waitcnt vmcnt(%3)" : "+v"(p0), "+v"(p1), "+v"(p2) : "n"(9) : "memory");
    const f32x4 lre = unpk4(p0), lim = unpk4(p1), ld = unpk4(p2) * splat4(1.44269504f);
    f32x4 dt, id = fma4(lre, lre, lim * lim);
#pragma unroll
    for (int r = 0; r < 4; ++r) { dt[r] = rbf(__builtin_amdgcn_exp2f(ld[r])); id[r] = __builtin_amdgcn_rcpf(id[r]); }
    const f32x4 A2 = dt * lre * splat4(1.44269504f), Bv = dt * lim * splat4(0.159154943f);
    cexp4(A2, Bv, lr, li);
    const f32x4 nr = lr - splat4(1.f);
    cr = fma4(nr, lre, li * lim) * id; ci = fma4(li, lre, nml4(nr, lim)) * id;
    const f32x4 ne = splat4((float)(8 * fr)), nf = splat4((float)((fr & 7) + 1));
    cexp4(A2 * ne, Bv * ne, Er, Ei);
    f32x4 tr, ti;
    cexp4(A2 * nf, Bv * nf, tr, ti);
    Fr = fma4(cr, tr, nml4(ci, ti)); Fi = fma4(cr, ti, ci * tr);
    const float a1 = (fr & 2) ? ((fr & 1) ? A2.w : A2.z) : ((fr & 1) ? A2.y : A2.x), b1 = (fr & 2) ? ((fr & 1) ? Bv.w : Bv.z) : ((fr & 1) ? Bv.y : Bv.x);
    const int e0 = c - 1 - (fr >> 2);
    const f32x4 nl = {(float)(128 * max(e0, 0)), (float)(128 * max(e0 - 4, 0)), (float)(128 * max(e0 - 8, 0)), (float)(128 * max(e0 - 12, 0))};
    cexp4(splat4(a1) * nl, splat4(b1) * nl, br_, bi_);
    asm volatile("" : "+v"(lr), "+v"(li), "+v"(cr), "+v"(ci), "+v"(Er), "+v"(Ei), "+v"(Fr), "+v"(Fi), "+v"(br_), "+v"(bi_));
  }
  asm volatile("s_waitcnt vmcnt(%9)" : "+v"(dv), "+v"(xv[0][0]), "+v"(xv[0][1]), "+v"(xv[1][0]), "+v"(xv[1][1]), "+v"(wv[0]), "+v"(wv[1]), "+v"(gv[0]), "+v"(gv[1]) : "n"(0) : "memory");
  {
    float sv[4];
#pragma unroll
    for (int r = 0; r < 2; ++r) {
      float sr = 0.f, sg_ = 0.f;
#pragma unroll
      for (int h = 0; h < 2; ++h)
#pragma unroll
        for (int e = 0; e < 4; ++e) {
          const unsigned xx = xv[r][h][e], ww = wv[h][e], gw = gv[h][e];
          sr = __builtin_amdgcn_fdot2_f32_bf16(__builtin_bit_cast(bf16x2_t, xx), __builtin_bit_cast(bf16x2_t, ww), sr, false);
          sg_ = __builtin_amdgcn_fdot2_f32_bf16(__builtin_bit_cast(bf16x2_t, xx), __builtin_bit_cast(bf16x2_t, gw), sg_, false);
        }
      sv[r] = sr; sv[2 + r] = sg_;
    }
    const bool b1 = lane & 1, b2 = lane & 2;
    const float k0 = (b1 ? sv[1] : sv[0]) + dpp1<0xb1>(b1 ? sv[0] : sv[1]), k1 = (b1 ? sv[3] : sv[2]) + dpp1<0xb1>(b1 ? sv[2] : sv[3]);
    float k2 = (b2 ? k1 : k0) + dpp1<0x4e>(b2 ? k0 : k1);
    k2 += dpp1<0x124>(k2);
    k2 += dpp1<0x128>(k2);
    k2 = sum16_32(k2);
    const unsigned vb = b2 ? f2bf(1.f / (1.f + __expf(-rbf(k2)))) : route_key(rbf(k2), 0) >> 16;
    const unsigned p01 = vb | ((unsigned)__builtin_amdgcn_update_dpp(0, (int)vb, 0xb1, 0xf, 0xf, true) << 16);
    asm volatile("ds_write_b32 %0, %1\n\ts_waitcnt lgkmcnt(0)" ::"v"((lane & 61) == 0 ? (unsigned)(L_RT + (lane * 2 + wid) * 4) : (unsigned)(L_JNK + lane * 4)), "v"(p01) : "memory");
  }
  __builtin_amdgcn_s_setprio(2);
  unsigned tagA = L_TAG;
  f32x4 acc[2][8];
  const unsigned ago = fr < 4 ? W_AGG + ((j * 16 + c) * 64 + wid * 16 + 4 * fq + fr) * 16u : 0xf0000000u;
  {
#pragma unroll
    for (int m = 0; m < 2; ++m)
#pragma unroll
      for (int n = 0; n < 8; ++n) acc[m][n] = (f32x4){0.f, 0.f, 0.f, 0.f};
    const int x0 = (fq ^ (fr & 7)) * 16, x1 = ((4 + fq) ^ (fr & 7)) * 16;
    unsigned pa0 = 16384 + (wid * 32 + fr) * 128 + x0, pa1 = 16384 + (wid * 32 + fr) * 128 + x1, pb0 = fr * 128 + x0, pb1 = fr * 128 + x1;
    unsigned sit, xad[4], s1[3], fv1 = 0;
#pragma unroll
    for (int r = 0; r < 4; ++r) xad[r] = 98304 + (32 * wid + 4 * fq + r) * 128 + (((fr >> 1) ^ (4 * (fq & 1) + r)) * 16) + (fr & 1) * 8;
    bf16x8 fa[2][2], fb[8];
    asm volatile(ASM_GEMM1 : [c00] "+v"(acc[0][0]), [c01] "+v"(acc[0][1]), [c02] "+v"(acc[0][2]), [c03] "+v"(acc[0][3]), [c04] "+v"(acc[0][4]), [c05] "+v"(acc[0][5]), [c06] "+v"(acc[0][6]), [c07] "+v"(acc[0][7]), [c10] "+v"(acc[1][0]), [c11] "+v"(acc[1][1]), [c12] "+v"(acc[1][2]), [c13] "+v"(acc[1][3]), [c14] "+v"(acc[1][4]), [c15] "+v"(acc[1][5]), [c16] "+v"(acc[1][6]), [c17] "+v"(acc[1][7]), [a00] "=&v"(fa[0][0]), [a01] "=&v"(fa[0][1]), [a10] "=&v"(fa[1][0]), [a11] "=&v"(fa[1][1]), [b0] "=&v"(fb[0]), [b1] "=&v"(fb[1]), [b2] "=&v"(fb[2]), [b3] "=&v"(fb[3]), [b4] "=&v"(fb[4]), [b5] "=&v"(fb[5]), [b6] "=&v"(fb[6]), [b7] "=&v"(fb[7]), [pa0] "+v"(pa0), [pa1] "+v"(pa1), [pb0] "+v"(pb0), [pb1] "+v"(pb1), [it] "=&s"(sit), [tg] "+v"(tagA), [xe00] "=&v"(xe[0][0]), [xe01] "=&v"(xe[0][1]), [xe02] "=&v"(xe[0][2]), [xe03] "=&v"(xe[0][3]), [xe10] "=&v"(xe[1][0]), [xe11] "=&v"(xe[1][1]), [xe12] "=&v"(xe[1][2]), [xe13] "=&v"(xe[1][3]) : [xa0] "v"(xad[0]), [xa1] "v"(xad[1]), [xa2] "v"(xad[2]), [xa3] "v"(xad[3]) : "memory", "scc", "v252", "v253", "v254", "v255");
  }
  __builtin_amdgcn_s_setprio(3);
  const unsigned tagB = tagA + 0x40000000u;
  {
#pragma unroll
    for (int n = 1; n < 8; ++n) {
      const f32x4 pr = acc[0][n - 1], pi = acc[1][n - 1];
      acc[0][n] = fma4(lr, pr, fnm4(li, pi, acc[0][n]));
      acc[1][n] = fma4(lr, pi, fma4(li, pr, acc[1][n]));
    }
    const f32x4 wr = dpp4<0x140>(Er), wi = dpp4<0x140>(Ei);
    const f32x4 zr = fma4(wr, acc[0][7], nml4(wi, acc[1][7])), zi = fma4(wr, acc[1][7], wi * acc[0][7]);
    const bool o1 = fr & 1, o2 = fr & 2;
    const float r01 = (o1 ? zr.y : zr.x) + dpp1<0xb1>(o1 ? zr.x : zr.y), r23 = (o1 ? zr.w : zr.z) + dpp1<0xb1>(o1 ? zr.z : zr.w);
    const float i01 = (o1 ? zi.y : zi.x) + dpp1<0xb1>(o1 ? zi.x : zi.y), i23 = (o1 ? zi.w : zi.z) + dpp1<0xb1>(o1 ? zi.z : zi.w);
    float sr = (o2 ? r23 : r01) + dpp1<0x4e>(o2 ? r01 : r23), si = (o2 ? i23 : i01) + dpp1<0x4e>(o2 ? i01 : i23);
    sr += dpp1<0x124>(sr); si += dpp1<0x124>(si);
    sr += dpp1<0x128>(sr); si += dpp1<0x128>(si);
    u32x4 av = {__float_as_uint(sr), tagA, __float_as_uint(si), tagB};
    asm volatile("s_nop 1\n\tbuffer_store_dwordx4 %0, %1, %2, 0 offen sc1\n\ts_nop 7" : "+v"(av) : "v"(ago), "s"(rws) : "memory");
  }
  unsigned ao[4];
  u32x4 g[4];
#pragma unroll
  for (int i = 0; i < 4; ++i) {
    ao[i] = (fr >> 2) + 4 * i < c ? W_AGG + ((j * 16 + (fr >> 2) + 4 * i) * 64 + wid * 16 + 4 * fq + (fr & 3)) * 16u : 0xf0000000u;
    g[i] = (u32x4){0u, 0u, 0u, 0u};
  }
  if (POLLN < 0) {
    FRESH();
#pragma unroll
    for (int i = 0; i < 4; ++i)
      if (4 * i < c) g[i] = ld128c(rws, ao[i]);
    FRESH();
  }
  f32x4 Pr, Pi, qr[8], qi[8];
  {
    f32x4 yr = acc[0][7], yi = acc[1][7];
#define KS_STEP(S, CTRL) { const f32x4 mr = dpp4<0x150 + S>(Er), mi = dpp4<0x150 + S>(Ei); const f32x4 sr = dpp4<CTRL>(yr), si = dpp4<CTRL>(yi); \
      yr = fma4(mr, sr, fnm4(mi, si, yr)); yi = fma4(mr, si, fma4(mi, sr, yi)); }
    KS_STEP(1, 0x111) KS_STEP(2, 0x112) KS_STEP(4, 0x114) KS_STEP(8, 0x118)
    Pr = dpp4<0x111>(yr); Pi = dpp4<0x111>(yi);
    asm volatile("" : "+v"(Pr), "+v"(Pi));
#pragma unroll
    for (int n = 0; n < 8; ++n) {
      if (n == POLLN) {
        FRESH();
#pragma unroll
        for (int i = 0; i < 4; ++i)
          if (4 * i < c) g[i] = ld128c(rws, ao[i]);
        FRESH();
      }
      bcast8(n, Fr, qr[n]); bcast8(n, Fi, qi[n]);
      const f32x4 br = fma4(cr, acc[0][n], nml4(ci, acc[1][n])), bi = fnm4(cr, acc[1][n], nml4(ci, acc[0][n]));
      acc[0][n] = br; acc[1][n] = bi;
      asm volatile("" : "+v"(acc[0][n]), "+v"(acc[1][n]), "+v"(qr[n]), "+v"(qi[n]));
    }
  }
  f32x4 Xr = Pr, Xi = Pi;
  if (POLLN == 8) {
    FRESH();
#pragma unroll
    for (int i = 0; i < 4; ++i)
      if (4 * i < c) g[i] = ld128c(rws, ao[i]);
    FRESH();
  }
  u32x2 xd[2][4];
  lds_wait_ge(FL_G, 2u);
  {
    const f32x4 df = unpk4(dv) * splat4(0.5f);
#pragma unroll
    for (int m = 0; m < 2; ++m) {
      const f32x4 ge = __builtin_bit_cast(f32x4, lds_rd128(L_GT + ((2 * wid + m) * 16 + 4 * fq) * 4));
#pragma unroll
      for (int r = 0; r < 4; ++r) {
        const f32x4 xf = unpk4(xe[m][r]), p = xf * df, o = xf * splat4(rbf(1.f - ge[r]));
        xd[m][r] = (u32x2){pk2(p.x, p.y), pk2(p.z, p.w)};
        xe[m][r] = (u32x2){pk2(o.x, o.y), pk2(o.z, o.w)};
        asm volatile("" : "+v"(xd[m][r]), "+v"(xe[m][r]));
      }
    }
  }
  if (POLLN > 9) __builtin_amdgcn_s_sleep(POLLN > 9 ? POLLN - 9 : 0);
  if (c > 0) {
#pragma unroll 1
    for (int it = 0; it < SPIN_MAX; ++it) {
      unsigned bad = 0;
#pragma unroll
      for (int i = 0; i < 4; ++i) bad |= ((g[i].y ^ tagA) | (g[i].w ^ tagB)) & ~(unsigned)((int)ao[i] >> 31);
      if (!__ballot(bad != 0u)) break;
      if (LBP && it >= LBP - 1) {
#pragma unroll 1
        for (int i2 = 0; i2 < SPIN_MAX; ++i2) {
          unsigned v = 0;
          asm volatile("buffer_atomic_or %0, %1, %2, 0 offen sc0 sc1\n\ts_waitcnt vmcnt(0)" : "+v"(v) : "v"(lane < c ? W_AGG + ((j * 16 + lane) * 64 + wid * 16 + 15) * 16u + 12u : 0xf0000000u), "s"(rws) : "memory");
          if (!__ballot(lane < c && v != tagB)) break;
        }
      }
      FRESH();
#pragma unroll
      for (int i = 0; i < 4; ++i)
        if (4 * i < c) g[i] = ld128c(rws, ao[i]);
    }
    const f32x4 sx = {__uint_as_float(g[0].x), __uint_as_float(g[1].x), __uint_as_float(g[2].x), __uint_as_float(g[3].x)};
    const f32x4 sy = {__uint_as_float(g[0].z), __uint_as_float(g[1].z), __uint_as_float(g[2].z), __uint_as_float(g[3].z)};
    const f32x4 tx = fma4(br_, sx, nml4(bi_, sy)), ty = fma4(br_, sy, bi_ * sx);
    float gx = (tx.x + tx.z) + (tx.y + tx.w), gy = (ty.x + ty.z) + (ty.y + ty.w);
    gx += dpp1<0x124>(gx); gy += dpp1<0x124>(gy);
    gx += dpp1<0x128>(gx); gy += dpp1<0x128>(gy);
    const f32x4 Gr = {dpp1<0x00>(gx), dpp1<0x55>(gx), dpp1<0xaa>(gx), dpp1<0xff>(gx)}, Gi = {dpp1<0x00>(gy), dpp1<0x55>(gy), dpp1<0xaa>(gy), dpp1<0xff>(gy)};
    Xr = fma4(Er, Gr, fnm4(Ei, Gi, Pr)); Xi = fma4(Er, Gi, fma4(Ei, Gr, Pi));
  }
  relane();
  const unsigned jnk = L_JNK + lane * 4;
  const unsigned ho = WIF == 3 ? 0xf0000000u : W_H + c * 524288 + (4 * pown + wid) * 8192 + lane * 16;
  u32x4 hv[8];
  __builtin_amdgcn_sched_barrier(0);
#define HST(t_) asm volatile("s_nop 1\n\tbuffer_store_dwordx4 %0, %1, %2, %3 offen offset:%4 sc1" : "+v"(hv[t_]) : "v"(ho), "s"(rws), "s"(((t_) >> 2) * 4096), "n"(((t_) & 3) * 1024) : "memory")
#pragma unroll
  for (int n = 0; n < 8; ++n) {
    const f32x4 hr = fma4(qr[n], Xr, fnm4(qi[n], Xi, acc[0][n])), hi = fnm4(qr[n], Xi, fnm4(qi[n], Xr, acc[1][n]));
    const auto p = __builtin_amdgcn_permlane16_swap(pk2(hr.x, hr.y), pk2(hi.x, hi.y), false, false);
    const auto q = __builtin_amdgcn_permlane16_swap(pk2(hr.z, hr.w), pk2(hi.z, hi.w), false, false);
    hv[n] = (u32x4){NS ? min(p[0], 0xfffffffeu) : p[0], q[0], NS ? min(p[1], 0xfffffffeu) : p[1], q[1]};
    if (NSI == 8) HST(n); else if (NSI == 4 ? (n & 1) : n < NSI) HST(NSI == 4 ? n >> 1 : n);
    __builtin_amdgcn_sched_barrier(0);
    if (!DSL && (n >> 1) != wid) asm volatile("ds_write_b128 %0, %1" ::"v"(L_XH + (((n >> 1) * 3 + wid - (wid > (n >> 1) ? 1 : 0)) * 2 + (n & 1)) * 1024 + lane * 16), "v"(hv[n]) : "memory");
  }
  if (DSL) {
#pragma unroll
    for (int w = 0; w < 4; ++w)
      if (w != wid) asm volatile("ds_write_b128 %0, %1\n\tds_write_b128 %0, %2 offset:1024" ::"v"(L_XH + (w * 3 + wid - (wid > w ? 1 : 0)) * 2048 + lane * 16), "v"(hv[2 * w]), "v"(hv[2 * w + 1]) : "memory");
  }
  if (lane == 0) lds_wr32(FL_HS + wid * 4, 1u);
  const rsrc_t rh = mk(ws + W_H + c * 524288, 524288);
  const unsigned vo = wid * 2048 + lane * 16;
  u32x32 hw0, hw1;
  unsigned st[12], pc[4];
  f32x4 ac2[2][4];
  const u64 mb_ = 1ull << (4 * pown + wid);
  u32x2 mf = {(unsigned)mb_, (unsigned)(mb_ >> 32)};
#define ACK() do { asm volatile("s_waitcnt vmcnt(0)" : "+v"(hv[0]), "+v"(hv[1]), "+v"(hv[2]), "+v"(hv[3]), "+v"(hv[4]), "+v"(hv[5]), "+v"(hv[6]), "+v"(hv[7])::"memory"); \
    if (BM) asm volatile("buffer_atomic_or_x2 %0, %1, %2, 0 offen sc0 sc1" : "+v"(mf) : "v"(lane == 0 ? W_HF + c * 128 : 0xf0000000u), "s"(rws) : "memory"); else \
    asm volatile("buffer_store_dwordx2 %0, %1, %2, 0 offen sc1" ::"v"((u32x2){tagA, tagB}), "v"(lane == 0 ? W_HF + (c * 16 + pown) * FSB + wid * FSW : 0xf0000000u), "s"(rws) : "memory"); \
    lds_wr32(lane == 0 ? (unsigned)(FL_FREE + wid * 4) : jnk, 9u); \
    if (NS) { if (RQD) __builtin_amdgcn_s_sleep(RQD); asm volatile(ASM_REQ : "=&{v[184:215]}"(hw0), "=&{v[216:247]}"(hw1), [hb0] "=&s"(st[0]), [hb1] "=&s"(st[1]), [hb2] "=&s"(st[2]), [hb3] "=&s"(st[3]) : [vo] "v"(vo), [own] "s"(pown), [srd] "s"(rh) : "memory", "scc"); } } while (0)
#pragma unroll
  for (int sg = 0; sg < 4; ++sg) pc[sg] = (fq & 1) * 8192 + fr * 128 + (((2 * sg + (fq >> 1)) ^ ((fr >> 1) & 7)) * 16);
  {
    lds_wait_ge(FL_C, 1u);
    u32x4 k[4][4], h[4][2];
#pragma unroll
    for (int i = 0; i < 4; ++i) {
      const int sw = i == 0 ? wid : i - (i <= wid ? 1 : 0);
      asm volatile("ds_read_b128 %0, %4\n\tds_read_b128 %1, %4 offset:2048\n\tds_read_b128 %2, %4 offset:4096\n\tds_read_b128 %3, %4 offset:6144"
                   : "=&v"(k[i][0]), "=&v"(k[i][1]), "=&v"(k[i][2]), "=&v"(k[i][3]) : "v"(pc[0] ^ (sw * 32)) : "memory");
    }
#pragma unroll
    for (int m = 0; m < 2; ++m)
#pragma unroll
      for (int r = 0; r < 4; ++r) {
        const f32x4 t = unpk4(xd[m][r]);
#pragma unroll
        for (int n = 0; n < 4; ++n) ac2[m][n][r] = t[n];
      }
    h[0][0] = hv[0]; h[0][1] = hv[1];
    if (wid == 1) { h[0][0] = hv[2]; h[0][1] = hv[3]; }
    if (wid == 2) { h[0][0] = hv[4]; h[0][1] = hv[5]; }
    if (wid == 3) { h[0][0] = hv[6]; h[0][1] = hv[7]; }
    asm volatile("" : "+v"(ac2[0][0]), "+v"(ac2[0][1]), "+v"(ac2[0][2]), "+v"(ac2[0][3]), "+v"(ac2[1][0]), "+v"(ac2[1][1]), "+v"(ac2[1][2]), "+v"(ac2[1][3]), "+v"(h[0][0]), "+v"(h[0][1]));
    if (NSI < 8) HST(NSI);
    if (MGP == 1) ACK();
    lds_wait_min4(FL_HS, 1u);
    asm volatile("ds_read_b128 %0, %6\n\tds_read_b128 %1, %6 offset:1024\n\tds_read_b128 %2, %6 offset:2048\n\tds_read_b128 %3, %6 offset:3072\n\tds_read_b128 %4, %6 offset:4096\n\tds_read_b128 %5, %6 offset:5120"
                 : "=&v"(h[1][0]), "=&v"(h[1][1]), "=&v"(h[2][0]), "=&v"(h[2][1]), "=&v"(h[3][0]), "=&v"(h[3][1]) : "v"(L_XH + wid * 6144 + lane * 16) : "memory");
    lds_wr32(lane == 0 ? (unsigned)(FL_CFREE + wid * 4) : jnk, 1u);
#pragma unroll
    for (int i = 0; i < 4; ++i) {
      if (i == 1) asm volatile("s_waitcnt lgkmcnt(0)" : "+v"(h[1][0]), "+v"(h[1][1]), "+v"(h[2][0]), "+v"(h[2][1]), "+v"(h[3][0]), "+v"(h[3][1])::"memory");
      asm volatile("" : "+v"(k[i][0]), "+v"(k[i][1]), "+v"(k[i][2]), "+v"(k[i][3]));
#pragma unroll
      for (int n = 0; n < 4; ++n) {
        ac2[0][n] = __builtin_amdgcn_mfma_f32_16x16x32_bf16(__builtin_bit_cast(bf16x8, h[i][0]), __builtin_bit_cast(bf16x8, k[i][n]), ac2[0][n], 0, 0, 0);
        ac2[1][n] = __builtin_amdgcn_mfma_f32_16x16x32_bf16(__builtin_bit_cast(bf16x8, h[i][1]), __builtin_bit_cast(bf16x8, k[i][n]), ac2[1][n], 0, 0, 0);
      }
      asm volatile("" : "+v"(ac2[0][0]), "+v"(ac2[0][1]), "+v"(ac2[0][2]), "+v"(ac2[0][3]), "+v"(ac2[1][0]), "+v"(ac2[1][1]), "+v"(ac2[1][2]), "+v"(ac2[1][3])::"memory");
      if (NSI + 1 + i < 8) HST(NSI + 1 + i < 8 ? NSI + 1 + i : 0);
    }
    static_assert(NSI >= 3 && (MGP == 0 || NSI >= 7), "all stores");
  }
  if (MGP == 0) ACK();
  f32x4 ov[2][4];
  float g2[2][4];
#pragma unroll
  for (int m = 0; m < 2; ++m) {
    const f32x4 ge = lds4(smem, L_GT + ((2 * wid + m) * 16 + 4 * fq) * 4);
#pragma unroll
    for (int r = 0; r < 4; ++r) {
      ov[m][r] = unpk4(xe[m][r]);
      g2[m][r] = ge[r] + ge[r];
      asm volatile("" : "+v"(ov[m][r]), "+v"(g2[m][r]));
    }
  }
  unsigned ra[2][4];
  lds_wait_ge(FL_SEL, 1u);
  const unsigned routed = __builtin_amdgcn_readfirstlane(lds_rd32(FL_SEL));
#pragma unroll
  for (int m = 0; m < 2; ++m) {
    const u32x4 me = lds_rd128(L_MAP + ((2 * wid + m) * 16 + 4 * fq) * 4);
#pragma unroll
    for (int r = 0; r < 4; ++r) { ra[m][r] = me[r] + fr * 16; asm volatile("" : "+v"(ra[m][r])); }
  }
  const rsrc_t ro = mk(out, 2048 * 1024 * 4);
  unsigned oo = (c * 128 + 32 * fq + 2 * wid) * 4096 + (j * 64 + 4 * fr) * 4;
  if (WIF == 1) oo = 0xf0000000u;
  if (WIF == 2 && (fq & 1)) oo = 0xf0000000u;
  asm volatile("" : "+v"(oo) : "s"(ro));
  if (BM) {
    asm volatile("s_waitcnt vmcnt(0)" : "+v"(mf)::"memory");
    asm volatile("ds_max_u32 %0, %1" ::"v"(lane == 0 ? (unsigned)(FL_C + 8) : jnk), "v"(lead(mf)) : "memory");
  }
  if (MPW && wid == MPV) {
#pragma unroll 1
    for (int it = 0; it < SPIN_MAX; ++it) {
      u32x2 f = {0u, 0u};
      asm volatile("buffer_atomic_or_x2 %0, %1, %2, 0 offen sc0 sc1\n\ts_waitcnt vmcnt(0)" : "+v"(f) : "v"(W_HF + (c * 16 + upos(lane >> 2)) * FSB + (lane & 3) * FSW), "s"(rws) : "memory");
      const u64 m = ~__ballot(lane < 4 || (f.x == tagA && f.y == tagB));
      const unsigned n = m ? ((unsigned)__builtin_ctzll(m) >> 2) - 1u : 99u;
      asm volatile("ds_max_u32 %0, %1" ::"v"(lane == 0 ? (unsigned)(FL_C + 8) : jnk), "v"(n) : "memory");
      if (n >= MPW) break;
    }
  }
  if (wid < MPS) __builtin_amdgcn_s_setprio(MPRIO2);

  {
    const unsigned fad = FL_C, fwd = lane == 0 ? (unsigned)(FL_CFREE + wid * 4) : jnk;
    bf16x8 kb[2][4];
    unsigned fv = 1;
#pragma unroll
    for (int sg = 0; sg < 4; ++sg) pc[sg] += 16384;
    asm volatile(ASM_GEMM2 : [c00] "+v"(ac2[0][0]), [c01] "+v"(ac2[0][1]), [c02] "+v"(ac2[0][2]), [c03] "+v"(ac2[0][3]), [c10] "+v"(ac2[1][0]), [c11] "+v"(ac2[1][1]), [c12] "+v"(ac2[1][2]), [c13] "+v"(ac2[1][3]), "=&{v[184:215]}"(hw0), "=&{v[216:247]}"(hw1), [k00] "=&v"(kb[0][0]), [k01] "=&v"(kb[0][1]), [k02] "=&v"(kb[0][2]), [k03] "=&v"(kb[0][3]), [k10] "=&v"(kb[1][0]), [k11] "=&v"(kb[1][1]), [k12] "=&v"(kb[1][2]), [k13] "=&v"(kb[1][3]), [pc0] "+v"(pc[0]), [pc1] "+v"(pc[1]), [pc2] "+v"(pc[2]), [pc3] "+v"(pc[3]), [fv] "+v"(fv), [s] "=&s"(st[0]), [s2] "=&s"(st[1]), [to] "=&s"(st[2]), [hn] "=&s"(st[3]), [cn] "=&s"(st[4]), [dl] "=&s"(st[5]), [nf] "=&s"(st[6]), [hr] "=&s"(st[7]), [hb0] "=&s"(st[8]), [hb1] "=&s"(st[9]), [hb2] "=&s"(st[10]), [hb3] "=&s"(st[11]) : [vo] "v"(vo), [own] "s"(pown), [srd] "s"(rh), [fa] "v"(fad), [fw] "v"(fwd), [d16] "s"(0x4000), [wr] "s"(0x500) : "memory", "scc", "vcc", "v250", "v251", "v252", "v253", "v254", "v255");
  }
  if (routed > 1u && WIF != 7) {
#pragma unroll 1
    for (int it = 0; it < SPIN_MAX * 4; ++it) {
      u32x2 f;
      f32x4 d[8];
      asm volatile("ds_read_b64 %8, %17\n\tds_read_b128 %0, %9\n\tds_read_b128 %1, %10\n\tds_read_b128 %2, %11\n\tds_read_b128 %3, %12\n\t"
                   "ds_read_b128 %4, %13\n\tds_read_b128 %5, %14\n\tds_read_b128 %6, %15\n\tds_read_b128 %7, %16\n\ts_waitcnt lgkmcnt(0)"
                   : "=&v"(d[0]), "=&v"(d[1]), "=&v"(d[2]), "=&v"(d[3]), "=&v"(d[4]), "=&v"(d[5]), "=&v"(d[6]), "=&v"(d[7]), "=&v"(f)
                   : "v"(ra[0][0]), "v"(ra[0][1]), "v"(ra[0][2]), "v"(ra[0][3]), "v"(ra[1][0]), "v"(ra[1][1]), "v"(ra[1][2]), "v"(ra[1][3]), "v"(FL_XD) : "memory");
      if (__builtin_amdgcn_readfirstlane(f.x & f.y)) {
#pragma unroll
        for (int e = 0; e < 8; ++e) ov[e >> 2][e & 3] += d[e];
        break;
      }
    }
  }
#pragma unroll
  for (int r = 0; r < 4; ++r)
#pragma unroll
    for (int m = 0; m < 2; ++m) {
#pragma unroll
      for (int n = 0; n < 4; ++n) ov[m][r][n] = fma1(g2[m][r], ac2[m][n][r], ov[m][r][n]);
      asm volatile("s_nop 1\n\tbuffer_store_dwordx4 %0, %1, %2, %3 offen" OST : "+v"(ov[m][r]) : "v"(oo), "s"(ro), "s"(r * 32768 + m * 4096) : "memory");
      __builtin_amdgcn_sched_barrier(0);
    }
  asm volatile("s_nop 7\n\ts_nop 7" ::"v"(ov[0][0]), "v"(ov[0][1]), "v"(ov[0][2]), "v"(ov[0][3]), "v"(ov[1][0]), "v"(ov[1][1]), "v"(ov[1][2]), "v"(ov[1][3]));
  asm volatile("Lk_m:");
#endif
}

extern "C" void kernel_launch(void* const* d_in, const int* in_sizes, int n_in, void* d_out, int out_size, void* d_ws, size_t ws_size,
                              hipStream_t stream) {
  (void)hipFuncSetAttribute((const void*)fused, hipFuncAttributeMaxDynamicSharedMemorySize, LDS_BYTES);
  fused<<<256, 512, LDS_BYTES, stream>>>((const u16*)d_in[0], (const u16*)d_in[1], (const u16*)d_in[2], (const u16*)d_in[3], (const u16*)d_in[4],
                                         (const u16*)d_in[5], (const u16*)d_in[6], (const u16*)d_in[7], (const u16*)d_in[8], (const u16*)d_in[10],
                                         (const u16*)d_in[11], (const u16*)d_in[12], (const u16*)d_in[13], (const u16*)d_in[14], (char*)d_ws + WS_BASE(ws_size),
                                         (float*)d_out);
}
```

```cpp
#include <hip/hip_runtime.h>
#include <hip/hip_bf16.h>
#include <stdint.h>

typedef __attribute__((ext_vector_type(8))) short bf16x8;
typedef __attribute__((ext_vector_type(4))) float f32x4;
typedef __attribute__((ext_vector_type(2))) float f32x2;
typedef __attribute__((ext_vector_type(4))) unsigned u32x4;
typedef __attribute__((ext_vector_type(2))) unsigned u32x2;
typedef __attribute__((ext_vector_type(32))) unsigned u32x32;
typedef unsigned short u16;
typedef unsigned long long u64;
typedef __attribute__((ext_vector_type(8))) unsigned long long u64x8;
#define LAS __attribute__((address_space(3)))
#define DEV __device__ __forceinline__

#define ASM_GEMM1 \
  "s_mov_b32 %[it], 0\n\t" \
  "s_barrier\n\t" \
  "ds_read_b128 %[a00], %[pa0] offset:0\n\t" \
  "ds_read_b128 %[a01], %[pa0] offset:2048\n\t" \
  "ds_read_b128 %[b0], %[pb0] offset:0\n\t" \
  "ds_read_b128 %[b1], %[pb0] offset:2048\n\t" \
  "ds_read_b128 %[b2], %[pb0] offset:4096\n\t" \
  "ds_read_b128 %[b3], %[pb0] offset:6144\n\t" \
  "ds_read_b128 %[b4], %[pb0] offset:8192\n\t" \
  "ds_read_b128 %[b5], %[pb0] offset:10240\n\t" \
  "ds_read_b128 %[b6], %[pb0] offset:12288\n\t" \
  "ds_read_b128 %[b7], %[pb0] offset:14336\n\t" \
  "Lg1_%=:\n\t" \
  "ds_read_b128 %[a10], %[pa1] offset:0\n\t" \
  "ds_read_b128 %[a11], %[pa1] offset:2048\n\t" \
  "s_waitcnt lgkmcnt(8)\n\t" \
  "v_mfma_f32_16x16x32_bf16 %[c00], %[a00], %[b0], %[c00]\n\t" \
  "v_mfma_f32_16x16x32_bf16 %[c10], %[a01], %[b0], %[c10]\n\t" \
  "ds_read_b128 %[b0], %[pb1] offset:0\n\t" \
  "v_mfma_f32_16x16x32_bf16 %[c01], %[a00], %[b1], %[c01]\n\t" \
  "v_mfma_f32_16x16x32_bf16 %[c11], %[a01], %[b1], %[c11]\n\t" \
  "ds_read_b128 %[b1], %[pb1] offset:2048\n\t" \
  "s_waitcnt lgkmcnt(8)\n\t" \
  "v_mfma_f32_16x16x32_bf16 %[c02], %[a00], %[b2], %[c02]\n\t" \
  "v_mfma_f32_16x16x32_bf16 %[c12], %[a01], %[b2], %[c12]\n\t" \
  "ds_read_b128 %[b2], %[pb1] offset:4096\n\t" \
  "v_mfma_f32_16x16x32_bf16 %[c03], %[a00], %[b3], %[c03]\n\t" \
  "v_mfma_f32_16x16x32_bf16 %[c13], %[a01], %[b3], %[c13]\n\t" \
  "ds_read_b128 %[b3], %[pb1] offset:6144\n\t" \
  "s_waitcnt lgkmcnt(8)\n\t" \
  "v_mfma_f32_16x16x32_bf16 %[c04], %[a00], %[b4], %[c04]\n\t" \
  "v_mfma_f32_16x16x32_bf16 %[c14], %[a01], %[b4], %[c14]\n\t" \
  "ds_read_b128 %[b4], %[pb1] offset:8192\n\t" \
  "v_mfma_f32_16x16x32_bf16 %[c05], %[a00], %[b5], %[c05]\n\t" \
  "v_mfma_f32_16x16x32_bf16 %[c15], %[a01], %[b5], %[c15]\n\t" \
  "ds_read_b128 %[b5], %[pb1] offset:10240\n\t" \
  "s_waitcnt lgkmcnt(8)\n\t" \
  "v_mfma_f32_16x16x32_bf16 %[c06], %[a00], %[b6], %[c06]\n\t" \
  "v_mfma_f32_16x16x32_bf16 %[c16], %[a01], %[b6], %[c16]\n\t" \
  "ds_read_b128 %[b6], %[pb1] offset:12288\n\t" \
  "v_mfma_f32_16x16x32_bf16 %[c07], %[a00], %[b7], %[c07]\n\t" \
  "v_mfma_f32_16x16x32_bf16 %[c17], %[a01], %[b7], %[c17]\n\t" \
  "ds_read_b128 %[b7], %[pb1] offset:14336\n\t" \
  "s_waitcnt lgkmcnt(6)\n\t" \
  "v_mfma_f32_16x16x32_bf16 %[c00], %[a10], %[b0], %[c00]\n\t" \
  "v_mfma_f32_16x16x32_bf16 %[c10], %[a11], %[b0], %[c10]\n\t" \
  "v_mfma_f32_16x16x32_bf16 %[c01], %[a10], %[b1], %[c01]\n\t" \
  "v_mfma_f32_16x16x32_bf16 %[c11], %[a11], %[b1], %[c11]\n\t" \
  "s_waitcnt lgkmcnt(4)\n\t" \
  "v_mfma_f32_16x16x32_bf16 %[c02], %[a10], %[b2], %[c02]\n\t" \
  "v_mfma_f32_16x16x32_bf16 %[c12], %[a11], %[b2], %[c12]\n\t" \
  "s_waitcnt lgkmcnt(0)\n\t" \
  "s_barrier\n\t" \
  "ds_read_b128 %[a00], %[pa0] offset:32768\n\t" \
  "ds_read_b128 %[a01], %[pa0] offset:34816\n\t" \
  "v_mfma_f32_16x16x32_bf16 %[c03], %[a10], %[b3], %[c03]\n\t" \
  "v_mfma_f32_16x16x32_bf16 %[c13], %[a11], %[b3], %[c13]\n\t" \
  "ds_read_b128 %[b0], %[pb0] offset:32768\n\t" \
  "ds_read_b128 %[b1], %[pb0] offset:34816\n\t" \
  "v_mfma_f32_16x16x32_bf16 %[c04], %[a10], %[b4], %[c04]\n\t" \
  "v_mfma_f32_16x16x32_bf16 %[c14], %[a11], %[b4], %[c14]\n\t" \
  "ds_read_b128 %[b2], %[pb0] offset:36864\n\t" \
  "ds_read_b128 %[b3], %[pb0] offset:38912\n\t" \
  "v_mfma_f32_16x16x32_bf16 %[c05], %[a10], %[b5], %[c05]\n\t" \
  "v_mfma_f32_16x16x32_bf16 %[c15], %[a11], %[b5], %[c15]\n\t" \
  "ds_read_b128 %[b4], %[pb0] offset:40960\n\t" \
  "ds_read_b128 %[b5], %[pb0] offset:43008\n\t" \
  "v_mfma_f32_16x16x32_bf16 %[c06], %[a10], %[b6], %[c06]\n\t" \
  "v_mfma_f32_16x16x32_bf16 %[c16], %[a11], %[b6], %[c16]\n\t" \
  "ds_read_b128 %[b6], %[pb0] offset:45056\n\t" \
  "v_mfma_f32_16x16x32_bf16 %[c07], %[a10], %[b7], %[c07]\n\t" \
  "v_mfma_f32_16x16x32_bf16 %[c17], %[a11], %[b7], %[c17]\n\t" \
  "ds_read_b128 %[b7], %[pb0] offset:47104\n\t" \
  "ds_read_b128 %[a10], %[pa1] offset:32768\n\t" \
  "ds_read_b128 %[a11], %[pa1] offset:34816\n\t" \
  "s_waitcnt lgkmcnt(8)\n\t" \
  "v_mfma_f32_16x16x32_bf16 %[c00], %[a00], %[b0], %[c00]\n\t" \
  "v_mfma_f32_16x16x32_bf16 %[c10], %[a01], %[b0], %[c10]\n\t" \
  "ds_read_b128 %[b0], %[pb1] offset:32768\n\t" \
  "v_mfma_f32_16x16x32_bf16 %[c01], %[a00], %[b1], %[c01]\n\t" \
  "v_mfma_f32_16x16x32_bf16 %[c11], %[a01], %[b1], %[c11]\n\t" \
  "ds_read_b128 %[b1], %[pb1] offset:34816\n\t" \
  "s_waitcnt lgkmcnt(8)\n\t" \
  "v_mfma_f32_16x16x32_bf16 %[c02], %[a00], %[b2], %[c02]\n\t" \
  "v_mfma_f32_16x16x32_bf16 %[c12], %[a01], %[b2], %[c12]\n\t" \
  "ds_read_b128 %[b2], %[pb1] offset:36864\n\t" \
  "v_mfma_f32_16x16x32_bf16 %[c03], %[a00], %[b3], %[c03]\n\t" \
  "v_mfma_f32_16x16x32_bf16 %[c13], %[a01], %[b3], %[c13]\n\t" \
  "ds_read_b128 %[b3], %[pb1] offset:38912\n\t" \
  "v_xor_b32 %[pa0], 0x10000, %[pa0]\n\t" \
  "s_waitcnt lgkmcnt(8)\n\t" \
  "v_mfma_f32_16x16x32_bf16 %[c04], %[a00], %[b4], %[c04]\n\t" \
  "v_mfma_f32_16x16x32_bf16 %[c14], %[a01], %[b4], %[c14]\n\t" \
  "ds_read_b128 %[b4], %[pb1] offset:40960\n\t" \
  "v_xor_b32 %[pb0], 0x10000, %[pb0]\n\t" \
  "v_mfma_f32_16x16x32_bf16 %[c05], %[a00], %[b5], %[c05]\n\t" \
  "v_mfma_f32_16x16x32_bf16 %[c15], %[a01], %[b5], %[c15]\n\t" \
  "ds_read_b128 %[b5], %[pb1] offset:43008\n\t" \
  "s_waitcnt lgkmcnt(8)\n\t" \
  "v_mfma_f32_16x16x32_bf16 %[c06], %[a00], %[b6], %[c06]\n\t" \
  "v_mfma_f32_16x16x32_bf16 %[c16], %[a01], %[b6], %[c16]\n\t" \
  "ds_read_b128 %[b6], %[pb1] offset:45056\n\t" \
  "s_cmp_eq_u32 %[it], 7\n\t" \
  "v_mfma_f32_16x16x32_bf16 %[c07], %[a00], %[b7], %[c07]\n\t" \
  "v_mfma_f32_16x16x32_bf16 %[c17], %[a01], %[b7], %[c17]\n\t" \
  "ds_read_b128 %[b7], %[pb1] offset:47104\n\t" \
  "s_cbranch_scc1 Lxe_%=\n\t" \
  "Lxb_%=:\n\t" \
  "s_waitcnt lgkmcnt(6)\n\t" \
  "v_mfma_f32_16x16x32_bf16 %[c00], %[a10], %[b0], %[c00]\n\t" \
  "v_mfma_f32_16x16x32_bf16 %[c10], %[a11], %[b0], %[c10]\n\t" \
  "v_mfma_f32_16x16x32_bf16 %[c01], %[a10], %[b1], %[c01]\n\t" \
  "v_mfma_f32_16x16x32_bf16 %[c11], %[a11], %[b1], %[c11]\n\t" \
  "v_xor_b32 %[pa1], 0x10000, %[pa1]\n\t" \
  "s_waitcnt lgkmcnt(4)\n\t" \
  "v_mfma_f32_16x16x32_bf16 %[c02], %[a10], %[b2], %[c02]\n\t" \
  "v_mfma_f32_16x16x32_bf16 %[c12], %[a11], %[b2], %[c12]\n\t" \
  "v_xor_b32 %[pb1], 0x10000, %[pb1]\n\t" \
  "s_waitcnt lgkmcnt(0)\n\t" \
  "s_barrier\n\t" \
  "s_cbranch_scc1 Lt1_%=\n\t" \
  "ds_read_b128 %[a00], %[pa0] offset:0\n\t" \
  "ds_read_b128 %[a01], %[pa0] offset:2048\n\t" \
  "v_mfma_f32_16x16x32_bf16 %[c03], %[a10], %[b3], %[c03]\n\t" \
  "v_mfma_f32_16x16x32_bf16 %[c13], %[a11], %[b3], %[c13]\n\t" \
  "ds_read_b128 %[b0], %[pb0] offset:0\n\t" \
  "ds_read_b128 %[b1], %[pb0] offset:2048\n\t" \
  "v_mfma_f32_16x16x32_bf16 %[c04], %[a10], %[b4], %[c04]\n\t" \
  "v_mfma_f32_16x16x32_bf16 %[c14], %[a11], %[b4], %[c14]\n\t" \
  "ds_read_b128 %[b2], %[pb0] offset:4096\n\t" \
  "ds_read_b128 %[b3], %[pb0] offset:6144\n\t" \
  "v_mfma_f32_16x16x32_bf16 %[c05], %[a10], %[b5], %[c05]\n\t" \
  "v_mfma_f32_16x16x32_bf16 %[c15], %[a11], %[b5], %[c15]\n\t" \
  "ds_read_b128 %[b4], %[pb0] offset:8192\n\t" \
  "ds_read_b128 %[b5], %[pb0] offset:10240\n\t" \
  "v_mfma_f32_16x16x32_bf16 %[c06], %[a10], %[b6], %[c06]\n\t" \
  "v_mfma_f32_16x16x32_bf16 %[c16], %[a11], %[b6], %[c16]\n\t" \
  "ds_read_b128 %[b6], %[pb0] offset:12288\n\t" \
  "s_add_u32 %[it], %[it], 1\n\t" \
  "v_mfma_f32_16x16x32_bf16 %[c07], %[a10], %[b7], %[c07]\n\t" \
  "v_mfma_f32_16x16x32_bf16 %[c17], %[a11], %[b7], %[c17]\n\t" \
  "ds_read_b128 %[b7], %[pb0] offset:14336\n\t" \
  "s_branch Lg1_%=\n\t" \
  "Lxe_%=:\n\t" \
  "ds_read_b64 %[xe00], %[xa0] offset:0\n\t" \
  "ds_read_b64 %[xe01], %[xa1] offset:0\n\t" \
  "ds_read_b64 %[xe02], %[xa2] offset:0\n\t" \
  "ds_read_b64 %[xe03], %[xa3] offset:0\n\t" \
  "ds_read_b64 %[xe10], %[xa0] offset:2048\n\t" \
  "ds_read_b64 %[xe11], %[xa1] offset:2048\n\t" \
  "ds_read_b64 %[xe12], %[xa2] offset:2048\n\t" \
  "ds_read_b64 %[xe13], %[xa3] offset:2048\n\t" \
  "ds_read_b32 %[tg], %[tg]\n\t" \
  "s_branch Lxb_%=\n\t" \
  "Lt1_%=:\n\t" \
  "v_mfma_f32_16x16x32_bf16 %[c03], %[a10], %[b3], %[c03]\n\t" \
  "v_mfma_f32_16x16x32_bf16 %[c13], %[a11], %[b3], %[c13]\n\t" \
  "v_mfma_f32_16x16x32_bf16 %[c04], %[a10], %[b4], %[c04]\n\t" \
  "v_mfma_f32_16x16x32_bf16 %[c14], %[a11], %[b4], %[c14]\n\t" \
  "v_mfma_f32_16x16x32_bf16 %[c05], %[a10], %[b5], %[c05]\n\t" \
  "v_mfma_f32_16x16x32_bf16 %[c15], %[a11], %[b5], %[c15]\n\t" \
  "v_mfma_f32_16x16x32_bf16 %[c06], %[a10], %[b6], %[c06]\n\t" \
  "v_mfma_f32_16x16x32_bf16 %[c16], %[a11], %[b6], %[c16]\n\t" \
  "v_mfma_f32_16x16x32_bf16 %[c07], %[a10], %[b7], %[c07]\n\t" \
  "v_mfma_f32_16x16x32_bf16 %[c17], %[a11], %[b7], %[c17]\n\t" \
  "s_nop 7\n\t"
#define ASM_GEMM2 \
  "s_nop 4\n\t" \
  "s_mov_b32 %[to], 0x1000000\n\t" \
  "s_mov_b32 %[cn], 1\n\t" \
  "Lc0_%=:\n\t" \
  "ds_read_b128 v[252:255], %[fa]\n\t" \
  "s_cmp_eq_u32 %[to], 0\n\t" \
  "s_cbranch_scc1 Lc0_%=x\n\t" \
  "s_sub_u32 %[to], %[to], 1\n\t" \
  "s_waitcnt lgkmcnt(0)\n\t" \
  "v_readfirstlane_b32 %[s], v253\n\t" \
  "s_cmp_ge_u32 %[s], 2\n\t" \
  "s_cbranch_scc0 Lc0_%=\n\t" \
  "Lc0_%=x:\n\t" \
  "ds_read_b128 %[k00], %[pc0] offset:0\n\t" \
  "ds_read_b128 %[k01], %[pc0] offset:2048\n\t" \
  "ds_read_b128 %[k02], %[pc0] offset:4096\n\t" \
  "ds_read_b128 %[k03], %[pc0] offset:6144\n\t" \
  "ds_read_b128 %[k10], %[pc1] offset:0\n\t" \
  "ds_read_b128 %[k11], %[pc1] offset:2048\n\t" \
  "ds_read_b128 %[k12], %[pc1] offset:4096\n\t" \
  "ds_read_b128 %[k13], %[pc1] offset:6144\n\t" \
  "s_mov_b32 %[hn], 3\n\t" \
  "s_cmp_gt_u32 1, %[own]\n\t" \
  "s_addc_u32 %[hb0], 1, -1\n\t" \
  "s_lshl_b32 %[hb0], %[hb0], 15\n\t" \
  "s_or_b32 %[hb1], %[hb0], 0x2000\n\t" \
  "s_or_b32 %[hb2], %[hb0], 0x4000\n\t" \
  "s_or_b32 %[hb3], %[hb0], 0x6000\n\t" \
  "Lh1_%=:\n\t" \
  "ds_read_b128 v[252:255], %[fa]\n\t" \
  "s_cmp_eq_u32 %[to], 0\n\t" \
  "s_cbranch_scc1 Lh1_%=x\n\t" \
  "s_sub_u32 %[to], %[to], 1\n\t" \
  "s_waitcnt lgkmcnt(0)\n\t" \
  "v_readfirstlane_b32 %[s], v254\n\t" \
  "s_cmp_ge_u32 %[s], 1\n\t" \
  "s_cbranch_scc0 Lh1_%=\n\t" \
  "Lh1_%=x:\n\t" \
  "buffer_load_dwordx4 v[216:219], %[vo], %[srd], %[hb0] offen offset:0 sc1\n\t" \
  "buffer_load_dwordx4 v[220:223], %[vo], %[srd], %[hb0] offen offset:1024 sc1\n\t" \
  "buffer_load_dwordx4 v[224:227], %[vo], %[srd], %[hb1] offen offset:0 sc1\n\t" \
  "buffer_load_dwordx4 v[228:231], %[vo], %[srd], %[hb1] offen offset:1024 sc1\n\t" \
  "buffer_load_dwordx4 v[232:235], %[vo], %[srd], %[hb2] offen offset:0 sc1\n\t" \
  "buffer_load_dwordx4 v[236:239], %[vo], %[srd], %[hb2] offen offset:1024 sc1\n\t" \
  "buffer_load_dwordx4 v[240:243], %[vo], %[srd], %[hb3] offen offset:0 sc1\n\t" \
  "buffer_load_dwordx4 v[244:247], %[vo], %[srd], %[hb3] offen offset:1024 sc1\n\t" \
  "s_cmp_gt_u32 2, %[own]\n\t" \
  "s_addc_u32 %[hb0], 2, -1\n\t" \
  "s_lshl_b32 %[hb0], %[hb0], 15\n\t" \
  "s_or_b32 %[hb1], %[hb0], 0x2000\n\t" \
  "s_or_b32 %[hb2], %[hb0], 0x4000\n\t" \
  "s_or_b32 %[hb3], %[hb0], 0x6000\n\t" \
  "s_cmp_ge_u32 %[s], 2\n\t" \
  "s_cbranch_scc1 Lh2_%=x\n\t" \
  "Lh2_%=:\n\t" \
  "ds_read_b128 v[252:255], %[fa]\n\t" \
  "s_cmp_eq_u32 %[to], 0\n\t" \
  "s_cbranch_scc1 Lh2_%=x\n\t" \
  "s_sub_u32 %[to], %[to], 1\n\t" \
  "s_waitcnt lgkmcnt(0)\n\t" \
  "v_readfirstlane_b32 %[s], v254\n\t" \
  "s_cmp_ge_u32 %[s], 2\n\t" \
  "s_cbranch_scc0 Lh2_%=\n\t" \
  "Lh2_%=x:\n\t" \
  "buffer_load_dwordx4 v[184:187], %[vo], %[srd], %[hb0] offen offset:0 sc1\n\t" \
  "buffer_load_dwordx4 v[188:191], %[vo], %[srd], %[hb0] offen offset:1024 sc1\n\t" \
  "buffer_load_dwordx4 v[192:195], %[vo], %[srd], %[hb1] offen offset:0 sc1\n\t" \
  "buffer_load_dwordx4 v[196:199], %[vo], %[srd], %[hb1] offen offset:1024 sc1\n\t" \
  "buffer_load_dwordx4 v[200:203], %[vo], %[srd], %[hb2] offen offset:0 sc1\n\t" \
  "buffer_load_dwordx4 v[204:207], %[vo], %[srd], %[hb2] offen offset:1024 sc1\n\t" \
  "buffer_load_dwordx4 v[208:211], %[vo], %[srd], %[hb3] offen offset:0 sc1\n\t" \
  "buffer_load_dwordx4 v[212:215], %[vo], %[srd], %[hb3] offen offset:1024 sc1\n\t" \
  "s_cmp_ge_u32 %[s], 3\n\t" \
  "s_cbranch_scc1 Lh3_%=x\n\t" \
  "Lh3_%=:\n\t" \
  "ds_read_b128 v[252:255], %[fa]\n\t" \
  "s_cmp_eq_u32 %[to], 0\n\t" \
  "s_cbranch_scc1 Lh3_%=x\n\t" \
  "s_sub_u32 %[to], %[to], 1\n\t" \
  "s_waitcnt lgkmcnt(0)\n\t" \
  "v_readfirstlane_b32 %[s], v254\n\t" \
  "s_cmp_ge_u32 %[s], 3\n\t" \
  "s_cbranch_scc0 Lh3_%=\n\t" \
  "Lh3_%=x:\n\t" \
  "s_branch Lin_%=\n\t" \
  "Lg2_%=:\n\t" \
  "s_waitcnt vmcnt(14) lgkmcnt(7)\n\t" \
  "ds_read_b128 v[252:255], %[fa]\n\t" \
  "v_mfma_f32_16x16x32_bf16 %[c00], v[184:187], %[k00], %[c00]\n\t" \
  "s_cmp_gt_u32 %[hn], %[own]\n\t" \
  "s_addc_u32 %[hb0], %[hn], -1\n\t" \
  "v_mfma_f32_16x16x32_bf16 %[c10], v[188:191], %[k00], %[c10]\n\t" \
  "ds_read_b128 %[k00], %[pc2] offset:0\n\t" \
  "s_waitcnt lgkmcnt(8)\n\t" \
  "v_mfma_f32_16x16x32_bf16 %[c01], v[184:187], %[k01], %[c01]\n\t" \
  "s_lshl_b32 %[hb0], %[hb0], 15\n\t" \
  "v_mfma_f32_16x16x32_bf16 %[c11], v[188:191], %[k01], %[c11]\n\t" \
  "ds_read_b128 %[k01], %[pc2] offset:2048\n\t" \
  "s_waitcnt lgkmcnt(8)\n\t" \
  "v_mfma_f32_16x16x32_bf16 %[c02], v[184:187], %[k02], %[c02]\n\t" \
  "s_or_b32 %[hb1], %[hb0], 0x2000\n\t" \
  "v_mfma_f32_16x16x32_bf16 %[c12], v[188:191], %[k02], %[c12]\n\t" \
  "ds_read_b128 %[k02], %[pc2] offset:4096\n\t" \
  "s_waitcnt lgkmcnt(8)\n\t" \
  "v_mfma_f32_16x16x32_bf16 %[c03], v[184:187], %[k03], %[c03]\n\t" \
  "s_add_u32 %[cn], %[cn], 1\n\t" \
  "v_mfma_f32_16x16x32_bf16 %[c13], v[188:191], %[k03], %[c13]\n\t" \
  "ds_read_b128 %[k03], %[pc2] offset:6144\n\t" \
  "buffer_load_dwordx4 v[184:187], %[vo], %[srd], %[hb0] offen offset:0 sc1\n\t" \
  "s_waitcnt vmcnt(13) lgkmcnt(8)\n\t" \
  "v_mfma_f32_16x16x32_bf16 %[c00], v[192:195], %[k10], %[c00]\n\t" \
  "s_cmp_eq_u32 %[cn], 9\n\t" \
  "s_cselect_b32 %[dl], 0x20000, %[d16]\n\t" \
  "v_mfma_f32_16x16x32_bf16 %[c10], v[196:199], %[k10], %[c10]\n\t" \
  "s_bitcmp1_b32 %[wr], %[cn]\n\t" \
  "s_cselect_b32 %[dl], 0xfffe4000, %[dl]\n\t" \
  "ds_read_b128 %[k10], %[pc3] offset:0\n\t" \
  "s_waitcnt lgkmcnt(8)\n\t" \
  "v_mfma_f32_16x16x32_bf16 %[c01], v[192:195], %[k11], %[c01]\n\t" \
  "v_add_u32 %[pc0], %[dl], %[pc0]\n\t" \
  "v_mfma_f32_16x16x32_bf16 %[c11], v[196:199], %[k11], %[c11]\n\t" \
  "s_or_b32 %[hb2], %[hb0], 0x4000\n\t" \
  "ds_read_b128 %[k11], %[pc3] offset:2048\n\t" \
  "buffer_load_dwordx4 v[188:191], %[vo], %[srd], %[hb0] offen offset:1024 sc1\n\t" \
  "s_waitcnt lgkmcnt(8)\n\t" \
  "v_mfma_f32_16x16x32_bf16 %[c02], v[192:195], %[k12], %[c02]\n\t" \
  "v_add_u32 %[pc1], %[dl], %[pc1]\n\t" \
  "v_mfma_f32_16x16x32_bf16 %[c12], v[196:199], %[k12], %[c12]\n\t" \
  "v_add_u32 %[fv], 1, %[fv]\n\t" \
  "ds_read_b128 %[k12], %[pc3] offset:4096\n\t" \
  "s_waitcnt lgkmcnt(7)\n\t" \
  "v_mfma_f32_16x16x32_bf16 %[c03], v[192:195], %[k13], %[c03]\n\t" \
  "v_readfirstlane_b32 %[s], v253\n\t" \
  "v_mfma_f32_16x16x32_bf16 %[c13], v[196:199], %[k13], %[c13]\n\t" \
  "ds_read_b128 %[k13], %[pc3] offset:6144\n\t" \
  "ds_write_b32 %[fw], %[fv]\n\t" \
  "s_cmp_gt_u32 %[s], %[cn]\n\t" \
  "s_cbranch_scc0 Lcs0_%=\n\t" \
  "Lcb0_%=:\n\t" \
  "buffer_load_dwordx4 v[192:195], %[vo], %[srd], %[hb1] offen offset:0 sc1\n\t" \
  "s_waitcnt vmcnt(13) lgkmcnt(8)\n\t" \
  "ds_read_b128 v[252:255], %[fa]\n\t" \
  "v_mfma_f32_16x16x32_bf16 %[c00], v[200:203], %[k00], %[c00]\n\t" \
  "s_or_b32 %[hb3], %[hb0], 0x6000\n\t" \
  "v_mfma_f32_16x16x32_bf16 %[c10], v[204:207], %[k00], %[c10]\n\t" \
  "ds_read_b128 %[k00], %[pc0] offset:0\n\t" \
  "s_waitcnt lgkmcnt(9)\n\t" \
  "v_mfma_f32_16x16x32_bf16 %[c01], v[200:203], %[k01], %[c01]\n\t" \
  "v_mfma_f32_16x16x32_bf16 %[c11], v[204:207], %[k01], %[c11]\n\t" \
  "ds_read_b128 %[k01], %[pc0] offset:2048\n\t" \
  "buffer_load_dwordx4 v[196:199], %[vo], %[srd], %[hb1] offen offset:1024 sc1\n\t" \
  "s_waitcnt lgkmcnt(9)\n\t" \
  "v_mfma_f32_16x16x32_bf16 %[c02], v[200:203], %[k02], %[c02]\n\t" \
  "v_mfma_f32_16x16x32_bf16 %[c12], v[204:207], %[k02], %[c12]\n\t" \
  "ds_read_b128 %[k02], %[pc0] offset:4096\n\t" \
  "s_waitcnt lgkmcnt(9)\n\t" \
  "v_mfma_f32_16x16x32_bf16 %[c03], v[200:203], %[k03], %[c03]\n\t" \
  "v_mfma_f32_16x16x32_bf16 %[c13], v[204:207], %[k03], %[c13]\n\t" \
  "ds_read_b128 %[k03], %[pc0] offset:6144\n\t" \
  "buffer_load_dwordx4 v[200:203], %[vo], %[srd], %[hb2] offen offset:0 sc1\n\t" \
  "s_waitcnt vmcnt(13) lgkmcnt(9)\n\t" \
  "v_mfma_f32_16x16x32_bf16 %[c00], v[208:211], %[k10], %[c00]\n\t" \
  "v_add_u32 %[pc2], %[dl], %[pc2]\n\t" \
  "v_mfma_f32_16x16x32_bf16 %[c10], v[212:215], %[k10], %[c10]\n\t" \
  "ds_read_b128 %[k10], %[pc1] offset:0\n\t" \
  "s_waitcnt lgkmcnt(9)\n\t" \
  "v_mfma_f32_16x16x32_bf16 %[c01], v[208:211], %[k11], %[c01]\n\t" \
  "v_add_u32 %[pc3], %[dl], %[pc3]\n\t" \
  "v_mfma_f32_16x16x32_bf16 %[c11], v[212:215], %[k11], %[c11]\n\t" \
  "ds_read_b128 %[k11], %[pc1] offset:2048\n\t" \
  "buffer_load_dwordx4 v[204:207], %[vo], %[srd], %[hb2] offen offset:1024 sc1\n\t" \
  "s_waitcnt lgkmcnt(6)\n\t" \
  "v_mfma_f32_16x16x32_bf16 %[c02], v[208:211], %[k12], %[c02]\n\t" \
  "v_readfirstlane_b32 %[s], v254\n\t" \
  "v_mfma_f32_16x16x32_bf16 %[c12], v[212:215], %[k12], %[c12]\n\t" \
  "ds_read_b128 %[k12], %[pc1] offset:4096\n\t" \
  "v_mfma_f32_16x16x32_bf16 %[c03], v[208:211], %[k13], %[c03]\n\t" \
  "s_add_u32 %[hn], %[hn], 1\n\t" \
  "v_mfma_f32_16x16x32_bf16 %[c13], v[212:215], %[k13], %[c13]\n\t" \
  "ds_read_b128 %[k13], %[pc1] offset:6144\n\t" \
  "buffer_load_dwordx4 v[208:211], %[vo], %[srd], %[hb3] offen offset:0 sc1\n\t" \
  "buffer_load_dwordx4 v[212:215], %[vo], %[srd], %[hb3] offen offset:1024 sc1\n\t" \
  "s_cmp_ge_u32 %[s], %[hn]\n\t" \
  "s_cbranch_scc0 Lhs0_%=\n\t" \
  "Lhb0_%=:\n\t" \
  "Lin_%=:\n\t" \
  "s_waitcnt vmcnt(14) lgkmcnt(7)\n\t" \
  "ds_read_b128 v[252:255], %[fa]\n\t" \
  "v_mfma_f32_16x16x32_bf16 %[c00], v[216:219], %[k00], %[c00]\n\t" \
  "s_cmp_gt_u32 %[hn], %[own]\n\t" \
  "s_addc_u32 %[hb0], %[hn], -1\n\t" \
  "v_mfma_f32_16x16x32_bf16 %[c10], v[220:223], %[k00], %[c10]\n\t" \
  "ds_read_b128 %[k00], %[pc2] offset:0\n\t" \
  "s_waitcnt lgkmcnt(8)\n\t" \
  "v_mfma_f32_16x16x32_bf16 %[c01], v[216:219], %[k01], %[c01]\n\t" \
  "s_lshl_b32 %[hb0], %[hb0], 15\n\t" \
  "v_mfma_f32_16x16x32_bf16 %[c11], v[220:223], %[k01], %[c11]\n\t" \
  "ds_read_b128 %[k01], %[pc2] offset:2048\n\t" \
  "s_waitcnt lgkmcnt(8)\n\t" \
  "v_mfma_f32_16x16x32_bf16 %[c02], v[216:219], %[k02], %[c02]\n\t" \
  "s_or_b32 %[hb1], %[hb0], 0x2000\n\t" \
  "v_mfma_f32_16x16x32_bf16 %[c12], v[220:223], %[k02], %[c12]\n\t" \
  "ds_read_b128 %[k02], %[pc2] offset:4096\n\t" \
  "s_waitcnt lgkmcnt(8)\n\t" \
  "v_mfma_f32_16x16x32_bf16 %[c03], v[216:219], %[k03], %[c03]\n\t" \
  "s_add_u32 %[cn], %[cn], 1\n\t" \
  "v_mfma_f32_16x16x32_bf16 %[c13], v[220:223], %[k03], %[c13]\n\t" \
  "ds_read_b128 %[k03], %[pc2] offset:6144\n\t" \
  "buffer_load_dwordx4 v[216:219], %[vo], %[srd], %[hb0] offen offset:0 sc1\n\t" \
  "s_waitcnt vmcnt(13) lgkmcnt(8)\n\t" \
  "v_mfma_f32_16x16x32_bf16 %[c00], v[224:227], %[k10], %[c00]\n\t" \
  "s_cmp_eq_u32 %[cn], 9\n\t" \
  "s_cselect_b32 %[dl], 0x20000, %[d16]\n\t" \
  "v_mfma_f32_16x16x32_bf16 %[c10], v[228:231], %[k10], %[c10]\n\t" \
  "s_bitcmp1_b32 %[wr], %[cn]\n\t" \
  "s_cselect_b32 %[dl], 0xfffe4000, %[dl]\n\t" \
  "ds_read_b128 %[k10], %[pc3] offset:0\n\t" \
  "s_waitcnt lgkmcnt(8)\n\t" \
  "v_mfma_f32_16x16x32_bf16 %[c01], v[224:227], %[k11], %[c01]\n\t" \
  "v_add_u32 %[pc0], %[dl], %[pc0]\n\t" \
  "v_mfma_f32_16x16x32_bf16 %[c11], v[228:231], %[k11], %[c11]\n\t" \
  "s_or_b32 %[hb2], %[hb0], 0x4000\n\t" \
  "ds_read_b128 %[k11], %[pc3] offset:2048\n\t" \
  "buffer_load_dwordx4 v[220:223], %[vo], %[srd], %[hb0] offen offset:1024 sc1\n\t" \
  "s_waitcnt lgkmcnt(8)\n\t" \
  "v_mfma_f32_16x16x32_bf16 %[c02], v[224:227], %[k12], %[c02]\n\t" \
  "v_add_u32 %[pc1], %[dl], %[pc1]\n\t" \
  "v_mfma_f32_16x16x32_bf16 %[c12], v[228:231], %[k12], %[c12]\n\t" \
  "v_add_u32 %[fv], 1, %[fv]\n\t" \
  "ds_read_b128 %[k12], %[pc3] offset:4096\n\t" \
  "s_waitcnt lgkmcnt(7)\n\t" \
  "v_mfma_f32_16x16x32_bf16 %[c03], v[224:227], %[k13], %[c03]\n\t" \
  "v_readfirstlane_b32 %[s], v252\n\t" \
  "v_mfma_f32_16x16x32_bf16 %[c13], v[228:231], %[k13], %[c13]\n\t" \
  "ds_read_b128 %[k13], %[pc3] offset:6144\n\t" \
  "ds_write_b32 %[fw], %[fv]\n\t" \
  "s_cmp_gt_u32 %[s], %[cn]\n\t" \
  "s_cbranch_scc0 Lcs1_%=\n\t" \
  "Lcb1_%=:\n\t" \
  "buffer_load_dwordx4 v[224:227], %[vo], %[srd], %[hb1] offen offset:0 sc1\n\t" \
  "s_waitcnt vmcnt(13) lgkmcnt(8)\n\t" \
  "ds_read_b128 v[252:255], %[fa]\n\t" \
  "v_mfma_f32_16x16x32_bf16 %[c00], v[232:235], %[k00], %[c00]\n\t" \
  "s_or_b32 %[hb3], %[hb0], 0x6000\n\t" \
  "v_mfma_f32_16x16x32_bf16 %[c10], v[236:239], %[k00], %[c10]\n\t" \
  "ds_read_b128 %[k00], %[pc0] offset:0\n\t" \
  "s_waitcnt lgkmcnt(9)\n\t" \
  "v_mfma_f32_16x16x32_bf16 %[c01], v[232:235], %[k01], %[c01]\n\t" \
  "v_mfma_f32_16x16x32_bf16 %[c11], v[236:239], %[k01], %[c11]\n\t" \
  "ds_read_b128 %[k01], %[pc0] offset:2048\n\t" \
  "buffer_load_dwordx4 v[228:231], %[vo], %[srd], %[hb1] offen offset:1024 sc1\n\t" \
  "s_waitcnt lgkmcnt(9)\n\t" \
  "v_mfma_f32_16x16x32_bf16 %[c02], v[232:235], %[k02], %[c02]\n\t" \
  "v_mfma_f32_16x16x32_bf16 %[c12], v[236:239], %[k02], %[c12]\n\t" \
  "ds_read_b128 %[k02], %[pc0] offset:4096\n\t" \
  "s_waitcnt lgkmcnt(9)\n\t" \
  "v_mfma_f32_16x16x32_bf16 %[c03], v[232:235], %[k03], %[c03]\n\t" \
  "v_mfma_f32_16x16x32_bf16 %[c13], v[236:239], %[k03], %[c13]\n\t" \
  "ds_read_b128 %[k03], %[pc0] offset:6144\n\t" \
  "buffer_load_dwordx4 v[232:235], %[vo], %[srd], %[hb2] offen offset:0 sc1\n\t" \
  "s_waitcnt vmcnt(13) lgkmcnt(9)\n\t" \
  "v_mfma_f32_16x16x32_bf16 %[c00], v[240:243], %[k10], %[c00]\n\t" \
  "v_add_u32 %[pc2], %[dl], %[pc2]\n\t" \
  "v_mfma_f32_16x16x32_bf16 %[c10], v[244:247], %[k10], %[c10]\n\t" \
  "ds_read_b128 %[k10], %[pc1] offset:0\n\t" \
  "s_waitcnt lgkmcnt(9)\n\t" \
  "v_mfma_f32_16x16x32_bf16 %[c01], v[240:243], %[k11], %[c01]\n\t" \
  "v_add_u32 %[pc3], %[dl], %[pc3]\n\t" \
  "v_mfma_f32_16x16x32_bf16 %[c11], v[244:247], %[k11], %[c11]\n\t" \
  "ds_read_b128 %[k11], %[pc1] offset:2048\n\t" \
  "buffer_load_dwordx4 v[236:239], %[vo], %[srd], %[hb2] offen offset:1024 sc1\n\t" \
  "s_waitcnt lgkmcnt(6)\n\t" \
  "v_mfma_f32_16x16x32_bf16 %[c02], v[240:243], %[k12], %[c02]\n\t" \
  "v_readfirstlane_b32 %[s], v254\n\t" \
  "v_mfma_f32_16x16x32_bf16 %[c12], v[244:247], %[k12], %[c12]\n\t" \
  "ds_read_b128 %[k12], %[pc1] offset:4096\n\t" \
  "v_mfma_f32_16x16x32_bf16 %[c03], v[240:243], %[k13], %[c03]\n\t" \
  "s_add_u32 %[hn], %[hn], 1\n\t" \
  "v_mfma_f32_16x16x32_bf16 %[c13], v[244:247], %[k13], %[c13]\n\t" \
  "ds_read_b128 %[k13], %[pc1] offset:6144\n\t" \
  "buffer_load_dwordx4 v[240:243], %[vo], %[srd], %[hb3] offen offset:0 sc1\n\t" \
  "buffer_load_dwordx4 v[244:247], %[vo], %[srd], %[hb3] offen offset:1024 sc1\n\t" \
  "s_cmp_ge_u32 %[s], %[hn]\n\t" \
  "s_cbranch_scc0 Lhs1_%=\n\t" \
  "Lhb1_%=:\n\t" \
  "s_cmp_lt_u32 %[cn], 14\n\t" \
  "s_cbranch_scc1 Lg2_%=\n\t" \
  "s_waitcnt vmcnt(14) lgkmcnt(7)\n\t" \
  "ds_read_b128 v[252:255], %[fa]\n\t" \
  "v_mfma_f32_16x16x32_bf16 %[c00], v[184:187], %[k00], %[c00]\n\t" \
  "v_mfma_f32_16x16x32_bf16 %[c10], v[188:191], %[k00], %[c10]\n\t" \
  "ds_read_b128 %[k00], %[pc2] offset:0\n\t" \
  "s_waitcnt lgkmcnt(8)\n\t" \
  "v_mfma_f32_16x16x32_bf16 %[c01], v[184:187], %[k01], %[c01]\n\t" \
  "v_mfma_f32_16x16x32_bf16 %[c11], v[188:191], %[k01], %[c11]\n\t" \
  "ds_read_b128 %[k01], %[pc2] offset:2048\n\t" \
  "s_waitcnt lgkmcnt(8)\n\t" \
  "v_mfma_f32_16x16x32_bf16 %[c02], v[184:187], %[k02], %[c02]\n\t" \
  "v_mfma_f32_16x16x32_bf16 %[c12], v[188:191], %[k02], %[c12]\n\t" \
  "ds_read_b128 %[k02], %[pc2] offset:4096\n\t" \
  "s_waitcnt lgkmcnt(8)\n\t" \
  "v_mfma_f32_16x16x32_bf16 %[c03], v[184:187], %[k03], %[c03]\n\t" \
  "s_add_u32 %[cn], %[cn], 1\n\t" \
  "v_mfma_f32_16x16x32_bf16 %[c13], v[188:191], %[k03], %[c13]\n\t" \
  "ds_read_b128 %[k03], %[pc2] offset:6144\n\t" \
  "s_waitcnt vmcnt(12) lgkmcnt(8)\n\t" \
  "v_mfma_f32_16x16x32_bf16 %[c00], v[192:195], %[k10], %[c00]\n\t" \
  "s_cmp_eq_u32 %[cn], 9\n\t" \
  "s_cselect_b32 %[dl], 0x20000, %[d16]\n\t" \
  "v_mfma_f32_16x16x32_bf16 %[c10], v[196:199], %[k10], %[c10]\n\t" \
  "s_bitcmp1_b32 %[wr], %[cn]\n\t" \
  "s_cselect_b32 %[dl], 0xfffe4000, %[dl]\n\t" \
  "ds_read_b128 %[k10], %[pc3] offset:0\n\t" \
  "s_waitcnt lgkmcnt(8)\n\t" \
  "v_mfma_f32_16x16x32_bf16 %[c01], v[192:195], %[k11], %[c01]\n\t" \
  "v_add_u32 %[pc0], %[dl], %[pc0]\n\t" \
  "v_mfma_f32_16x16x32_bf16 %[c11], v[196:199], %[k11], %[c11]\n\t" \
  "ds_read_b128 %[k11], %[pc3] offset:2048\n\t" \
  "s_waitcnt lgkmcnt(8)\n\t" \
  "v_mfma_f32_16x16x32_bf16 %[c02], v[192:195], %[k12], %[c02]\n\t" \
  "v_add_u32 %[pc1], %[dl], %[pc1]\n\t" \
  "v_mfma_f32_16x16x32_bf16 %[c12], v[196:199], %[k12], %[c12]\n\t" \
  "ds_read_b128 %[k12], %[pc3] offset:4096\n\t" \
  "s_waitcnt lgkmcnt(7)\n\t" \
  "v_mfma_f32_16x16x32_bf16 %[c03], v[192:195], %[k13], %[c03]\n\t" \
  "v_readfirstlane_b32 %[s], v253\n\t" \
  "v_mfma_f32_16x16x32_bf16 %[c13], v[196:199], %[k13], %[c13]\n\t" \
  "ds_read_b128 %[k13], %[pc3] offset:6144\n\t" \
  "s_cmp_gt_u32 %[s], %[cn]\n\t" \
  "s_cbranch_scc0 Lcs0t_%=\n\t" \
  "Lcb0t_%=:\n\t" \
  "s_waitcnt vmcnt(10) lgkmcnt(7)\n\t" \
  "v_mfma_f32_16x16x32_bf16 %[c00], v[200:203], %[k00], %[c00]\n\t" \
  "v_mfma_f32_16x16x32_bf16 %[c10], v[204:207], %[k00], %[c10]\n\t" \
  "ds_read_b128 %[k00], %[pc0] offset:0\n\t" \
  "s_waitcnt lgkmcnt(7)\n\t" \
  "v_mfma_f32_16x16x32_bf16 %[c01], v[200:203], %[k01], %[c01]\n\t" \
  "v_mfma_f32_16x16x32_bf16 %[c11], v[204:207], %[k01], %[c11]\n\t" \
  "ds_read_b128 %[k01], %[pc0] offset:2048\n\t" \
  "s_waitcnt lgkmcnt(7)\n\t" \
  "v_mfma_f32_16x16x32_bf16 %[c02], v[200:203], %[k02], %[c02]\n\t" \
  "v_mfma_f32_16x16x32_bf16 %[c12], v[204:207], %[k02], %[c12]\n\t" \
  "ds_read_b128 %[k02], %[pc0] offset:4096\n\t" \
  "s_waitcnt lgkmcnt(7)\n\t" \
  "v_mfma_f32_16x16x32_bf16 %[c03], v[200:203], %[k03], %[c03]\n\t" \
  "v_mfma_f32_16x16x32_bf16 %[c13], v[204:207], %[k03], %[c13]\n\t" \
  "ds_read_b128 %[k03], %[pc0] offset:6144\n\t" \
  "s_waitcnt vmcnt(8) lgkmcnt(7)\n\t" \
  "v_mfma_f32_16x16x32_bf16 %[c00], v[208:211], %[k10], %[c00]\n\t" \
  "v_add_u32 %[pc2], %[dl], %[pc2]\n\t" \
  "v_mfma_f32_16x16x32_bf16 %[c10], v[212:215], %[k10], %[c10]\n\t" \
  "ds_read_b128 %[k10], %[pc1] offset:0\n\t" \
  "s_waitcnt lgkmcnt(7)\n\t" \
  "v_mfma_f32_16x16x32_bf16 %[c01], v[208:211], %[k11], %[c01]\n\t" \
  "v_add_u32 %[pc3], %[dl], %[pc3]\n\t" \
  "v_mfma_f32_16x16x32_bf16 %[c11], v[212:215], %[k11], %[c11]\n\t" \
  "ds_read_b128 %[k11], %[pc1] offset:2048\n\t" \
  "s_waitcnt lgkmcnt(7)\n\t" \
  "v_mfma_f32_16x16x32_bf16 %[c02], v[208:211], %[k12], %[c02]\n\t" \
  "v_mfma_f32_16x16x32_bf16 %[c12], v[212:215], %[k12], %[c12]\n\t" \
  "ds_read_b128 %[k12], %[pc1] offset:4096\n\t" \
  "s_waitcnt lgkmcnt(7)\n\t" \
  "v_mfma_f32_16x16x32_bf16 %[c03], v[208:211], %[k13], %[c03]\n\t" \
  "v_mfma_f32_16x16x32_bf16 %[c13], v[212:215], %[k13], %[c13]\n\t" \
  "ds_read_b128 %[k13], %[pc1] offset:6144\n\t" \
  "s_waitcnt vmcnt(6) lgkmcnt(7)\n\t" \
  "v_mfma_f32_16x16x32_bf16 %[c00], v[216:219], %[k00], %[c00]\n\t" \
  "v_mfma_f32_16x16x32_bf16 %[c10], v[220:223], %[k00], %[c10]\n\t" \
  "ds_read_b128 %[k00], %[pc2] offset:0\n\t" \
  "s_waitcnt lgkmcnt(7)\n\t" \
  "v_mfma_f32_16x16x32_bf16 %[c01], v[216:219], %[k01], %[c01]\n\t" \
  "v_mfma_f32_16x16x32_bf16 %[c11], v[220:223], %[k01], %[c11]\n\t" \
  "ds_read_b128 %[k01], %[pc2] offset:2048\n\t" \
  "s_waitcnt lgkmcnt(7)\n\t" \
  "v_mfma_f32_16x16x32_bf16 %[c02], v[216:219], %[k02], %[c02]\n\t" \
  "v_mfma_f32_16x16x32_bf16 %[c12], v[220:223], %[k02], %[c12]\n\t" \
  "ds_read_b128 %[k02], %[pc2] offset:4096\n\t" \
  "s_waitcnt lgkmcnt(7)\n\t" \
  "v_mfma_f32_16x16x32_bf16 %[c03], v[216:219], %[k03], %[c03]\n\t" \
  "v_mfma_f32_16x16x32_bf16 %[c13], v[220:223], %[k03], %[c13]\n\t" \
  "ds_read_b128 %[k03], %[pc2] offset:6144\n\t" \
  "s_waitcnt vmcnt(4) lgkmcnt(7)\n\t" \
  "v_mfma_f32_16x16x32_bf16 %[c00], v[224:227], %[k10], %[c00]\n\t" \
  "v_mfma_f32_16x16x32_bf16 %[c10], v[228:231], %[k10], %[c10]\n\t" \
  "ds_read_b128 %[k10], %[pc3] offset:0\n\t" \
  "s_waitcnt lgkmcnt(7)\n\t" \
  "v_mfma_f32_16x16x32_bf16 %[c01], v[224:227], %[k11], %[c01]\n\t" \
  "v_mfma_f32_16x16x32_bf16 %[c11], v[228:231], %[k11], %[c11]\n\t" \
  "ds_read_b128 %[k11], %[pc3] offset:2048\n\t" \
  "s_waitcnt lgkmcnt(7)\n\t" \
  "v_mfma_f32_16x16x32_bf16 %[c02], v[224:227], %[k12], %[c02]\n\t" \
  "v_mfma_f32_16x16x32_bf16 %[c12], v[228:231], %[k12], %[c12]\n\t" \
  "ds_read_b128 %[k12], %[pc3] offset:4096\n\t" \
  "s_waitcnt lgkmcnt(7)\n\t" \
  "v_mfma_f32_16x16x32_bf16 %[c03], v[224:227], %[k13], %[c03]\n\t" \
  "v_mfma_f32_16x16x32_bf16 %[c13], v[228:231], %[k13], %[c13]\n\t" \
  "ds_read_b128 %[k13], %[pc3] offset:6144\n\t" \
  "s_waitcnt vmcnt(2) lgkmcnt(7)\n\t" \
  "v_mfma_f32_16x16x32_bf16 %[c00], v[232:235], %[k00], %[c00]\n\t" \
  "v_mfma_f32_16x16x32_bf16 %[c10], v[236:239], %[k00], %[c10]\n\t" \
  "s_waitcnt lgkmcnt(6)\n\t" \
  "v_mfma_f32_16x16x32_bf16 %[c01], v[232:235], %[k01], %[c01]\n\t" \
  "v_mfma_f32_16x16x32_bf16 %[c11], v[236:239], %[k01], %[c11]\n\t" \
  "s_waitcnt lgkmcnt(5)\n\t" \
  "v_mfma_f32_16x16x32_bf16 %[c02], v[232:235], %[k02], %[c02]\n\t" \
  "v_mfma_f32_16x16x32_bf16 %[c12], v[236:239], %[k02], %[c12]\n\t" \
  "s_waitcnt lgkmcnt(4)\n\t" \
  "v_mfma_f32_16x16x32_bf16 %[c03], v[232:235], %[k03], %[c03]\n\t" \
  "v_mfma_f32_16x16x32_bf16 %[c13], v[236:239], %[k03], %[c13]\n\t" \
  "s_waitcnt vmcnt(0) lgkmcnt(3)\n\t" \
  "v_mfma_f32_16x16x32_bf16 %[c00], v[240:243], %[k10], %[c00]\n\t" \
  "v_mfma_f32_16x16x32_bf16 %[c10], v[244:247], %[k10], %[c10]\n\t" \
  "s_waitcnt lgkmcnt(2)\n\t" \
  "v_mfma_f32_16x16x32_bf16 %[c01], v[240:243], %[k11], %[c01]\n\t" \
  "v_mfma_f32_16x16x32_bf16 %[c11], v[244:247], %[k11], %[c11]\n\t" \
  "s_waitcnt lgkmcnt(1)\n\t" \
  "v_mfma_f32_16x16x32_bf16 %[c02], v[240:243], %[k12], %[c02]\n\t" \
  "v_mfma_f32_16x16x32_bf16 %[c12], v[244:247], %[k12], %[c12]\n\t" \
  "s_waitcnt lgkmcnt(0)\n\t" \
  "v_mfma_f32_16x16x32_bf16 %[c03], v[240:243], %[k13], %[c03]\n\t" \
  "v_mfma_f32_16x16x32_bf16 %[c13], v[244:247], %[k13], %[c13]\n\t" \
  "s_branch Lend_%=\n\t" \
  "Lcs0_%=:\n\t" \
  "s_add_u32 %[s2], %[cn], 1\n\t" \
  "Lcr0_%=:\n\t" \
  "ds_read_b128 v[252:255], %[fa]\n\t" \
  "s_cmp_eq_u32 %[to], 0\n\t" \
  "s_cbranch_scc1 Lcb0_%=\n\t" \
  "s_sub_u32 %[to], %[to], 1\n\t" \
  "s_waitcnt lgkmcnt(0)\n\t" \
  "v_readfirstlane_b32 %[s], v253\n\t" \
  "s_cmp_ge_u32 %[s], %[s2]\n\t" \
  "s_cbranch_scc0 Lcr0_%=\n\t" \
  "s_branch Lcb0_%=\n\t" \
  "Lhs0_%=:\n\t" \
  "ds_read_b128 v[252:255], %[fa]\n\t" \
  "s_cmp_eq_u32 %[to], 0\n\t" \
  "s_cbranch_scc1 Lhb0_%=\n\t" \
  "s_sub_u32 %[to], %[to], 1\n\t" \
  "s_waitcnt lgkmcnt(0)\n\t" \
  "v_readfirstlane_b32 %[s], v254\n\t" \
  "s_cmp_ge_u32 %[s], %[hn]\n\t" \
  "s_cbranch_scc0 Lhs0_%=\n\t" \
  "s_branch Lhb0_%=\n\t" \
  "Lcs1_%=:\n\t" \
  "s_add_u32 %[s2], %[cn], 1\n\t" \
  "Lcr1_%=:\n\t" \
  "ds_read_b128 v[252:255], %[fa]\n\t" \
  "s_cmp_eq_u32 %[to], 0\n\t" \
  "s_cbranch_scc1 Lcb1_%=\n\t" \
  "s_sub_u32 %[to], %[to], 1\n\t" \
  "s_waitcnt lgkmcnt(0)\n\t" \
  "v_readfirstlane_b32 %[s], v252\n\t" \
  "s_cmp_ge_u32 %[s], %[s2]\n\t" \
  "s_cbranch_scc0 Lcr1_%=\n\t" \
  "s_branch Lcb1_%=\n\t" \
  "Lhs1_%=:\n\t" \
  "ds_read_b128 v[252:255], %[fa]\n\t" \
  "s_cmp_eq_u32 %[to], 0\n\t" \
  "s_cbranch_scc1 Lhb1_%=\n\t" \
  "s_sub_u32 %[to], %[to], 1\n\t" \
  "s_waitcnt lgkmcnt(0)\n\t" \
  "v_readfirstlane_b32 %[s], v254\n\t" \
  "s_cmp_ge_u32 %[s], %[hn]\n\t" \
  "s_cbranch_scc0 Lhs1_%=\n\t" \
  "s_branch Lhb1_%=\n\t" \
  "Lcs0t_%=:\n\t" \
  "s_add_u32 %[s2], %[cn], 1\n\t" \
  "Lcr0t_%=:\n\t" \
  "ds_read_b128 v[252:255], %[fa]\n\t" \
  "s_cmp_eq_u32 %[to], 0\n\t" \
  "s_cbranch_scc1 Lcb0t_%=\n\t" \
  "s_sub_u32 %[to], %[to], 1\n\t" \
  "s_waitcnt lgkmcnt(0)\n\t" \
  "v_readfirstlane_b32 %[s], v253\n\t" \
  "s_cmp_ge_u32 %[s], %[s2]\n\t" \
  "s_cbranch_scc0 Lcr0t_%=\n\t" \
  "s_branch Lcb0t_%=\n\t" \
  "Lend_%=:\n\t" \
  "s_nop 7\n\t"

#define YGATE 9
#define XG2 2
#define CPK 9
#define STN 6
#define NRG1 2
#define W1A 2
#define W1B 3
#define W1C 4
#define W1D 6
#define XUN 9
#define NFW 28
#define SPINLOG 18
constexpr int D_ = 1024, F_ = 3584;
constexpr int L_XH = 131072;
constexpr int MISC = 155648;
constexpr int L_GT = MISC;
constexpr int L_FLG = L_GT + 512;
constexpr int L_JNK = L_FLG + 256;
constexpr int L_SEL = L_JNK + 256;
constexpr int L_BROW = L_SEL + 128;
constexpr int L_ACT = L_BROW + 2048;
constexpr int L_CAND = L_ACT + 1024;
constexpr int L_ZERO = L_CAND + 256;
constexpr int L_MAP = L_ZERO + 256;
constexpr int L_TAG = L_MAP + 512;
constexpr int L_RT = L_TAG + 16;
constexpr int LDS_BYTES = 163840;
static_assert(L_RT + 32 <= LDS_BYTES, "lds");
constexpr int FL_FREE = L_FLG + 16, FL_C = L_FLG + 32  , FL_CFREE = L_FLG + 48, FL_XD = L_FLG + 80, FL_TK = L_FLG + 96, FL_SEL = L_FLG + 112,
              FL_G = L_FLG + 128, FL_W = L_FLG + 136, FL_HS = L_FLG + 144, FL_C8 = L_FLG + 160;
constexpr unsigned W_EPOCH = 0;
constexpr unsigned W_R2 = 256;
constexpr unsigned W_G2 = W_R2 + 8192;
constexpr unsigned W_HF = W_G2 + 8192;
constexpr unsigned W_AGG = W_HF + 32768;
constexpr unsigned W_GU = W_AGG + 262144 + 4096;
constexpr unsigned W_YP = W_GU + 56 * 16384;
constexpr unsigned YTILE = 56 * 256, YTOK = 16 * YTILE;
constexpr unsigned W_H = 8u << 20;
constexpr unsigned W_DBG = 20u << 20;
constexpr unsigned W_END = 21u << 20;
static_assert(W_YP + 256 * 7168 <= W_H, "ws");
#define WS_BASE(n) (((n) - W_END) & ~(size_t)0xfffff)
constexpr int SPIN_MAX = 1 << SPINLOG;

DEV float bf2f(unsigned v) { return __uint_as_float(v << 16); }
DEV float bfhi(unsigned v) { return __uint_as_float(v & 0xffff0000u); }
DEV u16 f2bf(float f) { return __builtin_bit_cast(u16, (__bf16)f); }
DEV float rbf(float f) { return bf2f(f2bf(f)); }
typedef __bf16 bf16x2_t __attribute__((ext_vector_type(2)));
DEV unsigned pk2(float a, float b) { return __builtin_bit_cast(unsigned, __builtin_convertvector((f32x2){a, b}, bf16x2_t)); }
DEV float fma1(float a, float b, float c) { float d; asm("v_fma_f32 %0, %1, %2, %3" : "=v"(d) : "v"(a), "v"(b), "v"(c)); return d; }
DEV float mul1(float a, float b) { float d; asm("v_mul_f32 %0, %1, %2" : "=v"(d) : "v"(a), "v"(b)); return d; }
template <int N> DEV void waitv() { asm volatile("s_waitcnt vmcnt(%0)" ::"n"(N) : "memory"); }
DEV void bar() { asm volatile("s_barrier" ::: "memory"); }
DEV unsigned lds_rd32(unsigned a) { unsigned v; asm volatile("ds_read_b32 %0, %1\n\ts_waitcnt lgkmcnt(0)" : "=v"(v) : "v"(a) : "memory"); return v; }
DEV u32x4 lds_rd128(unsigned a) { u32x4 v; asm volatile("ds_read_b128 %0, %1\n\ts_waitcnt lgkmcnt(0)" : "=v"(v) : "v"(a) : "memory"); return v; }
DEV void lds_wr32(unsigned a, unsigned v) { asm volatile("ds_write_b32 %0, %1" ::"v"(a), "v"(v) : "memory"); }
DEV void lds_wait_ge(unsigned a, unsigned need) {
#pragma unroll 1
  for (int it = 0; it < SPIN_MAX * 4; ++it) { if (__builtin_amdgcn_readfirstlane(lds_rd32(a)) >= need) break; __builtin_amdgcn_s_sleep(1); }
}
typedef __amdgpu_buffer_rsrc_t rsrc_t;
#define FRESH() asm volatile("" ::: "memory")
DEV u32x4 ld128c(rsrc_t r, unsigned vo) { return __builtin_amdgcn_raw_buffer_load_b128(r, vo, 0, 16); }
DEV void st128c(rsrc_t r, unsigned vo, u32x4 v) {
  asm volatile("s_nop 4\n\tbuffer_store_dwordx4 %0, %1, %2, 0 offen sc1\n\ts_nop 7\n\ts_nop 7" ::"v"(v), "v"(vo), "s"(r) : "memory");
}
DEV void st64c(rsrc_t r, unsigned vo, u32x2 v) { __builtin_amdgcn_raw_buffer_store_b64(v, r, vo, 0, 16); }
DEV rsrc_t mk(const void* p, unsigned bytes) { return __builtin_amdgcn_make_buffer_rsrc((void*)p, 0, bytes, 0x00020000); }

DEV unsigned wave_maxu(unsigned v) {
  v = max(v, (unsigned)__builtin_amdgcn_update_dpp((int)v, (int)v, 0x111, 0xf, 0xf, false));
  v = max(v, (unsigned)__builtin_amdgcn_update_dpp((int)v, (int)v, 0x112, 0xf, 0xf, false));
  v = max(v, (unsigned)__builtin_amdgcn_update_dpp((int)v, (int)v, 0x114, 0xf, 0xf, false));
  v = max(v, (unsigned)__builtin_amdgcn_update_dpp((int)v, (int)v, 0x118, 0xf, 0xf, false));
  v = max(v, (unsigned)__builtin_amdgcn_update_dpp((int)v, (int)v, 0x142, 0xa, 0xf, false));
  v = max(v, (unsigned)__builtin_amdgcn_update_dpp((int)v, (int)v, 0x143, 0xc, 0xf, false));
  return (unsigned)__builtin_amdgcn_readlane((int)v, 63);
}
DEV float sum16_32(float v) {
  { auto p = __builtin_amdgcn_permlane16_swap(__float_as_uint(v), __float_as_uint(v), false, false); v = __uint_as_float(p[0]) + __uint_as_float(p[1]); }
  { auto p = __builtin_amdgcn_permlane32_swap(__float_as_uint(v), __float_as_uint(v), false, false); v = __uint_as_float(p[0]) + __uint_as_float(p[1]); }
  return v;
}
DEV unsigned route_key(float r, int idx) {
  unsigned u = __float_as_uint(r) >> 16;
  u = (u & 0x8000u) ? (~u & 0xffffu) : (u | 0x8000u);
  return (u << 16) | (unsigned)(2047 - idx);
}
DEV float key_val(unsigned key) {
  unsigned u = key >> 16;
  u = (u & 0x8000u) ? (u & 0x7fffu) : (~u & 0xffffu);
  return __uint_as_float(u << 16);
}

DEV void unit_trn(u32x4 (&w)[8]) {
  u32x4 b[8];
#pragma unroll
  for (int j = 0; j < 8; ++j)
#pragma unroll
    for (int t = 0; t < 4; ++t) b[j][t] = __builtin_amdgcn_perm(w[2 * t + 1][j >> 1], w[2 * t][j >> 1], (j & 1) ? 0x07060302u : 0x05040100u);
#pragma unroll
  for (int j = 0; j < 8; ++j) w[j] = b[j];
}
DEV f32x4 fma4(f32x4 a, f32x4 b, f32x4 c) { return __builtin_elementwise_fma(a, b, c); }
DEV f32x4 fnm4(f32x4 a, f32x4 b, f32x4 c) {
  const f32x2 l = __builtin_elementwise_fma(-a.lo, b.lo, c.lo), h = __builtin_elementwise_fma(-a.hi, b.hi, c.hi);
  return (f32x4){l.x, l.y, h.x, h.y};
}
DEV f32x4 nml4(f32x4 a, f32x4 b) { const f32x2 l = -a.lo * b.lo, h = -a.hi * b.hi; return (f32x4){l.x, l.y, h.x, h.y}; }
template <int CTRL> DEV float dpp1(float v) { return __uint_as_float((unsigned)__builtin_amdgcn_update_dpp(0, (int)__float_as_uint(v), CTRL, 0xf, 0xf, true)); }
template <int CTRL> DEV f32x4 dpp4(f32x4 v) { return (f32x4){dpp1<CTRL>(v.x), dpp1<CTRL>(v.y), dpp1<CTRL>(v.z), dpp1<CTRL>(v.w)}; }
DEV f32x4 splat4(float v) { f32x2 p = {v, v}; asm volatile("" : "+v"(p)); return (f32x4){p.x, p.y, p.x, p.y}; }
DEV void cexp4(f32x4 a2, f32x4 bv, f32x4& re, f32x4& im) {
  f32x4 m, cs, sn;
#pragma unroll
  for (int r = 0; r < 4; ++r) { const float ph = __builtin_amdgcn_fractf(bv[r]); m[r] = __builtin_amdgcn_exp2f(a2[r]); cs[r] = __builtin_amdgcn_cosf(ph); sn[r] = __builtin_amdgcn_sinf(ph); }
  re = m * cs; im = m * sn;
}
DEV f32x4 unpk4(u32x2 v) { return (f32x4){bf2f(v.x & 0xffffu), bfhi(v.x), bf2f(v.y & 0xffffu), bfhi(v.y)}; }
DEV void bcast8(int n, f32x4 v, f32x4& o) {
  switch (n) {
    case 0: o = dpp4<0x150>(v); break; case 1: o = dpp4<0x151>(v); break; case 2: o = dpp4<0x152>(v); break; case 3: o = dpp4<0x153>(v); break;
    case 4: o = dpp4<0x154>(v); break; case 5: o = dpp4<0x155>(v); break; case 6: o = dpp4<0x156>(v); break; default: o = dpp4<0x157>(v); break;
  }
}
DEV f32x4 lds4(LAS char* smem, int a) { return *(const LAS f32x4*)(smem + a); }
DEV void lds_wait_min4(unsigned a, unsigned need) {
#pragma unroll 1
  for (int it = 0; it < SPIN_MAX * 4; ++it) {
    const u32x4 f = lds_rd128(a);
    if (__builtin_amdgcn_readfirstlane(min(min(f.x, f.y), min(f.z, f.w))) >= need) break;
    __builtin_amdgcn_s_sleep(1);
  }
}

DEV void dma4(rsrc_t r0, rsrc_t r1, LAS char* la, const unsigned (&v)[4], unsigned so) {
  __builtin_amdgcn_raw_ptr_buffer_load_lds(r0, (LAS void*)la, 16, v[0], so, 0, 0);
  __builtin_amdgcn_raw_ptr_buffer_load_lds(r0, (LAS void*)la, 16, v[1], so, 1024, 0);
  __builtin_amdgcn_raw_ptr_buffer_load_lds(r1, (LAS void*)la, 16, v[2], so, 2048, 0);
  __builtin_amdgcn_raw_ptr_buffer_load_lds(r1, (LAS void*)la, 16, v[3], so, 3072, 0);
}

__global__ __launch_bounds__(512, 2) void fused(const u16*, const u16*, const u16*, const u16*, const u16*, const u16*, const u16*, const u16*, const u16*, const u16*,
                                                const u16*, const u16*, const u16*, const u16*, char*, float*) {
#if __HIP_DEVICE_COMPILE__
  unsigned kw0, kw1;
  asm volatile("s_load_dword %0, %2, 0x0\n\ts_load_dword %1, %2, 0x40" : "=&s"(kw0), "=&s"(kw1) : "s"(__builtin_amdgcn_kernarg_segment_ptr()));
  extern __shared__ __attribute__((aligned(16))) char smem_[];
  LAS char* smem = (LAS char*)smem_;
  const int tid = threadIdx.x, wid = __builtin_amdgcn_readfirstlane(tid >> 6);
  const int bid = blockIdx.x, xcd = bid & 7, loc = bid >> 3;
  const int c = (xcd >> 1) * 4 + (loc >> 3), j = (xcd & 1) * 8 + (loc & 7);
  int lane, fr, fq;
  auto relane = [&]() {
    asm volatile("v_mbcnt_lo_u32_b32 %0, -1, 0\n\tv_mbcnt_hi_u32_b32 %0, -1, %0" : "=v"(lane));
    __builtin_assume(lane >= 0 && lane < 64);
    fr = lane & 15; fq = lane >> 4;
  };
  const int crot = (c & 1) + 4 * (c & 2) + (c >> 2) * 2;
  const int pown = (j - crot) & 15;
  auto upos = [&](int u) { return u == 0 ? pown : u - 1 + (u - 1 >= pown ? 1 : 0); };
  auto lead = [&](u32x2 f) {
    const u64 m = ~(((u64)(unsigned)__builtin_amdgcn_readfirstlane(f.y) << 32 | (unsigned)__builtin_amdgcn_readfirstlane(f.x)) | 15ull << (4 * pown));
    const int p = (int)__builtin_ctzll(m) >> 2;
    return m ? (unsigned)(p - (p > pown ? 1 : 0)) : 99u;
  };
  u64 pc0;
  unsigned l0, l1, l2;
  asm volatile("s_getpc_b64 %0\n\tLk_pc:\n\ts_mov_b32 %1, Lk_m-Lk_pc\n\ts_mov_b32 %2, Lk_l-Lk_pc\n\ts_mov_b32 %3, Lk_x-Lk_pc" : "=s"(pc0), "=s"(l0), "=s"(l1), "=s"(l2));
  auto codepull = [&]() {
    const unsigned po = min((unsigned)(loc * 16 + (lane & 15)) * 128u, max(l0, max(l1, l2)) - 4u);
    if (lane < 16) __builtin_amdgcn_global_load_lds((const __attribute__((address_space(1))) void*)(pc0 + po), (LAS void*)(smem + L_JNK), 4, 0, 0);
  };
  u32x2 p0, p1, p2, dv;
  u32x4 xv[2][2], wv[2], gv[2];
  if (wid < 4) {
    u64x8 ta, tb;
    asm volatile("s_load_dwordx16 %0, %2, 0x0\n\ts_load_dwordx16 %1, %2, 0x40" : "=&s"(ta), "=&s"(tb) : "s"(__builtin_amdgcn_kernarg_segment_ptr()));
    relane();
    if (wid == 0) codepull();
    asm volatile("s_waitcnt lgkmcnt(0)" : "+s"(ta), "+s"(tb));
    asm volatile("global_load_dwordx2 %0, %12, %16\n\tglobal_load_dwordx2 %1, %12, %17\n\tglobal_load_dwordx2 %2, %12, %18\n\t"
                 "global_load_dwordx4 %4, %13, %19\n\tglobal_load_dwordx4 %5, %13, %19 offset:1024\n\tglobal_load_dwordx4 %6, %13, %19 offset:2048\n\tglobal_load_dwordx4 %7, %13, %19 offset:3072\n\t"
                 "global_load_dwordx4 %8, %14, %20\n\tglobal_load_dwordx4 %9, %14, %20 offset:1024\n\tglobal_load_dwordx4 %10, %14, %21\n\tglobal_load_dwordx4 %11, %14, %21 offset:1024\n\t"
                 "global_load_dwordx2 %3, %15, %22"
                 : "=&v"(p0), "=&v"(p1), "=&v"(p2), "=&v"(dv), "=&v"(xv[0][0]), "=&v"(xv[0][1]), "=&v"(xv[1][0]), "=&v"(xv[1][1]), "=&v"(wv[0]), "=&v"(wv[1]), "=&v"(gv[0]), "=&v"(gv[1])
                 : "v"((j * 64 + wid * 16 + 4 * fq) * 2), "v"((c * 128 + j * 8 + wid * 2) * 2048 + lane * 16), "v"(lane * 16), "v"((j * 64 + 4 * fr) * 2),
                   "s"(ta[1]), "s"(ta[2]), "s"(ta[7]), "s"(ta[0]), "s"(tb[1]), "s"(tb[5]), "s"(tb[0]) : "memory");
  }
  const int l = wid & 1;
  if (__builtin_expect(wid >= 4, 1)) {
  u64x8 ka, kb;
  asm volatile("s_load_dwordx16 %0, %2, 0x0\n\ts_load_dwordx16 %1, %2, 0x40" : "=&s"(ka), "=&s"(kb) : "s"(__builtin_amdgcn_kernarg_segment_ptr()));
  relane();
  unsigned vo1[4];
#pragma unroll
  for (int e = 0; e < 4; ++e)
    vo1[e] = (l == 0 ? c * 128 + 8 * (lane >> 3) + 64 * (e & 1) + (e >> 1) : j * 64 + (lane >> 3) + 8 * (e & 1)) * 2048 + (((lane & 7) ^ (lane >> 3)) * 16) + 4096 - e * 1024;
  const unsigned s4 = (l == 0 ? 2 : 16) * 2048, so1 = wid >= 6 ? 2 * s4 : 0, la1 = l * 16384 + (wid >= 6 ? 8192 : 0);
  const int s0 = (c + j) & 15;
  unsigned sl0 = ((s0 + (s0 == j ? 1 : 0)) & 15) * 128 + so1;
  asm volatile("" : "+v"(vo1[0]), "+v"(vo1[1]), "+v"(vo1[2]), "+v"(vo1[3]) : "s"(sl0), "s"(s4), "s"(la1));
  asm volatile("s_waitcnt lgkmcnt(0)" : "+s"(ka), "+s"(kb) : "s"(kw0), "s"(kw1));
  const char *x = (const char*)ka[0], *Bre = (const char*)ka[3], *Bim = (const char*)ka[4], *Cre = (const char*)ka[5], *Cim = (const char*)ka[6], *w_gate = (const char*)kb[2],
             *w_up = (const char*)kb[3], *w_down = (const char*)kb[4];
  char* ws = (char*)kb[6];
  const rsrc_t r0 = mk((l == 0 ? x : Bre) - 4096, (l == 0 ? 1 << 22 : 1 << 21) + 4096), r1 = mk((l == 0 ? x : Bim) - 4096, (l == 0 ? 1 << 22 : 1 << 21) + 4096);
  auto issue = [&](unsigned sk, unsigned slot) { dma4(r0, r1, smem + slot + la1, vo1, sk); dma4(r0, r1, smem + slot + la1 + 4096, vo1, sk + s4); };
  issue(sl0, 0);
  __builtin_amdgcn_sched_barrier(0);
  const rsrc_t rws = mk(ws, W_END);
  const rsrc_t rx = mk(x, 2048 * 1024 * 2);
  u64 tbl;
  {
    const int n = (j - s0) & 15;
    const u64 seq = 0xfedcba9876543210ull, rot = s0 ? (seq >> (4 * s0)) | (seq << (64 - 4 * s0)) : seq, lo = (1ull << (4 * n)) - 1;
    tbl = (rot & lo) | ((rot >> 4) & ~lo) | ((u64)j << 60);
  }
  auto slice = [&](int k) { return ((unsigned)(tbl >> (4 * k)) & 15u) * 128u; };
  __builtin_amdgcn_sched_barrier(0);

  auto flag = [&](int a, unsigned v) { lds_wr32(lane == 0 ? (unsigned)a : (unsigned)(L_JNK + lane * 4), v); };
  auto flagmax = [&](int a, unsigned v) { asm volatile("ds_max_u32 %0, %1" ::"v"(lane == 0 ? (unsigned)a : (unsigned)(L_JNK + lane * 4)), "v"(v) : "memory"); };
    const rsrc_t rcr = mk(Cre, 1 << 21), rci = mk(Cim, 1 << 21);
    const unsigned ce = (j * 64 + 4 * (lane >> 3)) * 2048 + (((lane & 7) ^ (lane >> 4)) * 16);
    const unsigned co = (j * 64 + 32 + 4 * (lane >> 3)) * 2048 + (((lane & 7) ^ (4 + (lane >> 4))) * 16);
    auto cfill = [&](int u, int slot, int q0, int q1) {
      LAS char* dst = smem + slot * 16384;
#pragma unroll
      for (int q = q0; q < q1; ++q)
        __builtin_amdgcn_raw_ptr_buffer_load_lds(q < 8 ? rcr : rci, (LAS void*)(dst + q * 1024), 16, (q & 1) ? co : ce, ((q & 7) >> 1) * 2048 + ((upos(u) + crot) & 15) * 128, 0, 0);
    };
  if (__builtin_expect(wid == 4 || wid == 5, 1)) {
    __builtin_amdgcn_s_setprio(2);
    issue(slice(1), 32768);
    issue(slice(2), 65536);
    constexpr int K0 = 0;
    {
      u64 t = tbl >> (4 * K0 + 12);
#pragma unroll 1
      for (int k = K0; k < 13; ++k) {
        const unsigned sk = ((unsigned)t & 15u) << 7;
        waitv<16>();
        bar();
        issue(sk, ((k + 3) & 3) * 32768);
        if (k == CPK && l == 1) codepull();
        t >>= 4;
      }
    }
    waitv<16>(); bar();
    waitv<8>(); bar();
    waitv<0>(); bar();
    bar();
    lds_wait_ge(FL_W + l * 4, 1u);
    __builtin_amdgcn_s_setprio(1);
#pragma unroll 1
    for (int u = l; u < 8; u += 2) { cfill(u, u, 0, 8); waitv<16>(); cfill(u, u, 8, 16); waitv<16>(); if (u >= 2) flag(FL_C + l * 4, (unsigned)(u - 1)); }
    if (l == 0) {
      waitv<0>();
      flag(FL_C, 7u);
      __builtin_amdgcn_s_setprio(3);
      lds_wait_min4(FL_FREE, 9u);
#pragma unroll 1
      for (int it = 0; it < SPIN_MAX; ++it) {
        if (__builtin_amdgcn_readfirstlane(lds_rd32(FL_C + 8)) == 99u) break;
        u32x2 f = {0u, 0u};
        asm volatile("buffer_atomic_or_x2 %0, %1, %2, 0 offen sc0 sc1\n\ts_waitcnt vmcnt(0)" : "+v"(f) : "v"(lane == 0 ? W_HF + c * 128 : 0xf0000000u), "s"(rws) : "memory");
        flagmax(FL_C + 8, lead(f));
      }
      flagmax(FL_C + 8, 99u);
      __builtin_amdgcn_s_setprio(0);
      lds_wait_ge(FL_C8, 1u);
      flag(FL_C, 9u);
#pragma unroll 1
      for (int u = 10; u < 16; u += 2) {
        lds_wait_min4(FL_CFREE, (unsigned)(u - 8));
        cfill(u, u - 9, 0, 16);
        waitv<0>();
        flag(FL_C, u == 14 ? 99u : (unsigned)(u + 1));
      }
    } else {
      u32x4 cq[NRG1][16];
#pragma unroll
      for (int g = 0; g < NRG1; ++g) {
        waitv<16>();
        if (g == 1) flag(FL_C + 4, 8u);
#pragma unroll
        for (int q = 0; q < 16; ++q) {
          const unsigned so = ((q & 7) >> 1) * 2048 + ((upos(11 + 2 * g) + crot) & 15) * 128;
          if (q < 8) asm volatile("buffer_load_dwordx4 %0, %1, %2, %3 offen" : "=v"(cq[g][q]) : "v"((q & 1) ? co : ce), "s"(rcr), "s"(so) : "memory");
          else asm volatile("buffer_load_dwordx4 %0, %1, %2, %3 offen" : "=v"(cq[g][q]) : "v"((q & 1) ? co : ce), "s"(rci), "s"(so) : "memory");
          if ((q & 7) == 7) waitv<16>();
        }
      }
      waitv<0>();
      if (NRG1 < 2) flag(FL_C + 4, 8u);
      lds_wait_min4(FL_CFREE, 1u);
      cfill(8, 0, 0, 16);
      cfill(9, 8, 0, 16);
      waitv<0>();
      flag(FL_C8, 1u);
      flag(FL_C + 4, 10u);
      __builtin_amdgcn_s_setprio(0);
#pragma unroll
      for (int g = 0; g < 3; ++g) {
        const int u = 11 + 2 * g;
        lds_wait_min4(FL_CFREE, (unsigned)(u - 8));
        if (g < NRG1) {
#pragma unroll
          for (int q = 0; q < 16; ++q)
            asm volatile("ds_write_b128 %1, %0 offset:%2" : "+v"(cq[g < NRG1 ? g : 0][q]) : "v"((u - 9) * 16384 + lane * 16), "n"(q * 1024) : "memory");
        } else {
          cfill(u, u - 9, 0, 16);
          waitv<0>();
        }
        flag(FL_C + 4, g == 2 ? 99u : (unsigned)(u + 1));
      }
    }
    asm volatile("Lk_l:");
    return;
  }
  {
    const int xw = wid - 6;
    __builtin_amdgcn_s_setprio(2);
    u32x2 ep;
    unsigned tagA, tagB;
    if (xw == 0) lds_wr32(L_FLG + lane * 4, 0u);
    else asm volatile("ds_write_b64 %0, %1\n\tds_write_b64 %2, %3" ::"v"(L_CAND + lane * 8), "v"((u32x2){0u, 0u}), "v"(L_MAP + lane * 8), "v"((u32x2){L_ZERO, L_ZERO}) : "memory");
    int b_ = bid;
    asm volatile("" : "+s"(b_));
    const bool ffn = b_ < 8 * NFW;
    const int sg = (b_ & 7) * 7 + (b_ >> 5), kq = (b_ >> 3) & 3, sg1 = (sg & ~1) + l, gu1 = sg & 1;
    const unsigned gub = W_GU + ((sg1 * 4 + kq) * 2 + gu1) * 2048, ypb = W_YP + (kq * 4 + l * 2) * YTILE + sg * 256;
    u32x4 w1[4][8], w2[2][8];
    const rsrc_t rw1 = mk(gu1 ? w_up : w_gate, ffn ? D_ * F_ * 2 : 0), r2 = mk(w_down, ffn ? D_ * F_ * 2 : 0);
    unsigned o1 = (kq * 256 + 32 * (fr >> 3) + 8 * fq) * (F_ * 2) + (sg1 * 64 + 8 * (fr & 7)) * 2;
    unsigned o2 = (sg * 64 + 8 * fq) * 2048 + (kq * 256 + xw * 128 + 8 * fr) * 2;
#define LDW1(b) { _Pragma("unroll") for (int i = 4 * ((b) & 1); i < 4 * ((b) & 1) + 4; ++i) \
      asm volatile("buffer_load_dwordx4 %0, %1, %2, %3 offen nt" : "=v"(w1[(b) >> 1][i]) : "v"(o1), "s"(rw1), "s"((((b) >> 1) * 64 + i) * (F_ * 2)) : "memory"); }
#define LDW2(a, b) { _Pragma("unroll") for (int i = a; i < b; ++i) \
      asm volatile("buffer_load_dwordx4 %0, %1, %2, %3 offen nt" : "=v"(w2[i >> 3][i & 7]) : "v"(o2), "s"(r2), "s"(((i & 7) + (i >> 3) * 32) * 2048) : "memory"); }
#define TRN(w, n) { asm volatile("s_waitcnt vmcnt(%8)" : "+v"(w[0]), "+v"(w[1]), "+v"(w[2]), "+v"(w[3]), "+v"(w[4]), "+v"(w[5]), "+v"(w[6]), "+v"(w[7]) : "n"(n) : "memory"); \
      unit_trn(w); asm volatile("" : "+v"(w[0]), "+v"(w[1]), "+v"(w[2]), "+v"(w[3]), "+v"(w[4]), "+v"(w[5]), "+v"(w[6]), "+v"(w[7])); }
    unsigned key[16];
    unsigned rgo = W_R2 + l * 4096 + lane * 16, ggo = W_G2 + c * 512 + (lane & 31) * 16;
    u32x4 g[4], a;
    asm volatile("" : "+v"(o1), "+v"(o2), "+v"(rgo), "+v"(ggo) : "s"(rw1), "s"(r2));
    auto A_EP = [&]() { asm volatile("buffer_load_dwordx2 %0, off, %1, 0 sc1" : "=v"(ep) : "s"(rws) : "memory"); };
    auto A_TAG = [&]() {
      asm volatile("" : "+v"(ep));
      tagA = ep.x + 1u; tagB = tagA + 0x40000000u;
      if (xw == 0 && lane == 0) lds_wr32(L_TAG, tagA);
    };
    auto A_CLR = [&]() {
      asm volatile("buffer_store_dwordx2 %0, %1, %2, 0 offen sc1" ::"v"((u32x2){0u, 0u}), "v"(j == 0 && xw == 0 && lane == 0 ? W_HF + c * 128 : 0xf0000000u), "s"(rws) : "memory");
      asm volatile("s_nop 1\n\t"
                   "buffer_store_dwordx4 %0, %1, %3, 0 offen sc1\n\tbuffer_store_dwordx4 %0, %1, %3, 0 offen offset:1024 sc1\n\t"
                   "buffer_store_dwordx4 %0, %2, %3, 0 offen sc1\n\tbuffer_store_dwordx4 %0, %2, %3, %4 offen sc1\n\t"
                   "buffer_store_dwordx4 %0, %2, %3, %5 offen sc1\n\tbuffer_store_dwordx4 %0, %2, %3, %6 offen sc1\n\ts_nop 7"
                   ::"v"((u32x4){~0u, ~0u, ~0u, ~0u}), "v"(ffn ? gub + lane * 16 : 0xf0000000u), "v"(ffn ? ypb + (lane >> 5) * YTOK + ((lane >> 4) & 1) * YTILE + fr * 16 : 0xf0000000u), "s"(rws),
                   "s"(2 * YTOK), "s"(4 * YTOK), "s"(6 * YTOK) : "memory");
    };
    auto A_PUB = [&]() {
      u32x2 rt;
      asm volatile("ds_read_b64 %0, %1\n\ts_waitcnt lgkmcnt(0)" : "=v"(rt) : "v"(L_RT + (lane & 1) * 16 + xw * 8) : "memory");
      st128c(rws, lane < 2 ? (lane ? W_G2 : W_R2) + (c * 128 + j * 8 + xw * 4) * 4 : 0xf0000000u, (u32x4){rt.x, tagA, rt.y, tagB});
    };
    auto A_GAT = [&]() {
#pragma unroll
      for (int q = 0; q < 4; ++q) asm volatile("buffer_load_dwordx4 %0, %1, %2, 0 offen offset:%3 sc1" : "=v"(g[q]) : "v"(rgo), "s"(rws), "n"(q * 1024) : "memory");
      asm volatile("buffer_load_dwordx4 %0, %1, %2, 0 offen sc1" : "=v"(a) : "v"(ggo), "s"(rws) : "memory");
    };
    A_EP();
    issue(slice(1) + so1, 32768);
    issue(slice(2) + so1, 65536);
    asm volatile("s_waitcnt vmcnt(17) lgkmcnt(0)" ::: "memory"); bar();
    issue(slice(3) + so1, 98304);
    waitv<24>(); A_TAG();
    A_CLR();
    asm volatile("s_waitcnt vmcnt(23) lgkmcnt(0)" ::: "memory"); bar();
    issue(slice(4) + so1, 0);
    asm volatile("s_waitcnt vmcnt(23) lgkmcnt(0)" ::: "memory"); bar();
    issue(slice(5) + so1, 32768);
    asm volatile("s_waitcnt vmcnt(23) lgkmcnt(0)" ::: "memory"); bar();
    issue(slice(6) + so1, 65536);
    waitv<24>(); A_PUB();
    constexpr int K0 = 4;
    {
      const int ms = K0 + ((b_ >> 3) + (b_ & 7)) % STN;
      u64 t = tbl >> (4 * K0 + 12);
#pragma unroll XUN
      for (int k = K0; k < 13; ++k) {
        const unsigned sk = (((unsigned)t & 15u) << 7) + so1;
        if ((unsigned)(k - ms - 1) < 3u) waitv<32>(); else waitv<16>();
        bar();
        issue(sk, ((k + 3) & 3) * 32768);
        if (k < K0 + STN && k == ms) LDW2(0, 16)
        t >>= 4;
      }
    }
    A_GAT();
#define LDW1R(a, b) { _Pragma("unroll") for (int b_ = (a); b_ < (b); ++b_) LDW1(b_) }
    LDW1R(0, W1A)
    waitv<21 + 4 * W1A>(); bar(); LDW1R(W1A, W1B)
    waitv<13 + 4 * W1B>(); bar(); LDW1R(W1B, W1C)
    waitv<5 + 4 * W1C>(); bar(); LDW1R(W1C, W1D)
    bar();
    __builtin_amdgcn_s_setprio(1);
    LDW1R(W1D, 8)
    if (lane == 0) lds_wr32(FL_W + xw * 4, 1u);
    asm volatile("s_waitcnt vmcnt(32)" : "+v"(g[0]), "+v"(g[1]), "+v"(g[2]), "+v"(g[3]), "+v"(a)::"memory");
    {
#pragma unroll 1
      for (int it = 0; it < SPIN_MAX; ++it) {
        bool ok = true;
#pragma unroll
        for (int q = 0; q < 4; ++q) ok = ok && g[q].y == tagA && g[q].w == tagB;
        if (__all(ok && a.y == tagA && a.w == tagB)) break;
        __builtin_amdgcn_s_sleep(8);
        A_GAT();
        asm volatile("s_waitcnt vmcnt(0)" : "+v"(g[0]), "+v"(g[1]), "+v"(g[2]), "+v"(g[3]), "+v"(a)::"memory");
      }
      if (l == 0) {
        const unsigned gp = lane < 32 ? a.x : a.z, ga = L_GT + ((4 * (lane & 1) + 2 * (lane >> 5)) * 16 + ((lane & 31) >> 1)) * 4;
        lds_wr32(ga, gp << 16);
        lds_wr32(ga + 64, gp & 0xffff0000u);
      }
      asm volatile("ds_add_u32 %0, %1" ::"v"(lane == 0 ? (unsigned)FL_G : (unsigned)(L_JNK + lane * 4)), "v"(1u) : "memory");
#pragma unroll
      for (int q = 0; q < 4; ++q) {
        const unsigned r0 = 2047 - (l * 1024 + q * 256 + lane * 4);
        key[4 * q] = (g[q].x << 16) + r0; key[4 * q + 1] = (g[q].x & 0xffff0000u) + (r0 - 1);
        key[4 * q + 2] = (g[q].z << 16) + (r0 - 2); key[4 * q + 3] = (g[q].z & 0xffff0000u) + (r0 - 3);
        asm volatile("" : "+v"(key[4 * q]), "+v"(key[4 * q + 1]), "+v"(key[4 * q + 2]), "+v"(key[4 * q + 3]));
      }
      unsigned m = key[0], m2 = 0, m3 = 0;
#pragma unroll
      for (int i = 1; i < 16; ++i) {
        asm("v_med3_u32 %0, %1, %0, %3\n\tv_med3_u32 %1, %2, %1, %3\n\tv_max_u32 %2, %2, %3" : "+v"(m3), "+v"(m2), "+v"(m) : "v"(key[i]));
      }
      asm volatile("" : "+v"(m), "+v"(m2), "+v"(m3));
      unsigned T = 0;
#pragma unroll
      for (int b = 31; b >= 16; --b) {
        const unsigned t2 = T | (1u << b);
        T = __popcll(__ballot(m >= t2)) >= 8 ? t2 : T;
      }
      asm volatile("" : "+s"(T));
      const u64 k1 = __ballot(m >= T), k2 = __ballot(m2 >= T);
      const int n1 = __popcll(k1);
      if (__ballot(m3 >= T) || n1 + __popcll(k2) > 16) {
        unsigned cand = 0;
#pragma unroll 1
        for (int s = 0; s < 8; ++s) {
          unsigned mm = key[0];
#pragma unroll
          for (int i = 1; i < 16; ++i) mm = max(mm, key[i]);
          mm = wave_maxu(mm);
          cand = lane == s ? mm : cand;
#pragma unroll
          for (int i = 0; i < 16; ++i) key[i] = key[i] == mm ? 0u : key[i];
        }
        if (lane < 8) lds_wr32(L_CAND + l * 64 + lane * 4, cand);
      } else {
        const unsigned p1 = __builtin_amdgcn_mbcnt_hi((unsigned)(k1 >> 32), __builtin_amdgcn_mbcnt_lo((unsigned)k1, 0u));
        const unsigned p2 = __builtin_amdgcn_mbcnt_hi((unsigned)(k2 >> 32), __builtin_amdgcn_mbcnt_lo((unsigned)k2, (unsigned)n1));
        lds_wr32(m >= T ? L_CAND + l * 64 + p1 * 4 : L_JNK + lane * 4, m);
        if (k2) lds_wr32(m2 >= T ? L_CAND + l * 64 + p2 * 4 : L_JNK + lane * 4, m2);
      }
      if (lane == 0) lds_wr32(FL_TK + 8 + l * 4, 1u);
      lds_wait_ge(FL_TK + 8 + (l ^ 1) * 4, 1u);
      const unsigned ck = *(const volatile LAS unsigned*)(smem + L_CAND + (lane & 31) * 4);
      int rank = 0;
#pragma unroll
      for (int i = 0; i < 4; ++i) {
        const u32x4 o = *(const volatile LAS u32x4*)(smem + L_CAND + (lane >> 5) * 64 + i * 16);
        rank += (o.x > ck) + (o.y > ck) + (o.z > ck) + (o.w > ck);
      }
      { auto p = __builtin_amdgcn_permlane32_swap((unsigned)rank, (unsigned)rank, false, false); rank = (int)(p[0] + p[1]); }
      if (l == 0) {
        if (lane < 32 && ck != 0u && rank < 8) {
          const int t = 2047 - (int)(ck & 0xffffu);
          lds_wr32(L_SEL + rank * 4, ck);
          if ((t >> 7) == c) lds_wr32(L_MAP + ((t & 7) * 16 + ((t & 127) >> 3)) * 4, (unsigned)(L_BROW + rank * 256));
        }
        const unsigned any = __ballot(ck != 0u && rank < 8 && ((2047 - (int)(ck & 0xffffu)) >> 7) == c) ? 2u : 1u;
        flag(FL_SEL, any);
        if (bid == 0 && lane == 0) st64c(rws, W_EPOCH, (u32x2){tagA, tagB});
      }
    }
    lds_wait_ge(FL_SEL, 1u);
    const unsigned mine = lds_rd32(L_SEL + (lane & 7) * 4);
    {
      {
        u32x4 xa[4];
#pragma unroll
        for (int u = 0; u < 4; ++u)
          asm volatile("buffer_load_dwordx4 %0, %1, %2, 0 offen offset:%3" : "=v"(xa[u]) : "v"((2047 - (mine & 0xffffu)) * 2048 + (kq * 256 + 32 * (fr >> 3) + 8 * fq) * 2), "s"(rx), "n"(u * 128) : "memory");
        TRN(w1[0], 20) TRN(w1[1], 20) TRN(w1[2], 12) TRN(w1[3], 0)
        asm volatile("" : "+v"(xa[0]), "+v"(xa[1]), "+v"(xa[2]), "+v"(xa[3]));
        const bool val = (fq >> 1) == (fr >> 3), lo = lane < 32;
        auto publish = [&](f32x4 (&d)[8], unsigned o, unsigned sp) {
          u32x4 pv[4];
#pragma unroll
          for (int rr = 0; rr < 2; ++rr)
#pragma unroll
            for (int q = 0; q < 8; ++q) {
              auto p = __builtin_amdgcn_permlane32_swap(__float_as_uint(d[q][rr]), __float_as_uint(d[q][2 + rr]), false, false);
              const unsigned own = lo ? p[0] : p[1], oth = lo ? p[1] : p[0];
              pv[rr * 2 + (q >> 2)][q & 3] = min(__float_as_uint(__uint_as_float(own) + dpp1<0x128>(__uint_as_float(oth))), 0xfffffffeu);
            }
          asm volatile("s_nop 4\n\t"
                       "buffer_store_dwordx4 %0, %4, %5, 0 offen sc1\n\tbuffer_store_dwordx4 %1, %4, %5, %6 offen sc1\n\t"
                       "buffer_store_dwordx4 %2, %4, %5, %7 offen sc1\n\tbuffer_store_dwordx4 %3, %4, %5, %8 offen sc1\n\t"
                       "s_nop 7\n\ts_nop 7"
                       ::"v"(pv[0]), "v"(pv[1]), "v"(pv[2]), "v"(pv[3]), "v"(val && ffn ? o : 0xf0000000u), "s"(rws), "s"(sp), "s"(2 * sp), "s"(3 * sp) : "memory");
        };
        f32x4 d[8];
#pragma unroll
        for (int q = 0; q < 8; ++q) d[q] = (f32x4){0.f, 0.f, 0.f, 0.f};
#pragma unroll
        for (int u = 0; u < 4; ++u)
#pragma unroll
          for (int q = 0; q < 8; ++q) d[q] = __builtin_amdgcn_mfma_f32_16x16x32_bf16(__builtin_bit_cast(bf16x8, xa[u]), __builtin_bit_cast(bf16x8, w1[u][q]), d[q], 0, 0, 0);
        publish(d, gub + (16 * (fr >> 3) + 8 * (fq & 1) + (fr & 7)) * 16, 512);
        TRN(w2[0], 63) TRN(w2[1], 63)
        lds_wait_ge(FL_CFREE, XG2);
        __builtin_amdgcn_s_setprio(3);
        if (ffn) {
          f32x4 G, U;
#pragma unroll 1
          for (int it = 0; it < SPIN_MAX; ++it) {
            FRESH();
            u32x4 a_[8];
#pragma unroll
            for (int k = 0; k < 8; ++k) a_[k] = __builtin_amdgcn_raw_buffer_load_b128(rws, W_GU + sg * 16384 + xw * 1024 + lane * 16, k * 2048, 16);
            unsigned m = 0;
#pragma unroll
            for (int k = 0; k < 8; ++k) m = max(max(m, max(a_[k].x, a_[k].y)), max(a_[k].z, a_[k].w));
            G = (__builtin_bit_cast(f32x4, a_[0]) + __builtin_bit_cast(f32x4, a_[2])) + (__builtin_bit_cast(f32x4, a_[4]) + __builtin_bit_cast(f32x4, a_[6]));
            U = (__builtin_bit_cast(f32x4, a_[1]) + __builtin_bit_cast(f32x4, a_[3])) + (__builtin_bit_cast(f32x4, a_[5]) + __builtin_bit_cast(f32x4, a_[7]));
            if (__all(m != ~0u)) break;
            __builtin_amdgcn_s_sleep(8);
          }
          float a4[4];
#pragma unroll
          for (int e = 0; e < 4; ++e) { const float g = rbf(G[e]); a4[e] = rbf(g * __builtin_amdgcn_rcpf(1.f + __expf(-g))) * rbf(U[e]); }
          asm volatile("s_nop 0\n\tds_write_b64 %0, %1" ::"v"(L_ACT + (4 * ((lane >> 3) & 1) + 2 * ((lane >> 4) & 1) + xw) * 128 + (lane & 7) * 16 + (lane >> 5) * 8),
                       "v"((u32x2){pk2(a4[0], a4[1]), pk2(a4[2], a4[3])}) : "memory");
          if (lane == 0) lds_wr32(FL_TK + xw * 4, 1u);
          lds_wait_ge(FL_TK + (xw ^ 1) * 4, 1u);
        }
        const u32x4 af0 = lds_rd128(L_ACT + ((fr & 7) * 8 + fq) * 16), af1 = lds_rd128(L_ACT + ((fr & 7) * 8 + 4 + fq) * 16);
#pragma unroll
        for (int q = 0; q < 8; ++q) d[q] = __builtin_amdgcn_mfma_f32_16x16x32_bf16(__builtin_bit_cast(bf16x8, af0), __builtin_bit_cast(bf16x8, w2[0][q]), (f32x4){0.f, 0.f, 0.f, 0.f}, 0, 0, 0);
#pragma unroll
        for (int q = 0; q < 8; ++q) d[q] = __builtin_amdgcn_mfma_f32_16x16x32_bf16(__builtin_bit_cast(bf16x8, af1), __builtin_bit_cast(bf16x8, w2[1][q]), d[q], 0, 0, 0);
        {
          u32x4 pv[4];
#pragma unroll
          for (int r = 0; r < 4; ++r)
#pragma unroll
            for (int q = 0; q < 4; ++q) pv[r][q] = min(__float_as_uint(lo ? d[q][r] : d[4 + q][r]), 0xfffffffeu);
          asm volatile("s_nop 4\n\t"
                       "buffer_store_dwordx4 %0, %4, %5, 0 offen sc1\n\tbuffer_store_dwordx4 %1, %4, %5, %6 offen sc1\n\t"
                       "buffer_store_dwordx4 %2, %4, %5, %7 offen sc1\n\tbuffer_store_dwordx4 %3, %4, %5, %8 offen sc1\n\t"
                       "s_nop 7\n\ts_nop 7"
                       ::"v"(pv[0]), "v"(pv[1]), "v"(pv[2]), "v"(pv[3]), "v"(ffn ? ypb + (fq & 1) * 4 * YTOK + (fr >> 3) * YTILE + (lane >> 5) * 128 + (fr & 7) * 16 : 0xf0000000u),
                       "s"(rws), "s"(YTOK), "s"(2 * YTOK), "s"(3 * YTOK) : "memory");
        }
      }
    }
    __builtin_amdgcn_s_setprio(3);
    unsigned rest = (unsigned)__ballot(((2047 - (int)(mine & 0xffffu)) >> 7) == c) & 0xffu;
    if (xw) rest &= rest - 1;
#pragma unroll 1
    for (; rest; rest &= rest - 1, rest &= rest - 1) {
      const int s = __builtin_ctz(rest);
      const unsigned ks = (unsigned)__builtin_amdgcn_readlane((int)mine, s);
      const int t = 2047 - (int)(ks & 0xffffu);
      const float rs = rbf(1.f / (1.f + __expf(-key_val(ks))));
      const unsigned yb = W_YP + s * YTOK + j * YTILE;
      const u32x2 xr = __builtin_amdgcn_raw_buffer_load_b64(rx, t * 2048 + (j * 64 + 8 * (lane & 7) + 4 * ((lane >> 3) & 1)) * 2, 0, 0);
      lds_wait_ge(FL_CFREE, YGATE);
      const float omg = rbf(1.f - __uint_as_float(lds_rd32(L_GT + ((t & 7) * 16 + ((t & 127) >> 3)) * 4)));
      f32x4 y;
#pragma unroll 1
      for (int it = 0; it < SPIN_MAX; ++it) {
        FRESH();
        unsigned m = 0;
        f32x4 y2[2] = {{0.f, 0.f, 0.f, 0.f}, {0.f, 0.f, 0.f, 0.f}};
#pragma unroll
        for (int k = 0; k < 14; ++k) {
          const u32x4 a = __builtin_amdgcn_raw_buffer_load_b128(rws, yb + lane * 16, k * 1024, 16);
          m = max(max(m, max(a.x, a.y)), max(a.z, a.w));
          y2[k & 1] += __builtin_bit_cast(f32x4, a);
        }
        y = y2[0] + y2[1];
        if (__all(m != ~0u)) break;
        __builtin_amdgcn_s_sleep(6);
      }
      const f32x4 xf = unpk4(xr);
      f32x4 dl;
#pragma unroll
      for (int e = 0; e < 4; ++e) {
        const float v = sum16_32(y[e]);
        dl[e] = rbf(mul1(omg, rbf(xf[e] + rbf(mul1(rbf(v), rs))))) - rbf(mul1(omg, xf[e]));
      }
      if (lane < 16) asm volatile("s_nop 0\n\tds_write_b128 %0, %1\n\ts_nop 3" ::"v"(L_BROW + s * 256 + (lane & 7) * 32 + (lane >> 3) * 16), "v"(dl) : "memory");
    }
    if (lane == 0) lds_wr32(FL_XD + xw * 4, 1u);
    asm volatile("Lk_x:");
    return;
  }
  }

  __builtin_amdgcn_s_setprio(3);
  relane();
  u64x8 mb;
  asm volatile("s_load_dwordx16 %0, %1, 0x40\n\ts_waitcnt lgkmcnt(0)" : "=&s"(mb) : "s"(__builtin_amdgcn_kernarg_segment_ptr()), "s"(kw0), "s"(kw1));
  const char* out = (const char*)mb[7];
  char* ws = (char*)mb[6];
  const rsrc_t rws = mk(ws, W_END);
  f32x4 lr, li, cr, ci, Er, Ei, Fr, Fi, br_, bi_;
  u32x2 xe[2][4];
  {
    asm volatile("s_waitcnt vmcnt(%3)" : "+v"(p0), "+v"(p1), "+v"(p2) : "n"(9) : "memory");
    const f32x4 lre = unpk4(p0), lim = unpk4(p1), ld = unpk4(p2) * splat4(1.44269504f);
    f32x4 dt, id = fma4(lre, lre, lim * lim);
#pragma unroll
    for (int r = 0; r < 4; ++r) { dt[r] = rbf(__builtin_amdgcn_exp2f(ld[r])); id[r] = __builtin_amdgcn_rcpf(id[r]); }
    const f32x4 A2 = dt * lre * splat4(1.44269504f), Bv = dt * lim * splat4(0.159154943f);
    cexp4(A2, Bv, lr, li);
    const f32x4 nr = lr - splat4(1.f);
    cr = fma4(nr, lre, li * lim) * id; ci = fma4(li, lre, nml4(nr, lim)) * id;
    const f32x4 ne = splat4((float)(8 * fr)), nf = splat4((float)((fr & 7) + 1));
    cexp4(A2 * ne, Bv * ne, Er, Ei);
    f32x4 tr, ti;
    cexp4(A2 * nf, Bv * nf, tr, ti);
    Fr = fma4(cr, tr, nml4(ci, ti)); Fi = fma4(cr, ti, ci * tr);
    const float a1 = (fr & 2) ? ((fr & 1) ? A2.w : A2.z) : ((fr & 1) ? A2.y : A2.x), b1 = (fr & 2) ? ((fr & 1) ? Bv.w : Bv.z) : ((fr & 1) ? Bv.y : Bv.x);
    const int e0 = c - 1 - (fr >> 2);
    const f32x4 nl = {(float)(128 * max(e0, 0)), (float)(128 * max(e0 - 4, 0)), (float)(128 * max(e0 - 8, 0)), (float)(128 * max(e0 - 12, 0))};
    cexp4(splat4(a1) * nl, splat4(b1) * nl, br_, bi_);
    asm volatile("" : "+v"(lr), "+v"(li), "+v"(cr), "+v"(ci), "+v"(Er), "+v"(Ei), "+v"(Fr), "+v"(Fi), "+v"(br_), "+v"(bi_));
  }
  asm volatile("s_waitcnt vmcnt(%9)" : "+v"(dv), "+v"(xv[0][0]), "+v"(xv[0][1]), "+v"(xv[1][0]), "+v"(xv[1][1]), "+v"(wv[0]), "+v"(wv[1]), "+v"(gv[0]), "+v"(gv[1]) : "n"(0) : "memory");
  {
    float sv[4];
#pragma unroll
    for (int r = 0; r < 2; ++r) {
      float sr = 0.f, sg_ = 0.f;
#pragma unroll
      for (int h = 0; h < 2; ++h)
#pragma unroll
        for (int e = 0; e < 4; ++e) {
          const unsigned xx = xv[r][h][e], ww = wv[h][e], gw = gv[h][e];
          sr = __builtin_amdgcn_fdot2_f32_bf16(__builtin_bit_cast(bf16x2_t, xx), __builtin_bit_cast(bf16x2_t, ww), sr, false);
          sg_ = __builtin_amdgcn_fdot2_f32_bf16(__builtin_bit_cast(bf16x2_t, xx), __builtin_bit_cast(bf16x2_t, gw), sg_, false);
        }
      sv[r] = sr; sv[2 + r] = sg_;
    }
    const bool b1 = lane & 1, b2 = lane & 2;
    const float k0 = (b1 ? sv[1] : sv[0]) + dpp1<0xb1>(b1 ? sv[0] : sv[1]), k1 = (b1 ? sv[3] : sv[2]) + dpp1<0xb1>(b1 ? sv[2] : sv[3]);
    float k2 = (b2 ? k1 : k0) + dpp1<0x4e>(b2 ? k0 : k1);
    k2 += dpp1<0x124>(k2);
    k2 += dpp1<0x128>(k2);
    k2 = sum16_32(k2);
    const unsigned vb = b2 ? f2bf(1.f / (1.f + __expf(-rbf(k2)))) : route_key(rbf(k2), 0) >> 16;
    const unsigned p01 = vb | ((unsigned)__builtin_amdgcn_update_dpp(0, (int)vb, 0xb1, 0xf, 0xf, true) << 16);
    asm volatile("ds_write_b32 %0, %1\n\ts_waitcnt lgkmcnt(0)" ::"v"((lane & 61) == 0 ? (unsigned)(L_RT + (lane * 2 + wid) * 4) : (unsigned)(L_JNK + lane * 4)), "v"(p01) : "memory");
  }
  __builtin_amdgcn_s_setprio(2);
  unsigned tagA = L_TAG;
  f32x4 acc[2][8];
  const unsigned ago = fr < 4 ? W_AGG + ((j * 16 + c) * 64 + wid * 16 + 4 * fq + fr) * 16u : 0xf0000000u;
  {
#pragma unroll
    for (int m = 0; m < 2; ++m)
#pragma unroll
      for (int n = 0; n < 8; ++n) acc[m][n] = (f32x4){0.f, 0.f, 0.f, 0.f};
    const int x0 = (fq ^ (fr & 7)) * 16, x1 = ((4 + fq) ^ (fr & 7)) * 16;
    unsigned pa0 = 16384 + (wid * 32 + fr) * 128 + x0, pa1 = 16384 + (wid * 32 + fr) * 128 + x1, pb0 = fr * 128 + x0, pb1 = fr * 128 + x1;
    unsigned sit, xad[4];
#pragma unroll
    for (int r = 0; r < 4; ++r) xad[r] = 98304 + (32 * wid + 4 * fq + r) * 128 + (((fr >> 1) ^ (4 * (fq & 1) + r)) * 16) + (fr & 1) * 8;
    bf16x8 fa[2][2], fb[8];
    asm volatile(ASM_GEMM1 : [c00] "+v"(acc[0][0]), [c01] "+v"(acc[0][1]), [c02] "+v"(acc[0][2]), [c03] "+v"(acc[0][3]), [c04] "+v"(acc[0][4]), [c05] "+v"(acc[0][5]), [c06] "+v"(acc[0][6]), [c07] "+v"(acc[0][7]), [c10] "+v"(acc[1][0]), [c11] "+v"(acc[1][1]), [c12] "+v"(acc[1][2]), [c13] "+v"(acc[1][3]), [c14] "+v"(acc[1][4]), [c15] "+v"(acc[1][5]), [c16] "+v"(acc[1][6]), [c17] "+v"(acc[1][7]), [a00] "=&v"(fa[0][0]), [a01] "=&v"(fa[0][1]), [a10] "=&v"(fa[1][0]), [a11] "=&v"(fa[1][1]), [b0] "=&v"(fb[0]), [b1] "=&v"(fb[1]), [b2] "=&v"(fb[2]), [b3] "=&v"(fb[3]), [b4] "=&v"(fb[4]), [b5] "=&v"(fb[5]), [b6] "=&v"(fb[6]), [b7] "=&v"(fb[7]), [pa0] "+v"(pa0), [pa1] "+v"(pa1), [pb0] "+v"(pb0), [pb1] "+v"(pb1), [it] "=&s"(sit), [tg] "+v"(tagA), [xe00] "=&v"(xe[0][0]), [xe01] "=&v"(xe[0][1]), [xe02] "=&v"(xe[0][2]), [xe03] "=&v"(xe[0][3]), [xe10] "=&v"(xe[1][0]), [xe11] "=&v"(xe[1][1]), [xe12] "=&v"(xe[1][2]), [xe13] "=&v"(xe[1][3]) : [xa0] "v"(xad[0]), [xa1] "v"(xad[1]), [xa2] "v"(xad[2]), [xa3] "v"(xad[3]) : "memory", "scc", "v252", "v253", "v254", "v255");
  }
  __builtin_amdgcn_s_setprio(3);
  const unsigned tagB = tagA + 0x40000000u;
  {
#pragma unroll
    for (int n = 1; n < 8; ++n) {
      const f32x4 pr = acc[0][n - 1], pi = acc[1][n - 1];
      acc[0][n] = fma4(lr, pr, fnm4(li, pi, acc[0][n]));
      acc[1][n] = fma4(lr, pi, fma4(li, pr, acc[1][n]));
    }
    const f32x4 wr = dpp4<0x140>(Er), wi = dpp4<0x140>(Ei);
    const f32x4 zr = fma4(wr, acc[0][7], nml4(wi, acc[1][7])), zi = fma4(wr, acc[1][7], wi * acc[0][7]);
    const bool o1 = fr & 1, o2 = fr & 2;
    const float r01 = (o1 ? zr.y : zr.x) + dpp1<0xb1>(o1 ? zr.x : zr.y), r23 = (o1 ? zr.w : zr.z) + dpp1<0xb1>(o1 ? zr.z : zr.w);
    const float i01 = (o1 ? zi.y : zi.x) + dpp1<0xb1>(o1 ? zi.x : zi.y), i23 = (o1 ? zi.w : zi.z) + dpp1<0xb1>(o1 ? zi.z : zi.w);
    float sr = (o2 ? r23 : r01) + dpp1<0x4e>(o2 ? r01 : r23), si = (o2 ? i23 : i01) + dpp1<0x4e>(o2 ? i01 : i23);
    sr += dpp1<0x124>(sr); si += dpp1<0x124>(si);
    sr += dpp1<0x128>(sr); si += dpp1<0x128>(si);
    u32x4 av = {__float_as_uint(sr), tagA, __float_as_uint(si), tagB};
    asm volatile("s_nop 1\n\tbuffer_store_dwordx4 %0, %1, %2, 0 offen sc1\n\ts_nop 7" : "+v"(av) : "v"(ago), "s"(rws) : "memory");
  }
  unsigned ao[4];
  u32x4 g[4];
#pragma unroll
  for (int i = 0; i < 4; ++i) {
    ao[i] = (fr >> 2) + 4 * i < c ? W_AGG + ((j * 16 + (fr >> 2) + 4 * i) * 64 + wid * 16 + 4 * fq + (fr & 3)) * 16u : 0xf0000000u;
    g[i] = (u32x4){0u, 0u, 0u, 0u};
  }
  f32x4 Pr, Pi, qr[8], qi[8];
  {
    f32x4 yr = acc[0][7], yi = acc[1][7];
#define KS_STEP(S, CTRL) { const f32x4 mr = dpp4<0x150 + S>(Er), mi = dpp4<0x150 + S>(Ei); const f32x4 sr = dpp4<CTRL>(yr), si = dpp4<CTRL>(yi); \
      yr = fma4(mr, sr, fnm4(mi, si, yr)); yi = fma4(mr, si, fma4(mi, sr, yi)); }
    KS_STEP(1, 0x111) KS_STEP(2, 0x112) KS_STEP(4, 0x114) KS_STEP(8, 0x118)
    Pr = dpp4<0x111>(yr); Pi = dpp4<0x111>(yi);
    asm volatile("" : "+v"(Pr), "+v"(Pi));
#pragma unroll
    for (int n = 0; n < 8; ++n) {
      bcast8(n, Fr, qr[n]); bcast8(n, Fi, qi[n]);
      const f32x4 br = fma4(cr, acc[0][n], nml4(ci, acc[1][n])), bi = fnm4(cr, acc[1][n], nml4(ci, acc[0][n]));
      acc[0][n] = br; acc[1][n] = bi;
      asm volatile("" : "+v"(acc[0][n]), "+v"(acc[1][n]), "+v"(qr[n]), "+v"(qi[n]));
    }
  }
  f32x4 Xr = Pr, Xi = Pi;
  {
    FRESH();
#pragma unroll
    for (int i = 0; i < 4; ++i)
      if (4 * i < c) g[i] = ld128c(rws, ao[i]);
    FRESH();
  }
  u32x2 xd[2][4];
  lds_wait_ge(FL_G, 2u);
  {
    const f32x4 df = unpk4(dv) * splat4(0.5f);
#pragma unroll
    for (int m = 0; m < 2; ++m) {
      const f32x4 ge = __builtin_bit_cast(f32x4, lds_rd128(L_GT + ((2 * wid + m) * 16 + 4 * fq) * 4));
#pragma unroll
      for (int r = 0; r < 4; ++r) {
        const f32x4 xf = unpk4(xe[m][r]), p = xf * df, o = xf * splat4(rbf(1.f - ge[r]));
        xd[m][r] = (u32x2){pk2(p.x, p.y), pk2(p.z, p.w)};
        xe[m][r] = (u32x2){pk2(o.x, o.y), pk2(o.z, o.w)};
        asm volatile("" : "+v"(xd[m][r]), "+v"(xe[m][r]));
      }
    }
  }
  if (c > 0) {
#pragma unroll 1
    for (int it = 0; it < SPIN_MAX; ++it) {
      unsigned bad = 0;
#pragma unroll
      for (int i = 0; i < 4; ++i) bad |= ((g[i].y ^ tagA) | (g[i].w ^ tagB)) & ~(unsigned)((int)ao[i] >> 31);
      if (!__ballot(bad != 0u)) break;
      FRESH();
#pragma unroll
      for (int i = 0; i < 4; ++i)
        if (4 * i < c) g[i] = ld128c(rws, ao[i]);
    }
    const f32x4 sx = {__uint_as_float(g[0].x), __uint_as_float(g[1].x), __uint_as_float(g[2].x), __uint_as_float(g[3].x)};
    const f32x4 sy = {__uint_as_float(g[0].z), __uint_as_float(g[1].z), __uint_as_float(g[2].z), __uint_as_float(g[3].z)};
    const f32x4 tx = fma4(br_, sx, nml4(bi_, sy)), ty = fma4(br_, sy, bi_ * sx);
    float gx = (tx.x + tx.z) + (tx.y + tx.w), gy = (ty.x + ty.z) + (ty.y + ty.w);
    gx += dpp1<0x124>(gx); gy += dpp1<0x124>(gy);
    gx += dpp1<0x128>(gx); gy += dpp1<0x128>(gy);
    const f32x4 Gr = {dpp1<0x00>(gx), dpp1<0x55>(gx), dpp1<0xaa>(gx), dpp1<0xff>(gx)}, Gi = {dpp1<0x00>(gy), dpp1<0x55>(gy), dpp1<0xaa>(gy), dpp1<0xff>(gy)};
    Xr = fma4(Er, Gr, fnm4(Ei, Gi, Pr)); Xi = fma4(Er, Gi, fma4(Ei, Gr, Pi));
  }
  relane();
  const unsigned jnk = L_JNK + lane * 4;
  const unsigned ho = W_H + c * 524288 + (4 * pown + wid) * 8192 + lane * 16;
  u32x4 hv[8];
  __builtin_amdgcn_sched_barrier(0);
#pragma unroll
  for (int n = 0; n < 8; ++n) {
    const f32x4 hr = fma4(qr[n], Xr, fnm4(qi[n], Xi, acc[0][n])), hi = fnm4(qr[n], Xi, fnm4(qi[n], Xr, acc[1][n]));
    const auto p = __builtin_amdgcn_permlane16_swap(pk2(hr.x, hr.y), pk2(hi.x, hi.y), false, false);
    const auto q = __builtin_amdgcn_permlane16_swap(pk2(hr.z, hr.w), pk2(hi.z, hi.w), false, false);
    hv[n] = (u32x4){p[0], q[0], p[1], q[1]};
    asm volatile("s_nop 1\n\tbuffer_store_dwordx4 %0, %1, %2, %3 offen offset:%4 sc1" : "+v"(hv[n]) : "v"(ho), "s"(rws), "s"((n >> 2) * 4096), "n"((n & 3) * 1024) : "memory");
    __builtin_amdgcn_sched_barrier(0);
  }
  {
#pragma unroll
    for (int w = 0; w < 4; ++w)
      if (w != wid) asm volatile("ds_write_b128 %0, %1\n\tds_write_b128 %0, %2 offset:1024" ::"v"(L_XH + (w * 3 + wid - (wid > w ? 1 : 0)) * 2048 + lane * 16), "v"(hv[2 * w]), "v"(hv[2 * w + 1]) : "memory");
  }
  if (lane == 0) lds_wr32(FL_HS + wid * 4, 1u);
  const rsrc_t rh = mk(ws + W_H + c * 524288, 524288);
  const unsigned vo = wid * 2048 + lane * 16;
  u32x32 hw0, hw1;
  unsigned st[10], pc[4];
  f32x4 ac2[2][4];
  const u64 mb_ = 1ull << (4 * pown + wid);
  u32x2 mf = {(unsigned)mb_, (unsigned)(mb_ >> 32)};
#pragma unroll
  for (int sg = 0; sg < 4; ++sg) pc[sg] = (fq & 1) * 8192 + fr * 128 + (((2 * sg + (fq >> 1)) ^ ((fr >> 1) & 7)) * 16);
  {
    lds_wait_ge(FL_C, 1u);
    u32x4 k[4][4], h[4][2];
#pragma unroll
    for (int i = 0; i < 4; ++i) {
      const int sw = i == 0 ? wid : i - (i <= wid ? 1 : 0);
      asm volatile("ds_read_b128 %0, %4\n\tds_read_b128 %1, %4 offset:2048\n\tds_read_b128 %2, %4 offset:4096\n\tds_read_b128 %3, %4 offset:6144"
                   : "=&v"(k[i][0]), "=&v"(k[i][1]), "=&v"(k[i][2]), "=&v"(k[i][3]) : "v"(pc[0] ^ (sw * 32)) : "memory");
    }
#pragma unroll
    for (int m = 0; m < 2; ++m)
#pragma unroll
      for (int r = 0; r < 4; ++r) {
        const f32x4 t = unpk4(xd[m][r]);
#pragma unroll
        for (int n = 0; n < 4; ++n) ac2[m][n][r] = t[n];
      }
    h[0][0] = hv[0]; h[0][1] = hv[1];
    if (wid == 1) { h[0][0] = hv[2]; h[0][1] = hv[3]; }
    if (wid == 2) { h[0][0] = hv[4]; h[0][1] = hv[5]; }
    if (wid == 3) { h[0][0] = hv[6]; h[0][1] = hv[7]; }
    asm volatile("" : "+v"(ac2[0][0]), "+v"(ac2[0][1]), "+v"(ac2[0][2]), "+v"(ac2[0][3]), "+v"(ac2[1][0]), "+v"(ac2[1][1]), "+v"(ac2[1][2]), "+v"(ac2[1][3]), "+v"(h[0][0]), "+v"(h[0][1]));
    asm volatile("s_waitcnt vmcnt(0)" : "+v"(hv[0]), "+v"(hv[1]), "+v"(hv[2]), "+v"(hv[3]), "+v"(hv[4]), "+v"(hv[5]), "+v"(hv[6]), "+v"(hv[7])::"memory");
    asm volatile("buffer_atomic_or_x2 %0, %1, %2, 0 offen sc0 sc1" : "+v"(mf) : "v"(lane == 0 ? W_HF + c * 128 : 0xf0000000u), "s"(rws) : "memory");
    lds_wr32(lane == 0 ? (unsigned)(FL_FREE + wid * 4) : jnk, 9u);
    lds_wait_min4(FL_HS, 1u);
    asm volatile("ds_read_b128 %0, %6\n\tds_read_b128 %1, %6 offset:1024\n\tds_read_b128 %2, %6 offset:2048\n\tds_read_b128 %3, %6 offset:3072\n\tds_read_b128 %4, %6 offset:4096\n\tds_read_b128 %5, %6 offset:5120"
                 : "=&v"(h[1][0]), "=&v"(h[1][1]), "=&v"(h[2][0]), "=&v"(h[2][1]), "=&v"(h[3][0]), "=&v"(h[3][1]) : "v"(L_XH + wid * 6144 + lane * 16) : "memory");
    lds_wr32(lane == 0 ? (unsigned)(FL_CFREE + wid * 4) : jnk, 1u);
#pragma unroll
    for (int i = 0; i < 4; ++i) {
      if (i == 1) asm volatile("s_waitcnt lgkmcnt(0)" : "+v"(h[1][0]), "+v"(h[1][1]), "+v"(h[2][0]), "+v"(h[2][1]), "+v"(h[3][0]), "+v"(h[3][1])::"memory");
      asm volatile("" : "+v"(k[i][0]), "+v"(k[i][1]), "+v"(k[i][2]), "+v"(k[i][3]));
#pragma unroll
      for (int n = 0; n < 4; ++n) {
        ac2[0][n] = __builtin_amdgcn_mfma_f32_16x16x32_bf16(__builtin_bit_cast(bf16x8, h[i][0]), __builtin_bit_cast(bf16x8, k[i][n]), ac2[0][n], 0, 0, 0);
        ac2[1][n] = __builtin_amdgcn_mfma_f32_16x16x32_bf16(__builtin_bit_cast(bf16x8, h[i][1]), __builtin_bit_cast(bf16x8, k[i][n]), ac2[1][n], 0, 0, 0);
      }
      asm volatile("" : "+v"(ac2[0][0]), "+v"(ac2[0][1]), "+v"(ac2[0][2]), "+v"(ac2[0][3]), "+v"(ac2[1][0]), "+v"(ac2[1][1]), "+v"(ac2[1][2]), "+v"(ac2[1][3])::"memory");
    }
  }
  f32x4 ov[2][4];
  float g2[2][4];
#pragma unroll
  for (int m = 0; m < 2; ++m) {
    const f32x4 ge = lds4(smem, L_GT + ((2 * wid + m) * 16 + 4 * fq) * 4);
#pragma unroll
    for (int r = 0; r < 4; ++r) {
      ov[m][r] = unpk4(xe[m][r]);
      g2[m][r] = ge[r] + ge[r];
      asm volatile("" : "+v"(ov[m][r]), "+v"(g2[m][r]));
    }
  }
  unsigned ra[2][4];
  lds_wait_ge(FL_SEL, 1u);
  const unsigned routed = __builtin_amdgcn_readfirstlane(lds_rd32(FL_SEL));
#pragma unroll
  for (int m = 0; m < 2; ++m) {
    const u32x4 me = lds_rd128(L_MAP + ((2 * wid + m) * 16 + 4 * fq) * 4);
#pragma unroll
    for (int r = 0; r < 4; ++r) { ra[m][r] = me[r] + fr * 16; asm volatile("" : "+v"(ra[m][r])); }
  }
  const rsrc_t ro = mk(out, 2048 * 1024 * 4);
  unsigned oo = (c * 128 + 32 * fq + 2 * wid) * 4096 + (j * 64 + 4 * fr) * 4;
  asm volatile("" : "+v"(oo) : "s"(ro));
  {
    asm volatile("s_waitcnt vmcnt(0)" : "+v"(mf)::"memory");
    asm volatile("ds_max_u32 %0, %1" ::"v"(lane == 0 ? (unsigned)(FL_C + 8) : jnk), "v"(lead(mf)) : "memory");
  }
  __builtin_amdgcn_s_setprio(2);

  {
    const unsigned fad = FL_C, fwd = lane == 0 ? (unsigned)(FL_CFREE + wid * 4) : jnk;
    bf16x8 kb[2][4];
    unsigned fv = 1;
#pragma unroll
    for (int sg = 0; sg < 4; ++sg) pc[sg] += 16384;
    asm volatile(ASM_GEMM2 : [c00] "+v"(ac2[0][0]), [c01] "+v"(ac2[0][1]), [c02] "+v"(ac2[0][2]), [c03] "+v"(ac2[0][3]), [c10] "+v"(ac2[1][0]), [c11] "+v"(ac2[1][1]), [c12] "+v"(ac2[1][2]), [c13] "+v"(ac2[1][3]), "=&{v[184:215]}"(hw0), "=&{v[216:247]}"(hw1), [k00] "=&v"(kb[0][0]), [k01] "=&v"(kb[0][1]), [k02] "=&v"(kb[0][2]), [k03] "=&v"(kb[0][3]), [k10] "=&v"(kb[1][0]), [k11] "=&v"(kb[1][1]), [k12] "=&v"(kb[1][2]), [k13] "=&v"(kb[1][3]), [pc0] "+v"(pc[0]), [pc1] "+v"(pc[1]), [pc2] "+v"(pc[2]), [pc3] "+v"(pc[3]), [fv] "+v"(fv), [s] "=&s"(st[0]), [s2] "=&s"(st[1]), [to] "=&s"(st[2]), [hn] "=&s"(st[3]), [cn] "=&s"(st[4]), [dl] "=&s"(st[5]), [hb0] "=&s"(st[6]), [hb1] "=&s"(st[7]), [hb2] "=&s"(st[8]), [hb3] "=&s"(st[9]) : [vo] "v"(vo), [own] "s"(pown), [srd] "s"(rh), [fa] "v"(fad), [fw] "v"(fwd), [d16] "s"(0x4000), [wr] "s"(0x500) : "memory", "scc", "vcc", "v250", "v251", "v252", "v253", "v254", "v255");
  }
  if (routed > 1u) {
#pragma unroll 1
    for (int it = 0; it < SPIN_MAX * 4; ++it) {
      u32x2 f;
      f32x4 d[8];
      asm volatile("ds_read_b64 %8, %17\n\tds_read_b128 %0, %9\n\tds_read_b128 %1, %10\n\tds_read_b128 %2, %11\n\tds_read_b128 %3, %12\n\t"
                   "ds_read_b128 %4, %13\n\tds_read_b128 %5, %14\n\tds_read_b128 %6, %15\n\tds_read_b128 %7, %16\n\ts_waitcnt lgkmcnt(0)"
                   : "=&v"(d[0]), "=&v"(d[1]), "=&v"(d[2]), "=&v"(d[3]), "=&v"(d[4]), "=&v"(d[5]), "=&v"(d[6]), "=&v"(d[7]), "=&v"(f)
                   : "v"(ra[0][0]), "v"(ra[0][1]), "v"(ra[0][2]), "v"(ra[0][3]), "v"(ra[1][0]), "v"(ra[1][1]), "v"(ra[1][2]), "v"(ra[1][3]), "v"(FL_XD) : "memory");
      if (__builtin_amdgcn_readfirstlane(f.x & f.y)) {
#pragma unroll
        for (int e = 0; e < 8; ++e) ov[e >> 2][e & 3] += d[e];
        break;
      }
    }
  }
#pragma unroll
  for (int r = 0; r < 4; ++r)
#pragma unroll
    for (int m = 0; m < 2; ++m) {
#pragma unroll
      for (int n = 0; n < 4; ++n) ov[m][r][n] = fma1(g2[m][r], ac2[m][n][r], ov[m][r][n]);
      asm volatile("s_nop 1\n\tbuffer_store_dwordx4 %0, %1, %2, %3 offen sc1 nt" : "+v"(ov[m][r]) : "v"(oo), "s"(ro), "s"(r * 32768 + m * 4096) : "memory");
      __builtin_amdgcn_sched_barrier(0);
    }
  asm volatile("s_nop 7\n\ts_nop 7" ::"v"(ov[0][0]), "v"(ov[0][1]), "v"(ov[0][2]), "v"(ov[0][3]), "v"(ov[1][0]), "v"(ov[1][1]), "v"(ov[1][2]), "v"(ov[1][3]));
  asm volatile("Lk_m:");
#endif
}

extern "C" void kernel_launch(void* const* d_in, const int* in_sizes, int n_in, void* d_out, int out_size, void* d_ws, size_t ws_size,
                              hipStream_t stream) {
  (void)hipFuncSetAttribute((const void*)fused, hipFuncAttributeMaxDynamicSharedMemorySize, LDS_BYTES);
  fused<<<256, 512, LDS_BYTES, stream>>>((const u16*)d_in[0], (const u16*)d_in[1], (const u16*)d_in[2], (const u16*)d_in[3], (const u16*)d_in[4],
                                         (const u16*)d_in[5], (const u16*)d_in[6], (const u16*)d_in[7], (const u16*)d_in[8], (const u16*)d_in[10],
                                         (const u16*)d_in[11], (const u16*)d_in[12], (const u16*)d_in[13], (const u16*)d_in[14], (char*)d_ws + WS_BASE(ws_size),
                                         (float*)d_out);
}
```
